```python
import torch

def _softmax(x, axis=-1):
    return torch.softmax(x, dim=axis)
def _gelu(x):
    return torch.nn.functional.gelu(x, approximate="tanh")
def _silu(x):
    return torch.nn.functional.silu(x)
def _sigmoid(x):
    return torch.sigmoid(x)
def _layernorm(x, axis=-1, eps=1e-5):
    m = x.mean(dim=axis, keepdim=True)
    v = x.var(dim=axis, keepdim=True, unbiased=False)
    return (x - m) / torch.sqrt(v + eps)
def _scatter_add(tgt, idx, src, axis=0):
    return tgt.clone().index_add_(axis, idx, src)
def _segment_sum(src, seg, n):
    out = torch.zeros((n,) + src.shape[1:], dtype=src.dtype)
    return out.index_add_(0, seg, src)
def _pad(x, pw):
    flat = tuple(v for pair in reversed(pw) for v in pair)
    return torch.nn.functional.pad(x, flat)

kernel_name = 'parallel_sum__sliding_local_dilated__moe_arch_mod__prompt'

def setup_inputs():

    _w = (lambda a: (lambda t: t.to(torch.bfloat16) if t.is_floating_point() else t)(torch.as_tensor(a)))
    def _A_setup():
        B, S, D = 1, 2048, 1024
        return {
            "x": _w((lambda *s: torch.randn(*s, dtype=torch.bfloat16) * 0.05)(B, S, D)),
            "wqkv": _w((lambda *s: torch.randn(*s, dtype=torch.bfloat16) * 0.05)(D, 3 * D)),
            "wo": _w((lambda *s: torch.randn(*s, dtype=torch.bfloat16) * 0.05)(D, D)),
        }
    _A_in = _A_setup()

    def _B_setup():
        B, S, D, F, C = 1, 2048, 1024, 3584, 8
        return {
            "x": _w((lambda *s: torch.randn(*s, dtype=torch.bfloat16) * 0.05)(B, S, D)),
            "w_r": _w((lambda *s: torch.randn(*s, dtype=torch.bfloat16) * 0.05)(D)),
            "w_gate": _w((lambda *s: torch.randn(*s, dtype=torch.bfloat16) * 0.05)(D, F)),
            "w_up": _w((lambda *s: torch.randn(*s, dtype=torch.bfloat16) * 0.05)(D, F)),
            "w_down": _w((lambda *s: torch.randn(*s, dtype=torch.bfloat16) * 0.05)(F, D)),
        }
    _B_in = _B_setup()

    return {
        "A_x": _A_in["x"],
        "A_wqkv": _A_in["wqkv"],
        "A_wo": _A_in["wo"],
        "B_x": _B_in["x"],
        "B_w_r": _B_in["w_r"],
        "B_w_gate": _B_in["w_gate"],
        "B_w_up": _B_in["w_up"],
        "B_w_down": _B_in["w_down"],
    }

def reference(A_x, A_wqkv, A_wo, B_x, B_w_r, B_w_gate, B_w_up, B_w_down):
    def _A_ref(x, wqkv, wo):
        B, S, D = x.shape
        H = 32; Dh = D // H
        W, dil = 8, 2
        qkv = (lambda x, *d: x.permute(*d))((x @ wqkv).reshape(B, S, 3, H, Dh), 2, 0, 3, 1, 4)
        q, k, v = qkv[0], qkv[1], qkv[2]
        scale = Dh ** -0.5
        pos = torch.arange(S)
        off = torch.arange(W) * dil
        idx = pos[:, None] - off[None, :]
        mask = idx >= 0
        idx_c = torch.where(mask, idx, 0).reshape(-1)
        kg = k[:, :, idx_c, :].reshape(B, H, S, W, Dh)
        vg = v[:, :, idx_c, :].reshape(B, H, S, W, Dh)
        logits = torch.einsum("bhsd,bhswd->bhsw", q, kg) * scale
        logits = torch.where(mask[None, None, :, :], logits, -1e9)
        attn = _softmax(logits, axis=-1)
        o = torch.einsum("bhsw,bhswd->bhsd", attn, vg)
        return (lambda x, *d: x.permute(*d))(o, 0, 2, 1, 3).reshape(B, S, D) @ wo

    def _B_ref(x, w_r, w_gate, w_up, w_down):
        B, S, D = x.shape
        C = 8
        r = x @ w_r
        idx = torch.argsort(-r, axis=-1)[:, :C]
        idx_e = (lambda x, n, axis: torch.repeat_interleave(x, n, dim=axis))(idx.reshape(B, C, 1), D, axis=2)
        x_sel = (lambda x, i, axis: torch.take_along_dim(x, i, dim=axis))(x, idx_e, axis=1)
        g = _silu(x_sel @ w_gate)
        u = x_sel @ w_up
        y = (g * u) @ w_down
        r_sel = _sigmoid((lambda x, i, axis: torch.take_along_dim(x, i, dim=axis))(r, idx, axis=1))
        y = y * r_sel.reshape(B, C, 1)
        b_off = (torch.arange(B) * S).reshape(B, 1)
        flat_idx = (idx + b_off).reshape(B * C)
        zero = (x * 0.0).reshape(B * S, D)
        upd = _scatter_add(zero, flat_idx, y.reshape(B * C, D), axis=0)
        return x + upd.reshape(B, S, D)

    x = A_x
    xn = _layernorm(x, axis=-1)
    return x + _A_ref(xn, A_wqkv, A_wo) + _B_ref(xn, B_w_r, B_w_gate, B_w_up, B_w_down)
```

```cpp
#include <hip/hip_runtime.h>
#include <hip/hip_bf16.h>
#include <stdint.h>

typedef __attribute__((ext_vector_type(8))) short bf16x8;
typedef __attribute__((ext_vector_type(4))) short s16x4;
typedef __attribute__((ext_vector_type(4))) float f32x4;
typedef __attribute__((ext_vector_type(2))) float f32x2;
typedef __attribute__((ext_vector_type(2))) __bf16 bf16x2_t;
typedef __attribute__((ext_vector_type(4))) unsigned u32x4;
typedef unsigned short u16;
#define LAS __attribute__((address_space(3)))
#define GAS __attribute__((address_space(1)))
#define RLX_AGENT __ATOMIC_RELAXED, __HIP_MEMORY_SCOPE_AGENT

#define S_TOK 2048
#define DM 1024
#define NQKV 3072
#define FF 3584

typedef __attribute__((ext_vector_type(2))) unsigned u32x2;

#define WS_EP   0
#define WS_R    4096
#define WS_K    (WS_R + 32768)
#define WS_OF   (WS_K + 8192)
#define WS_CS   (WS_OF + 4096)
#define WS_GU   (WS_CS + 16 * 2 * 16 * 1024)
#define WS_Y    (WS_GU + 56 * 4 * 8192)
#define WS_O    (8u << 20)

__device__ const unsigned k_tags[32] = {0,  1,  2,  3,  4,  5,  6,  7,  8,  9,  10, 11, 12, 13, 14, 15,
                                        16, 17, 18, 19, 20, 21, 22, 23, 24, 25, 26, 27, 28, 29, 30, 31};

__device__ __forceinline__ unsigned pack2(float a, float b) {
  f32x2 v = {a, b};
  bf16x2_t r = __builtin_convertvector(v, bf16x2_t);
  return __builtin_bit_cast(unsigned, r);
}
__device__ __forceinline__ float rbf(float f) { return __uint_as_float(pack2(f, f) << 16); }
__device__ __forceinline__ float lo2f(unsigned w) { return __uint_as_float(w << 16); }
__device__ __forceinline__ float hi2f(unsigned w) { return __uint_as_float(w & 0xffff0000u); }

__device__ __forceinline__ bf16x8 lds_b128(const char* p) { return *(const bf16x8*)p; }
__device__ __forceinline__ bf16x8 lds_tr2(const char* p) {
  s16x4 lo = __builtin_amdgcn_ds_read_tr16_b64_v4i16((LAS s16x4*)p);
  s16x4 hi = __builtin_amdgcn_ds_read_tr16_b64_v4i16((LAS s16x4*)(p + 512));
  return __builtin_shufflevector(lo, hi, 0, 1, 2, 3, 4, 5, 6, 7);
}
#define MFMA16(a, b, c) __builtin_amdgcn_mfma_f32_16x16x32_bf16(a, b, c, 0, 0, 0)
#define WAITV(n) asm volatile("s_waitcnt vmcnt(%0)" ::"i"(n) : "memory")
#define WAITL0() __builtin_amdgcn_s_waitcnt(0xC07F)
#define BAR() __builtin_amdgcn_s_barrier()
#define SCHED0() __builtin_amdgcn_sched_barrier(0)
#define NAP(n) asm volatile("s_sleep %0" ::"i"(n) : "memory")
__device__ __forceinline__ float xsum16_32(float v) {
  unsigned u = __float_as_uint(v);
  auto a = __builtin_amdgcn_permlane16_swap(u, u, false, false);
  v = __uint_as_float(a[0]) + __uint_as_float(a[1]);
  u = __float_as_uint(v);
  auto b = __builtin_amdgcn_permlane32_swap(u, u, false, false);
  return __uint_as_float(b[0]) + __uint_as_float(b[1]);
}
__device__ __forceinline__ bf16x8 pack8(const f32x4& a, const f32x4& b) {
  u32x4 r = {pack2(a[0], a[1]), pack2(a[2], a[3]), pack2(b[0], b[1]), pack2(b[2], b[3])};
  return __builtin_bit_cast(bf16x8, r);
}
__device__ __forceinline__ unsigned wave_max_u32(unsigned v) {
  v = max(v, (unsigned)__builtin_amdgcn_update_dpp(0, (int)v, 0x111, 0xf, 0xf, false));
  v = max(v, (unsigned)__builtin_amdgcn_update_dpp(0, (int)v, 0x112, 0xf, 0xf, false));
  v = max(v, (unsigned)__builtin_amdgcn_update_dpp(0, (int)v, 0x114, 0xf, 0xf, false));
  v = max(v, (unsigned)__builtin_amdgcn_update_dpp(0, (int)v, 0x118, 0xf, 0xf, false));
  v = max(v, (unsigned)__builtin_amdgcn_update_dpp(0, (int)v, 0x142, 0xa, 0xf, false));
  v = max(v, (unsigned)__builtin_amdgcn_update_dpp(0, (int)v, 0x143, 0xc, 0xf, false));
  return (unsigned)__builtin_amdgcn_readlane((int)v, 63);
}
__device__ __forceinline__ bf16x8 tr_frag(const u32x4 (&w)[8], int i) {
  const unsigned sel = (i & 1) ? 0x07060302u : 0x05040100u;
  u32x4 r = {__builtin_amdgcn_perm(w[1][i >> 1], w[0][i >> 1], sel), __builtin_amdgcn_perm(w[3][i >> 1], w[2][i >> 1], sel),
             __builtin_amdgcn_perm(w[5][i >> 1], w[4][i >> 1], sel), __builtin_amdgcn_perm(w[7][i >> 1], w[6][i >> 1], sel)};
  return __builtin_bit_cast(bf16x8, r);
}

#define SLOT 43008
#define XB 18432
#define NBAR 14
#define L_WO 20224
#define L_XCH (L_WO + 131072)
#define L_HF (L_XCH + 6144)
#define L_UP (L_HF + 1024)
#define L_CSF (L_UP + 2048)
#define L_QST (L_CSF + 1536)
#define L_FLG (L_QST + 1152)
#define L_SIZE (L_FLG + 256)
#define F_FULL (L_FLG + 0)
#define F_ST (L_FLG + 8)
#define F_WO (L_FLG + 16)
#define F_FREE (L_FLG + 32)
#define F_XCH (L_FLG + 40)
#define F_ODONE (L_FLG + 48)
#define F_SELOK (L_FLG + 52)
#define F_GO (L_FLG + 56)
#define F_OL (L_FLG + 60)
#define F_H (L_FLG + 64)
#define F_TAG (L_FLG + 68)
#define F_UP (L_FLG + 72)
#define F_SEL (L_FLG + 96)
#define OD 7
#define SPIN_CAP (1u << 18)
#define WR 54
#define LF 20
#define NW 3
#define TW0 3
#define CODE_BYTES 53440

__device__ __forceinline__ unsigned lds_addr(const char* p) { return (unsigned)(size_t)(LAS const char*)p; }
__device__ __forceinline__ void lds_post(char* smem, int off, unsigned v) {
  asm volatile("s_mov_b64 exec, 1\n\tds_write_b8 %0, %1\n\ts_mov_b64 exec, -1" ::"v"(lds_addr(smem + off)), "v"(v) : "memory");
}
__device__ __forceinline__ unsigned lds_word(const char* smem, int off) { return *(const volatile LAS unsigned*)(LAS const char*)(smem + off); }
__device__ __forceinline__ u32x4 lds_word4(const char* smem, int off) { return *(const volatile LAS u32x4*)(LAS const char*)(smem + off); }
template <unsigned MASK, bool COND = false>
__device__ __forceinline__ unsigned wait_bytes(const char* smem, int off, unsigned need, unsigned skip = 0) {
  unsigned t, f;
  asm volatile(
      ".if %6\n\ts_cmp_lg_u32 %5, 0\n\ts_cbranch_scc1 2f\n\t.endif\n\t"
      "1:\n\t"
      "ds_read_b32 %1, %2\n\t"
      "s_waitcnt lgkmcnt(0)\n\t"
      "v_readfirstlane_b32 %0, %1\n\t"
      "s_or_b32 %0, %0, 0x80808080\n\t"
      "s_sub_u32 %0, %0, %3\n\t"
      "s_and_b32 %0, %0, %4\n\t"
      "s_cmp_eq_u32 %0, %4\n\t"
      "s_cbranch_scc1 2f\n\t"
      "s_sleep 1\n\t"
      "s_branch 1b\n\t"
      "2:"
      : "=&s"(t), "=&v"(f)
      : "v"(lds_addr(smem + off)), "s"(need * 0x01010101u), "s"(MASK), "s"(COND ? skip : MASK), "i"((int)COND)
      : "scc", "memory");
  return f;
}
__device__ __forceinline__ void wait_full(const char* smem, int off, unsigned need, unsigned cur = 0) {
  unsigned a, b;
  asm volatile(
      "s_cmp_ge_u32 %0, %4\n\t"
      "s_cbranch_scc1 3f\n\t"
      "s_branch 2f\n\t"
      "1:\n\t"
      "s_sleep 1\n\t"
      "2:\n\t"
      "ds_read_b32 %1, %3\n\t"
      "ds_read_b32 %2, %3 offset:4\n\t"
      "s_waitcnt lgkmcnt(0)\n\t"
      "v_min_u32 %1, %1, %2\n\t"
      "s_nop 0\n\t"
      "v_readfirstlane_b32 %0, %1\n\t"
      "s_cmp_lt_u32 %0, %4\n\t"
      "s_cbranch_scc1 1b\n\t"
      "3:"
      : "+s"(cur), "=&v"(a), "=&v"(b)
      : "v"(lds_addr(smem + off)), "s"(need)
      : "scc", "memory");
}
__device__ __forceinline__ unsigned uni(unsigned v) { return (unsigned)__builtin_amdgcn_readfirstlane((int)v); }
__device__ __forceinline__ void dma_tag(char* smem, int off, int n) {
  unsigned keep;
  asm volatile("s_nop 2\n\ts_mov_b32 %0, m0\n\ts_mov_b32 m0, %1\n\ts_mov_b64 exec, 1\n\tglobal_load_lds_dword %2, %3\n\ts_mov_b64 exec, -1\n\ts_mov_b32 m0, %0\n\ts_nop 0"
               : "=&s"(keep) : "s"(uni(lds_addr(smem + off))), "v"(n * 4), "s"(k_tags) : "memory");
}
struct QFrag { bf16x8 xa[5]; bf16x8 w[3][2]; };
__device__ __forceinline__ void q_read(QFrag& f, const char* sa, const char* sb0, const char* sb1, int ks) {
#pragma unroll
  for (int i = 0; i < 5; ++i) f.xa[i] = lds_b128(sa + i * 2048);
#pragma unroll
  for (int s = 0; s < 3; ++s) {
    f.w[s][0] = lds_tr2(sb0 + s * 8192 + ks * 4096);
    f.w[s][1] = lds_tr2(sb1 + s * 8192 + ks * 4096);
  }
}
__device__ constexpr int acc_no(int HALF, int m, int i, int t) {
  int n = 0;
  for (int ii = 0; ii < 5; ++ii)
    for (int tt = 0; tt < 2; ++tt)
      for (int mm = 0; mm < 3; ++mm) {
        const bool on = mm == 0 ? (HALF == 1 || (ii >= 1 && ii <= 3)) : (HALF == 0 || ii >= 1);
        if (mm == m && ii == i && tt == t) return on ? n : -1;
        if (on) ++n;
      }
  return -1;
}
__device__ constexpr int seq_of(int HALF, int z) {
  int n = 0;
  for (int a = 0; a < 30; ++a) {
    const int m = a / 10, t0 = (a / 5) % 2;
    const int i = m < 2 ? (t0 ? a % 5 : 4 - a % 5) : 4 - (a - 20) / 2, t = m < 2 ? t0 : (a ^ i) & 1;
    if (acc_no(HALF, m, i, t) >= 0) { if (n == z) return m * 100 + i * 10 + t; ++n; }
  }
  return -1;
}
template <int HALF, int Z, int E, bool FIRST>
__device__ __forceinline__ void q_one(const QFrag& f, f32x4 (&acc)[26]) {
  if constexpr (Z < E) {
    constexpr int m = seq_of(HALF, Z) / 100, i = seq_of(HALF, Z) / 10 % 10, t = seq_of(HALF, Z) % 10;
    constexpr int n = acc_no(HALF, m, i, t);
    const f32x4 c = FIRST ? f32x4{0, 0, 0, 0} : acc[n];
    acc[n] = m == 2 ? MFMA16(f.xa[i], f.w[2][t], c) : MFMA16(f.w[m][t], f.xa[i], c);
    q_one<HALF, Z + 1, E, FIRST>(f, acc);
  }
}
template <int HALF, int B, int E, int NRD, bool FIRST = false>
__device__ __forceinline__ void q_mma(const QFrag& f, f32x4 (&acc)[26]) {
  q_one<HALF, B, E, FIRST>(f, acc);
#pragma unroll
  for (int i = 0; i < NRD; ++i) {
    __builtin_amdgcn_sched_group_barrier(0x008, 1, 0);
    __builtin_amdgcn_sched_group_barrier(0x100, 1, 0);
  }
  __builtin_amdgcn_sched_group_barrier(0x008, E - B - NRD, 0);
}

__device__ __forceinline__ void tr_all(u32x4 (&w)[8]) {
  u32x4 t[8];
#pragma unroll
  for (int i = 0; i < 8; ++i) t[i] = __builtin_bit_cast(u32x4, tr_frag(w, i));
#pragma unroll
  for (int i = 0; i < 8; ++i) w[i] = t[i];
  asm volatile("" : "+v"(w[0]), "+v"(w[1]), "+v"(w[2]), "+v"(w[3]), "+v"(w[4]), "+v"(w[5]), "+v"(w[6]), "+v"(w[7]));
}
typedef __amdgpu_buffer_rsrc_t rsrc_t;
__device__ __forceinline__ rsrc_t mk_rsrc(const void* p, unsigned bytes) { return __builtin_amdgcn_make_buffer_rsrc((void*)p, 0, bytes, 0x00020000); }
__device__ __forceinline__ u32x4 ld16(rsrc_t rs, unsigned voff, unsigned soff) { return __builtin_amdgcn_raw_buffer_load_b128(rs, voff, soff, 0); }
__device__ __forceinline__ u32x4 ld16c(rsrc_t rs, unsigned voff, unsigned soff) { return __builtin_amdgcn_raw_buffer_load_b128(rs, voff, soff, 16); }
__device__ __forceinline__ void st16c(rsrc_t rs, unsigned voff, u32x4 v) { __builtin_amdgcn_raw_buffer_store_b128(v, rs, voff, 0, 16); }
__device__ __forceinline__ void st8c(rsrc_t rs, unsigned voff, unsigned v, unsigned tag) { __builtin_amdgcn_raw_buffer_store_b64(u32x2{v, tag}, rs, voff, 0, 16); }
__device__ __forceinline__ unsigned chk(unsigned bad, const u32x4& v, unsigned tag) {
  return __builtin_amdgcn_bitop3_b32(__builtin_amdgcn_bitop3_b32(bad, v[1], tag, 0xF6), v[3], tag, 0xF6);
}
__device__ __forceinline__ float f_of(unsigned u) { return __uint_as_float(u); }
#define LANE_VARS                                                                   \
  int lane = lane_in; asm volatile("" : "+v"(lane));                                \
  const int g = lane >> 4, r16 = lane & 15, q = (lane >> 2) & 3, p = lane & 3;      \
  const int hk = (q >> 1) | ((g & 1) << 1), brow = (8 * g + q) * 128 + 8 * p;       \
  (void)r16; (void)hk; (void)brow;

template <bool MAX>
__device__ __forceinline__ float rows_reduce(const float (&v)[4]) {
  auto op = [](unsigned a, unsigned b) {
    return __float_as_uint(MAX ? __builtin_elementwise_maximum(__uint_as_float(a), __uint_as_float(b)) : __uint_as_float(a) + __uint_as_float(b));
  };
  auto p = __builtin_amdgcn_permlane16_swap(__float_as_uint(v[0]), __float_as_uint(v[1]), false, false);
  auto q = __builtin_amdgcn_permlane16_swap(__float_as_uint(v[2]), __float_as_uint(v[3]), false, false);
  auto t = __builtin_amdgcn_permlane32_swap(op(p[0], p[1]), op(q[0], q[1]), false, false);
  return __uint_as_float(op(t[0], t[1]));
}
__device__ __forceinline__ void rows_spread(float t, float (&v)[4]) {
  auto h = __builtin_amdgcn_permlane32_swap(__float_as_uint(t), __float_as_uint(t), false, false);
  auto p = __builtin_amdgcn_permlane16_swap(h[0], h[0], false, false);
  auto q = __builtin_amdgcn_permlane16_swap(h[1], h[1], false, false);
  v[0] = __uint_as_float(p[0]); v[1] = __uint_as_float(p[1]); v[2] = __uint_as_float(q[0]); v[3] = __uint_as_float(q[1]);
}
typedef __attribute__((ext_vector_type(16))) unsigned u32x16;

template <int CTL>
__device__ __forceinline__ float dpp_f(float v) { return __uint_as_float(__builtin_amdgcn_update_dpp(0, (int)__float_as_uint(v), CTL, 0xf, 0xf, false)); }
__device__ __forceinline__ float sum16(float v) {
  v += dpp_f<0xB1>(v);
  v += dpp_f<0x4E>(v);
  v += dpp_f<0x141>(v);
  return v + dpp_f<0x140>(v);
}
__device__ __forceinline__ float dot2(unsigned a, unsigned b, float c) {
  return __builtin_amdgcn_fdot2_f32_bf16(__builtin_bit_cast(bf16x2_t, a), __builtin_bit_cast(bf16x2_t, b), c, false);
}
__device__ __forceinline__ float sum32(float v) {
  v = sum16(v);
  auto a = __builtin_amdgcn_permlane16_swap(__float_as_uint(v), __float_as_uint(v), false, false);
  return __uint_as_float(a[0]) + __uint_as_float(a[1]);
}
struct RowRegs { u32x4 x[4], w[2]; unsigned ep, keep; };
__device__ __forceinline__ void row_request(RowRegs& r, const u16* __restrict__ x, const u16* __restrict__ wr, char* ws, int row0, int lane, bool on) {
  if (!on) return;
  const rsrc_t xrs = mk_rsrc(x, S_TOK * DM * 2), rrs = mk_rsrc(wr, DM * 2);
  const unsigned xo = (unsigned)(row0 + (lane >> 5)) * 2048u + (lane & 31) * 16, wo = lane * 16;
  asm volatile("buffer_load_dword %0, off, %1, 0 sc1" : "=v"(r.ep) : "s"(mk_rsrc(ws + WS_EP, 4)) : "memory");
#pragma unroll
  for (int j = 0; j < 4; ++j) asm volatile("buffer_load_dwordx4 %0, %1, %2, 0 offen offset:%3" : "=v"(r.x[j]) : "v"(xo), "s"(xrs), "i"(j * 512) : "memory");
#pragma unroll
  for (int j = 0; j < 2; ++j) asm volatile("buffer_load_dwordx4 %0, %1, %2, 0 offen offset:%3" : "=v"(r.w[j]) : "v"(wo), "s"(rrs), "i"(j * 1024) : "memory");
}
__device__ __forceinline__ unsigned row_stats(RowRegs& r, char* ws, int row0, int lane) {
  float s0 = 0.f, s1 = 0.f;
  asm volatile("s_waitcnt vmcnt(2)" : "+v"(r.x[0]), "+v"(r.x[1]), "+v"(r.x[2]), "+v"(r.x[3]), "+v"(r.ep), "+v"(r.keep) :: "memory");
  const unsigned tag = uni(r.ep) + 1u;
#pragma unroll
  for (int j = 0; j < 4; ++j) {
    s0 = dot2(r.x[j][0], 0x3f803f80u, s0); s1 = dot2(r.x[j][1], 0x3f803f80u, s1);
    s0 = dot2(r.x[j][2], 0x3f803f80u, s0); s1 = dot2(r.x[j][3], 0x3f803f80u, s1);
  }
  const float mean = sum32(s0 + s1) * (1.0f / DM), mb = rbf(mean);
  s0 = 0.f; s1 = 0.f;
#pragma unroll
  for (int j = 0; j < 4; ++j)
#pragma unroll
    for (int i = 0; i < 4; ++i) {
      const float a = lo2f(r.x[j][i]) - mb, b = hi2f(r.x[j][i]) - mb;
      s0 = fmaf(a, a, s0); s1 = fmaf(b, b, s1);
      r.x[j][i] = pack2(a, b);
    }
  const float dm = mean - mb;
  const float var = rbf(fmaxf(sum32(s0 + s1) * (1.0f / DM) - dm * dm, 0.f));
  const float den = rbf(__builtin_amdgcn_sqrtf(rbf(var + 1e-5f))), rd = __builtin_amdgcn_rcpf(den);
  s0 = 0.f; s1 = 0.f;
  asm volatile("s_waitcnt vmcnt(0)" : "+v"(r.w[0]), "+v"(r.w[1]) :: "memory");
#pragma unroll
  for (int j = 0; j < 4; ++j)
#pragma unroll
    for (int i = 0; i < 4; ++i) {
      const unsigned n = pack2(lo2f(r.x[j][i]) * rd, hi2f(r.x[j][i]) * rd);
      const unsigned w = __builtin_amdgcn_permlane32_swap(r.w[j >> 1][i], r.w[j >> 1][i], false, false)[j & 1];
      if (i & 1) s1 = dot2(n, w, s1); else s0 = dot2(n, w, s0);
    }
  const float dot = sum32(s0 + s1);
  unsigned k = pack2(dot, dot) & 0xffffu;
  k = (k & 0x8000u) ? (~k & 0xffffu) : (k | 0x8000u);
  const rsrc_t wrs = mk_rsrc(ws, WS_O);
  st16c(wrs, (lane & 31) == 0 ? WS_R + (unsigned)(row0 + (lane >> 5)) * 16u : 0x7fffffffu,
        u32x4{(k << 16) | (__float_as_uint(mb) >> 16), tag, __float_as_uint(den), tag});
  const unsigned k01 = k | ((unsigned)__builtin_amdgcn_readlane((int)k, 32) << 16);
  st8c(wrs, lane == 0 ? WS_K + (unsigned)(((row0 >> 2) & 7) * 1024 + (row0 >> 5) * 16 + (row0 & 2) * 4) : 0x7fffffffu, k01, tag);
  return tag;
}
__device__ __forceinline__ void k_order(int tm, int tn, int& pa, int& pb) {
  pb = tn & 3;
  pa = (tm * 0x1111) ^ ((tn & 4) * 0x51 & 0x144) ^ ((~tn & 2) * 0x2200) ^ ((~tn & 1) << 9) ^ (pb == 3 ? 1 : tm & 1);
}
__device__ __forceinline__ int k_tile(int t, int pa, int pb) { return ((((t + pb) & 12) ? t + pb : t & 3) ^ (pa >> ((t + pb) & 12))) & 15; }
struct EpiRegs { int a0, b0, tx; unsigned xr, go; u32x4 xv[2][2]; unsigned t[16]; };
__device__ __forceinline__ float opq(float v) { asm("" : "+v"(v)); return v; }
typedef __attribute__((ext_vector_type(10))) unsigned u32x10;
#define GRP(N, M, K, C) _Pragma("unroll") for (int z_ = 0; z_ < (N); ++z_) { __builtin_amdgcn_sched_group_barrier(0x008, M, 0); __builtin_amdgcn_sched_group_barrier(K, C, 0); }
__device__ constexpr int tail_of(int HALF, int z) {
  int n = 0;
  for (int a = 0; a < 30; ++a) {
    const int i = HALF ? a / 6 : 4 - a / 6, m = 2 - a % 6 / 2, t = a & 1;
    if (acc_no(HALF, m, i, t) >= 0 && (m < 2 || (HALF == 0 && i == 4))) { if (n == z) return m * 100 + i * 10 + t; ++n; }
  }
  return -1;
}
__device__ __forceinline__ void qkv_setup(char* smem, int wid, int lane_in, int tm, int tn, EpiRegs& e) {
  LANE_VARS
  e.a0 = (wid & 1) * 8192 + r16 * 128 + ((g ^ (r16 >> 1)) << 4);
  e.b0 = XB + brow + (((wid & 2) ^ hk) << 5);
  int pa, pb;
  k_order(tm, tn, pa, pb);
  e.tx = __builtin_ctz((unsigned)__ballot(k_tile(r16, pa, pb) == tn));
  e.xr = lds_addr(smem) + (16 + wid * 32 + r16) * 128 + ((((g & 1) * 2 + (g >> 1)) ^ (r16 >> 1)) << 4);
  asm volatile("" : "+v"(e.a0), "+v"(e.b0), "+v"(e.xr));
  asm volatile("" : "+s"(e.tx));
}
template <int HALF>
__device__ __forceinline__ void qkv_attention(char* smem, int wid, int lane_in, int tm, int tn, char* ws, unsigned tag,
                                              const u16* __restrict__ x, EpiRegs& e) {
  const int h = wid >> 1;
  f32x4 acc[26];
  float ra[5], rb[5], sa[4], sb[4];
  QFrag f0, f1;
  u32x4 fl;
  unsigned aq, ac, aa, ab, xa, xv, xf, ooff, sel[4];
  {
    LANE_VARS
    const int a0 = e.a0, b0 = e.b0, tx = uni(e.tx);
    const unsigned xr = e.xr;
    asm volatile("" : "=v"(e.xv[0][0]), "=v"(e.xv[0][1]), "=v"(e.xv[1][0]), "=v"(e.xv[1][1]));
    auto keep = [&](int t, int slot, int tx) {
      unsigned p0, p1;
      asm volatile("s_cmp_lg_u32 %7, %8\n\ts_cbranch_scc1 1f\n\tv_add_u32 %4, %9, %6\n\tv_xor_b32 %5, 64, %4\n\tds_read_b128 %0, %4\n\tds_read_b128 %1, %5\n\t"
                   "ds_read_b128 %2, %4 offset:2048\n\tds_read_b128 %3, %5 offset:2048\n\t1:"
                   : "+v"(e.xv[0][0]), "+v"(e.xv[0][1]), "+v"(e.xv[1][0]), "+v"(e.xv[1][1]), "=&v"(p0), "=&v"(p1) : "v"(xr), "s"(t), "s"(tx), "s"(slot) : "scc", "memory");
    };
    SCHED0();
    WAITL0();
    BAR();
    q_read(f0, smem + a0, smem + b0, smem + (b0 ^ 32), 0);
    SCHED0();
    q_read(f1, smem + (a0 ^ 64), smem + b0, smem + (b0 ^ 32), 1);
    q_mma<HALF, 0, 26, 17, true>(f0, acc);
    SCHED0();
    keep(0, 0, tx);
    WAITL0();
    BAR();
    int so = SLOT;
#pragma unroll 1
    for (int t = 1; t < NBAR - 1; ++t) {
      SCHED0();
      q_read(f0, smem + so + a0, smem + so + b0, smem + so + (b0 ^ 32), 0);
      q_mma<HALF, 0, 26, 17>(f1, acc);
      SCHED0();
      q_read(f1, smem + so + (a0 ^ 64), smem + so + b0, smem + so + (b0 ^ 32), 1);
      q_mma<HALF, 0, 26, 17>(f0, acc);
      SCHED0();
      keep(t, so, tx);
      so = (so == 2 * SLOT) ? 0 : so + SLOT;
      WAITL0();
      BAR();
    }
    int qa = so + a0, qc = so + (a0 ^ 64), qb = so + b0, qd = so + (b0 ^ 32);
    asm volatile("" : "+v"(qa), "+v"(qb), "+v"(qc), "+v"(qd));
#pragma unroll 1
    for (int t = NBAR + 1; t < 17; ) {
      SCHED0();
      q_read(f0, smem + qa, smem + qb, smem + qd, 0);
      q_mma<HALF, 0, 26, 17>(f1, acc);
      SCHED0();
      q_read(f1, smem + qc, smem + qb, smem + qd, 1);
      const u32x4 fl = lds_word4(smem, F_FULL);
      q_mma<HALF, 0, 21, 18>(f0, acc);
      SCHED0();
      const unsigned cur = uni(min(fl[0], fl[1])), need = t;
      SCHED0();
      q_mma<HALF, 21, 22, 0>(f0, acc);
      SCHED0();
      keep(t, so, tx + 2);
      SCHED0();
      qa += SLOT; qb += SLOT; qc += SLOT; qd += SLOT; so += SLOT; ++t;
      q_one<HALF, 22, 25, false>(f0, acc);
      GRP(2, 1, 0x002, 2);
      asm volatile("" : "+v"(qa), "+v"(qb), "+v"(qc), "+v"(qd), "+s"(so), "+s"(t));
      SCHED0();
      q_mma<HALF, 25, 26, 0>(f0, acc);
      SCHED0();
      wait_full(smem, F_FULL, need, cur);
    }
    SCHED0();
    q_read(f0, smem + a0, smem + b0, smem + (b0 ^ 32), 0);
    q_one<HALF, 0, 26, false>(f1, acc);
    {
      const int pa = 4 * g + (r16 & 1);
      const bool odd = r16 & 1, cur_a = pa <= r16, cur_b = pa + 2 <= r16;
      const unsigned qs = lds_addr(smem + L_QST + HALF * 512);
      aq = qs + r16 * 8; ac = lds_addr(smem + L_CSF + h * 256) + r16 * 8;
      aa = qs + pa * 8 + (cur_a ? 128 : 0); ab = qs + pa * 8 + (cur_b ? 128 : 0);
      xa = lds_addr(smem + L_XCH + h * 3072) + lane * 16; xv = xa + 2048 - lane * 8; xf = lds_addr(smem + F_XCH + h * 4);
      const unsigned la = odd ? 0x01000c0cu : 0x0c0c0100u, lb = odd ? 0x03020c0cu : 0x0c0c0302u;
      sel[0] = cur_a ? 0x0c0c0c0cu : la; sel[1] = cur_b ? 0x0c0c0c0cu : lb; sel[2] = cur_a ? la : 0x0c0c0c0cu; sel[3] = cur_b ? lb : 0x0c0c0c0cu;
      ooff = (unsigned)(tm * 8 + HALF * 4) * 32768u + (tn * 2 + h) * 1024 + lane * 16;
#pragma unroll
      for (int z = 0; z < 17; ++z) { GRP(1, 1, 0x100, 1); __builtin_amdgcn_sched_group_barrier(0x002, 1, 0); }
      GRP(9, 1, 0x002, 3);
      asm volatile("" : "+v"(aq), "+v"(ac), "+v"(aa), "+v"(ab), "+v"(xa), "+v"(xv), "+v"(xf), "+v"(ooff));
      asm volatile("" : "+v"(sel[0]), "+v"(sel[1]), "+v"(sel[2]), "+v"(sel[3]));
    }
    SCHED0();
#pragma unroll
    for (int i = 0; i < 5; ++i) f1.xa[i] = lds_b128(smem + (a0 ^ 64) + i * 2048);
#pragma unroll
    for (int s = 0; s < 3; ++s) {
      if (s == 2) fl = lds_word4(smem, F_FULL);
      f1.w[s][0] = lds_tr2(smem + b0 + s * 8192 + 4096);
      f1.w[s][1] = lds_tr2(smem + (b0 ^ 32) + s * 8192 + 4096);
    }
    q_mma<HALF, 0, 26 - 0, 18>(f0, acc);
    SCHED0();
    keep(15, 0, tx);
    SCHED0();
  }
  LANE_VARS
  auto mm = [&](const QFrag& f, int m, int i, int t) {
    const int n = max(acc_no(HALF, m, i, t), 0);
    acc[n] = m == 2 ? MFMA16(f.xa[i], f.w[2][t], acc[n]) : MFMA16(f.w[m][t], f.xa[i], acc[n]);
  };
  auto last = [&](int z0, int z1) {
#pragma unroll
    for (int z = z0; z < z1; ++z) {
      const int c = tail_of(HALF, HALF ? z : z < 6 ? z + 2 : z < 8 ? z - 6 : z);
      mm(f1, c / 100, c / 10 % 10, c % 10);
    }
  };
  last(0, 3);
  SCHED0();
  const unsigned cur = uni(min(fl[2], fl[3]));
  SCHED0();
  last(3, 3 + 1);
  SCHED0();
  wait_full(smem, F_ST, 1, cur);
  SCHED0();
  u32x2 sq[5], cs[3][2];
#pragma unroll
  for (int i = 0; i < 5; ++i) sq[i] = *(const LAS u32x2*)(aq + i * 128);
#pragma unroll
  for (int s = 0; s < 3; ++s)
#pragma unroll
    for (int t = 0; t < 2; ++t) cs[s][t] = *(const LAS u32x2*)(ac + s * 512 + t * 128);
#pragma unroll
  for (int i = 1; i < 5; ++i) { ra[i] = *(const LAS float*)(aa + (i - 1) * 128 + 4); rb[i] = *(const LAS float*)(ab + (i - 1) * 128 + 20); }
  last(3 + 1, 3 + 5);
  __builtin_amdgcn_sched_group_barrier(0x100, 3, 0); GRP(3, 1, 0x100, 3);
  SCHED0();
  lds_post(smem, F_FREE + wid, 16);
  SCHED0();
  last(3 + 5, 18);
  const unsigned gm = g == 0 ? ~0u : 0u;
  u32x2 lx[5];
#pragma unroll
  for (int i = 0; i < 5; ++i) lx[i] = u32x2{sq[i][0] & gm, sq[i][0] & gm & 0xffffu};
#pragma unroll
  for (int i = 1; i < 5; ++i) {
    const float c = f_of(sq[i][1]) * (0.17677669529663687f * 1.4426950408889634f);
    sa[i - 1] = opq(c * ra[i]); sb[i - 1] = opq(c * rb[i]);
  }
  __builtin_amdgcn_sched_group_barrier(0x008, 5 - 3, 0); GRP(8, 1, 0x002, 3);
  asm volatile("" : "+v"(lx[0]), "+v"(lx[1]), "+v"(lx[2]), "+v"(lx[3]), "+v"(lx[4]));
  asm volatile("" : "+v"(sa[0]), "+v"(sa[1]), "+v"(sa[2]), "+v"(sa[3]), "+v"(sb[0]), "+v"(sb[1]), "+v"(sb[2]), "+v"(sb[3]));
  SCHED0();
  auto ln = [&](int m, int i, int t) {
    const int n = max(acc_no(HALF, m, i, t), 0);
    const s16x4 lxi = __builtin_bit_cast(s16x4, lx[i]), lw = __builtin_bit_cast(s16x4, cs[m][t]);
    acc[n] = m == 2 ? __builtin_amdgcn_mfma_f32_16x16x16bf16_1k(lxi, lw, acc[n], 0, 0, 0) : __builtin_amdgcn_mfma_f32_16x16x16bf16_1k(lw, lxi, acc[n], 0, 0, 0);
  };
#pragma unroll
  for (int z = 0; z < 18; ++z) ln(tail_of(HALF, z) / 100, tail_of(HALF, z) / 10 % 10, tail_of(HALF, z) % 10);
  bf16x8 qf[5], kf[5];
  u32x10 vf[2];
  auto pack_qk = [&](int i) {
    if (acc_no(HALF, 0, i, 0) >= 0) qf[i] = pack8(acc[max(acc_no(HALF, 0, i, 0), 0)], acc[max(acc_no(HALF, 0, i, 1), 0)]);
    if (acc_no(HALF, 1, i, 0) >= 0) kf[i] = pack8(acc[max(acc_no(HALF, 1, i, 0), 0)], acc[max(acc_no(HALF, 1, i, 1), 0)]);
  };
  auto pack_v = [&](int i, int t) {
    const f32x4& v = acc[max(acc_no(HALF, 2, i, t), 0)];
    return u32x2{pack2(v[0], v[1]), pack2(v[2], v[3])};
  };
  u32x2 vx[2];
  pack_qk(HALF == 0 ? 4 : 0);
  if (HALF == 0) {
    vx[0] = pack_v(4, 0); vx[1] = pack_v(4, 1);
    asm volatile("ds_write_b128 %0, %2 offset:1024\n\tds_write2st64_b64 %1, %3, %4 offset1:1" :: "v"(xa), "v"(xv), "v"(kf[4]), "v"(vx[0]), "v"(vx[1]) : "memory");
  } else {
    asm volatile("ds_write_b128 %0, %1" :: "v"(xa), "v"(qf[0]) : "memory");
  }
  asm volatile("s_mov_b64 exec, 1\n\tds_write_b8 %0, %0 offset:%1\n\ts_mov_b64 exec, -1" ::"v"(xf), "i"(HALF) : "memory");
  static_assert((F_XCH & 0xf) != 0 && ((F_XCH + 4) & 0xf) != 0, "the low byte of the address is the flag");
#pragma unroll
  for (int i = 0; i < 4; ++i) pack_qk(HALF == 0 ? 3 - i : i + 1);
  const f32x4 z4 = {0.f, 0.f, 0.f, 0.f};
  f32x4 sp[4], sc[4];
#pragma unroll
  for (int j = 0; j < 3; ++j) {
    const int i = HALF == 0 ? 3 - j : j + 2;
    sc[i - 1] = MFMA16(kf[i], qf[i], z4); sp[i - 1] = MFMA16(kf[i - 1], qf[i], z4);
  }
  if (HALF) sc[0] = MFMA16(kf[1], qf[1], z4);
  GRP(1, 6, 0x002, 4); GRP(12, 1, 0x002, 2); GRP(HALF ? 7 : 6, 1, 0x002, 3);
  SCHED0();
  unsigned flag;
  bf16x8 xq;
  if (HALF == 0) asm volatile("ds_read_b32 %0, %2\n\tds_read_b128 %1, %3" : "=&v"(flag), "=&v"(xq) : "v"(xf), "v"(xa) : "memory");
  else asm volatile("ds_read_b32 %0, %4\n\tds_read_b128 %1, %5 offset:1024\n\tds_read_b64 %2, %6\n\tds_read_b64 %3, %6 offset:512"
                    : "=&v"(flag), "=&v"(xq), "=&v"(vx[0]), "=&v"(vx[1]) : "v"(xf), "v"(xa), "v"(xv) : "memory");
  SCHED0();
  const bool odd = r16 & 1, cur_a = sel[0] == 0x0c0c0c0cu, cur_b = sel[1] == 0x0c0c0c0cu;
  float a[4], b[4], m[4];
  auto logits = [&](int j) {
    const f32x4 p4 = sp[j], c4 = sc[j];
    const float p0 = p4[0], p1 = p4[1], p2 = p4[2], p3 = p4[3], c0 = c4[0], c1 = c4[1], c2 = c4[2], c3 = c4[3];
    const float ca = odd ? c1 : c0, cb = odd ? c3 : c2, pa_ = odd ? p1 : p0, pb = odd ? p3 : p2;
    a[j] = opq((cur_a ? ca : pa_) * sa[j]);
    b[j] = opq((cur_b ? cb : pb) * sb[j]);
    if (HALF == 0 && j == 0) {
      a[j] = (!cur_a && tm == 0) ? -INFINITY : a[j];
      b[j] = (!cur_b && tm == 0) ? -INFINITY : b[j];
    }
    m[j] = __builtin_elementwise_maximum(a[j], b[j]);
  };
  auto late = [&](int z0, int z1) {
#pragma unroll
    for (int y = z0; y < z1; ++y) {
      const int c = seq_of(HALF, 18 + ((y + 8 - 0) & 7));
      if (y < 8 + 0) mm(y < 0 ? f0 : f1, 2, c / 10 % 10, c % 10);
    }
  };
#pragma unroll
  for (int j = 0; j < 2; ++j) logits(HALF == 0 ? 2 - j : j + 1);
  late(0, 6);
  GRP(6, 1, 0x002, 3);
  asm volatile("" : "+v"(m[1]), "+v"(m[2]));
  SCHED0();
  {
    unsigned t;
#define XWAIT(RD) "s_waitcnt lgkmcnt(0)\n\tv_readfirstlane_b32 %0, %1\n\ts_and_b32 %0, %0, %[mask]\n\ts_cbranch_scc1 2f\n\t1:\n\t" RD \
                  "\n\ts_waitcnt lgkmcnt(0)\n\tv_readfirstlane_b32 %0, %1\n\ts_and_b32 %0, %0, %[mask]\n\ts_cbranch_scc0 1b\n\t2:"
    if (HALF == 0) asm volatile(XWAIT("ds_read_b32 %1, %3\n\tds_read_b128 %2, %4") : "=&s"(t), "+v"(flag), "+v"(xq) : "v"(xf), "v"(xa), [mask] "s"(0xff00u) : "scc", "memory");
    else asm volatile(XWAIT("ds_read_b32 %1, %5\n\tds_read_b128 %2, %6 offset:1024\n\tds_read_b64 %3, %7\n\tds_read_b64 %4, %7 offset:512")
                      : "=&s"(t), "+v"(flag), "+v"(xq), "+v"(vx[0]), "+v"(vx[1]) : "v"(xf), "v"(xa), "v"(xv), [mask] "s"(0xffu) : "scc", "memory");
  }
  SCHED0();
  if (HALF == 0) { sc[3] = MFMA16(kf[4], xq, z4); sp[3] = MFMA16(kf[3], xq, z4); }
  else sp[0] = MFMA16(xq, qf[1], z4);
  logits(HALF == 0 ? 0 : 3);
  late(6, 8);
  GRP(1, HALF ? 1 : 2, 0x002, 3); GRP(2, 1, 0x002, 3);
  asm volatile("" : "+v"(m[HALF ? 3 : 0]));
  SCHED0();
  logits(HALF == 0 ? 3 : 0);
  rows_spread(rows_reduce<true>(m), m);
#pragma unroll
  for (int j = 0; j < 4; ++j) {
    a[j] = __builtin_amdgcn_exp2f(opq(a[j] - m[j]));
    b[j] = __builtin_amdgcn_exp2f(opq(b[j] - m[j]));
    m[j] = opq(a[j] + b[j]);
    a[j] = opq(a[j] * ra[j + 1]); b[j] = opq(b[j] * rb[j + 1]);
  }
  late(8, 16);
#pragma unroll
  for (int z = 0; z < 8; ++z) ln(2, seq_of(HALF, 18 + z) / 10 % 10, seq_of(HALF, 18 + z) % 10);
  GRP(0, 1, 0x402, 3); GRP(8, 1, 0x402, 2);
  asm volatile("" : "+v"(m[0]), "+v"(m[1]), "+v"(m[2]), "+v"(m[3]));
  SCHED0();
#pragma unroll
  for (int t = 0; t < 2; ++t) {
#pragma unroll
    for (int i = 0; i < 5; ++i) {
      const u32x2 v = i == (HALF ? 0 : 4) ? vx[t] : pack_v(i, t);
      vf[t][2 * i] = v[0]; vf[t][2 * i + 1] = v[1];
    }
    asm("" : "+v"(vf[t]));
  }
  rows_spread(__builtin_amdgcn_rcpf(rows_reduce<false>(m)), m);
  const rsrc_t ors = mk_rsrc(ws + WS_O, S_TOK * DM * 2);
#pragma unroll
  for (int j = 0; j < 4; ++j) {
    const unsigned pk = pack2(a[j] * m[j], b[j] * m[j]);
    const u32x4 pw = {__builtin_amdgcn_perm(0u, pk, sel[0]), __builtin_amdgcn_perm(0u, pk, sel[1]), __builtin_amdgcn_perm(0u, pk, sel[2]), __builtin_amdgcn_perm(0u, pk, sel[3])};
    const u32x4 v0 = {vf[0][2 * j], vf[0][2 * j + 1], vf[0][2 * j + 2], vf[0][2 * j + 3]}, v1 = {vf[1][2 * j], vf[1][2 * j + 1], vf[1][2 * j + 2], vf[1][2 * j + 3]};
    sp[j] = MFMA16(__builtin_bit_cast(bf16x8, v0), __builtin_bit_cast(bf16x8, pw), z4);
    sc[j] = MFMA16(__builtin_bit_cast(bf16x8, v1), __builtin_bit_cast(bf16x8, pw), z4);
  }
#pragma unroll
  for (int j = 0; j < 4; ++j) { qf[j] = pack8(sp[j], sc[j]); st16c(ors, ooff + j * 32768, __builtin_bit_cast(u32x4, qf[j])); }
  SCHED0();
  asm volatile("ds_read_b32 %0, %1 offset:%2" : "=v"(e.go) : "v"(xf - h * 4), "i"(F_GO - F_XCH) : "memory");
  wait_bytes<0x80808080u>(smem, F_FREE, 16);
  asm volatile("" : "+v"(e.go));
  asm volatile("ds_write_b128 %0, %1\n\tds_write_b128 %0, %2 offset:1024\n\tds_write_b128 %0, %3 offset:4096\n\tds_write_b128 %0, %4 offset:5120"
               :: "v"(lds_addr(smem) + HALF * 8192 + h * 2048 + lane * 16), "v"(qf[0]), "v"(qf[1]), "v"(qf[2]), "v"(qf[3]) : "memory");
  lds_post(smem, F_OL + wid, 1);
}

__device__ __forceinline__ unsigned ln2s(unsigned w, float mb, float r) {
  const unsigned d = pack2(opq(lo2f(w) - mb), opq(hi2f(w) - mb));
  return pack2(opq(lo2f(d) * r), opq(hi2f(d) * r));
}

__device__ __forceinline__ void await_attention(char* smem, int lane, int tm, int tn, char* ws, unsigned tag) {
  const rsrc_t wrs = mk_rsrc(ws, WS_O);
  const unsigned off = WS_OF + (tm * 64 + ((lane >> 3) & 1) * 32 + (lane & 7)) * 4;
  const int own = tn >> 3;
  bool first = true;
  unsigned seen = 0, since = 0;
#pragma unroll 1
  for (unsigned n = 0; n < SPIN_CAP; ++n) {
    unsigned want = ~seen & 0xffffu;
    if (since) ++since; else since = uni(lds_word(smem, F_ODONE)) >> 2;
    if (since <= (first ? 2 : 0)) want &= own ? 0x00ffu : 0xff00u;
    const unsigned f = __builtin_amdgcn_raw_buffer_load_b32(wrs, ((want >> (lane & 15)) & 1) ? off : 0x7fffffffu, 0, 16);
    seen |= (unsigned)__ballot(f == tag) & want;
    if (seen == 0xffffu) break;
    if (first && ((seen & 0xffu) == 0xffu || (seen >> 8) == 0xffu)) { lds_post(smem, F_GO, (seen & 0xffu) == 0xffu ? 1 : 2); first = false; NAP(8); }
    NAP(2);
  }
  lds_post(smem, F_GO + 1, 1);
  if (first) lds_post(smem, F_GO, 1);
}

__device__ __forceinline__ void out_proj(char* smem, int wid, int lane_in, int tm, int tn, char* ws, unsigned tag,
                                         u16* __restrict__ out, EpiRegs& e, unsigned rot2) {
  LANE_VARS
  const rsrc_t ors = mk_rsrc(ws + WS_O, S_TOK * DM * 2), yrs = mk_rsrc(out, S_TOK * DM * 2);
  const int tok = tm * 128 + wid * 32 + r16, co = (g & 1) * 16 + (g >> 1) * 8;
  unsigned eoff = (unsigned)(tok * DM + tn * 64 + co) * 2u;
  const unsigned vo = (unsigned)(tm * 8 + wid * 2) * 32768u + lane * 16;
  bf16x8 wb[3][4], oa[OD][2];
  int bc[4];
#pragma unroll
  for (int c = 0; c < 4; ++c) bc[c] = L_WO + brow + ((c ^ hk) << 5);
  f32x4 acc[2][4];
  SCHED0();
  unsigned go, oth = (tn & 8) * 2 ^ 16;
  asm volatile("" : "+s"(oth));
  auto o_request = [&](int s) {
    asm volatile("buffer_load_dwordx4 %0, %2, %4, %5 offen sc1\n\tbuffer_load_dwordx4 %1, %3, %4, %5 offen sc1" : "+v"(oa[s % OD][0]), "+v"(oa[s % OD][1])
                 : "v"(vo), "v"(vo + 32768), "s"(ors), "s"(((s >> 1 == 7 ? oth : (s & 16) ^ ((go & 2) * 8)) + ((rot2 >> (s & 14) * 2) & 14) + (s & 1)) * 1024) : "memory");
  };
  auto start = [&](unsigned word) {
    go = uni(word) & 3;
    if (go) {
#pragma unroll
      for (int s = 0; s < OD - 1; ++s) o_request(s);
    }
    SCHED0();
  };
#pragma unroll
  for (int s = 0; s < OD; ++s) asm volatile("" : "=v"(oa[s][0]), "=v"(oa[s][1]));
  {
    const unsigned w0 = uni(e.go);
    if (w0 & 3) wait_bytes<0x80808080u>(smem, F_OL, 1);
    start(w0);
  }
  asm volatile("s_waitcnt vmcnt(%1)\n\ts_cmp_lg_u32 %0, 0\n\ts_cbranch_scc1 1f\n\ts_waitcnt vmcnt(0)\n\t1:" :: "s"(go), "i"(2 * OD - 2) : "scc", "memory");
  unsigned old = 0;
  if (lane == 0) old = __hip_atomic_fetch_add((LAS unsigned*)(LAS char*)(smem + F_ODONE), 1u, __ATOMIC_RELAXED, __HIP_MEMORY_SCOPE_WORKGROUP);
  if (uni(old) == 3u) {
    if (lane == 0) __hip_atomic_store((GAS unsigned*)(ws + WS_OF) + tm * 64 + (tn >> 3) * 32 + (tn & 7), tag, RLX_AGENT);
  }
  SCHED0();
  const unsigned w1 = lds_word(smem, F_GO);
  const unsigned srow = 2047u - (*(const unsigned*)(smem + F_SEL + (lane & 7) * 4) & 0xffffu);
  f32x4 res[2][4];
  const uint2 st[2] = {*(const uint2*)(smem + L_QST + (16 + wid * 32 + r16) * 8), *(const uint2*)(smem + L_QST + (32 + wid * 32 + r16) * 8)};
  auto norm = [&](int u) {
    const float rd = f_of(st[u].y), k = 1.0f + rd, b = lo2f(st[u].x) * rd;
#pragma unroll
    for (int pr = 0; pr < 2; ++pr) {
      auto sa = __builtin_amdgcn_permlane16_swap(e.xv[u][pr][0], e.xv[u][pr][2], false, false);
      auto sb = __builtin_amdgcn_permlane16_swap(e.xv[u][pr][1], e.xv[u][pr][3], false, false);
#pragma unroll
      for (int z = 0; z < 2; ++z) res[u][2 * pr + z] = f32x4{lo2f(sa[z]), hi2f(sa[z]), lo2f(sb[z]), hi2f(sb[z])} * k + b;
    }
    asm volatile("" : "+v"(res[u][0]), "+v"(res[u][1]), "+v"(res[u][2]), "+v"(res[u][3]));
  };
  f32x4 ac2[2][4], (&ac3)[2][4] = res;
  auto w_frag = [&](int slot, const int (&b)[4], int z) {
#pragma unroll
    for (int c = 0; c < 4; ++c) wb[slot][c] = lds_tr2(smem + b[c] + z * 4096);
  };
  auto step = [&](f32x4 (&d)[2][4], int slot, const bf16x8 (&o)[2], bool fresh) {
#pragma unroll
    for (int c = 0; c < 4; ++c)
#pragma unroll
      for (int uu = 0; uu < 2; ++uu) {
        const int u = uu ^ (c & 1);
        d[u][c] = MFMA16(wb[slot][c], o[u], fresh ? (f32x4{0, 0, 0, 0}) : d[u][c]);
      }
  };
  int b3[4];
#pragma unroll
  for (int c = 0; c < 4; ++c) b3[c] = bc[c] + 28 * 4096;
  auto own_heads = [&]() {
    w_frag(0, b3, 2); w_frag(1, b3, 3);
    const unsigned w2 = lds_word(smem, F_GO);
    SCHED0();
    if (wid < 2) wait_bytes<0x00800080u>(smem, F_OL, 1); else wait_bytes<0x80008000u>(smem, F_OL, 1);
    if (!go) start(w2);
    bf16x8 own[2][2];
#pragma unroll
    for (int z = 0; z < 4; ++z) own[z >> 1][z & 1] = lds_b128(smem + wid * 4096 + lane * 16 + z * 1024);
    step(ac3, 0, own[0], false); step(ac3, 1, own[1], false);
    asm volatile("" : "+v"(ac3[0][0]), "+v"(ac3[0][1]), "+v"(ac3[0][2]), "+v"(ac3[0][3]), "+v"(ac3[1][0]), "+v"(ac3[1][1]), "+v"(ac3[1][2]), "+v"(ac3[1][3]));
    SCHED0();
  };
  unsigned mine = (unsigned)__ballot((srow >> 5) == (unsigned)(tm * 4 + wid)) & 0xffu;
  asm volatile("" : "+s"(mine));
  asm volatile("" : "+v"(eoff));
  if (!go) start(w1);
  norm(0);
  norm(1);
  SCHED0();
  own_heads();
  if (!go) start(wait_bytes<0x80u>(smem, F_GO, 1));
  go = uni(go);
  int b2[4];
  const int mine1st = ((go >> 1) ^ (tn >> 3) ^ 1) & 1;
#pragma unroll
  for (int c = 0; c < 4; ++c) { b2[c] = bc[c] + mine1st * 57344; bc[c] += (mine1st ^ 1) * 57344; }
  unsigned so[15];
#pragma unroll
  for (int z = (OD - 1) / 2; z < 15; ++z) { so[z] = ((z == 7 ? oth : (2 * z & 16) ^ ((go & 2) * 8)) + ((rot2 >> (z & 7) * 4) & 14)) * 1024; asm volatile("" : "+s"(so[z])); }
  s16x4 wh[3][8];
  auto w_half = [&](int s, int z0, int z1) {
#pragma unroll
    for (int z = z0; z < z1; ++z)
      if (s < 30) wh[s % 3][z] = __builtin_amdgcn_ds_read_tr16_b64_v4i16((LAS s16x4*)(smem + (s < 14 ? bc[z >> 1] + s * 4096 : s < 16 ? b3[z >> 1] + (s - 14) * 4096 : b2[z >> 1] + (s - 16) * 4096) + (z & 1) * 512));
  };
  wait_full(smem, F_WO, 1);
  w_half(0, 0, 8); w_half(1, 0, 8);
  SCHED0();
  asm volatile("s_waitcnt vmcnt(%2)" : "+v"(oa[0][0]), "+v"(oa[0][1]) : "i"(2 * OD - 4) : "memory");
  auto one = [&](int s) __attribute__((always_inline)) {
    f32x4 (&d)[2][4] = s < 14 ? ac2 : s < 16 ? ac3 : acc;
    auto part = [&](int z0, int z1) {
      SCHED0();
      w_half(s + 2, z0, z1);
#pragma unroll
      for (int z = z0; z < z1; ++z) {
        const int c = z >> 1, u = (z ^ c) & 1;
        d[u][c] = MFMA16(__builtin_shufflevector(wh[s % 3][2 * c], wh[s % 3][2 * c + 1], 0, 1, 2, 3, 4, 5, 6, 7), oa[s % OD][u], s == 0 || s == 16 ? (f32x4{0, 0, 0, 0}) : d[u][c]);
        __builtin_amdgcn_sched_group_barrier(0x008, 1, 0);
        __builtin_amdgcn_sched_group_barrier(0x100, 1, 0);
      }
      SCHED0();
    };
    const int n = s + OD - 1;
    auto ask = [&](int u) {
      if (n < 30) asm volatile("buffer_load_dwordx4 %0, %1, %2, %3 offen offset:%4 sc1" : "+v"(oa[n % OD][u]) : "v"(vo + u * 32768), "s"(ors), "s"(so[n >> 1]), "i"((n & 1) * 1024) : "memory");
    };
    part(0, 1);
    if (n == 14 || n == 16) wait_bytes<0x8000u, true>(smem, F_GO, 1, n == 14 ? mine1st ^ 1 : mine1st);
    ask(0);
    part(1, 3);
    ask(1);
    part(3, 5);
    if (s < 29) asm volatile("s_waitcnt vmcnt(%2)" : "+v"(oa[(s + 1) % OD][0]), "+v"(oa[(s + 1) % OD][1]) : "i"(2 * ((n < 29 ? n : 29) - s - 1)) : "memory");
    part(5, 8);
    asm volatile("" : "+v"(d[0][0]), "+v"(d[0][1]), "+v"(d[0][2]), "+v"(d[0][3]), "+v"(d[1][0]), "+v"(d[1][1]), "+v"(d[1][2]), "+v"(d[1][3]));
  };
#pragma unroll
  for (int s = 0; s < 10; ++s) one(s);
#pragma unroll
  for (int s = 10; s < 20; ++s) one(s);
#pragma unroll
  for (int s = 20; s < 28; ++s) one(s);
  asm volatile("s_waitcnt vmcnt(0)" : "+v"(oa[29 % OD][0]), "+v"(oa[29 % OD][1]) :: "memory");
  if (mine != 0) {
    wait_bytes<0x80808080u>(smem, F_UP, 1);
#pragma unroll 1
    for (; mine != 0; mine &= mine - 1) {
      const int c = __builtin_ctz(mine), row = __builtin_amdgcn_readlane((int)srow, c) & 31;
#pragma unroll
      for (int u = 0; u < 2; ++u)
        if (row == u * 16 + r16) {
#pragma unroll
          for (int z = 0; z < 4; ++z) res[u][z] += *(const f32x4*)(smem + L_UP + c * 256 + (z * 16 + g * 4) * 4);
        }
    }
  }
  SCHED0();
#pragma unroll
  for (int u = 0; u < 2; ++u)
#pragma unroll
    for (int z = 0; z < 8; ++z)
      acc[u][z & 3] = MFMA16(__builtin_shufflevector(wh[(28 + (z >> 2)) % 3][2 * (z & 3)], wh[(28 + (z >> 2)) % 3][2 * (z & 3) + 1], 0, 1, 2, 3, 4, 5, 6, 7), oa[(28 + (z >> 2)) % OD][u], acc[u][z & 3]);
#pragma unroll
  for (int u = 0; u < 2; ++u)
#pragma unroll
    for (int pr = 0; pr < 2; ++pr) {
      const f32x4 c0 = (acc[u][2 * pr] + ac2[u][2 * pr]) + ac3[u][2 * pr], c1 = (acc[u][2 * pr + 1] + ac2[u][2 * pr + 1]) + ac3[u][2 * pr + 1];
      auto sa = __builtin_amdgcn_permlane16_swap(pack2(c0[0], c0[1]), pack2(c1[0], c1[1]), false, false);
      auto sb = __builtin_amdgcn_permlane16_swap(pack2(c0[2], c0[3]), pack2(c1[2], c1[3]), false, false);
      __builtin_amdgcn_raw_buffer_store_b128(u32x4{sa[0], sb[0], sa[1], sb[1]}, yrs, eoff + pr * 64 + u * 16 * DM * 2, 0, 16);
    }
  __builtin_amdgcn_sched_group_barrier(0x008, 9, 0);
  GRP(7, 1, 0x002, 4);
  SCHED0();
}

__device__ __forceinline__ unsigned med3(unsigned a, unsigned b, unsigned c) { return max(min(a, b), min(max(a, b), c)); }
#define PIN8(a) asm volatile("" : "+v"(a[0]), "+v"(a[1]), "+v"(a[2]), "+v"(a[3]), "+v"(a[4]), "+v"(a[5]), "+v"(a[6]), "+v"(a[7]))
__device__ __forceinline__ void tick(int& nb) {
  SCHED0();
  if (nb < NBAR) { BAR(); ++nb; }
  asm volatile("" ::: "memory");
  SCHED0();
}
__device__ __forceinline__ void hidden_half(char* smem, int lane, int l, int cg, char* ws, unsigned tag, u32x4 (&fd)[2][8]) {
  const rsrc_t wrs = mk_rsrc(ws, WS_O);
  const unsigned off = WS_GU + (unsigned)(cg * 32768 + l * 2048 + lane * 16);
  u32x4 ps[2][8];
  auto request = [&]() {
#pragma unroll
    for (int p = 0; p < 16; ++p) ps[p & 1][p >> 1] = ld16c(wrs, off + (p & 1) * 1024, (p >> 1) * 4096);
    SCHED0();
  };
  auto good = [&]() {
    unsigned bad = 0;
#pragma unroll
    for (int p = 0; p < 16; ++p) bad = chk(bad, ps[p & 1][p >> 1], tag);
    return __all(bad == 0);
  };
  request();
  tr_all(fd[0]); tr_all(fd[1]);
  SCHED0();
#pragma unroll 1
  for (unsigned n = 0; n < SPIN_CAP && !good(); ++n) { NAP(4); request(); }
  unsigned hp[2];
#pragma unroll
  for (int j = 0; j < 2; ++j) {
    float hv[2];
#pragma unroll
    for (int z = 0; z < 2; ++z) {
      const float gb = rbf((f_of(ps[j][0][2 * z]) + f_of(ps[j][2][2 * z])) + (f_of(ps[j][4][2 * z]) + f_of(ps[j][6][2 * z])));
      const float ub = rbf((f_of(ps[j][1][2 * z]) + f_of(ps[j][3][2 * z])) + (f_of(ps[j][5][2 * z]) + f_of(ps[j][7][2 * z])));
      hv[z] = rbf(gb / (1.0f + __expf(-gb))) * ub;
    }
    hp[j] = pack2(hv[0], hv[1]);
  }
#pragma unroll
  for (int j = 0; j < 2; ++j)
    *(unsigned*)(smem + L_HF + (lane & 7) * 128 + (((lane >> 4) & 1) * 4 + 2 * l + j) * 16 + ((lane >> 3) & 1) * 4 + (lane >> 5) * 8) = hp[j];
  lds_post(smem, F_H + l, 1);
}

struct Stager {
  rsrc_t xrs, qrs;
  LAS char* lds;
  unsigned va, vb;
  int arow, pa, pb;
  __device__ __forceinline__ Stager(char* smem, int l, int lane, int tm, int tn, const u16* x, const u16* w) {
    asm volatile("" : "+v"(lane));
    const int kr = lane >> 3;
    xrs = mk_rsrc(x, S_TOK * DM * 2); qrs = mk_rsrc(w, DM * NQKV * 2);
    lds = (LAS char*)smem + l * 1024;
    va = (unsigned)(kr + 8 * l) * 2048u + (((lane & 7) ^ (kr >> 1) ^ (l << 2)) << 4);
    vb = (unsigned)(kr + 8 * l) * 6144u + tn * 128 + (((lane & 7) ^ (((kr >> 1) & 1) << 1) ^ (l << 2)) << 4);
    arow = tm * 128 - 16;
    k_order(tm, tn, pa, pb);
  }
};
#define DM1(l, o) "s_add_u32 m0, %2, " #l "\n\ts_add_u32 %0, %5, " #o "\n\tbuffer_load_dwordx4 %3, %4, %0 offen lds\n\t"
#define DMA_(str, v_, rs_, so_, s0_) asm volatile("s_mov_b32 %1, m0\n\t" str "s_mov_b32 m0, %1" : "=&s"(tmp), "=&s"(keep) : "s"(base), "v"(v_), "s"(rs_), "s"(so_), "s"(s0_) : "scc", "memory")
__device__ __forceinline__ void stage_asm(const Stager& g, int t, int so) {
  const int kt = k_tile(t, g.pa, g.pb);
  const unsigned base = (unsigned)(size_t)g.lds + so;
  unsigned keep, tmp;
  DMA_("s_mov_b32 m0, %2\n\ts_nop 0\n\tbuffer_load_dwordx4 %3, %4, %6 offen lds\n\t" DM1(2048, 32768) DM1(4096, 65536) DM1(6144, 98304) DM1(8192, 131072)
       DM1(10240, 163840) DM1(12288, 196608) DM1(14336, 229376) DM1(16384, 262144), g.va, g.xrs, g.arow * 2048 + kt * 128, max(g.arow, 0) * 2048 + kt * 128);
  DMA_(DM1(18432, 0) DM1(20480, 98304) DM1(22528, 196608) DM1(24576, 294912) DM1(26624, 2048) DM1(28672, 100352) DM1(30720, 198656) DM1(32768, 296960)
       DM1(34816, 4096) DM1(36864, 102400) DM1(38912, 200704) DM1(40960, 299008), g.vb, g.qrs, kt * 64 * 6144, 0);
}
__device__ __forceinline__ int unit_of(int bid) {
  const int off = bid >> 3;
  return (off >> 2) == 3 ? off + 16 : (off >> 2) == 7 ? off - 16 : off;
}
__device__ constexpr int wo_early(int t) { return t < TW0 ? 0 : ((t - TW0) * NW < 64 - WR + 2 * LF ? (t - TW0) * NW : 64 - WR + 2 * LF); }
__device__ __forceinline__ void loader(char* smem, int l, int lane, int tm, int tn, unsigned rot2, const u16* __restrict__ x,
                                       const u16* __restrict__ w, const u16* __restrict__ wo, char* ws) {
  const Stager stage(smem, l, lane, tm, tn, x, w);
  dma_tag(smem, F_FULL + l * 4, 0);
  const rsrc_t prs = mk_rsrc(wo, DM * DM * 2);
  const int kr = lane >> 3;
  const unsigned vo = (unsigned)((kr < 4 ? kr : 12 + kr) + 4 * l) * 2048u + tn * 128 + (((lane & 7) ^ (((kr >> 1) & 1) << 1) ^ (l << 2)) << 4);
  u32x4 pc[2 * LF];
  int oth = (tn & 8) * 2 ^ 16;
  asm volatile("" : "+s"(oth));
  auto source = [&](int n) {
    const int k = n >> 1, half = k < 14 || k >= 30 ? (tn & 8) * 2 : oth;
    return (unsigned)((half + ((rot2 >> ((k < 14 ? k : k < 28 ? k - 14 : 14) & 14) * 2) & 14) + (k & 1)) * 32 + 8 * (n & 1)) * 2048;
  };
  auto request = [&](int z) {
    unsigned keep;
    if (z >= 64 - WR) asm volatile("s_nop 4\n\tbuffer_load_dwordx4 %0, %1, %2, %3 offen" : "=v"(pc[(z + 2 * LF - 64 + WR) % (2 * LF)])
                                   : "v"(vo), "s"(prs), "s"(source(z - 64 + WR + (z - 64 + WR < LF ? 0 : 28 - LF))) : "memory");
    else asm volatile("s_mov_b32 %0, m0\n\ts_add_u32 m0, %4, %5\n\ts_nop 2\n\tbuffer_load_dwordx4 %1, %2, %3 offen lds\n\ts_mov_b32 m0, %0"
                      : "=&s"(keep) : "v"(vo), "s"(prs), "s"(source(z + WR)), "s"((unsigned)(size_t)stage.lds), "i"(L_WO + (z + WR) * 2048) : "scc", "memory");
  };
  static_assert(L_WO + WR * 2048 >= 3 * SLOT && WR <= 60 && wo_early(NBAR - 4) >= 64 - WR, "beyond the ring, and early");
#pragma unroll
  for (int t = -1; t < NBAR - 2; ++t) {
    if (t >= 0) stage_asm(stage, t + 3, ((t + 3) % 3) * SLOT);
    SCHED0();
#pragma unroll
    for (int i = wo_early(t); i < wo_early(t + 1); ++i) request(i);
    WAITV(21 + (t < 0) + wo_early(t + 1) - wo_early(t - 1));
    BAR();
  }
  dma_tag(smem, F_FULL + l * 4, 15);
  stage_asm(stage, 15, 0);
  dma_tag(smem, F_FULL + l * 4, 16);
  SCHED0();
  {
    const unsigned d0 = lds_addr(smem + L_WO + l * 1024 + lane * 16), d1 = d0 + 65536;
#pragma unroll
    for (int i = wo_early(NBAR - 2); i < 64 - WR + 2 * LF; ++i) request(i);
    wait_bytes<0x80808080u>(smem, F_FREE, 16);
    WAITV(0);
#pragma unroll
    for (int i = 0; i < 2 * LF; ++i) {
      const int n = i < LF ? i : i - LF + 28;
      asm volatile("ds_write_b128 %1, %0 offset:%2" : "+v"(pc[i]) : "v"(n < 32 ? d0 : d1), "i"((n & 31) * 2048) : "memory");
    }
    if (l == 0) {
      const unsigned v1 = (vo + 4 * 2048) ^ 64;
      wait_bytes<0x80808080u>(smem, F_OL, 1);
#pragma unroll
      for (int y = 0; y < WR - 2 * LF; ++y) {
        const int n = y < 28 - LF ? LF + y : y + 2 * LF;
        unsigned keep;
        asm volatile("s_mov_b32 %0, m0\n\ts_add_u32 m0, %4, %5\n\ts_nop 0\n\tbuffer_load_dwordx4 %1, %2, %3 offen lds\n\ts_add_u32 m0, m0, 1024\n\ts_nop 0\n\tbuffer_load_dwordx4 %6, %2, %3 offen lds\n\ts_mov_b32 m0, %0"
                     : "=&s"(keep) : "v"(vo), "s"(prs), "s"(source(n)), "s"((unsigned)(size_t)stage.lds), "i"(L_WO + n * 2048), "v"(v1) : "scc", "memory");
      }
      dma_tag(smem, F_WO, 1);
    } else lds_post(smem, F_WO + 4, 1);
  }
  __builtin_amdgcn_s_setprio(1);
  if (l) await_attention(smem, lane, tm, tn, ws, uni(lds_word(smem, F_TAG)));
}

__device__ __forceinline__ void updates(char* smem, int w, int lane, int tm, int tn, char* ws) {
  const rsrc_t wrs = mk_rsrc(ws, WS_O);
  const unsigned tag = uni(lds_word(smem, F_TAG));
  const int g = lane >> 4, li = lane & 15;
  wait_bytes<0x80u>(smem, F_SELOK, 1);
  const unsigned selkey = *(const unsigned*)(smem + F_SEL + (lane & 7) * 4);
  unsigned mine = (unsigned)__ballot(((2047u - (selkey & 0xffffu)) >> 7) == (unsigned)tm) & 0xffu;
#pragma unroll
  for (int i = 0; i < 3; ++i) mine &= i < w ? mine - 1 : ~0u;
  if (mine != 0 && w < 3) {
    wait_bytes<0x80u>(smem, F_GO, 1);
    NAP(24);
  }
  for (; mine != 0; mine &= mine - 1, mine &= mine - 1, mine &= mine - 1, mine &= mine - 1) {
    const int c = __builtin_ctz(mine);
    const unsigned yoff = WS_Y + (unsigned)((g * 14 * 8 + c) * DM + tn * 64 + li * 4) * 4u;
    u32x4 yv[14];
    const unsigned key = (unsigned)__builtin_amdgcn_readlane((int)selkey, c), kb = key >> 16;
#pragma unroll 1
    for (unsigned n = 0; n < SPIN_CAP; ++n) {
      unsigned bad = 0;
#pragma unroll
      for (int j = 0; j < 14; ++j) yv[j] = ld16c(wrs, yoff, j * (8 * DM * 4));
#pragma unroll
      for (int j = 0; j < 14; ++j) bad = chk(bad, yv[j], tag);
      if (__all(bad == 0)) break;
      NAP(8);
    }
    float y[4] = {0.f, 0.f, 0.f, 0.f};
#pragma unroll
    for (int j = 0; j < 14; ++j) { y[0] += lo2f(yv[j][0]); y[1] += hi2f(yv[j][0]); y[2] += lo2f(yv[j][2]); y[3] += hi2f(yv[j][2]); }
    const float r = __uint_as_float(((kb & 0x8000u) ? (kb & 0x7fffu) : (~kb & 0xffffu)) << 16);
    const float rs = rbf(1.0f / (1.0f + __expf(-r)));
#pragma unroll
    for (int e = 0; e < 4; ++e) y[e] = rbf(rbf(xsum16_32(y[e])) * rs);
    *(f32x4*)(smem + L_UP + c * 256 + li * 16) = f32x4{y[0], y[1], y[2], y[3]};
  }
  lds_post(smem, F_UP + w, 1);
}

__device__ __forceinline__ void ld8k(u32x4 (&d)[8], rsrc_t rs, unsigned off) {
#define L1K(i, so, o) "buffer_load_dwordx4 %" #i ", %8, %9, " so " offen offset:" #o " sc1\n\t"
  asm volatile("s_nop 4\n\t" L1K(0, "0", 0) L1K(1, "0", 1024) L1K(2, "0", 2048) L1K(3, "0", 3072) L1K(4, "%10", 0) L1K(5, "%10", 1024) L1K(6, "%10", 2048) L1K(7, "%10", 3072)
               : "=&v"(d[0]), "=&v"(d[1]), "=&v"(d[2]), "=&v"(d[3]), "=&v"(d[4]), "=&v"(d[5]), "=&v"(d[6]), "=&v"(d[7]) : "v"(off), "s"(rs), "s"(4096) : "memory");
  SCHED0();
}
template <int V> struct IC { static constexpr int v = V; };
__device__ __forceinline__ void side_wave(char* smem, int F, int lane, int bid, int tm, int tn, const u16* __restrict__ x,
                                          const u16* __restrict__ wgu, const u16* __restrict__ wd, char* ws) {
  const rsrc_t wrs = mk_rsrc(ws, WS_O), xrs = mk_rsrc(x, S_TOK * DM * 2);
  const int u = unit_of(bid), cg = (bid & 7) * 7 + ((u >> 2) % 7), q = u & 3;
  const int g = lane >> 4, li = lane & 15;
  u32x4 fw[4][8];
  const rsrc_t grs = mk_rsrc(wgu, u < 28 ? DM * FF * 2 : 0);
  const int cu = (bid & 7) * 7 + (((u >> 2) + F) % 7);
  const unsigned fo = (unsigned)(q * 256 + (li >> 3) * 128 + 8 * g) * (FF * 2) + cu * 128 + (li & 7) * 16;
#pragma unroll
  for (int s = 0; s < 4; ++s) asm volatile("" : "=v"(fw[s][0]), "=v"(fw[s][1]), "=v"(fw[s][2]), "=v"(fw[s][3]), "=v"(fw[s][4]), "=v"(fw[s][5]), "=v"(fw[s][6]), "=v"(fw[s][7]));
  unsigned due = (0x3012 >> ((tn & 3) * 4)) & 15, iss = 0;
#define FW(n) fw[(n) >> 3][(n) & 7]
#define LD(i) "s_mov_b32 %[t], (((%[n0]+" #i ")>>3)*32+((%[n0]+" #i ")&7))*7168\n\tbuffer_load_dwordx4 %" #i ", %[fo], %[rs], %[t] offen nt\n\t"
#define LD8 LD(0) LD(1) LD(2) LD(3) LD(4) LD(5) LD(6) LD(7)
#define R8(n) "+v"(FW(n)), "+v"(FW(n + 1)), "+v"(FW(n + 2)), "+v"(FW(n + 3)), "+v"(FW(n + 4)), "+v"(FW(n + 5)), "+v"(FW(n + 6)), "+v"(FW(n + 7))
#define SITE(lds_, ...) asm volatile("s_cmp_lg_u32 %[iss], %[k]\n\ts_cbranch_scc1 1f\n\ts_cmp_le_u32 %[nb], %[due]\n\ts_cbranch_scc1 1f\n\ts_sleep %[dly]\n\t" lds_ \
    "s_add_u32 %[iss], %[iss], 1\n\ts_add_u32 %[due], %[due], 4\n\t1:" : __VA_ARGS__, [iss] "+s"(iss), [due] "+s"(due), [t] "=&s"(t) \
    : [fo] "v"(fo), [rs] "s"(grs), [nb] "s"(nb), [k] "i"(K), [n0] "i"(n0), [dly] "i"(D) : "scc", "memory")
  auto site = [&](auto kk, auto dd, unsigned nb) {
    constexpr int K = decltype(kk)::v, D = decltype(dd)::v, n0 = K == 0 ? 0 : 8 + K * 8;
    unsigned t;
    SCHED0();
    if constexpr (K != 0) SITE(LD8, R8(n0));
    else SITE(LD8 LD(8) LD(9) LD(10) LD(11) LD(12) LD(13) LD(14) LD(15), R8(n0), R8(n0 + 8));
    SCHED0();
  };
#define CH(k, d, n) site(IC<k>{}, IC<d>{}, n)
#define WAITD(K, d) asm volatile("s_waitcnt vmcnt(%1)\n\ts_cmp_lg_u32 %0, %3\n\ts_cbranch_scc1 1f\n\ts_waitcnt vmcnt(%2)\n\t1:" : "+s"(iss) : "i"((K) + 8), "i"(K), "i"(d) : "scc", "memory")
  unsigned tag;
  const int csb = WS_CS + ((tn * 2 + F) * 16 + (tm & 1) * 8) * 1024;
  {
    const int qq = (lane >> 2) & 3, hk = (qq >> 1) | ((g & 1) << 1);
    const char* pb = smem + XB + (8 * g + qq) * 128 + 8 * (lane & 3);
    const bf16x8 ones = {0x3f80, 0x3f80, 0x3f80, 0x3f80, 0x3f80, 0x3f80, 0x3f80, 0x3f80};
    f32x4 a[6];
#pragma unroll
    for (int t = 0; t < 2; ++t) {
      SCHED0(); BAR(); SCHED0();
      __builtin_amdgcn_s_setprio(3);
#pragma unroll
      for (int c = 0; c < 6; ++c) {
        const char* p = pb + t * SLOT + (c >> 1) * 8192 + (((2 * F + (c & 1)) ^ hk) << 5);
        a[c] = MFMA16(ones, lds_tr2(p), t ? a[c] : (f32x4{0, 0, 0, 0}));
        a[c] = MFMA16(ones, lds_tr2(p + 4096), a[c]);
      }
      asm volatile("" : "+v"(a[0]), "+v"(a[1]), "+v"(a[2]), "+v"(a[3]), "+v"(a[4]), "+v"(a[5]));
      if (t == 0) { tag = uni(lds_word(smem, F_TAG)); CH(0, 4, 1); }
    }
    const unsigned off = csb + (tm >> 1) * 1024 + lane * 8;
    st8c(wrs, off, __float_as_uint(g == 0 ? a[0][0] : g == 1 ? a[1][0] : g == 2 ? a[2][0] : a[3][0]), tag);
    st8c(wrs, lane < 32 ? off + 512 : 0x7fffffffu, __float_as_uint(g == 0 ? a[4][0] : a[5][0]), tag);
    __builtin_amdgcn_s_setprio(0);
    CH(0, 4, 2);
  }
  SCHED0(); BAR(); SCHED0();
  CH(0, 8, 3);
  SCHED0(); BAR(); SCHED0();
  int nb = 4;
  u32x4 kd[8], cs[8], rec[2];
  auto gather = [&]() {
    ld8k(kd, wrs, WS_K + lane * 16);
  };
  auto gathered = [&]() {
    unsigned bad = 0;
#pragma unroll
    for (int j = 0; j < 8; ++j) bad = chk(bad, kd[j], tag);
    return __all(bad == 0);
  };
  auto gather2 = [&]() {
    ld8k(cs, wrs, csb + min(lane, 47) * 16);
    asm volatile("buffer_load_dwordx4 %0, %2, %4, 0 offen sc1\n\tbuffer_load_dwordx4 %1, %3, %4, 0 offen sc1" : "=&v"(rec[0]), "=&v"(rec[1])
                 : "v"(WS_R + (unsigned)max(tm * 128 - 16 + F * 72 + lane, 0) * 16), "v"(WS_R + (unsigned)max(tm * 128 - 8 + F * 72 + lane, 0) * 16), "s"(wrs) : "memory");
    SCHED0();
  };
  auto gathered2 = [&]() {
    unsigned bad = chk(chk(0u, rec[0], tag), rec[1], tag);
#pragma unroll
    for (int j = 0; j < 8; ++j) bad = chk(bad, cs[j], tag);
    return __all(bad == 0);
  };
  NAP(8);
  CH(0, 0, 99);
  gather();
  tick(nb);
  CH(1, 8, nb);
  tick(nb);
  WAITD(0, 1); PIN8(kd);
#pragma unroll 1
  for (unsigned n = 0; n < SPIN_CAP && !gathered(); ++n) { NAP(8); gather(); tick(nb); tick(nb); WAITV(0); PIN8(kd); }
  if (bid == 0 && F == 0 && lane == 0) __hip_atomic_store((GAS unsigned*)(ws + WS_EP), tag, RLX_AGENT);
  unsigned m1 = 0, m2 = 0, m3 = 0, m4 = 0;
#pragma unroll
  for (int j = 0; j < 8; ++j) {
    kd[j] = u32x4{(kd[j][0] << 16) | (31 - 4 * j), (kd[j][0] & 0xffff0000u) | (30 - 4 * j), (kd[j][2] << 16) | (29 - 4 * j), (kd[j][2] & 0xffff0000u) | (28 - 4 * j)};
#pragma unroll
    for (int i = 0; i < 4; ++i) {
      const unsigned v = kd[j][i];
      m4 = med3(m3, m4, v); m3 = med3(m2, m3, v); m2 = med3(m1, m2, v); m1 = max(m1, v);
    }
    if (j == 3 || j == 7) { PIN8(kd); asm volatile("" : "+v"(m1), "+v"(m2), "+v"(m3), "+v"(m4)); SCHED0(); CH(1, 0, nb); if (j == 7) gather2(); tick(nb); }
  }
  const unsigned lb = (63 - lane) << 5;
  m1 |= lb; m2 |= lb; m3 |= lb; m4 |= lb;
  unsigned selkey = 0;
#pragma unroll
  for (int r = 0; r < 8; ++r) {
    const unsigned wm = wave_max_u32(m1);
    selkey = ((lane & 7) == r) ? wm : selkey;
    const bool won = m1 == wm;
    m1 = won ? m2 : m1; m2 = won ? m3 : m2; m3 = won ? m4 : m3; m4 = won ? 0u : m4;
    if (r == 3) { asm volatile("" : "+v"(m1), "+v"(m2), "+v"(m3), "+v"(m4), "+v"(selkey)); CH(1, 0, 99); tick(nb); }
  }
  if (__any(m1 == 0)) {
    unsigned wm = 0;
#pragma unroll 1
    for (int r = 0; r < 8; ++r) {
      tick(nb);
      unsigned m = 0;
#pragma unroll
      for (int j = 0; j < 8; ++j)
#pragma unroll
        for (int i = 0; i < 4; ++i) m = max(m, (kd[j][i] | lb) - wm);
      wm += wave_max_u32(m);
      selkey = ((lane & 7) == r) ? wm : selkey;
    }
  }
  const unsigned tok = 2047u - (selkey & 0xffffu);
  u32x4 xf[4], trec;
  asm volatile("buffer_load_dwordx4 %0, %5, %6, 0 offen\n\tbuffer_load_dwordx4 %1, %5, %6, 0 offen offset:64\n\tbuffer_load_dwordx4 %2, %5, %6, 0 offen offset:128\n\t"
               "buffer_load_dwordx4 %3, %5, %6, 0 offen offset:192\n\tbuffer_load_dwordx4 %4, %7, %8, 0 offen sc1"
               : "=&v"(xf[0]), "=&v"(xf[1]), "=&v"(xf[2]), "=&v"(xf[3]), "=&v"(trec) : "v"(tok * 2048u + q * 512 + (li >> 3) * 256 + g * 16), "s"(xrs), "v"(WS_R + tok * 16), "s"(wrs) : "memory");
  CH(2, 0, nb);
  tick(nb);
  WAITD(5, 2); PIN8(fw[0]); PIN8(fw[1]); asm volatile("" : "+v"(rec[0]), "+v"(rec[1])); PIN8(cs);
  tr_all(fw[0]); tr_all(fw[1]);
  SCHED0();
#pragma unroll 1
  for (unsigned n = 0; n < SPIN_CAP && !gathered2(); ++n) { NAP(8); gather2(); tick(nb); tick(nb); WAITV(0); asm volatile("" : "+v"(rec[0]), "+v"(rec[1])); PIN8(cs); }
  CH(2, 0, nb);
  tick(nb);
  u32x4 tc;
  uint2 tq[2];
  {
    float csum[2] = {f_of(cs[0][0]), f_of(cs[0][2])};
#pragma unroll
    for (int r = 1; r < 8; ++r) { csum[0] = opq(csum[0] + f_of(cs[r][0])); csum[1] = opq(csum[1] + f_of(cs[r][2])); }
    const float h0 = rbf(csum[0]), d0 = opq(csum[0] - h0), c0 = rbf(d0), h1 = rbf(csum[1]), d1 = opq(csum[1] - h1), c1 = rbf(d1);
    tc = u32x4{pack2(h0, c0), pack2(opq(d0 - c0), 0.f), pack2(h1, c1), pack2(opq(d1 - c1), 0.f)};
    asm volatile("" : "+v"(tc));
  }
#pragma unroll
  for (int j = 0; j < 2; ++j) {
    const unsigned nm = (rec[j][0] & 0xffffu) ^ 0x8000u;
    tq[j] = make_uint2(nm | (nm << 16), __float_as_uint(__builtin_amdgcn_rcpf(f_of(rec[j][2]))));
    asm volatile("" : "+v"(tq[j].x), "+v"(tq[j].y));
  }
  WAITD(0, 2); asm volatile("" : "+v"(xf[0]), "+v"(xf[1]), "+v"(xf[2]), "+v"(xf[3]), "+v"(trec));
#pragma unroll 1
  for (unsigned n = 0; n < SPIN_CAP && !__all(chk(0u, trec, tag) == 0); ++n) {
    NAP(1);
    asm volatile("buffer_load_dwordx4 %0, %1, %2, 0 offen sc1" : "=&v"(trec) : "v"(WS_R + tok * 16), "s"(wrs) : "memory");
    tick(nb);
    WAITV(0); asm volatile("" : "+v"(trec));
  }
  unsigned ta[3] = {lds_addr(smem + L_CSF + (min(lane >> 4, 2) * 64 + F * 32 + (lane < 48 ? li : 15) * 2) * 8), lds_addr(smem + L_QST + (F * 72 + lane) * 8),
                    lds_addr(smem + (lane < 8 ? F_SEL + lane * 4 : lane == 8 ? F_SELOK : F_ST + F * 4))}, tv = lane < 8 ? selkey : 1u;
  asm volatile("" : "+v"(ta[0]), "+v"(ta[1]), "+v"(ta[2]), "+v"(tv));
  unsigned todo = 1;
  auto tables = [&](unsigned now) {
    asm volatile("s_and_b32 %0, %0, %8\n\ts_cbranch_scc0 1f\n\tds_write_b128 %1, %2\n\tds_write2_b64 %3, %4, %5 offset1:8\n\ts_mov_b64 exec, 0x3ff\n\tds_write_b32 %6, %7\n\ts_mov_b64 exec, -1\n\t1:"
                 : "+s"(now) : "v"(ta[0]), "v"(tc), "v"(ta[1]), "v"(tq[0]), "v"(tq[1]), "v"(ta[2]), "v"(tv), "s"(todo) : "scc", "memory");
    todo &= ~now;
  };
  auto slice = [&]() {
    if (nb < NBAR) tick(nb);
    else { const u32x2 fl = *(const volatile LAS u32x2*)(LAS const char*)(smem + F_FULL); tables(uni(min(fl[0], fl[1])) >> 4); SCHED0(); }
  };
  if (nb == NBAR) { __builtin_amdgcn_s_setprio(1); wait_full(smem, F_FULL, 16); tables(1); }
  {
    const float mb = lo2f(trec[0]), rd = 1.0f / f_of(trec[2]);
    f32x4 acc[8];
    auto norm = [&](int s) {
#pragma unroll
      for (int j = 0; j < 4; ++j) xf[s][j] = ln2s(xf[s][j], mb, rd);
      asm volatile("" : "+v"(xf[s]));
      SCHED0();
    };
    auto mma = [&](int s) {
      SCHED0(); __builtin_amdgcn_s_setprio(3); SCHED0();
#pragma unroll
      for (int i = 0; i < 8; ++i) acc[i] = MFMA16(__builtin_bit_cast(bf16x8, fw[s][i]), __builtin_bit_cast(bf16x8, xf[s]), s ? acc[i] : (f32x4{0, 0, 0, 0}));
      SCHED0(); __builtin_amdgcn_s_setprio(0); SCHED0();
    };
    norm(0);
    CH(2, 0, 99);
    slice();
    mma(0); norm(1); mma(1); norm(2);
    PIN8(acc);
    slice();
    norm(3);
    WAITV(0); PIN8(fw[2]); PIN8(fw[3]);
    tr_all(fw[2]);
    mma(2);
    PIN8(acc);
    slice();
    tr_all(fw[3]);
    mma(3);
    PIN8(acc);
    if (nb == NBAR) slice();
#pragma unroll
    for (int r = 0; r < 4; ++r) {
      float v[2];
#pragma unroll
      for (int z = 0; z < 2; ++z) {
        auto a = __builtin_amdgcn_permlane32_swap(__float_as_uint(acc[z][r]), __float_as_uint(acc[z + 4][r]), false, false);
        auto b = __builtin_amdgcn_permlane32_swap(__float_as_uint(acc[z + 2][r]), __float_as_uint(acc[z + 6][r]), false, false);
        v[z] = f_of(li < 8 ? a[0] : b[1]) + dpp_f<0x128>(f_of(li < 8 ? b[0] : a[1]));
      }
      st16c(wrs, u < 28 ? WS_GU + (unsigned)(((cu * 4 + q) * 2 + F) * 4096 + r * 1024 + lane * 16) : 0x7fffffffu, u32x4{__float_as_uint(v[0]), tag, __float_as_uint(v[1]), tag});
    }
  }
  while (nb < NBAR) tick(nb);
  __builtin_amdgcn_s_setprio(1);
  u32x4 fd[2][8];
  {
    const rsrc_t drs = mk_rsrc(wd, u < 28 ? DM * FF * 2 : 0);
    const unsigned dof = (unsigned)(cg * 64 + 8 * g) * (DM * 2) + q * 512 + F * 256 + li * 16;
    wait_full(smem, F_FULL, 15);
#pragma unroll
    for (int s = 0; s < 2; ++s)
#pragma unroll
      for (int j = 0; j < 8; ++j) fd[s][j] = __builtin_amdgcn_raw_buffer_load_b128(drs, dof + (j & 1) * (DM * 2), (s * 32 + (j & 6)) * (DM * 2), 2);
    SCHED0();
  }
  wait_full(smem, F_FULL, 16);
  tables(1);
  if (u < 28) {
    hidden_half(smem, lane, F, cg, ws, tag, fd);
    wait_bytes<0x8080u>(smem, F_H, 1);
    const bf16x8 h0 = lds_b128(smem + L_HF + (li & 7) * 128 + g * 16), h1 = lds_b128(smem + L_HF + (li & 7) * 128 + 64 + g * 16);
    f32x4 acc[8];
#pragma unroll
    for (int i = 0; i < 8; ++i) {
      acc[i] = MFMA16(h0, __builtin_bit_cast(bf16x8, fd[0][i]), (f32x4{0, 0, 0, 0}));
      acc[i] = MFMA16(h1, __builtin_bit_cast(bf16x8, fd[1][i]), acc[i]);
    }
    const unsigned off = WS_Y + (unsigned)((cg * 8 + (g & 1) * 4) * DM + q * 256 + F * 128 + li * 8 + (g >> 1) * 4) * 4u;
#pragma unroll
    for (int r = 0; r < 4; ++r) {
      const unsigned lo = pack2(acc[0][r], acc[1][r]), hi = pack2(acc[2][r], acc[3][r]), lo2 = pack2(acc[4][r], acc[5][r]), hi2 = pack2(acc[6][r], acc[7][r]);
      st16c(wrs, off + r * (DM * 4), u32x4{g < 2 ? lo : lo2, tag, g < 2 ? hi : hi2, tag});
    }
  }
  __builtin_amdgcn_s_waitcnt(0x0F70);
}

__global__ __launch_bounds__(512, 2) void fused_block(const u16* __restrict__ x, const u16* __restrict__ wqkv, const u16* __restrict__ wo,
                                                      const u16* __restrict__ wr, const u16* __restrict__ wg, const u16* __restrict__ wu,
                                                      const u16* __restrict__ wd, char* ws, u16* __restrict__ out) {
  __shared__ __attribute__((aligned(128))) char smem[L_SIZE];
  unsigned long long pc;
  unsigned first;
  asm volatile("v_cmp_gt_u32 vcc, 24, %2\n\ts_getpc_b64 %0\n\ts_cbranch_vccz 1f\n\ts_mov_b64 exec, vcc\n\tv_lshlrev_b32 %1, 7, %2\n\tglobal_load_dword %1, %1, %0\n\ts_mov_b64 exec, -1\n\t1:"
               : "=s"(pc), "=&v"(first) : "v"(threadIdx.x) : "vcc", "memory");
  SCHED0();
  unsigned long long t00; asm volatile("s_memrealtime %0\n\ts_waitcnt lgkmcnt(0)" : "=s"(t00) :: "memory");
  const int lane = threadIdx.x & 63;
  const int wid = __builtin_amdgcn_readfirstlane(threadIdx.x >> 6);
  const int bid = blockIdx.x, xcd = bid & 7, off = bid >> 3;
  const int tm = (xcd & 1) * 8 + (xcd & 2) * 2 + (off & 3), tn = (xcd >> 2) * 8 + (off >> 2);
  if (wid == 7) {
    unsigned d;
    asm volatile("s_getpc_b64 %0" : "=s"(pc));
    asm volatile("buffer_load_dword %0, %1, %2, 0 offen\n\ts_waitcnt vmcnt(0)" : "=&v"(d)
                 : "v"((unsigned)(lane * 32 + off) * 128u), "s"(mk_rsrc((const void*)(pc & ~255ull), CODE_BYTES)) : "memory");
  }
  asm volatile("" : "+s"(x), "+s"(wqkv), "+s"(wr), "+s"(ws), "+s"(out), "+s"(wo), "+s"(wd), "+s"(wg), "+s"(wu));
  unsigned rot2 = ((((unsigned)(0x65432106543210ull >> (off & 3) * 8) & 0xfffffffu) + ((tn & 7) + 1) * 0x1111111u) & 0x7777777u) * 2 | (unsigned)tn << 29;
  unsigned tag = 0;
  RowRegs rr;
  EpiRegs e;
  row_request(rr, x, wr, ws, tm * 128 + tn * 8 + wid * 2, lane, wid < 4);
  rr.keep = first;
  SCHED0();
  if ((wid >> 1) == 2) {
    __builtin_amdgcn_s_setprio(2);
    const Stager stage(smem, wid - 4, lane, tm, tn, x, wqkv);
    NAP(4);
    stage_asm(stage, 0, 0); stage_asm(stage, 1, SLOT);
    WAITV(21);
    BAR();
    stage_asm(stage, 2, 2 * SLOT);
  }
  asm volatile("" ::: "memory");
  if (wid < 4) {
    __builtin_amdgcn_s_setprio(2);
    qkv_setup(smem, wid, lane, tm, tn, e);
    asm volatile("" : "+s"(rot2));
    SCHED0();
    tag = row_stats(rr, ws, tm * 128 + tn * 8 + wid * 2, lane);
    if (wid == 0) *(unsigned*)(smem + L_FLG + lane * 4) = lane * 4 == F_TAG - L_FLG ? tag : 0u;
  }
  asm volatile("" ::: "memory");
  if (wid < 4) {
    if (wid & 1) qkv_attention<1>(smem, wid, lane, tm, tn, ws, tag, x, e);
    else qkv_attention<0>(smem, wid, lane, tm, tn, ws, tag, x, e);
    out_proj(smem, wid, lane, tm, tn, ws, tag, out, e, rot2);

  } else {
    if (wid < 6) loader(smem, wid - 4, lane, tm, tn, rot2, x, wqkv, wo, ws);
    else side_wave(smem, wid - 6, lane, bid, tm, tn, x, wid == 6 ? wg : wu, wd, ws);
    updates(smem, (0x2130 >> ((wid - 4) * 4)) & 3, lane, tm, tn, ws);
    __builtin_amdgcn_s_waitcnt(0x0F70);
  }
}

__global__ __launch_bounds__(64) void first_k(char* ws) {
  unsigned long long t00; asm volatile("s_memrealtime %0\n\ts_waitcnt lgkmcnt(0)" : "=s"(t00) :: "memory");
  if (threadIdx.x == 0) __builtin_amdgcn_raw_buffer_store_b32((unsigned)t00, mk_rsrc(ws + (32u << 20), 4096), 0, 0, 16);
}
__global__ __launch_bounds__(64) void last_k(char* ws) {
  const unsigned t0 = uni(__builtin_amdgcn_raw_buffer_load_b32(mk_rsrc(ws + (32u << 20), 4096), 0, 0, 16));
  for (int n = 0; n < 200000 && (unsigned)__builtin_amdgcn_s_memrealtime() - t0 < 5000u; ++n) NAP(1);
}
extern "C" void kernel_launch(void* const* d_in, const int* in_sizes, int n_in, void* d_out, int out_size, void* d_ws,
                              size_t ws_size, hipStream_t stream) {
  first_k<<<1, 64, 0, stream>>>((char*)d_ws);
  fused_block<<<256, 512, 0, stream>>>((const u16*)d_in[0], (const u16*)d_in[1], (const u16*)d_in[2], (const u16*)d_in[4],
                                       (const u16*)d_in[5], (const u16*)d_in[6], (const u16*)d_in[7], (char*)d_ws, (u16*)d_out);
  last_k<<<1, 64, 0, stream>>>((char*)d_ws);
}
```

```cpp
#define LC 1
#define EP 1
#include <hip/hip_runtime.h>
#include <hip/hip_bf16.h>
#include <stdint.h>

typedef __attribute__((ext_vector_type(8))) short bf16x8;
typedef __attribute__((ext_vector_type(4))) short s16x4;
typedef __attribute__((ext_vector_type(4))) float f32x4;
typedef __attribute__((ext_vector_type(2))) float f32x2;
typedef __attribute__((ext_vector_type(2))) __bf16 bf16x2_t;
typedef __attribute__((ext_vector_type(4))) unsigned u32x4;
typedef unsigned short u16;
#define LAS __attribute__((address_space(3)))
#define GAS __attribute__((address_space(1)))
#define RLX_AGENT __ATOMIC_RELAXED, __HIP_MEMORY_SCOPE_AGENT

#define S_TOK 2048
#define DM 1024
#define NQKV 3072
#define FF 3584

typedef __attribute__((ext_vector_type(2))) unsigned u32x2;

#define WS_EP   0
#define WS_R    4096
#define WS_K    (WS_R + 32768)
#define WS_OF   (WS_K + 8192)
#define WS_CS   (WS_OF + 4096)
#define WS_GU   (WS_CS + 16 * 2 * 16 * 1024)
#define WS_Y    (WS_GU + 56 * 4 * 8192)
#define WS_S    (WS_Y + 56 * 8 * DM * 4)
#define WS_O    (8u << 20)

__device__ const unsigned k_tags[32] = {0,  1,  2,  3,  4,  5,  6,  7,  8,  9,  10, 11, 12, 13, 14, 15,
                                        16, 17, 18, 19, 20, 21, 22, 23, 24, 25, 26, 27, 28, 29, 30, 31};

__device__ __forceinline__ unsigned pack2(float a, float b) {
  f32x2 v = {a, b};
  bf16x2_t r = __builtin_convertvector(v, bf16x2_t);
  return __builtin_bit_cast(unsigned, r);
}
__device__ __forceinline__ float rbf(float f) { return __uint_as_float(pack2(f, f) << 16); }
__device__ __forceinline__ float lo2f(unsigned w) { return __uint_as_float(w << 16); }
__device__ __forceinline__ float hi2f(unsigned w) { return __uint_as_float(w & 0xffff0000u); }

__device__ __forceinline__ bf16x8 lds_b128(const char* p) { return *(const bf16x8*)p; }
__device__ __forceinline__ bf16x8 lds_tr2(const char* p) {
  s16x4 lo = __builtin_amdgcn_ds_read_tr16_b64_v4i16((LAS s16x4*)p);
  s16x4 hi = __builtin_amdgcn_ds_read_tr16_b64_v4i16((LAS s16x4*)(p + 512));
  return __builtin_shufflevector(lo, hi, 0, 1, 2, 3, 4, 5, 6, 7);
}
#define MFMA16(a, b, c) __builtin_amdgcn_mfma_f32_16x16x32_bf16(a, b, c, 0, 0, 0)
#define WAITV(n) asm volatile("s_waitcnt vmcnt(%0)" ::"i"(n) : "memory")
#define WAITL0() __builtin_amdgcn_s_waitcnt(0xC07F)
#define BAR() __builtin_amdgcn_s_barrier()
#define SCHED0() __builtin_amdgcn_sched_barrier(0)
#define NAP(n) asm volatile("s_sleep %0" ::"i"(n) : "memory")
__device__ __forceinline__ float xsum16_32(float v) {
  unsigned u = __float_as_uint(v);
  auto a = __builtin_amdgcn_permlane16_swap(u, u, false, false);
  v = __uint_as_float(a[0]) + __uint_as_float(a[1]);
  u = __float_as_uint(v);
  auto b = __builtin_amdgcn_permlane32_swap(u, u, false, false);
  return __uint_as_float(b[0]) + __uint_as_float(b[1]);
}
__device__ __forceinline__ bf16x8 pack8(const f32x4& a, const f32x4& b) {
  u32x4 r = {pack2(a[0], a[1]), pack2(a[2], a[3]), pack2(b[0], b[1]), pack2(b[2], b[3])};
  return __builtin_bit_cast(bf16x8, r);
}
__device__ __forceinline__ unsigned wave_max_u32(unsigned v) {
  v = max(v, (unsigned)__builtin_amdgcn_update_dpp(0, (int)v, 0x111, 0xf, 0xf, false));
  v = max(v, (unsigned)__builtin_amdgcn_update_dpp(0, (int)v, 0x112, 0xf, 0xf, false));
  v = max(v, (unsigned)__builtin_amdgcn_update_dpp(0, (int)v, 0x114, 0xf, 0xf, false));
  v = max(v, (unsigned)__builtin_amdgcn_update_dpp(0, (int)v, 0x118, 0xf, 0xf, false));
  v = max(v, (unsigned)__builtin_amdgcn_update_dpp(0, (int)v, 0x142, 0xa, 0xf, false));
  v = max(v, (unsigned)__builtin_amdgcn_update_dpp(0, (int)v, 0x143, 0xc, 0xf, false));
  return (unsigned)__builtin_amdgcn_readlane((int)v, 63);
}
__device__ __forceinline__ bf16x8 tr_frag(const u32x4 (&w)[8], int i) {
  const unsigned sel = (i & 1) ? 0x07060302u : 0x05040100u;
  u32x4 r = {__builtin_amdgcn_perm(w[1][i >> 1], w[0][i >> 1], sel), __builtin_amdgcn_perm(w[3][i >> 1], w[2][i >> 1], sel),
             __builtin_amdgcn_perm(w[5][i >> 1], w[4][i >> 1], sel), __builtin_amdgcn_perm(w[7][i >> 1], w[6][i >> 1], sel)};
  return __builtin_bit_cast(bf16x8, r);
}

#define SLOT 43008
#define XB 18432
#define NBAR 14
#define L_WO 20224
#define L_XCH (L_WO + 131072)
#define L_HF (L_XCH + 6144)
#define L_UP (L_HF + 1024)
#define L_CSF (L_UP + 2048)
#define L_QST (L_CSF + 1536)
#define L_FLG (L_QST + 1152)
#define L_SIZE (L_FLG + 256)
#define F_FULL (L_FLG + 0)
#define F_ST (L_FLG + 8)
#define F_WO (L_FLG + 16)
#define F_FREE (L_FLG + 32)
#define F_XCH (L_FLG + 40)
#define F_ODONE (L_FLG + 48)
#define F_SELOK (L_FLG + 52)
#define F_GO (L_FLG + 56)
#define F_OL (L_FLG + 60)
#define F_H (L_FLG + 64)
#define F_TAG (L_FLG + 68)
#define F_UP (L_FLG + 72)
#define F_SEL (L_FLG + 96)
#define F_CND (L_FLG + 128)
#define OD 7
#define SPIN_CAP (1u << 18)
#define WR 54
#define LF 20
#define NW 3
#define TW0 3
#define CD 4
#define EV 4
#define PN 2
#define YD 24
#define OK 2
#define ON 8
#define HN 2
#define SD 0
#define MG (LC == 1)
#define XSL 10
#define KQ 1
#define TCH 3
#define TS 0
#define SX 0
#define CODE_BYTES 53888

#define RSC(i)
#define STAMP(i)
#define RST(i)

__device__ __forceinline__ unsigned lds_addr(const char* p) { return (unsigned)(size_t)(LAS const char*)p; }
__device__ __forceinline__ void lds_post(char* smem, int off, unsigned v) {
  asm volatile("s_mov_b64 exec, 1\n\tds_write_b8 %0, %1\n\ts_mov_b64 exec, -1" ::"v"(lds_addr(smem + off)), "v"(v) : "memory");
}
__device__ __forceinline__ unsigned lds_word(const char* smem, int off) { return *(const volatile LAS unsigned*)(LAS const char*)(smem + off); }
__device__ __forceinline__ u32x4 lds_word4(const char* smem, int off) { return *(const volatile LAS u32x4*)(LAS const char*)(smem + off); }
template <unsigned MASK, bool COND = false>
__device__ __forceinline__ unsigned wait_bytes(const char* smem, int off, unsigned need, unsigned skip = 0) {
  unsigned t, f;
  asm volatile(
      ".if %6\n\ts_cmp_lg_u32 %5, 0\n\ts_cbranch_scc1 2f\n\t.endif\n\t"
      "1:\n\t"
      "ds_read_b32 %1, %2\n\t"
      "s_waitcnt lgkmcnt(0)\n\t"
      "v_readfirstlane_b32 %0, %1\n\t"
      "s_or_b32 %0, %0, 0x80808080\n\t"
      "s_sub_u32 %0, %0, %3\n\t"
      "s_and_b32 %0, %0, %4\n\t"
      "s_cmp_eq_u32 %0, %4\n\t"
      "s_cbranch_scc1 2f\n\t"
      "s_sleep 1\n\t"
      "s_branch 1b\n\t"
      "2:"
      : "=&s"(t), "=&v"(f)
      : "v"(lds_addr(smem + off)), "s"(need * 0x01010101u), "s"(MASK), "s"(COND ? skip : MASK), "i"((int)COND)
      : "scc", "memory");
  return f;
}
__device__ __forceinline__ void wait_full(const char* smem, int off, unsigned need, unsigned cur = 0) {
  unsigned a, b;
  asm volatile(
      "s_cmp_ge_u32 %0, %4\n\t"
      "s_cbranch_scc1 3f\n\t"
      "s_branch 2f\n\t"
      "1:\n\t"
      "s_sleep 1\n\t"
      "2:\n\t"
      "ds_read_b32 %1, %3\n\t"
      "ds_read_b32 %2, %3 offset:4\n\t"
      "s_waitcnt lgkmcnt(0)\n\t"
      "v_min_u32 %1, %1, %2\n\t"
      "s_nop 0\n\t"
      "v_readfirstlane_b32 %0, %1\n\t"
      "s_cmp_lt_u32 %0, %4\n\t"
      "s_cbranch_scc1 1b\n\t"
      "3:"
      : "+s"(cur), "=&v"(a), "=&v"(b)
      : "v"(lds_addr(smem + off)), "s"(need)
      : "scc", "memory");
}
__device__ __forceinline__ unsigned uni(unsigned v) { return (unsigned)__builtin_amdgcn_readfirstlane((int)v); }
__device__ __forceinline__ void dma_tag(char* smem, int off, int n) {
  unsigned keep;
  asm volatile("s_nop 2\n\ts_mov_b32 %0, m0\n\ts_mov_b32 m0, %1\n\ts_mov_b64 exec, 1\n\tglobal_load_lds_dword %2, %3\n\ts_mov_b64 exec, -1\n\ts_mov_b32 m0, %0\n\ts_nop 0"
               : "=&s"(keep) : "s"(uni(lds_addr(smem + off))), "v"(n * 4), "s"(k_tags) : "memory");
}
struct QFrag { bf16x8 xa[5]; bf16x8 w[3][2]; };
__device__ __forceinline__ void q_read(QFrag& f, const char* sa, const char* sb0, const char* sb1, int ks) {
#pragma unroll
  for (int i = 0; i < 5; ++i) f.xa[i] = lds_b128(sa + i * 2048);
#pragma unroll
  for (int s = 0; s < 3; ++s) {
    f.w[s][0] = lds_tr2(sb0 + s * 8192 + ks * 4096);
    f.w[s][1] = lds_tr2(sb1 + s * 8192 + ks * 4096);
  }
}
__device__ constexpr int acc_no(int HALF, int m, int i, int t) {
  int n = 0;
  for (int ii = 0; ii < 5; ++ii)
    for (int tt = 0; tt < 2; ++tt)
      for (int mm = 0; mm < 3; ++mm) {
        const bool on = mm == 0 ? (HALF == 1 || (ii >= 1 && ii <= 3)) : (HALF == 0 || ii >= 1);
        if (mm == m && ii == i && tt == t) return on ? n : -1;
        if (on) ++n;
      }
  return -1;
}
__device__ constexpr int seq_of(int HALF, int z) {
  int n = 0;
  for (int a = 0; a < 30; ++a) {
    const int m = a / 10, t0 = (a / 5) % 2;
    const int i = m < 2 ? (t0 ? a % 5 : 4 - a % 5) : 4 - (a - 20) / 2, t = m < 2 ? t0 : (a ^ i) & 1;
    if (acc_no(HALF, m, i, t) >= 0) { if (n == z) return m * 100 + i * 10 + t; ++n; }
  }
  return -1;
}
template <int HALF, int Z, int E, bool FIRST>
__device__ __forceinline__ void q_one(const QFrag& f, f32x4 (&acc)[26]) {
  if constexpr (Z < E) {
    constexpr int m = seq_of(HALF, Z) / 100, i = seq_of(HALF, Z) / 10 % 10, t = seq_of(HALF, Z) % 10;
    constexpr int n = acc_no(HALF, m, i, t);
    const f32x4 c = FIRST ? f32x4{0, 0, 0, 0} : acc[n];
    acc[n] = m == 2 ? MFMA16(f.xa[i], f.w[2][t], c) : MFMA16(f.w[m][t], f.xa[i], c);
    q_one<HALF, Z + 1, E, FIRST>(f, acc);
  }
}
template <int HALF, int B, int E, int NRD, bool FIRST = false>
__device__ __forceinline__ void q_mma(const QFrag& f, f32x4 (&acc)[26]) {
  q_one<HALF, B, E, FIRST>(f, acc);
#pragma unroll
  for (int i = 0; i < NRD; ++i) {
    __builtin_amdgcn_sched_group_barrier(0x008, 1, 0);
    __builtin_amdgcn_sched_group_barrier(0x100, 1, 0);
  }
  __builtin_amdgcn_sched_group_barrier(0x008, E - B - NRD, 0);
}

__device__ __forceinline__ void tr_all(u32x4 (&w)[8]) {
  u32x4 t[8];
#pragma unroll
  for (int i = 0; i < 8; ++i) t[i] = __builtin_bit_cast(u32x4, tr_frag(w, i));
#pragma unroll
  for (int i = 0; i < 8; ++i) w[i] = t[i];
  asm volatile("" : "+v"(w[0]), "+v"(w[1]), "+v"(w[2]), "+v"(w[3]), "+v"(w[4]), "+v"(w[5]), "+v"(w[6]), "+v"(w[7]));
}
typedef __amdgpu_buffer_rsrc_t rsrc_t;
__device__ __forceinline__ rsrc_t mk_rsrc(const void* p, unsigned bytes) { return __builtin_amdgcn_make_buffer_rsrc((void*)p, 0, bytes, 0x00020000); }
__device__ __forceinline__ u32x4 ld16(rsrc_t rs, unsigned voff, unsigned soff) { return __builtin_amdgcn_raw_buffer_load_b128(rs, voff, soff, 0); }
__device__ __forceinline__ u32x4 ld16c(rsrc_t rs, unsigned voff, unsigned soff) { return __builtin_amdgcn_raw_buffer_load_b128(rs, voff, soff, 16); }
__device__ __forceinline__ void st16c(rsrc_t rs, unsigned voff, u32x4 v) { __builtin_amdgcn_raw_buffer_store_b128(v, rs, voff, 0, 16); }
__device__ __forceinline__ void st8c(rsrc_t rs, unsigned voff, unsigned v, unsigned tag) { __builtin_amdgcn_raw_buffer_store_b64(u32x2{v, tag}, rs, voff, 0, 16); }
__device__ __forceinline__ unsigned chk(unsigned bad, const u32x4& v, unsigned tag) {
  return __builtin_amdgcn_bitop3_b32(__builtin_amdgcn_bitop3_b32(bad, v[1], tag, 0xF6), v[3], tag, 0xF6);
}
__device__ __forceinline__ float f_of(unsigned u) { return __uint_as_float(u); }
#define LANE_VARS                                                                   \
  int lane = lane_in; asm volatile("" : "+v"(lane));                                \
  const int g = lane >> 4, r16 = lane & 15, q = (lane >> 2) & 3, p = lane & 3;      \
  const int hk = (q >> 1) | ((g & 1) << 1), brow = (8 * g + q) * 128 + 8 * p;       \
  (void)r16; (void)hk; (void)brow;

template <bool MAX>
__device__ __forceinline__ float rows_reduce(const float (&v)[4]) {
  auto op = [](unsigned a, unsigned b) {
    return __float_as_uint(MAX ? __builtin_elementwise_maximum(__uint_as_float(a), __uint_as_float(b)) : __uint_as_float(a) + __uint_as_float(b));
  };
  auto p = __builtin_amdgcn_permlane16_swap(__float_as_uint(v[0]), __float_as_uint(v[1]), false, false);
  auto q = __builtin_amdgcn_permlane16_swap(__float_as_uint(v[2]), __float_as_uint(v[3]), false, false);
  auto t = __builtin_amdgcn_permlane32_swap(op(p[0], p[1]), op(q[0], q[1]), false, false);
  return __uint_as_float(op(t[0], t[1]));
}
__device__ __forceinline__ void rows_spread(float t, float (&v)[4]) {
  auto h = __builtin_amdgcn_permlane32_swap(__float_as_uint(t), __float_as_uint(t), false, false);
  auto p = __builtin_amdgcn_permlane16_swap(h[0], h[0], false, false);
  auto q = __builtin_amdgcn_permlane16_swap(h[1], h[1], false, false);
  v[0] = __uint_as_float(p[0]); v[1] = __uint_as_float(p[1]); v[2] = __uint_as_float(q[0]); v[3] = __uint_as_float(q[1]);
}
typedef __attribute__((ext_vector_type(16))) unsigned u32x16;

template <int CTL>
__device__ __forceinline__ float dpp_f(float v) { return __uint_as_float(__builtin_amdgcn_update_dpp(0, (int)__float_as_uint(v), CTL, 0xf, 0xf, false)); }
__device__ __forceinline__ float sum16(float v) {
  v += dpp_f<0xB1>(v);
  v += dpp_f<0x4E>(v);
  v += dpp_f<0x141>(v);
  return v + dpp_f<0x140>(v);
}
__device__ __forceinline__ float dot2(unsigned a, unsigned b, float c) {
  return __builtin_amdgcn_fdot2_f32_bf16(__builtin_bit_cast(bf16x2_t, a), __builtin_bit_cast(bf16x2_t, b), c, false);
}
__device__ __forceinline__ float sum32(float v) {
  v = sum16(v);
  auto a = __builtin_amdgcn_permlane16_swap(__float_as_uint(v), __float_as_uint(v), false, false);
  return __uint_as_float(a[0]) + __uint_as_float(a[1]);
}
struct RowRegs { u32x4 x[4], w[2]; unsigned ep, keep; };
__device__ __forceinline__ void row_request(RowRegs& r, const u16* __restrict__ x, const u16* __restrict__ wr, char* ws, int row0, int lane, bool on) {
  if (!on) return;
  const rsrc_t xrs = mk_rsrc(x, S_TOK * DM * 2), rrs = mk_rsrc(wr, DM * 2);
  const unsigned xo = (unsigned)(row0 + (lane >> 5)) * 2048u + (lane & 31) * 16, wo = lane * 16;
  asm volatile("buffer_load_dword %0, off, %1, 0 sc1" : "=v"(r.ep) : "s"(mk_rsrc(ws + WS_EP, 4)) : "memory");
#pragma unroll
  for (int j = 0; j < 4; ++j) asm volatile("buffer_load_dwordx4 %0, %1, %2, 0 offen offset:%3" : "=v"(r.x[j]) : "v"(xo), "s"(xrs), "i"(j * 512) : "memory");
#pragma unroll
  for (int j = 0; j < 2; ++j) asm volatile("buffer_load_dwordx4 %0, %1, %2, 0 offen offset:%3" : "=v"(r.w[j]) : "v"(wo), "s"(rrs), "i"(j * 1024) : "memory");
}
__device__ __forceinline__ unsigned row_stats(RowRegs& r, char* ws, int row0, int lane) {
  float s0 = 0.f, s1 = 0.f;
  asm volatile("s_waitcnt vmcnt(2)" : "+v"(r.x[0]), "+v"(r.x[1]), "+v"(r.x[2]), "+v"(r.x[3]), "+v"(r.ep), "+v"(r.keep) :: "memory");
  const unsigned tag = uni(r.ep) + 1u;
#pragma unroll
  for (int j = 0; j < 4; ++j) {
    s0 = dot2(r.x[j][0], 0x3f803f80u, s0); s1 = dot2(r.x[j][1], 0x3f803f80u, s1);
    s0 = dot2(r.x[j][2], 0x3f803f80u, s0); s1 = dot2(r.x[j][3], 0x3f803f80u, s1);
  }
  const float mean = sum32(s0 + s1) * (1.0f / DM), mb = rbf(mean);
  s0 = 0.f; s1 = 0.f;
#pragma unroll
  for (int j = 0; j < 4; ++j)
#pragma unroll
    for (int i = 0; i < 4; ++i) {
      const float a = lo2f(r.x[j][i]) - mb, b = hi2f(r.x[j][i]) - mb;
      s0 = fmaf(a, a, s0); s1 = fmaf(b, b, s1);
      r.x[j][i] = pack2(a, b);
    }
  const float dm = mean - mb;
  const float var = rbf(fmaxf(sum32(s0 + s1) * (1.0f / DM) - dm * dm, 0.f));
  const float den = rbf(__builtin_amdgcn_sqrtf(rbf(var + 1e-5f))), rd = __builtin_amdgcn_rcpf(den);
  s0 = 0.f; s1 = 0.f;
  asm volatile("s_waitcnt vmcnt(0)" : "+v"(r.w[0]), "+v"(r.w[1]) :: "memory");
#pragma unroll
  for (int j = 0; j < 4; ++j)
#pragma unroll
    for (int i = 0; i < 4; ++i) {
      const unsigned n = pack2(lo2f(r.x[j][i]) * rd, hi2f(r.x[j][i]) * rd);
      const unsigned w = __builtin_amdgcn_permlane32_swap(r.w[j >> 1][i], r.w[j >> 1][i], false, false)[j & 1];
      if (i & 1) s1 = dot2(n, w, s1); else s0 = dot2(n, w, s0);
    }
  const float dot = sum32(s0 + s1);
  unsigned k = pack2(dot, dot) & 0xffffu;
  k = (k & 0x8000u) ? (~k & 0xffffu) : (k | 0x8000u);
  const rsrc_t wrs = mk_rsrc(ws, WS_O);
  st16c(wrs, (lane & 31) == 0 ? WS_R + (unsigned)(row0 + (lane >> 5)) * 16u : 0x7fffffffu,
        u32x4{(k << 16) | (__float_as_uint(mb) >> 16), tag, __float_as_uint(den), tag});
  const unsigned k01 = k | ((unsigned)__builtin_amdgcn_readlane((int)k, 32) << 16);
  st8c(wrs, lane == 0 ? WS_K + (unsigned)(((row0 >> 2) & 7) * 1024 + (row0 >> 5) * 16 + (row0 & 2) * 4) : 0x7fffffffu, k01, tag);
  return tag;
}
__device__ __forceinline__ void k_order(int tm, int tn, int& pa, int& pb) {
  pb = tn & 3;
  pa = (tm * 0x1111) ^ ((tn & 4) * 0x51 & 0x144) ^ ((~tn & 2) * 0x2200) ^ ((~tn & 1) << 9) ^ (pb == 3 ? 1 : tm & 1);
}
__device__ __forceinline__ int k_tile(int t, int pa, int pb) { return ((((t + pb) & 12) ? t + pb : t & 3) ^ (pa >> ((t + pb) & 12))) & 15; }
struct EpiRegs { int a0, b0, tx; unsigned xr, go; u32x4 xv[2][2]; unsigned t[16]; };
__device__ __forceinline__ float opq(float v) { asm("" : "+v"(v)); return v; }
typedef __attribute__((ext_vector_type(10))) unsigned u32x10;
#define DV 0
#define PRE 3
#define GRP(N, M, K, C) _Pragma("unroll") for (int z_ = 0; z_ < (N); ++z_) { __builtin_amdgcn_sched_group_barrier(0x008, M, 0); __builtin_amdgcn_sched_group_barrier(K, C, 0); }
__device__ constexpr int tail_of(int HALF, int z) {
  int n = 0;
  for (int a = 0; a < 30; ++a) {
    const int i = HALF ? a / 6 : 4 - a / 6, m = 2 - a % 6 / 2, t = a & 1;
    if (acc_no(HALF, m, i, t) >= 0 && (m < 2 || (HALF == 0 && i == 4))) { if (n == z) return m * 100 + i * 10 + t; ++n; }
  }
  return -1;
}
__device__ __forceinline__ void qkv_setup(char* smem, int wid, int lane_in, int tm, int tn, EpiRegs& e) {
  LANE_VARS
  e.a0 = (wid & 1) * 8192 + r16 * 128 + ((g ^ (r16 >> 1)) << 4);
  e.b0 = XB + brow + (((wid & 2) ^ hk) << 5);
  int pa, pb;
  k_order(tm, tn, pa, pb);
  e.tx = __builtin_ctz((unsigned)__ballot(k_tile(r16, pa, pb) == tn));
  e.xr = lds_addr(smem) + (16 + wid * 32 + r16) * 128 + ((((g & 1) * 2 + (g >> 1)) ^ (r16 >> 1)) << 4);
  asm volatile("" : "+v"(e.a0), "+v"(e.b0), "+v"(e.xr));
  asm volatile("" : "+s"(e.tx));
}
template <int HALF>
__device__ __forceinline__ void qkv_attention(char* smem, int wid, int lane_in, int tm, int tn, char* ws, unsigned tag,
                                              const u16* __restrict__ x, EpiRegs& e) {
  const int h = wid >> 1;
  f32x4 acc[26];
  float ra[5], rb[5], sa[4], sb[4];
  QFrag f0, f1;
  u32x4 fl;
  unsigned aq, ac, aa, ab, xa, xv, xf, ooff, sel[4];
  {
    LANE_VARS
    const int a0 = e.a0, b0 = e.b0, tx = uni(e.tx);
    const unsigned xr = e.xr;
    asm volatile("" : "=v"(e.xv[0][0]), "=v"(e.xv[0][1]), "=v"(e.xv[1][0]), "=v"(e.xv[1][1]));
    auto keep = [&](int t, int slot, int tx) {
      unsigned p0, p1;
      asm volatile("s_cmp_lg_u32 %7, %8\n\ts_cbranch_scc1 1f\n\tv_add_u32 %4, %9, %6\n\tv_xor_b32 %5, 64, %4\n\tds_read_b128 %0, %4\n\tds_read_b128 %1, %5\n\t"
                   "ds_read_b128 %2, %4 offset:2048\n\tds_read_b128 %3, %5 offset:2048\n\t1:"
                   : "+v"(e.xv[0][0]), "+v"(e.xv[0][1]), "+v"(e.xv[1][0]), "+v"(e.xv[1][1]), "=&v"(p0), "=&v"(p1) : "v"(xr), "s"(t), "s"(tx), "s"(slot) : "scc", "memory");
    };
    SCHED0();
    WAITL0();
    BAR();
    STAMP(1);
    q_read(f0, smem + a0, smem + b0, smem + (b0 ^ 32), 0);
    SCHED0();
    q_read(f1, smem + (a0 ^ 64), smem + b0, smem + (b0 ^ 32), 1);
    int qa = SLOT + a0, qc = SLOT + (a0 ^ 64), qb = SLOT + b0, qd = SLOT + (b0 ^ 32), so = SLOT;
    q_mma<HALF, 0, 26, 17, true>(f0, acc);
    asm volatile("" : "+v"(qa), "+v"(qb), "+v"(qc), "+v"(qd));
    SCHED0();
    keep(0, 0, tx);
#pragma unroll 1
    for (int t = 1; t < NBAR - 1; ) {
      SCHED0();
      WAITL0();
      BAR();
      SCHED0();
      q_read(f0, smem + qa, smem + qb, smem + qd, 0);
      q_mma<HALF, 0, 26, 17>(f1, acc);
      SCHED0();
      q_read(f1, smem + qc, smem + qb, smem + qd, 1);
      q_mma<HALF, 0, 19, 17>(f0, acc);
      SCHED0();
      int d = so == 2 * SLOT ? -2 * SLOT : SLOT;
      asm volatile("" : "+s"(d));
      SCHED0();
      q_mma<HALF, 19, 20, 0>(f0, acc);
      SCHED0();
      keep(t, so, tx);
      SCHED0();
      q_mma<HALF, 20, 21, 0>(f0, acc);
      SCHED0(); qa += d; qb += d; asm volatile("" : "+v"(qa), "+v"(qb)); SCHED0();
      q_mma<HALF, 21, 22, 0>(f0, acc);
      SCHED0(); qc += d; qd += d; asm volatile("" : "+v"(qc), "+v"(qd)); SCHED0();
      q_mma<HALF, 22, 23, 0>(f0, acc);
      SCHED0(); so += d; ++t; asm volatile("" : "+s"(so), "+s"(t)); SCHED0();
      q_mma<HALF, 23, 26, 0>(f0, acc);
      SCHED0();
    }
    WAITL0();
    BAR();
    STAMP(2);
#pragma unroll 1
    for (int t = NBAR + 1; t < 17; ) {
      SCHED0();
      q_read(f0, smem + qa, smem + qb, smem + qd, 0);
      q_mma<HALF, 0, 26, 17>(f1, acc);
      SCHED0();
      q_read(f1, smem + qc, smem + qb, smem + qd, 1);
      const u32x4 fl = lds_word4(smem, F_FULL);
      q_mma<HALF, 0, 21, 18>(f0, acc);
      SCHED0();
      const unsigned cur = uni(min(fl[0], fl[1])), need = t;
      SCHED0();
      q_mma<HALF, 21, 22, 0>(f0, acc);
      SCHED0();
      keep(t, so, tx + 2);
      SCHED0();
      qa += SLOT; qb += SLOT; qc += SLOT; qd += SLOT; so += SLOT; ++t;
      q_one<HALF, 22, 25, false>(f0, acc);
      GRP(2, 1, 0x002, 2);
      asm volatile("" : "+v"(qa), "+v"(qb), "+v"(qc), "+v"(qd), "+s"(so), "+s"(t));
      SCHED0();
      q_mma<HALF, 25, 26, 0>(f0, acc);
      SCHED0();
      wait_full(smem, F_FULL, need, cur);
    }
    SCHED0();
    q_read(f0, smem + a0, smem + b0, smem + (b0 ^ 32), 0);
    q_one<HALF, 0, 26, false>(f1, acc);
    {
      const int pa = 4 * g + (r16 & 1);
      const bool odd = r16 & 1, cur_a = pa <= r16, cur_b = pa + 2 <= r16;
      const unsigned qs = lds_addr(smem + L_QST + HALF * 512);
      aq = qs + r16 * 8; ac = lds_addr(smem + L_CSF + h * 256) + r16 * 8;
      aa = qs + pa * 8 + (cur_a ? 128 : 0); ab = qs + pa * 8 + (cur_b ? 128 : 0);
      xa = lds_addr(smem + L_XCH + h * 3072) + lane * 16; xv = xa + 2048 - lane * 8; xf = lds_addr(smem + F_XCH + h * 4);
      const unsigned la = odd ? 0x01000c0cu : 0x0c0c0100u, lb = odd ? 0x03020c0cu : 0x0c0c0302u;
      sel[0] = cur_a ? 0x0c0c0c0cu : la; sel[1] = cur_b ? 0x0c0c0c0cu : lb; sel[2] = cur_a ? la : 0x0c0c0c0cu; sel[3] = cur_b ? lb : 0x0c0c0c0cu;
      ooff = (unsigned)(tm * 8 + HALF * 4) * 32768u + (tn * 2 + h) * 1024 + lane * 16;
#pragma unroll
      for (int z = 0; z < 17; ++z) { GRP(1, 1, 0x100, 1); __builtin_amdgcn_sched_group_barrier(0x002, 1, 0); }
      GRP(9, 1, 0x002, 3);
      asm volatile("" : "+v"(aq), "+v"(ac), "+v"(aa), "+v"(ab), "+v"(xa), "+v"(xv), "+v"(xf), "+v"(ooff));
      asm volatile("" : "+v"(sel[0]), "+v"(sel[1]), "+v"(sel[2]), "+v"(sel[3]));
    }
    SCHED0();
#pragma unroll
    for (int i = 0; i < 5; ++i) f1.xa[i] = lds_b128(smem + (a0 ^ 64) + i * 2048);
#pragma unroll
    for (int s = 0; s < 3; ++s) {
      if (s == 2) fl = lds_word4(smem, F_FULL);
      f1.w[s][0] = lds_tr2(smem + b0 + s * 8192 + 4096);
      f1.w[s][1] = lds_tr2(smem + (b0 ^ 32) + s * 8192 + 4096);
    }
    q_mma<HALF, 0, 26 - DV, 18>(f0, acc);
    SCHED0();
    keep(15, 0, tx);
    SCHED0();
  }
  LANE_VARS
  auto mm = [&](const QFrag& f, int m, int i, int t) {
    const int n = max(acc_no(HALF, m, i, t), 0);
    acc[n] = m == 2 ? MFMA16(f.xa[i], f.w[2][t], acc[n]) : MFMA16(f.w[m][t], f.xa[i], acc[n]);
  };
  auto last = [&](int z0, int z1) {
#pragma unroll
    for (int z = z0; z < z1; ++z) {
      const int c = tail_of(HALF, HALF ? z : z < 6 ? z + 2 : z < 8 ? z - 6 : z);
      mm(f1, c / 100, c / 10 % 10, c % 10);
    }
  };
  last(0, PRE);
  SCHED0();
  const unsigned cur = uni(min(fl[2], fl[3]));
  SCHED0();
  last(PRE, PRE + 1);
  SCHED0();
  wait_full(smem, F_ST, 1, cur);
  SCHED0();
  u32x2 sq[5], cs[3][2];
#pragma unroll
  for (int i = 0; i < 5; ++i) sq[i] = *(const LAS u32x2*)(aq + i * 128);
#pragma unroll
  for (int s = 0; s < 3; ++s)
#pragma unroll
    for (int t = 0; t < 2; ++t) cs[s][t] = *(const LAS u32x2*)(ac + s * 512 + t * 128);
#pragma unroll
  for (int i = 1; i < 5; ++i) { ra[i] = *(const LAS float*)(aa + (i - 1) * 128 + 4); rb[i] = *(const LAS float*)(ab + (i - 1) * 128 + 20); }
  last(PRE + 1, PRE + 5);
  __builtin_amdgcn_sched_group_barrier(0x100, 3, 0); GRP(3, 1, 0x100, 3);
  SCHED0();
  lds_post(smem, F_FREE + wid, 16);
  SCHED0();
  last(PRE + 5, 18);
  const unsigned gm = g == 0 ? ~0u : 0u;
  u32x2 lx[5];
#pragma unroll
  for (int i = 0; i < 5; ++i) lx[i] = u32x2{sq[i][0] & gm, sq[i][0] & gm & 0xffffu};
#pragma unroll
  for (int i = 1; i < 5; ++i) {
    const float c = f_of(sq[i][1]) * (0.17677669529663687f * 1.4426950408889634f);
    sa[i - 1] = opq(c * ra[i]); sb[i - 1] = opq(c * rb[i]);
  }
  __builtin_amdgcn_sched_group_barrier(0x008, 5 - PRE, 0); GRP(8, 1, 0x002, 3);
  asm volatile("" : "+v"(lx[0]), "+v"(lx[1]), "+v"(lx[2]), "+v"(lx[3]), "+v"(lx[4]));
  asm volatile("" : "+v"(sa[0]), "+v"(sa[1]), "+v"(sa[2]), "+v"(sa[3]), "+v"(sb[0]), "+v"(sb[1]), "+v"(sb[2]), "+v"(sb[3]));
  SCHED0();
  auto ln = [&](int m, int i, int t) {
    const int n = max(acc_no(HALF, m, i, t), 0);
    const s16x4 lxi = __builtin_bit_cast(s16x4, lx[i]), lw = __builtin_bit_cast(s16x4, cs[m][t]);
    acc[n] = m == 2 ? __builtin_amdgcn_mfma_f32_16x16x16bf16_1k(lxi, lw, acc[n], 0, 0, 0) : __builtin_amdgcn_mfma_f32_16x16x16bf16_1k(lw, lxi, acc[n], 0, 0, 0);
  };
#pragma unroll
  for (int z = 0; z < 18; ++z) ln(tail_of(HALF, z) / 100, tail_of(HALF, z) / 10 % 10, tail_of(HALF, z) % 10);
  STAMP(3);
  bf16x8 qf[5], kf[5];
  u32x10 vf[2];
  auto pack_qk = [&](int i) {
    if (acc_no(HALF, 0, i, 0) >= 0) qf[i] = pack8(acc[max(acc_no(HALF, 0, i, 0), 0)], acc[max(acc_no(HALF, 0, i, 1), 0)]);
    if (acc_no(HALF, 1, i, 0) >= 0) kf[i] = pack8(acc[max(acc_no(HALF, 1, i, 0), 0)], acc[max(acc_no(HALF, 1, i, 1), 0)]);
  };
  auto pack_v = [&](int i, int t) {
    const f32x4& v = acc[max(acc_no(HALF, 2, i, t), 0)];
    return u32x2{pack2(v[0], v[1]), pack2(v[2], v[3])};
  };
  u32x2 vx[2];
  pack_qk(HALF == 0 ? 4 : 0);
  if (HALF == 0) {
    vx[0] = pack_v(4, 0); vx[1] = pack_v(4, 1);
    asm volatile("ds_write_b128 %0, %2 offset:1024\n\tds_write2st64_b64 %1, %3, %4 offset1:1" :: "v"(xa), "v"(xv), "v"(kf[4]), "v"(vx[0]), "v"(vx[1]) : "memory");
  } else {
    asm volatile("ds_write_b128 %0, %1" :: "v"(xa), "v"(qf[0]) : "memory");
  }
  asm volatile("s_mov_b64 exec, 1\n\tds_write_b8 %0, %0 offset:%1\n\ts_mov_b64 exec, -1" ::"v"(xf), "i"(HALF) : "memory");
  static_assert((F_XCH & 0xf) != 0 && ((F_XCH + 4) & 0xf) != 0, "the low byte of the address is the flag");
#pragma unroll
  for (int i = 0; i < 4; ++i) pack_qk(HALF == 0 ? 3 - i : i + 1);
  const f32x4 z4 = {0.f, 0.f, 0.f, 0.f};
  f32x4 sp[4], sc[4];
#pragma unroll
  for (int j = 0; j < 3; ++j) {
    const int i = HALF == 0 ? 3 - j : j + 2;
    sc[i - 1] = MFMA16(kf[i], qf[i], z4); sp[i - 1] = MFMA16(kf[i - 1], qf[i], z4);
  }
  if (HALF) sc[0] = MFMA16(kf[1], qf[1], z4);
  GRP(1, 6, 0x002, 4); GRP(12, 1, 0x002, 2); GRP(HALF ? 7 : 6, 1, 0x002, 3);
  SCHED0();
  unsigned flag;
  bf16x8 xq;
  if (HALF == 0) asm volatile("ds_read_b32 %0, %2\n\tds_read_b128 %1, %3" : "=&v"(flag), "=&v"(xq) : "v"(xf), "v"(xa) : "memory");
  else asm volatile("ds_read_b32 %0, %4\n\tds_read_b128 %1, %5 offset:1024\n\tds_read_b64 %2, %6\n\tds_read_b64 %3, %6 offset:512"
                    : "=&v"(flag), "=&v"(xq), "=&v"(vx[0]), "=&v"(vx[1]) : "v"(xf), "v"(xa), "v"(xv) : "memory");
  SCHED0();
  const bool odd = r16 & 1, cur_a = sel[0] == 0x0c0c0c0cu, cur_b = sel[1] == 0x0c0c0c0cu;
  float a[4], b[4], m[4];
  auto logits = [&](int j) {
    const f32x4 p4 = sp[j], c4 = sc[j];
    const float p0 = p4[0], p1 = p4[1], p2 = p4[2], p3 = p4[3], c0 = c4[0], c1 = c4[1], c2 = c4[2], c3 = c4[3];
    const float ca = odd ? c1 : c0, cb = odd ? c3 : c2, pa_ = odd ? p1 : p0, pb = odd ? p3 : p2;
    a[j] = opq((cur_a ? ca : pa_) * sa[j]);
    b[j] = opq((cur_b ? cb : pb) * sb[j]);
    if (HALF == 0 && j == 0) {
      a[j] = (!cur_a && tm == 0) ? -INFINITY : a[j];
      b[j] = (!cur_b && tm == 0) ? -INFINITY : b[j];
    }
    m[j] = __builtin_elementwise_maximum(a[j], b[j]);
  };
  auto late = [&](int z0, int z1) {
#pragma unroll
    for (int y = z0; y < z1; ++y) {
      const int c = seq_of(HALF, 18 + ((y + 8 - DV) & 7));
      if (y < 8 + DV) mm(y < DV ? f0 : f1, 2, c / 10 % 10, c % 10);
    }
  };
#pragma unroll
  for (int j = 0; j < 2; ++j) logits(HALF == 0 ? 2 - j : j + 1);
  late(0, 6);
  GRP(6, 1, 0x002, 3);
  asm volatile("" : "+v"(m[1]), "+v"(m[2]));
  SCHED0();
  {
    unsigned t;
#define XWAIT(RD) "s_waitcnt lgkmcnt(0)\n\tv_readfirstlane_b32 %0, %1\n\ts_and_b32 %0, %0, %[mask]\n\ts_cbranch_scc1 2f\n\t1:\n\t" RD \
                  "\n\ts_waitcnt lgkmcnt(0)\n\tv_readfirstlane_b32 %0, %1\n\ts_and_b32 %0, %0, %[mask]\n\ts_cbranch_scc0 1b\n\t2:"
    if (HALF == 0) asm volatile(XWAIT("ds_read_b32 %1, %3\n\tds_read_b128 %2, %4") : "=&s"(t), "+v"(flag), "+v"(xq) : "v"(xf), "v"(xa), [mask] "s"(0xff00u) : "scc", "memory");
    else asm volatile(XWAIT("ds_read_b32 %1, %5\n\tds_read_b128 %2, %6 offset:1024\n\tds_read_b64 %3, %7\n\tds_read_b64 %4, %7 offset:512")
                      : "=&s"(t), "+v"(flag), "+v"(xq), "+v"(vx[0]), "+v"(vx[1]) : "v"(xf), "v"(xa), "v"(xv), [mask] "s"(0xffu) : "scc", "memory");
  }
  SCHED0();
  if (HALF == 0) { sc[3] = MFMA16(kf[4], xq, z4); sp[3] = MFMA16(kf[3], xq, z4); }
  else sp[0] = MFMA16(xq, qf[1], z4);
  logits(HALF == 0 ? 0 : 3);
  late(6, 8);
  GRP(1, HALF ? 1 : 2, 0x002, 3); GRP(2, 1, 0x002, 3);
  asm volatile("" : "+v"(m[HALF ? 3 : 0]));
  SCHED0();
  logits(HALF == 0 ? 3 : 0);
  rows_spread(rows_reduce<true>(m), m);
#pragma unroll
  for (int j = 0; j < 4; ++j) {
    a[j] = __builtin_amdgcn_exp2f(opq(a[j] - m[j]));
    b[j] = __builtin_amdgcn_exp2f(opq(b[j] - m[j]));
    m[j] = opq(a[j] + b[j]);
    a[j] = opq(a[j] * ra[j + 1]); b[j] = opq(b[j] * rb[j + 1]);
  }
  late(8, 16);
#pragma unroll
  for (int z = 0; z < 8; ++z) ln(2, seq_of(HALF, 18 + z) / 10 % 10, seq_of(HALF, 18 + z) % 10);
  GRP(DV, 1, 0x402, 3); GRP(8, 1, 0x402, 2);
  asm volatile("" : "+v"(m[0]), "+v"(m[1]), "+v"(m[2]), "+v"(m[3]));
  SCHED0();
#pragma unroll
  for (int t = 0; t < 2; ++t) {
#pragma unroll
    for (int i = 0; i < 5; ++i) {
      const u32x2 v = i == (HALF ? 0 : 4) ? vx[t] : pack_v(i, t);
      vf[t][2 * i] = v[0]; vf[t][2 * i + 1] = v[1];
    }
    asm("" : "+v"(vf[t]));
  }
  rows_spread(__builtin_amdgcn_rcpf(rows_reduce<false>(m)), m);
  const rsrc_t ors = mk_rsrc(ws + WS_O, S_TOK * DM * 2);
#pragma unroll
  for (int j = 0; j < 4; ++j) {
    const unsigned pk = pack2(a[j] * m[j], b[j] * m[j]);
    const u32x4 pw = {__builtin_amdgcn_perm(0u, pk, sel[0]), __builtin_amdgcn_perm(0u, pk, sel[1]), __builtin_amdgcn_perm(0u, pk, sel[2]), __builtin_amdgcn_perm(0u, pk, sel[3])};
    const u32x4 v0 = {vf[0][2 * j], vf[0][2 * j + 1], vf[0][2 * j + 2], vf[0][2 * j + 3]}, v1 = {vf[1][2 * j], vf[1][2 * j + 1], vf[1][2 * j + 2], vf[1][2 * j + 3]};
    sp[j] = MFMA16(__builtin_bit_cast(bf16x8, v0), __builtin_bit_cast(bf16x8, pw), z4);
    sc[j] = MFMA16(__builtin_bit_cast(bf16x8, v1), __builtin_bit_cast(bf16x8, pw), z4);
  }
#pragma unroll
  for (int j = 0; j < 4; ++j) { qf[j] = pack8(sp[j], sc[j]); st16c(ors, ooff + j * 32768, __builtin_bit_cast(u32x4, qf[j])); }
  RST(4);
  SCHED0();
  asm volatile("ds_read_b32 %0, %1 offset:%2" : "=v"(e.go) : "v"(xf - h * 4), "i"(F_GO - F_XCH) : "memory");
  wait_bytes<0x80808080u>(smem, F_FREE, 16);
  asm volatile("" : "+v"(e.go));
  asm volatile("ds_write_b128 %0, %1\n\tds_write_b128 %0, %2 offset:1024\n\tds_write_b128 %0, %3 offset:4096\n\tds_write_b128 %0, %4 offset:5120"
               :: "v"(lds_addr(smem) + HALF * 8192 + h * 2048 + lane * 16), "v"(qf[0]), "v"(qf[1]), "v"(qf[2]), "v"(qf[3]) : "memory");
  lds_post(smem, F_OL + wid, 1);
}

__device__ __forceinline__ unsigned ln2s(unsigned w, float mb, float r) {
  const unsigned d = pack2(opq(lo2f(w) - mb), opq(hi2f(w) - mb));
  return pack2(opq(lo2f(d) * r), opq(hi2f(d) * r));
}

__device__ __forceinline__ void await_attention(char* smem, int lane, int tm, int tn, char* ws, unsigned tag, unsigned& tp) {
  const rsrc_t wrs = mk_rsrc(ws, WS_O);
  const unsigned off = WS_OF + (tm * 64 + ((lane >> 3) & 1) * 32 + (lane & 7)) * 4;
  const int own = tn >> 3;
  bool first = true;
  unsigned seen = 0, since = 0;
#pragma unroll 1
  for (unsigned n = 0; n < SPIN_CAP; ++n) {
    unsigned want = ~seen & 0xffffu;
    if (since) ++since; else since = uni(lds_word(smem, F_ODONE)) >> 2;
    if (since <= (first ? OK : 0)) want &= own ? 0x00ffu : 0xff00u;
    const unsigned f = __builtin_amdgcn_raw_buffer_load_b32(wrs, ((want >> (lane & 15)) & 1) ? off : 0x7fffffffu, 0, 16);
    seen |= (unsigned)__ballot(f == tag) & want;
    if (seen == 0xffffu) break;
    if (first && ((seen & 0xffu) == 0xffu || (seen >> 8) == 0xffu)) { lds_post(smem, F_GO, (seen & 0xffu) == 0xffu ? 1 : 2); first = false; NAP(ON); }
    NAP(PN);
  }
  lds_post(smem, F_GO + 1, 1);
  if (first) lds_post(smem, F_GO, 1);
}

__device__ __forceinline__ void out_proj(char* smem, int wid, int lane_in, int tm, int tn, char* ws, unsigned tag,
                                         u16* __restrict__ out, EpiRegs& e, unsigned rot2) {
  LANE_VARS
  const rsrc_t ors = mk_rsrc(ws + WS_O, S_TOK * DM * 2), yrs = mk_rsrc(out, S_TOK * DM * 2);
  const int tok = tm * 128 + wid * 32 + r16, co = (g & 1) * 16 + (g >> 1) * 8;
  unsigned eoff = (unsigned)(tok * DM + tn * 64 + co) * 2u;
  const unsigned vo = (unsigned)(tm * 8 + wid * 2) * 32768u + lane * 16;
  bf16x8 wb[3][4], oa[OD][2];
  int bc[4];
#pragma unroll
  for (int c = 0; c < 4; ++c) bc[c] = L_WO + brow + ((c ^ hk) << 5);
  f32x4 acc[2][4];
  SCHED0();
  unsigned go, oth = (tn & 8) * 2 ^ 16;
  asm volatile("" : "+s"(oth));
  auto o_request = [&](int s) {
    asm volatile("buffer_load_dwordx4 %0, %2, %4, %5 offen sc1\n\tbuffer_load_dwordx4 %1, %3, %4, %5 offen sc1" : "+v"(oa[s % OD][0]), "+v"(oa[s % OD][1])
                 : "v"(vo), "v"(vo + 32768), "s"(ors), "s"(((s >> 1 == 7 ? oth : (s & 16) ^ ((go & 2) * 8)) + ((rot2 >> (s & 14) * 2) & 14) + (s & 1)) * 1024) : "memory");
  };
  auto start = [&](unsigned word) {
    go = uni(word) & 3;
    if (go) {
#pragma unroll
      for (int s = 0; s < OD - 1; ++s) o_request(s);
    }
    SCHED0();
  };
#pragma unroll
  for (int s = 0; s < OD; ++s) asm volatile("" : "=v"(oa[s][0]), "=v"(oa[s][1]));
  {
    const unsigned w0 = uni(e.go);
    if (w0 & 3) wait_bytes<0x80808080u>(smem, F_OL, 1);
    start(w0);
  }
  asm volatile("s_waitcnt vmcnt(%1)\n\ts_cmp_lg_u32 %0, 0\n\ts_cbranch_scc1 1f\n\ts_waitcnt vmcnt(0)\n\t1:" :: "s"(go), "i"(2 * OD - 2) : "scc", "memory");
  unsigned old = 0;
  if (lane == 0) old = __hip_atomic_fetch_add((LAS unsigned*)(LAS char*)(smem + F_ODONE), 1u, __ATOMIC_RELAXED, __HIP_MEMORY_SCOPE_WORKGROUP);
  if (uni(old) == 3u) {
    if (lane == 0) __hip_atomic_store((GAS unsigned*)(ws + WS_OF) + tm * 64 + (tn >> 3) * 32 + (tn & 7), tag, RLX_AGENT);
  }
  RST(5);
  SCHED0();
  const unsigned w1 = lds_word(smem, F_GO);
  const unsigned srow = 2047u - (*(const unsigned*)(smem + F_SEL + (lane & 7) * 4) & 0xffffu);
  f32x4 res[2][4];
  const uint2 st[2] = {*(const uint2*)(smem + L_QST + (16 + wid * 32 + r16) * 8), *(const uint2*)(smem + L_QST + (32 + wid * 32 + r16) * 8)};
  auto norm = [&](int u) {
    const float rd = f_of(st[u].y), k = 1.0f + rd, b = lo2f(st[u].x) * rd;
#pragma unroll
    for (int pr = 0; pr < 2; ++pr) {
      auto sa = __builtin_amdgcn_permlane16_swap(e.xv[u][pr][0], e.xv[u][pr][2], false, false);
      auto sb = __builtin_amdgcn_permlane16_swap(e.xv[u][pr][1], e.xv[u][pr][3], false, false);
#pragma unroll
      for (int z = 0; z < 2; ++z) res[u][2 * pr + z] = f32x4{lo2f(sa[z]), hi2f(sa[z]), lo2f(sb[z]), hi2f(sb[z])} * k + b;
    }
    asm volatile("" : "+v"(res[u][0]), "+v"(res[u][1]), "+v"(res[u][2]), "+v"(res[u][3]));
  };
  f32x4 ac2[2][4], (&ac3)[2][4] = res;
  auto w_frag = [&](int slot, const int (&b)[4], int z) {
#pragma unroll
    for (int c = 0; c < 4; ++c) wb[slot][c] = lds_tr2(smem + b[c] + z * 4096);
  };
  auto step = [&](f32x4 (&d)[2][4], int slot, const bf16x8 (&o)[2], bool fresh) {
#pragma unroll
    for (int c = 0; c < 4; ++c)
#pragma unroll
      for (int uu = 0; uu < 2; ++uu) {
        const int u = uu ^ (c & 1);
        d[u][c] = MFMA16(wb[slot][c], o[u], fresh ? (f32x4{0, 0, 0, 0}) : d[u][c]);
      }
  };
  int b3[4];
#pragma unroll
  for (int c = 0; c < 4; ++c) b3[c] = bc[c] + 28 * 4096;
  auto own_heads = [&]() {
    w_frag(0, b3, 2); w_frag(1, b3, 3);
    const unsigned w2 = lds_word(smem, F_GO);
    SCHED0();
    if (wid < 2) wait_bytes<0x00800080u>(smem, F_OL, 1); else wait_bytes<0x80008000u>(smem, F_OL, 1);
    if (!go) start(w2);
    bf16x8 own[2][2];
#pragma unroll
    for (int z = 0; z < 4; ++z) own[z >> 1][z & 1] = lds_b128(smem + wid * 4096 + lane * 16 + z * 1024);
    step(ac3, 0, own[0], false); step(ac3, 1, own[1], false);
    asm volatile("" : "+v"(ac3[0][0]), "+v"(ac3[0][1]), "+v"(ac3[0][2]), "+v"(ac3[0][3]), "+v"(ac3[1][0]), "+v"(ac3[1][1]), "+v"(ac3[1][2]), "+v"(ac3[1][3]));
    SCHED0();
  };
  unsigned mine = (unsigned)__ballot((srow >> 5) == (unsigned)(tm * 4 + wid)) & 0xffu;
  asm volatile("" : "+s"(mine));
  asm volatile("" : "+v"(eoff));
  if (!go) start(w1);
  norm(0);
  norm(1);
  SCHED0();
  own_heads();
  if (!go) start(wait_bytes<0x80u>(smem, F_GO, 1));
  go = uni(go);
  RST(6);
  int b2[4];
  const int mine1st = ((go >> 1) ^ (tn >> 3) ^ 1) & 1;
#pragma unroll
  for (int c = 0; c < 4; ++c) { b2[c] = bc[c] + mine1st * 57344; bc[c] += (mine1st ^ 1) * 57344; }
  unsigned so[15];
#pragma unroll
  for (int z = (OD - 1) / 2; z < 15; ++z) { so[z] = ((z == 7 ? oth : (2 * z & 16) ^ ((go & 2) * 8)) + ((rot2 >> (z & 7) * 4) & 14)) * 1024; asm volatile("" : "+s"(so[z])); }
  s16x4 wh[3][8];
  auto w_half = [&](int s, int z0, int z1) {
#pragma unroll
    for (int z = z0; z < z1; ++z)
      if (s < 30) wh[s % 3][z] = __builtin_amdgcn_ds_read_tr16_b64_v4i16((LAS s16x4*)(smem + (s < 14 ? bc[z >> 1] + s * 4096 : s < 16 ? b3[z >> 1] + (s - 14) * 4096 : b2[z >> 1] + (s - 16) * 4096) + (z & 1) * 512));
  };
  wait_full(smem, F_WO, 1);
  RST(7);
  RSC(0);
  w_half(0, 0, 8); w_half(1, 0, 8);
  SCHED0();
  asm volatile("s_waitcnt vmcnt(%2)" : "+v"(oa[0][0]), "+v"(oa[0][1]) : "i"(2 * OD - 4) : "memory");
  auto one = [&](int s) __attribute__((always_inline)) {
    f32x4 (&d)[2][4] = s < 14 ? ac2 : s < 16 ? ac3 : acc;
    auto part = [&](int z0, int z1) {
      SCHED0();
      w_half(s + 2, z0, z1);
#pragma unroll
      for (int z = z0; z < z1; ++z) {
        const int c = z >> 1, u = (z ^ c) & 1;
        d[u][c] = MFMA16(__builtin_shufflevector(wh[s % 3][2 * c], wh[s % 3][2 * c + 1], 0, 1, 2, 3, 4, 5, 6, 7), oa[s % OD][u], s == 0 || s == 16 ? (f32x4{0, 0, 0, 0}) : d[u][c]);
        __builtin_amdgcn_sched_group_barrier(0x008, 1, 0);
        __builtin_amdgcn_sched_group_barrier(0x100, 1, 0);
      }
      SCHED0();
    };
    const int n = s + OD - 1;
    auto ask = [&](int u) {
      if (n < 30) asm volatile("buffer_load_dwordx4 %0, %1, %2, %3 offen offset:%4 sc1" : "+v"(oa[n % OD][u]) : "v"(vo + u * 32768), "s"(ors), "s"(so[n >> 1]), "i"((n & 1) * 1024) : "memory");
    };
    part(0, 1);
    if (n == 14 || n == 16) wait_bytes<0x8000u, true>(smem, F_GO, 1, n == 14 ? mine1st ^ 1 : mine1st);
    ask(0);
    part(1, 3);
    ask(1);
    part(3, 5);
    if (s < 29) asm volatile("s_waitcnt vmcnt(%2)" : "+v"(oa[(s + 1) % OD][0]), "+v"(oa[(s + 1) % OD][1]) : "i"(2 * ((n < 29 ? n : 29) - s - 1)) : "memory");
    part(5, 8);
    asm volatile("" : "+v"(d[0][0]), "+v"(d[0][1]), "+v"(d[0][2]), "+v"(d[0][3]), "+v"(d[1][0]), "+v"(d[1][1]), "+v"(d[1][2]), "+v"(d[1][3]));
    if (s == 0) { RST(14); RSC(1); }
    if (s == 14) { RST(15); RSC(2); }
  };
#pragma unroll
  for (int s = 0; s < 10; ++s) one(s);
#pragma unroll
  for (int s = 10; s < 20; ++s) one(s);
#pragma unroll
  for (int s = 20; s < 28; ++s) one(s);
  asm volatile("s_waitcnt vmcnt(0)" : "+v"(oa[29 % OD][0]), "+v"(oa[29 % OD][1]) :: "memory");
  if (mine != 0) {
    wait_bytes<0x80808080u>(smem, F_UP, 1);
#pragma unroll 1
    for (; mine != 0; mine &= mine - 1) {
      const int c = __builtin_ctz(mine), row = __builtin_amdgcn_readlane((int)srow, c) & 31;
#pragma unroll
      for (int u = 0; u < 2; ++u)
        if (row == u * 16 + r16) {
#pragma unroll
          for (int z = 0; z < 4; ++z) res[u][z] += *(const f32x4*)(smem + L_UP + c * 256 + (z * 16 + g * 4) * 4);
        }
    }
  }
  SCHED0();
#pragma unroll
  for (int u = 0; u < 2; ++u)
#pragma unroll
    for (int z = 0; z < 8; ++z)
      acc[u][z & 3] = MFMA16(__builtin_shufflevector(wh[(28 + (z >> 2)) % 3][2 * (z & 3)], wh[(28 + (z >> 2)) % 3][2 * (z & 3) + 1], 0, 1, 2, 3, 4, 5, 6, 7), oa[(28 + (z >> 2)) % OD][u], acc[u][z & 3]);
#pragma unroll
  for (int u = 0; u < 2; ++u)
#pragma unroll
    for (int pr = 0; pr < 2; ++pr) {
      const f32x4 c0 = (acc[u][2 * pr] + ac2[u][2 * pr]) + ac3[u][2 * pr], c1 = (acc[u][2 * pr + 1] + ac2[u][2 * pr + 1]) + ac3[u][2 * pr + 1];
      auto sa = __builtin_amdgcn_permlane16_swap(pack2(c0[0], c0[1]), pack2(c1[0], c1[1]), false, false);
      auto sb = __builtin_amdgcn_permlane16_swap(pack2(c0[2], c0[3]), pack2(c1[2], c1[3]), false, false);
      __builtin_amdgcn_raw_buffer_store_b128(u32x4{sa[0], sb[0], sa[1], sb[1]}, yrs, eoff + pr * 64 + u * 16 * DM * 2, 0, 16);
    }
  __builtin_amdgcn_sched_group_barrier(0x008, 9, 0);
  GRP(7, 1, 0x002, EV);
  SCHED0();
  RST(8);
  STAMP(9);
}

__device__ __forceinline__ unsigned med3(unsigned a, unsigned b, unsigned c) { return max(min(a, b), min(max(a, b), c)); }
#define PIN8(a) asm volatile("" : "+v"(a[0]), "+v"(a[1]), "+v"(a[2]), "+v"(a[3]), "+v"(a[4]), "+v"(a[5]), "+v"(a[6]), "+v"(a[7]))
__device__ __forceinline__ void tick(int& nb) {
  SCHED0();
  if (nb < NBAR) { BAR(); ++nb; if (HN) NAP(HN); }
  asm volatile("" ::: "memory");
  SCHED0();
}
__device__ __forceinline__ void hidden_half(char* smem, int lane, int l, int cg, char* ws, unsigned tag, u32x4 (&fd)[2][8]) {
  const rsrc_t wrs = mk_rsrc(ws, WS_O);
  const unsigned off = WS_GU + (unsigned)(cg * 32768 + l * 2048 + lane * 16);
  u32x4 ps[2][8];
  auto request = [&]() {
#pragma unroll
    for (int p = 0; p < 16; ++p) ps[p & 1][p >> 1] = ld16c(wrs, off + (p & 1) * 1024, (p >> 1) * 4096);
    SCHED0();
  };
  auto good = [&]() {
    unsigned bad = 0;
#pragma unroll
    for (int p = 0; p < 16; ++p) bad = chk(bad, ps[p & 1][p >> 1], tag);
    return __all(bad == 0);
  };
  request();
  tr_all(fd[0]); tr_all(fd[1]);
  SCHED0();
#pragma unroll 1
  for (unsigned n = 0; n < SPIN_CAP && !good(); ++n) { NAP(4); request(); }
  unsigned hp[2];
#pragma unroll
  for (int j = 0; j < 2; ++j) {
    float hv[2];
#pragma unroll
    for (int z = 0; z < 2; ++z) {
      const float gb = rbf((f_of(ps[j][0][2 * z]) + f_of(ps[j][2][2 * z])) + (f_of(ps[j][4][2 * z]) + f_of(ps[j][6][2 * z])));
      const float ub = rbf((f_of(ps[j][1][2 * z]) + f_of(ps[j][3][2 * z])) + (f_of(ps[j][5][2 * z]) + f_of(ps[j][7][2 * z])));
      hv[z] = rbf(gb / (1.0f + __expf(-gb))) * ub;
    }
    hp[j] = pack2(hv[0], hv[1]);
  }
#pragma unroll
  for (int j = 0; j < 2; ++j)
    *(unsigned*)(smem + L_HF + (lane & 7) * 128 + (((lane >> 4) & 1) * 4 + 2 * l + j) * 16 + ((lane >> 3) & 1) * 4 + (lane >> 5) * 8) = hp[j];
  lds_post(smem, F_H + l, 1);
}

struct Stager {
  rsrc_t xrs, qrs;
  LAS char* lds;
  unsigned va, vb;
  int arow, pa, pb;
  __device__ __forceinline__ Stager(char* smem, int l, int lane, int tm, int tn, const u16* x, const u16* w) {
    asm volatile("" : "+v"(lane));
    const int kr = lane >> 3;
    xrs = mk_rsrc(x, S_TOK * DM * 2); qrs = mk_rsrc(w, DM * NQKV * 2);
    lds = (LAS char*)smem + l * 1024;
    va = (unsigned)(kr + 8 * l) * 2048u + (((lane & 7) ^ (kr >> 1) ^ (l << 2)) << 4);
    vb = (unsigned)(kr + 8 * l) * 6144u + tn * 128 + (((lane & 7) ^ (((kr >> 1) & 1) << 1) ^ (l << 2)) << 4);
    arow = tm * 128 - 16;
    k_order(tm, tn, pa, pb);
  }
};
#define DM1(l, o) "s_add_u32 m0, %2, " #l "\n\ts_add_u32 %0, %5, " #o "\n\tbuffer_load_dwordx4 %3, %4, %0 offen lds\n\t"
#define DMA_(str, v_, rs_, so_, s0_) asm volatile("s_mov_b32 %1, m0\n\t" str "s_mov_b32 m0, %1" : "=&s"(tmp), "=&s"(keep) : "s"(base), "v"(v_), "s"(rs_), "s"(so_), "s"(s0_) : "scc", "memory")
__device__ __forceinline__ void stage_asm(const Stager& g, int t, int so) {
  const int kt = k_tile(t, g.pa, g.pb);
  const unsigned base = (unsigned)(size_t)g.lds + so;
  unsigned keep, tmp;
  DMA_("s_mov_b32 m0, %2\n\ts_nop 0\n\tbuffer_load_dwordx4 %3, %4, %6 offen lds\n\t" DM1(2048, 32768) DM1(4096, 65536) DM1(6144, 98304) DM1(8192, 131072)
       DM1(10240, 163840) DM1(12288, 196608) DM1(14336, 229376) DM1(16384, 262144), g.va, g.xrs, g.arow * 2048 + kt * 128, max(g.arow, 0) * 2048 + kt * 128);
  DMA_(DM1(18432, 0) DM1(20480, 98304) DM1(22528, 196608) DM1(24576, 294912) DM1(26624, 2048) DM1(28672, 100352) DM1(30720, 198656) DM1(32768, 296960)
       DM1(34816, 4096) DM1(36864, 102400) DM1(38912, 200704) DM1(40960, 299008), g.vb, g.qrs, kt * 64 * 6144, 0);
}
__device__ __forceinline__ int unit_of(int bid) {
  const int off = bid >> 3;
  return (off >> 2) == 3 ? off + 16 : (off >> 2) == 7 ? off - 16 : off;
}
__device__ constexpr int wo_early(int t) { return t < TW0 ? 0 : ((t - TW0) * NW < 64 - WR + 2 * LF ? (t - TW0) * NW : 64 - WR + 2 * LF); }
__device__ __forceinline__ void loader(char* smem, int l, int lane, int tm, int tn, unsigned rot2, const u16* __restrict__ x,
                                       const u16* __restrict__ w, const u16* __restrict__ wo, char* ws) {
  const Stager stage(smem, l, lane, tm, tn, x, w);
  dma_tag(smem, F_FULL + l * 4, 0);
  const rsrc_t prs = mk_rsrc(wo, DM * DM * 2);
  const int kr = lane >> 3;
  const unsigned vo = (unsigned)((kr < 4 ? kr : 12 + kr) + 4 * l) * 2048u + tn * 128 + (((lane & 7) ^ (((kr >> 1) & 1) << 1) ^ (l << 2)) << 4);
  u32x4 pc[2 * LF];
  int oth = (tn & 8) * 2 ^ 16;
  asm volatile("" : "+s"(oth));
  auto source = [&](int n) {
    const int k = n >> 1, half = k < 14 || k >= 30 ? (tn & 8) * 2 : oth;
    return (unsigned)((half + ((rot2 >> ((k < 14 ? k : k < 28 ? k - 14 : 14) & 14) * 2) & 14) + (k & 1)) * 32 + 8 * (n & 1)) * 2048;
  };
  auto request = [&](int z) {
    unsigned keep;
    if (z >= 64 - WR) asm volatile("s_nop 4\n\tbuffer_load_dwordx4 %0, %1, %2, %3 offen" : "=v"(pc[(z + 2 * LF - 64 + WR) % (2 * LF)])
                                   : "v"(vo), "s"(prs), "s"(source(z - 64 + WR + (z - 64 + WR < LF ? 0 : 28 - LF))) : "memory");
    else asm volatile("s_mov_b32 %0, m0\n\ts_add_u32 m0, %4, %5\n\ts_nop 2\n\tbuffer_load_dwordx4 %1, %2, %3 offen lds\n\ts_mov_b32 m0, %0"
                      : "=&s"(keep) : "v"(vo), "s"(prs), "s"(source(z + WR)), "s"((unsigned)(size_t)stage.lds), "i"(L_WO + (z + WR) * 2048) : "scc", "memory");
  };
  static_assert(L_WO + WR * 2048 >= 3 * SLOT && WR <= 60 && wo_early(NBAR - 4) >= 64 - WR, "beyond the ring, and early");
#pragma unroll
  for (int t = -1; t < NBAR - 2; ++t) {
    if (t >= 0) stage_asm(stage, t + 3, ((t + 3) % 3) * SLOT);
    SCHED0();
#pragma unroll
    for (int i = wo_early(t); i < wo_early(t + 1); ++i) request(i);
    WAITV(21 + (t < 0) + wo_early(t + 1) - wo_early(t - 1));
    BAR();
  }
  dma_tag(smem, F_FULL + l * 4, 15);
  stage_asm(stage, 15, 0);
  dma_tag(smem, F_FULL + l * 4, 16);
  SCHED0();
  {
    const unsigned d0 = lds_addr(smem + L_WO + l * 1024 + lane * 16), d1 = d0 + 65536;
#pragma unroll
    for (int i = wo_early(NBAR - 2); i < 64 - WR + 2 * LF; ++i) request(i);
    wait_bytes<0x80808080u>(smem, F_FREE, 16);
    WAITV(0);
#pragma unroll
    for (int i = 0; i < 2 * LF; ++i) {
      const int n = i < LF ? i : i - LF + 28;
      asm volatile("ds_write_b128 %1, %0 offset:%2" : "+v"(pc[i]) : "v"(n < 32 ? d0 : d1), "i"((n & 31) * 2048) : "memory");
    }
    if (l == 0) {
      const unsigned v1 = (vo + 4 * 2048) ^ 64;
      wait_bytes<0x80808080u>(smem, F_OL, 1);
#pragma unroll
      for (int y = 0; y < WR - 2 * LF; ++y) {
        const int n = y < 28 - LF ? LF + y : y + 2 * LF;
        unsigned keep;
        asm volatile("s_mov_b32 %0, m0\n\ts_add_u32 m0, %4, %5\n\ts_nop 0\n\tbuffer_load_dwordx4 %1, %2, %3 offen lds\n\ts_add_u32 m0, m0, 1024\n\ts_nop 0\n\tbuffer_load_dwordx4 %6, %2, %3 offen lds\n\ts_mov_b32 m0, %0"
                     : "=&s"(keep) : "v"(vo), "s"(prs), "s"(source(n)), "s"((unsigned)(size_t)stage.lds), "i"(L_WO + n * 2048), "v"(v1) : "scc", "memory");
      }
      dma_tag(smem, F_WO, 1);
    } else lds_post(smem, F_WO + 4, 1);
  }
  unsigned tp = 0;
  __builtin_amdgcn_s_setprio(1);
  if (l) await_attention(smem, lane, tm, tn, ws, uni(lds_word(smem, F_TAG)), tp);
}

__device__ __forceinline__ void updates(char* smem, int w, int lane, int tm, int tn, char* ws) {
  const rsrc_t wrs = mk_rsrc(ws, WS_O);
  const unsigned tag = uni(lds_word(smem, F_TAG));
  const int g = lane >> 4, li = lane & 15;
  wait_bytes<0x80u>(smem, F_SELOK, 1);
  const unsigned selkey = *(const unsigned*)(smem + F_SEL + (lane & 7) * 4);
  unsigned mine = (unsigned)__ballot(((2047u - (selkey & 0xffffu)) >> 7) == (unsigned)tm) & 0xffu;
#pragma unroll
  for (int i = 0; i < 3; ++i) mine &= i < w ? mine - 1 : ~0u;
  if (mine != 0 && w < 3) {
    wait_bytes<0x80u>(smem, F_GO, 1);
    NAP(YD);
  }
  for (; mine != 0; mine &= mine - 1, mine &= mine - 1, mine &= mine - 1, mine &= mine - 1) {
    const int c = __builtin_ctz(mine);
    const unsigned yoff = WS_Y + (unsigned)((g * 14 * 8 + c) * DM + tn * 64 + li * 4) * 4u;
    u32x4 yv[14];
    const unsigned key = (unsigned)__builtin_amdgcn_readlane((int)selkey, c), kb = key >> 16;
#pragma unroll 1
    for (unsigned n = 0; n < SPIN_CAP; ++n) {
      unsigned bad = 0;
#pragma unroll
      for (int j = 0; j < 14; ++j) yv[j] = ld16c(wrs, yoff, j * (8 * DM * 4));
#pragma unroll
      for (int j = 0; j < 14; ++j) bad = chk(bad, yv[j], tag);
      if (__all(bad == 0)) break;
      NAP(8);
    }
    float y[4] = {0.f, 0.f, 0.f, 0.f};
#pragma unroll
    for (int j = 0; j < 14; ++j) { y[0] += lo2f(yv[j][0]); y[1] += hi2f(yv[j][0]); y[2] += lo2f(yv[j][2]); y[3] += hi2f(yv[j][2]); }
    const float r = __uint_as_float(((kb & 0x8000u) ? (kb & 0x7fffu) : (~kb & 0xffffu)) << 16);
    const float rs = rbf(1.0f / (1.0f + __expf(-r)));
#pragma unroll
    for (int e = 0; e < 4; ++e) y[e] = rbf(rbf(xsum16_32(y[e])) * rs);
    *(f32x4*)(smem + L_UP + c * 256 + li * 16) = f32x4{y[0], y[1], y[2], y[3]};
  }
  lds_post(smem, F_UP + w, 1);
  STAMP(7 + (w == 0 || w == 3 ? -5 : 0));
}

__device__ __forceinline__ void ld8k(u32x4 (&d)[8], rsrc_t rs, unsigned off) {
#define L1K(i, so, o) "buffer_load_dwordx4 %" #i ", %8, %9, " so " offen offset:" #o " sc1\n\t"
  asm volatile("s_nop 4\n\t" L1K(0, "0", 0) L1K(1, "0", 1024) L1K(2, "0", 2048) L1K(3, "0", 3072) L1K(4, "%10", 0) L1K(5, "%10", 1024) L1K(6, "%10", 2048) L1K(7, "%10", 3072)
               : "=&v"(d[0]), "=&v"(d[1]), "=&v"(d[2]), "=&v"(d[3]), "=&v"(d[4]), "=&v"(d[5]), "=&v"(d[6]), "=&v"(d[7]) : "v"(off), "s"(rs), "s"(4096) : "memory");
  SCHED0();
}
template <int V> struct IC { static constexpr int v = V; };
__device__ __forceinline__ void side_wave(char* smem, int F, int lane, int bid, int tm, int tn, const u16* __restrict__ x,
                                          const u16* __restrict__ wgu, const u16* __restrict__ wd, char* ws) {
  const rsrc_t wrs = mk_rsrc(ws, WS_O), xrs = mk_rsrc(x, S_TOK * DM * 2);
  const int u = unit_of(bid), cg = (bid & 7) * 7 + ((u >> 2) % 7), q = u & 3;
  const int g = lane >> 4, li = lane & 15;
  u32x4 fw[4][8];
  const rsrc_t grs = mk_rsrc(wgu, u < 28 ? DM * FF * 2 : 0);
  const int cu = (bid & 7) * 7 + (((u >> 2) + F) % 7);
  const unsigned fo = (unsigned)(q * 256 + (li >> 3) * 128 + 8 * g) * (FF * 2) + cu * 128 + (li & 7) * 16;
#pragma unroll
  for (int s = 0; s < 4; ++s) asm volatile("" : "=v"(fw[s][0]), "=v"(fw[s][1]), "=v"(fw[s][2]), "=v"(fw[s][3]), "=v"(fw[s][4]), "=v"(fw[s][5]), "=v"(fw[s][6]), "=v"(fw[s][7]));
  const int csb = WS_CS + ((tn * 2 + F) * 16 + (tm & 1) * 8) * 1024;
  u32x4 cs[8], rec[2];
  unsigned ca = csb + min(lane, 47) * 16, cb = ca + 4096, ra = WS_R + (unsigned)max(tm * 128 - 16 + F * 72 + lane, 0) * 16, rb = WS_R + (unsigned)max(tm * 128 - 8 + F * 72 + lane, 0) * 16;
  asm volatile("" : "+v"(ca), "+v"(cb), "+v"(ra), "+v"(rb));
  unsigned due = (0x3012 >> ((tn & 3) * 4)) & 15, iss = 0;
#define FW(n) fw[(n) >> 3][(n) & 7]
#define LD(i) "s_mov_b32 %[t], (((%[n0]+" #i ")>>3)*32+((%[n0]+" #i ")&7))*7168\n\tbuffer_load_dwordx4 %" #i ", %[fo], %[rs], %[t] offen nt\n\t"
#define LD8 LD(0) LD(1) LD(2) LD(3) LD(4) LD(5) LD(6) LD(7)
#define R8(n) "+v"(FW(n)), "+v"(FW(n + 1)), "+v"(FW(n + 2)), "+v"(FW(n + 3)), "+v"(FW(n + 4)), "+v"(FW(n + 5)), "+v"(FW(n + 6)), "+v"(FW(n + 7))
#define SITE(lds_, ...) asm volatile("s_cmp_lg_u32 %[iss], %[k]\n\ts_cbranch_scc1 1f\n\ts_cmp_le_u32 %[nb], %[due]\n\ts_cbranch_scc1 1f\n\ts_sleep %[dly]\n\t" lds_ \
    "s_add_u32 %[iss], %[iss], 1\n\ts_add_u32 %[due], %[due], 4\n\t1:" : __VA_ARGS__, [iss] "+s"(iss), [due] "+s"(due), [t] "=&s"(t) \
    : [fo] "v"(fo), [rs] "s"(grs), [nb] "s"(nb), [k] "i"(K), [n0] "i"(n0), [dly] "i"(D), [ca] "v"(ca), [cb] "v"(cb), [ra] "v"(ra), [rb] "v"(rb), [ws] "s"(wrs) : "scc", "memory")
#define C1K(i, a, o) "buffer_load_dwordx4 %" #i ", %[" #a "], %[ws], 0 offen offset:" #o " sc1\n\t"
#define G2N C1K(0, ca, 0) C1K(1, ca, 1024) C1K(2, ca, 2048) C1K(3, ca, 3072) C1K(4, cb, 0) C1K(5, cb, 1024) C1K(6, cb, 2048) C1K(7, cb, 3072) C1K(8, ra, 0) C1K(9, rb, 0)
#define G2 C1K(8, ca, 0) C1K(9, ca, 1024) C1K(10, ca, 2048) C1K(11, ca, 3072) C1K(12, cb, 0) C1K(13, cb, 1024) C1K(14, cb, 2048) C1K(15, cb, 3072) C1K(16, ra, 0) C1K(17, rb, 0)
  auto site = [&](auto kk, auto dd, unsigned nb) {
    constexpr int K = decltype(kk)::v, D = decltype(dd)::v, n0 = K == 0 ? 0 : K == 3 ? 16 : 8 + K * 8;
    unsigned t;
    SCHED0();
    if constexpr (K == 1 && LC == 3) SITE(G2N, "+v"(cs[0]), "+v"(cs[1]), "+v"(cs[2]), "+v"(cs[3]), "+v"(cs[4]), "+v"(cs[5]), "+v"(cs[6]), "+v"(cs[7]), "+v"(rec[0]), "+v"(rec[1]));
    else if constexpr (K == 3) SITE(LD8, R8(16));
    else if constexpr (K == 1) SITE(G2 LD8, R8(n0), "+v"(cs[0]), "+v"(cs[1]), "+v"(cs[2]), "+v"(cs[3]), "+v"(cs[4]), "+v"(cs[5]), "+v"(cs[6]), "+v"(cs[7]), "+v"(rec[0]), "+v"(rec[1]));
    else if constexpr (K == 2) SITE(LD8, R8(n0));
    else SITE(LD8 LD(8) LD(9) LD(10) LD(11) LD(12) LD(13) LD(14) LD(15), R8(n0), R8(n0 + 8));
    SCHED0();
  };
#define CH(k, d, n) site(IC<k>{}, IC<d>{}, n)
#define WAITD(K, d) asm volatile("s_waitcnt vmcnt(%1)\n\ts_cmp_lg_u32 %0, %3\n\ts_cbranch_scc1 1f\n\ts_waitcnt vmcnt(%2)\n\t1:" : "+s"(iss) : "i"((K) + 8), "i"(K), "i"(d) : "scc", "memory")
  unsigned tag;
  {
    const int qq = (lane >> 2) & 3, hk = (qq >> 1) | ((g & 1) << 1);
    const char* pb = smem + XB + (8 * g + qq) * 128 + 8 * (lane & 3);
    const bf16x8 ones = {0x3f80, 0x3f80, 0x3f80, 0x3f80, 0x3f80, 0x3f80, 0x3f80, 0x3f80};
    f32x4 a[6];
#pragma unroll
    for (int t = 0; t < 2; ++t) {
      SCHED0(); BAR(); SCHED0();
      __builtin_amdgcn_s_setprio(3);
#pragma unroll
      for (int c = 0; c < 6; ++c) {
        const char* p = pb + t * SLOT + (c >> 1) * 8192 + (((2 * F + (c & 1)) ^ hk) << 5);
        a[c] = MFMA16(ones, lds_tr2(p), t ? a[c] : (f32x4{0, 0, 0, 0}));
        a[c] = MFMA16(ones, lds_tr2(p + 4096), a[c]);
      }
      asm volatile("" : "+v"(a[0]), "+v"(a[1]), "+v"(a[2]), "+v"(a[3]), "+v"(a[4]), "+v"(a[5]));
      if (t == 0) { tag = uni(lds_word(smem, F_TAG)); CH(0, CD, 1); }
    }
    const unsigned off = csb + (tm >> 1) * 1024 + lane * 8;
    st8c(wrs, off, __float_as_uint(g == 0 ? a[0][0] : g == 1 ? a[1][0] : g == 2 ? a[2][0] : a[3][0]), tag);
    st8c(wrs, lane < 32 ? off + 512 : 0x7fffffffu, __float_as_uint(g == 0 ? a[4][0] : a[5][0]), tag);
    __builtin_amdgcn_s_setprio(0);
    CH(0, CD, 2);
  }
#define SST(i)
  int nb = 2;
  tick(nb);
  const unsigned xs_off = WS_S + (unsigned)(((bid & 7) * 4 + q) * 6144 + lane * 16);
  u32x4 tc;
  uint2 tq[2];
  unsigned ta[3], tv, todo = 1;
  auto gather2 = [&]() {
    asm volatile(G2N : "=&v"(cs[0]), "=&v"(cs[1]), "=&v"(cs[2]), "=&v"(cs[3]), "=&v"(cs[4]), "=&v"(cs[5]), "=&v"(cs[6]), "=&v"(cs[7]), "=&v"(rec[0]), "=&v"(rec[1])
                 : [ca] "v"(ca), [cb] "v"(cb), [ra] "v"(ra), [rb] "v"(rb), [ws] "s"(wrs) : "memory");
    SCHED0();
  };
  auto prepare = [&]() {
#pragma unroll 1
    for (unsigned n = 0; n < SPIN_CAP; ++n) {
      unsigned bad = chk(chk(0u, rec[0], tag), rec[1], tag);
#pragma unroll
      for (int j = 0; j < 8; ++j) bad = chk(bad, cs[j], tag);
      if (__all(bad == 0)) break;
      NAP(8);
      gather2();
      tick(nb); tick(nb); WAITV(0); asm volatile("" : "+v"(rec[0]), "+v"(rec[1])); PIN8(cs);
    }
    float csum[2] = {f_of(cs[0][0]), f_of(cs[0][2])};
#pragma unroll
    for (int r = 1; r < 8; ++r) { csum[0] = opq(csum[0] + f_of(cs[r][0])); csum[1] = opq(csum[1] + f_of(cs[r][2])); }
    const float h0 = rbf(csum[0]), d0 = opq(csum[0] - h0), c0 = rbf(d0), h1 = rbf(csum[1]), d1 = opq(csum[1] - h1), c1 = rbf(d1);
    tc = u32x4{pack2(h0, c0), pack2(opq(d0 - c0), 0.f), pack2(h1, c1), pack2(opq(d1 - c1), 0.f)};
    asm volatile("" : "+v"(tc));
#pragma unroll
    for (int j = 0; j < 2; ++j) {
      const unsigned nm = (rec[j][0] & 0xffffu) ^ 0x8000u;
      tq[j] = make_uint2(nm | (nm << 16), __float_as_uint(__builtin_amdgcn_rcpf(f_of(rec[j][2]))));
      asm volatile("" : "+v"(tq[j].x), "+v"(tq[j].y));
    }
    ta[0] = lds_addr(smem + L_CSF + (min(lane >> 4, 2) * 64 + F * 32 + (lane < 48 ? li : 15) * 2) * 8);
    ta[1] = lds_addr(smem + L_QST + (F * 72 + lane) * 8);
    ta[2] = lds_addr(smem + (lane < 8 ? F_SEL + lane * 4 : lane == 8 ? F_SELOK : F_ST + F * 4));
    asm volatile("" : "+v"(ta[0]), "+v"(ta[1]), "+v"(ta[2]));
    SCHED0();
  };
  auto tables = [&](unsigned now) {
    asm volatile("s_and_b32 %0, %0, %8\n\ts_cbranch_scc0 1f\n\tds_write_b128 %1, %2\n\tds_write2_b64 %3, %4, %5 offset1:8\n\ts_mov_b64 exec, 0x3ff\n\tds_write_b32 %6, %7\n\ts_mov_b64 exec, -1\n\t1:"
                 : "+s"(now) : "v"(ta[0]), "v"(tc), "v"(ta[1]), "v"(tq[0]), "v"(tq[1]), "v"(ta[2]), "v"(tv), "s"(todo) : "scc", "memory");
    todo &= ~now;
  };
  auto slice = [&]() {
    if (nb < NBAR) tick(nb);
    else { const u32x2 fl = *(const volatile LAS u32x2*)(LAS const char*)(smem + F_FULL); tables(uni(min(fl[0], fl[1])) >> 4); SCHED0(); }
  };
  if (u >= 28) {
    u32x4 kd[4];
#pragma unroll 1
    for (unsigned n = 0; n < SPIN_CAP; ++n) {
#define L1K(i, o) "buffer_load_dwordx4 %" #i ", %4, %5, 0 offen offset:" #o " sc1\n\t"
      asm volatile("s_nop 4\n\t" L1K(0, 0) L1K(1, 1024) L1K(2, 2048) L1K(3, 3072)
                   : "=&v"(kd[0]), "=&v"(kd[1]), "=&v"(kd[2]), "=&v"(kd[3]) : "v"(WS_K + F * 4096 + lane * 16), "s"(wrs) : "memory");
#undef L1K
      tick(nb);
      asm volatile("s_waitcnt vmcnt(0)" : "+v"(kd[0]), "+v"(kd[1]), "+v"(kd[2]), "+v"(kd[3]) :: "memory");
      if (__all(chk(chk(chk(chk(0u, kd[0], tag), kd[1], tag), kd[2], tag), kd[3], tag) == 0)) break;
      NAP(4);
    }
    SST(8);
    unsigned m1 = 0, m2 = 0, m3 = 0, m4 = 0;
#pragma unroll
    for (int j = 0; j < 4; ++j) {
      kd[j] = u32x4{(kd[j][0] << 16) | (15 - 4 * j), (kd[j][0] & 0xffff0000u) | (14 - 4 * j), (kd[j][2] << 16) | (13 - 4 * j), (kd[j][2] & 0xffff0000u) | (12 - 4 * j)};
#pragma unroll
      for (int i = 0; i < 4; ++i) {
        const unsigned v = kd[j][i];
        m4 = med3(m3, m4, v); m3 = med3(m2, m3, v); m2 = med3(m1, m2, v); m1 = max(m1, v);
      }
    }
    const unsigned lb = ((63 - lane) << 5) | (F ? 0 : 16);
    m1 |= lb; m2 |= lb; m3 |= lb; m4 |= lb;
    asm volatile("" : "+v"(m1), "+v"(m2), "+v"(m3), "+v"(m4));
    tick(nb);
    unsigned best = 0;
#pragma unroll
    for (int r = 0; r < 8; ++r) {
      const unsigned wm = wave_max_u32(m1);
      best = (lane & 7) == r ? wm : best;
      const bool won = m1 == wm;
      m1 = won ? m2 : m1; m2 = won ? m3 : m2; m3 = won ? m4 : m3; m4 = won ? 0u : m4;
      if (r == 3) { asm volatile("" : "+v"(m1), "+v"(m2), "+v"(m3), "+v"(m4), "+v"(best)); tick(nb); }
    }
    if (__any(m1 == 0)) {
      unsigned wm = 0;
#pragma unroll 1
      for (int r = 0; r < 8; ++r) {
        if (r & 1) tick(nb);
        unsigned m = 0;
#pragma unroll
        for (int j = 0; j < 4; ++j)
#pragma unroll
          for (int i = 0; i < 4; ++i) m = max(m, (kd[j][i] | lb) - wm);
        wm += wave_max_u32(m);
        best = (lane & 7) == r ? wm : best;
      }
    }
    *(volatile LAS unsigned*)(LAS char*)(smem + F_CND + (F ? 60 - (lane & 7) * 4 : (lane & 7) * 4)) = best + 1;
    unsigned d = 0;
    if (TCH & 1) {
      const unsigned ct = 2047u - (best & 0xffffu);
      asm volatile("buffer_load_dword %0, %1, %2, 0 offen\n\tbuffer_load_dword %0, %3, %4, 0 offen sc1" : "=&v"(d) : "v"(lane < 32 ? ct * 2048u + q * 512 + (lane >> 3) * 128 : 0x7fffffffu), "s"(xrs), "v"(lane < 8 ? WS_R + ct * 16 : 0x7fffffffu), "s"(wrs) : "memory");
    }
    SST(9);
    gather2();
    unsigned ka, kb;
#pragma unroll 1
    for (unsigned n = 0; n < SPIN_CAP; ++n) {
      tick(nb);
      ka = *(const volatile LAS unsigned*)(LAS const char*)(smem + F_CND + (lane & 7) * 4);
      kb = *(const volatile LAS unsigned*)(LAS const char*)(smem + F_CND + 32 + (lane & 7) * 4);
      if (__all(min(ka, kb) != 0)) break;
      NAP(1);
    }
    const unsigned selkey = max(ka, kb) - 1, tok = 2047u - (selkey & 0xffffu);
    u32x4 xf[2], trec;
#pragma unroll 1
    for (unsigned n = 0; n < SPIN_CAP; ++n) {
      asm volatile("buffer_load_dwordx4 %0, %3, %4, 0 offen\n\tbuffer_load_dwordx4 %1, %3, %4, 0 offen offset:64\n\tbuffer_load_dwordx4 %2, %5, %6, 0 offen sc1"
                   : "=&v"(xf[0]), "=&v"(xf[1]), "=&v"(trec) : "v"(tok * 2048u + q * 512 + (li >> 3) * 256 + g * 16 + F * 128), "s"(xrs), "v"(WS_R + tok * 16), "s"(wrs) : "memory");
      if (!KQ || n) tick(nb);
      asm volatile("s_waitcnt vmcnt(0)" : "+v"(xf[0]), "+v"(xf[1]), "+v"(trec), "+v"(rec[0]), "+v"(rec[1]), "+v"(d) :: "memory"); PIN8(cs);
      if (__all(chk(0u, trec, tag) == 0)) break;
      NAP(1);
    }
    const float mb = lo2f(trec[0]), rd = 1.0f / f_of(trec[2]);
#pragma unroll
    for (int s = 0; s < 2; ++s)
#pragma unroll
      for (int j = 0; j < 4; ++j) xf[s][j] = ln2s(xf[s][j], mb, rd);
    st16c(wrs, xs_off + F * 3072, u32x4{xf[0][0], xf[0][1], xf[0][2], tag});
    st16c(wrs, xs_off + F * 3072 + 1024, u32x4{xf[1][0], xf[1][1], xf[1][2], tag});
    st16c(wrs, xs_off + F * 3072 + 2048, u32x4{xf[0][3], xf[1][3], selkey, tag});
    tv = lane < 8 ? selkey : 1u;
    SST(2);
    tick(nb);
    if (TCH & 2) {
      asm volatile("s_waitcnt vmcnt(0)\n\tbuffer_load_dword %0, %1, %2, 0 offen sc1" : "=&v"(d) : "v"(lane < 24 ? xs_off + F * 3072 + lane * 112 : 0x7fffffffu), "s"(wrs) : "memory");
    }
    prepare();
    asm volatile("s_waitcnt vmcnt(0)" : "+v"(d) :: "memory");
    while (nb < NBAR) tick(nb);
    __builtin_amdgcn_s_setprio(1);
    wait_full(smem, F_FULL, 16);
    tables(1);
  } else {
    u32x4 xs[6];
    auto ask = [&]() {
#define X1K(i, o) "buffer_load_dwordx4 %" #i ", %6, %7, 0 offen offset:" #o " sc1\n\t"
#define X2K(i, o) "buffer_load_dwordx4 %" #i ", %6, %7, %8 offen offset:" #o " sc1\n\t"
      asm volatile(X1K(0, 0) X1K(1, 1024) X1K(2, 2048) X1K(3, 3072) X2K(4, 0) X2K(5, 1024)
                   : "=&v"(xs[0]), "=&v"(xs[1]), "=&v"(xs[2]), "=&v"(xs[3]), "=&v"(xs[4]), "=&v"(xs[5]) : "v"(xs_off), "s"(wrs), "s"(4096) : "memory");
      SCHED0();
    };
    auto good = [&]() {
      unsigned bad = xs[0][3] ^ tag;
#pragma unroll
      for (int j = 1; j < 6; ++j) bad = __builtin_amdgcn_bitop3_b32(bad, xs[j][3], tag, 0xF6);
      return __all(bad == 0);
    };
#define PIN6(a) asm volatile("" : "+v"(a[0]), "+v"(a[1]), "+v"(a[2]), "+v"(a[3]), "+v"(a[4]), "+v"(a[5]))
#define WAITS(snap, n) asm volatile("s_waitcnt vmcnt(%2)\n\ts_cmp_lg_u32 %0, %1\n\ts_cbranch_scc1 1f\n\ts_waitcnt vmcnt(0)\n\t1:" : "+s"(iss) : "s"(snap), "i"(n) : "scc", "memory")
    asm volatile("" : "=v"(cs[0]), "=v"(cs[1]), "=v"(cs[2]), "=v"(cs[3]), "=v"(cs[4]), "=v"(cs[5]), "=v"(cs[6]), "=v"(cs[7]), "=v"(rec[0]), "=v"(rec[1]));
    CH(0, 8 - HN, 3);
    tick(nb);
    CH(0, 8 - HN, 99);
    tick(nb);
    CH(1, 8 - HN, nb);
    if (MG) CH(2, 0, 99);
    tick(nb);
    WAITS(1, MG ? 26 : LC == 3 ? 10 : 18); PIN8(fw[0]); PIN8(fw[1]);
    tr_all(fw[0]);
    CH(1, 3, nb);
    if (MG) CH(2, 0, 99);
    tick(nb);
    tr_all(fw[1]);
    CH(1, 3, nb);
    if (MG) CH(2, 0, 99);
    tick(nb);
    CH(1, 8 - HN, 99);
    if (MG) CH(2, 0, 99);
    tick(nb);
    if (LC == 0) CH(2, 8 - HN, nb);
    tick(nb);
    WAITS(2, 8);
    asm volatile("" : "+v"(rec[0]), "+v"(rec[1])); PIN8(cs); if (LC < 3) PIN8(fw[2]);
    if (!SD) ask();
    prepare();
    if (SD) ask();
    unsigned snap = iss;
    asm volatile("" : "+s"(snap));
    if (LC == 0) CH(2, 0, nb);
    tick(nb);
    WAITS(snap, 8); PIN6(xs);
    SST(1);
    if (!good()) {
      if (LC == 0) CH(2, 0, 99);
#pragma unroll 1
      for (unsigned n = 0; n < SPIN_CAP; ++n) {
        ask(); tick(nb); WAITV(0); PIN6(xs);
        if (good()) break;
        NAP(4);
      }
    }
    SST(2);
    tv = lane < 8 ? xs[2][2] : 1u;
    xs[0][3] = xs[2][0]; xs[1][3] = xs[2][1]; xs[3][3] = xs[5][0]; xs[4][3] = xs[5][1];
    PIN6(xs); asm volatile("" : "+v"(tv));
    if (nb == NBAR) { __builtin_amdgcn_s_setprio(1); wait_full(smem, F_FULL, 16); tables(1); }
    f32x4 acc[8];
    auto mma = [&](int s) {
      SCHED0(); __builtin_amdgcn_s_setprio(3); SCHED0();
#pragma unroll
      for (int i = 0; i < 8; ++i) acc[i] = MFMA16(__builtin_bit_cast(bf16x8, fw[s][i]), __builtin_bit_cast(bf16x8, xs[s + (s >> 1)]), s ? acc[i] : (f32x4{0, 0, 0, 0}));
      SCHED0(); __builtin_amdgcn_s_setprio(0); SCHED0();
      PIN8(acc);
    };
    mma(0);
    if (LC < 3) tr_all(fw[2]);
    if (LC == 0) CH(2, 3, 99);
    slice();
    mma(1);
    if (LC < 2) mma(2);
    if (LC == 1) { PIN8(fw[3]); tr_all(fw[3]); }
    slice();
    if (LC == 0) { WAITV(0); PIN8(fw[3]); tr_all(fw[3]); }
    if (LC < 2) mma(3);
    if (LC == 2) mma(2);
    auto finish = [&]() {
#pragma unroll
      for (int r = 0; r < 4; ++r) {
        float v[2];
#pragma unroll
        for (int z = 0; z < 2; ++z) {
          auto a = __builtin_amdgcn_permlane32_swap(__float_as_uint(acc[z][r]), __float_as_uint(acc[z + 4][r]), false, false);
          auto b = __builtin_amdgcn_permlane32_swap(__float_as_uint(acc[z + 2][r]), __float_as_uint(acc[z + 6][r]), false, false);
          v[z] = f_of(li < 8 ? a[0] : b[1]) + dpp_f<0x128>(f_of(li < 8 ? b[0] : a[1]));
        }
        st16c(wrs, WS_GU + (unsigned)(((cu * 4 + q) * 2 + F) * 4096 + r * 1024 + lane * 16), u32x4{__float_as_uint(v[0]), tag, __float_as_uint(v[1]), tag});
      }
      SCHED0();
      SST(3);
    };
    if (LC < 2 && EP == 1) finish();
    while (nb < NBAR) tick(nb);
    if (LC < 2 && EP == 0) { slice(); finish(); }
    __builtin_amdgcn_s_setprio(1);
    u32x4 fd[2][8];
    {
      const rsrc_t drs = mk_rsrc(wd, DM * FF * 2);
      const unsigned dof = (unsigned)(cg * 64 + 8 * g) * (DM * 2) + q * 512 + F * 256 + li * 16;
      wait_full(smem, F_FULL, 15);
      if (LC == 3) { iss = 3; CH(3, 0, 99); iss = 2; }
      if (LC >= 2) CH(2, 0, 99);
#pragma unroll
      for (int s = 0; s < 2; ++s)
#pragma unroll
        for (int j = 0; j < 8; ++j) fd[s][j] = __builtin_amdgcn_raw_buffer_load_b128(drs, dof + (j & 1) * (DM * 2), (s * 32 + (j & 6)) * (DM * 2), 2);
      SCHED0();
    }
    wait_full(smem, F_FULL, 16);
    tables(1);
    SST(4);
    if (LC == 3) { WAITV(24); PIN8(fw[2]); tr_all(fw[2]); mma(2); }
    if (LC >= 2) { WAITV(16); PIN8(fw[3]); tr_all(fw[3]); mma(3); __builtin_amdgcn_s_setprio(1); finish(); }
    if (bid == 0 && F == 0 && lane == 0) __hip_atomic_store((GAS unsigned*)(ws + WS_EP), tag, RLX_AGENT);
    hidden_half(smem, lane, F, cg, ws, tag, fd);
    SST(5);
    wait_bytes<0x8080u>(smem, F_H, 1);
    const bf16x8 h0 = lds_b128(smem + L_HF + (li & 7) * 128 + g * 16), h1 = lds_b128(smem + L_HF + (li & 7) * 128 + 64 + g * 16);
#pragma unroll
    for (int i = 0; i < 8; ++i) {
      acc[i] = MFMA16(h0, __builtin_bit_cast(bf16x8, fd[0][i]), (f32x4{0, 0, 0, 0}));
      acc[i] = MFMA16(h1, __builtin_bit_cast(bf16x8, fd[1][i]), acc[i]);
    }
    const unsigned off = WS_Y + (unsigned)((cg * 8 + (g & 1) * 4) * DM + q * 256 + F * 128 + li * 8 + (g >> 1) * 4) * 4u;
#pragma unroll
    for (int r = 0; r < 4; ++r) {
      const unsigned lo = pack2(acc[0][r], acc[1][r]), hi = pack2(acc[2][r], acc[3][r]), lo2 = pack2(acc[4][r], acc[5][r]), hi2 = pack2(acc[6][r], acc[7][r]);
      st16c(wrs, off + r * (DM * 4), u32x4{g < 2 ? lo : lo2, tag, g < 2 ? hi : hi2, tag});
    }
    SST(6);
  }
  __builtin_amdgcn_s_waitcnt(0x0F70);
}

__global__ __launch_bounds__(512, 2) void fused_block(const u16* __restrict__ x, const u16* __restrict__ wqkv, const u16* __restrict__ wo,
                                                      const u16* __restrict__ wr, const u16* __restrict__ wg, const u16* __restrict__ wu,
                                                      const u16* __restrict__ wd, char* ws, u16* __restrict__ out) {
  __shared__ __attribute__((aligned(128))) char smem[L_SIZE];
  unsigned long long pc;
  unsigned first;
  asm volatile("v_cmp_gt_u32 vcc, 24, %2\n\ts_getpc_b64 %0\n\ts_cbranch_vccz 1f\n\ts_mov_b64 exec, vcc\n\tv_lshlrev_b32 %1, 7, %2\n\tglobal_load_dword %1, %1, %0\n\ts_mov_b64 exec, -1\n\t1:"
               : "=s"(pc), "=&v"(first) : "v"(threadIdx.x) : "vcc", "memory");
  SCHED0();
  unsigned long long t00; asm volatile("s_memrealtime %0\n\ts_waitcnt lgkmcnt(0)" : "=s"(t00) :: "memory");
  const int lane = threadIdx.x & 63;
  const int wid = __builtin_amdgcn_readfirstlane(threadIdx.x >> 6);
  const int bid = blockIdx.x, xcd = bid & 7, off = bid >> 3;
  const int tm = (xcd & 1) * 8 + (xcd & 2) * 2 + (off & 3), tn = (xcd >> 2) * 8 + (off >> 2);
  STAMP(0);
  if (wid == 7) {
    unsigned d;
    asm volatile("s_getpc_b64 %0" : "=s"(pc));
    asm volatile("buffer_load_dword %0, %1, %2, 0 offen\n\ts_waitcnt vmcnt(0)" : "=&v"(d)
                 : "v"((unsigned)(lane * 32 + off) * 128u), "s"(mk_rsrc((const void*)(pc & ~255ull), CODE_BYTES)) : "memory");
  }
  asm volatile("" : "+s"(x), "+s"(wqkv), "+s"(wr), "+s"(ws), "+s"(out), "+s"(wo), "+s"(wd), "+s"(wg), "+s"(wu));
  unsigned rot2 = ((((unsigned)(0x65432106543210ull >> (off & 3) * 8) & 0xfffffffu) + ((tn & 7) + 1) * 0x1111111u) & 0x7777777u) * 2 | (unsigned)tn << 29;
  unsigned tag = 0;
  RowRegs rr;
  EpiRegs e;
  row_request(rr, x, wr, ws, tm * 128 + tn * 8 + wid * 2, lane, wid < 4);
  rr.keep = first;
  SCHED0();
  if ((wid >> 1) == 2) {
    __builtin_amdgcn_s_setprio(2);
    const Stager stage(smem, wid - 4, lane, tm, tn, x, wqkv);
    NAP(4);
    stage_asm(stage, 0, 0); stage_asm(stage, 1, SLOT);
    WAITV(21);
    BAR();
    stage_asm(stage, 2, 2 * SLOT);
  }
  asm volatile("" ::: "memory");
  if (wid < 4) {
    __builtin_amdgcn_s_setprio(2);
    qkv_setup(smem, wid, lane, tm, tn, e);
    asm volatile("" : "+s"(rot2));
    SCHED0();
    tag = row_stats(rr, ws, tm * 128 + tn * 8 + wid * 2, lane);
    if (wid == 0) *(unsigned*)(smem + L_FLG + lane * 4) = lane * 4 == F_TAG - L_FLG ? tag : 0u;
    STAMP(10);
  }
  asm volatile("" ::: "memory");
  if (wid < 4) {
    if (wid & 1) qkv_attention<1>(smem, wid, lane, tm, tn, ws, tag, x, e);
    else qkv_attention<0>(smem, wid, lane, tm, tn, ws, tag, x, e);
    out_proj(smem, wid, lane, tm, tn, ws, tag, out, e, rot2);
    {
      unsigned t0 = (unsigned)t00;
      const rsrc_t prs = mk_rsrc(ws + (32u << 20), 4096);
      if (wid == 0) __builtin_amdgcn_raw_buffer_store_b32(t0, prs, lane == 0 ? bid * 4 : 0x7fffffff, 0, 16);
      for (int n = 0; n < 100000 && (unsigned)__builtin_amdgcn_s_memrealtime() - t0 < 3200u; ++n) NAP(2);
      const u32x4 v = ld16c(prs, lane * 16, 0);
      int d = min(min((int)(v[0] - t0), (int)(v[1] - t0)), min((int)(v[2] - t0), (int)(v[3] - t0)));
      int e2 = max(max((int)(v[0] - t0), (int)(v[1] - t0)), max((int)(v[2] - t0), (int)(v[3] - t0)));
      const int mn = 100000 - (int)wave_max_u32((unsigned)(100000 - d));
      const int ea = (int)wave_max_u32((lane & 1) ? 0u : (unsigned)(100000 + e2)) - 100000, eb = (int)wave_max_u32((lane & 1) ? (unsigned)(100000 + e2) : 0u) - 100000;
      t0 += mn;
      const unsigned until = 4000u + 0u;
      for (int n = 0; n < 100000 && (unsigned)__builtin_amdgcn_s_memrealtime() - t0 < until; ++n) NAP(2);
    }
  } else {
    if (wid < 6) loader(smem, wid - 4, lane, tm, tn, rot2, x, wqkv, wo, ws);
    else side_wave(smem, wid - 6, lane, bid, tm, tn, x, wid == 6 ? wg : wu, wd, ws);
    updates(smem, (0x2130 >> ((wid - 4) * 4)) & 3, lane, tm, tn, ws);
    __builtin_amdgcn_s_waitcnt(0x0F70);
  }
}

extern "C" void kernel_launch(void* const* d_in, const int* in_sizes, int n_in, void* d_out, int out_size, void* d_ws,
                              size_t ws_size, hipStream_t stream) {
  fused_block<<<256, 512, 0, stream>>>((const u16*)d_in[0], (const u16*)d_in[1], (const u16*)d_in[2], (const u16*)d_in[4],
                                       (const u16*)d_in[5], (const u16*)d_in[6], (const u16*)d_in[7], (char*)d_ws, (u16*)d_out);
}
```

```cpp
#define LC 1
#define EP 1
#include <hip/hip_runtime.h>
#include <hip/hip_bf16.h>
#include <stdint.h>

typedef __attribute__((ext_vector_type(8))) short bf16x8;
typedef __attribute__((ext_vector_type(4))) short s16x4;
typedef __attribute__((ext_vector_type(4))) float f32x4;
typedef __attribute__((ext_vector_type(2))) float f32x2;
typedef __attribute__((ext_vector_type(2))) __bf16 bf16x2_t;
typedef __attribute__((ext_vector_type(4))) unsigned u32x4;
typedef unsigned short u16;
#define LAS __attribute__((address_space(3)))
#define GAS __attribute__((address_space(1)))
#define RLX_AGENT __ATOMIC_RELAXED, __HIP_MEMORY_SCOPE_AGENT

#define S_TOK 2048
#define DM 1024
#define NQKV 3072
#define FF 3584

typedef __attribute__((ext_vector_type(2))) unsigned u32x2;

#define WS_EP   0
#define WS_R    4096
#define WS_K    (WS_R + 32768)
#define WS_OF   (WS_K + 8192)
#define WS_CS   (WS_OF + 4096)
#define WS_GU   (WS_CS + 16 * 2 * 16 * 1024)
#define WS_Y    (WS_GU + 56 * 4 * 8192)
#define WS_S    (WS_Y + 56 * 8 * DM * 4)
#define WS_O    (8u << 20)

__device__ const unsigned k_tags[32] = {0,  1,  2,  3,  4,  5,  6,  7,  8,  9,  10, 11, 12, 13, 14, 15,
                                        16, 17, 18, 19, 20, 21, 22, 23, 24, 25, 26, 27, 28, 29, 30, 31};

__device__ __forceinline__ unsigned pack2(float a, float b) {
  f32x2 v = {a, b};
  bf16x2_t r = __builtin_convertvector(v, bf16x2_t);
  return __builtin_bit_cast(unsigned, r);
}
__device__ __forceinline__ float rbf(float f) { return __uint_as_float(pack2(f, f) << 16); }
__device__ __forceinline__ float lo2f(unsigned w) { return __uint_as_float(w << 16); }
__device__ __forceinline__ float hi2f(unsigned w) { return __uint_as_float(w & 0xffff0000u); }

__device__ __forceinline__ bf16x8 lds_b128(const char* p) { return *(const bf16x8*)p; }
__device__ __forceinline__ bf16x8 lds_tr2(const char* p) {
  s16x4 lo = __builtin_amdgcn_ds_read_tr16_b64_v4i16((LAS s16x4*)p);
  s16x4 hi = __builtin_amdgcn_ds_read_tr16_b64_v4i16((LAS s16x4*)(p + 512));
  return __builtin_shufflevector(lo, hi, 0, 1, 2, 3, 4, 5, 6, 7);
}
#define MFMA16(a, b, c) __builtin_amdgcn_mfma_f32_16x16x32_bf16(a, b, c, 0, 0, 0)
#define WAITV(n) asm volatile("s_waitcnt vmcnt(%0)" ::"i"(n) : "memory")
#define WAITL0() __builtin_amdgcn_s_waitcnt(0xC07F)
#define BAR() __builtin_amdgcn_s_barrier()
#define SCHED0() __builtin_amdgcn_sched_barrier(0)
#define NAP(n) asm volatile("s_sleep %0" ::"i"(n) : "memory")
__device__ __forceinline__ float xsum16_32(float v) {
  unsigned u = __float_as_uint(v);
  auto a = __builtin_amdgcn_permlane16_swap(u, u, false, false);
  v = __uint_as_float(a[0]) + __uint_as_float(a[1]);
  u = __float_as_uint(v);
  auto b = __builtin_amdgcn_permlane32_swap(u, u, false, false);
  return __uint_as_float(b[0]) + __uint_as_float(b[1]);
}
__device__ __forceinline__ bf16x8 pack8(const f32x4& a, const f32x4& b) {
  u32x4 r = {pack2(a[0], a[1]), pack2(a[2], a[3]), pack2(b[0], b[1]), pack2(b[2], b[3])};
  return __builtin_bit_cast(bf16x8, r);
}
__device__ __forceinline__ unsigned wave_max_u32(unsigned v) {
  v = max(v, (unsigned)__builtin_amdgcn_update_dpp(0, (int)v, 0x111, 0xf, 0xf, false));
  v = max(v, (unsigned)__builtin_amdgcn_update_dpp(0, (int)v, 0x112, 0xf, 0xf, false));
  v = max(v, (unsigned)__builtin_amdgcn_update_dpp(0, (int)v, 0x114, 0xf, 0xf, false));
  v = max(v, (unsigned)__builtin_amdgcn_update_dpp(0, (int)v, 0x118, 0xf, 0xf, false));
  v = max(v, (unsigned)__builtin_amdgcn_update_dpp(0, (int)v, 0x142, 0xa, 0xf, false));
  v = max(v, (unsigned)__builtin_amdgcn_update_dpp(0, (int)v, 0x143, 0xc, 0xf, false));
  return (unsigned)__builtin_amdgcn_readlane((int)v, 63);
}
__device__ __forceinline__ bf16x8 tr_frag(const u32x4 (&w)[8], int i) {
  const unsigned sel = (i & 1) ? 0x07060302u : 0x05040100u;
  u32x4 r = {__builtin_amdgcn_perm(w[1][i >> 1], w[0][i >> 1], sel), __builtin_amdgcn_perm(w[3][i >> 1], w[2][i >> 1], sel),
             __builtin_amdgcn_perm(w[5][i >> 1], w[4][i >> 1], sel), __builtin_amdgcn_perm(w[7][i >> 1], w[6][i >> 1], sel)};
  return __builtin_bit_cast(bf16x8, r);
}

#define SLOT 43008
#define XB 18432
#define NBAR 14
#define L_WO 20224
#define L_XCH (L_WO + 131072)
#define L_HF (L_XCH + 6144)
#define L_UP (L_HF + 1024)
#define L_CSF (L_UP + 2048)
#define L_QST (L_CSF + 1536)
#define L_FLG (L_QST + 1152)
#define L_SIZE (L_FLG + 256)
#define F_FULL (L_FLG + 0)
#define F_ST (L_FLG + 8)
#define F_WO (L_FLG + 16)
#define F_FREE (L_FLG + 32)
#define F_XCH (L_FLG + 40)
#define F_ODONE (L_FLG + 48)
#define F_SELOK (L_FLG + 52)
#define F_GO (L_FLG + 56)
#define F_OL (L_FLG + 60)
#define F_H (L_FLG + 64)
#define F_TAG (L_FLG + 68)
#define F_UP (L_FLG + 72)
#define F_SEL (L_FLG + 96)
#define F_CND (L_FLG + 128)
#define OD 7
#define SPIN_CAP (1u << 18)
#define WR 54
#define LF 20
#define NW 3
#define TW0 3
#define CD 4
#define EV 4
#define PN 2
#define YD 24
#define OK 2
#define ON 8
#define HN 2
#define SD 0
#define MG (LC == 1)
#define XSL 10
#define KQ 1
#define TCH 3
#define TS 0
#define SX 0
#define CODE_BYTES 53888

#define RSC(i)
#define STAMP(i)
#define RST(i)

__device__ __forceinline__ unsigned lds_addr(const char* p) { return (unsigned)(size_t)(LAS const char*)p; }
__device__ __forceinline__ void lds_post(char* smem, int off, unsigned v) {
  asm volatile("s_mov_b64 exec, 1\n\tds_write_b8 %0, %1\n\ts_mov_b64 exec, -1" ::"v"(lds_addr(smem + off)), "v"(v) : "memory");
}
__device__ __forceinline__ unsigned lds_word(const char* smem, int off) { return *(const volatile LAS unsigned*)(LAS const char*)(smem + off); }
__device__ __forceinline__ u32x4 lds_word4(const char* smem, int off) { return *(const volatile LAS u32x4*)(LAS const char*)(smem + off); }
template <unsigned MASK, bool COND = false>
__device__ __forceinline__ unsigned wait_bytes(const char* smem, int off, unsigned need, unsigned skip = 0) {
  unsigned t, f;
  asm volatile(
      ".if %6\n\ts_cmp_lg_u32 %5, 0\n\ts_cbranch_scc1 2f\n\t.endif\n\t"
      "1:\n\t"
      "ds_read_b32 %1, %2\n\t"
      "s_waitcnt lgkmcnt(0)\n\t"
      "v_readfirstlane_b32 %0, %1\n\t"
      "s_or_b32 %0, %0, 0x80808080\n\t"
      "s_sub_u32 %0, %0, %3\n\t"
      "s_and_b32 %0, %0, %4\n\t"
      "s_cmp_eq_u32 %0, %4\n\t"
      "s_cbranch_scc1 2f\n\t"
      "s_sleep 1\n\t"
      "s_branch 1b\n\t"
      "2:"
      : "=&s"(t), "=&v"(f)
      : "v"(lds_addr(smem + off)), "s"(need * 0x01010101u), "s"(MASK), "s"(COND ? skip : MASK), "i"((int)COND)
      : "scc", "memory");
  return f;
}
__device__ __forceinline__ void wait_full(const char* smem, int off, unsigned need, unsigned cur = 0) {
  unsigned a, b;
  asm volatile(
      "s_cmp_ge_u32 %0, %4\n\t"
      "s_cbranch_scc1 3f\n\t"
      "s_branch 2f\n\t"
      "1:\n\t"
      "s_sleep 1\n\t"
      "2:\n\t"
      "ds_read_b32 %1, %3\n\t"
      "ds_read_b32 %2, %3 offset:4\n\t"
      "s_waitcnt lgkmcnt(0)\n\t"
      "v_min_u32 %1, %1, %2\n\t"
      "s_nop 0\n\t"
      "v_readfirstlane_b32 %0, %1\n\t"
      "s_cmp_lt_u32 %0, %4\n\t"
      "s_cbranch_scc1 1b\n\t"
      "3:"
      : "+s"(cur), "=&v"(a), "=&v"(b)
      : "v"(lds_addr(smem + off)), "s"(need)
      : "scc", "memory");
}
__device__ __forceinline__ unsigned uni(unsigned v) { return (unsigned)__builtin_amdgcn_readfirstlane((int)v); }
__device__ __forceinline__ void dma_tag(char* smem, int off, int n) {
  unsigned keep;
  asm volatile("s_nop 2\n\ts_mov_b32 %0, m0\n\ts_mov_b32 m0, %1\n\ts_mov_b64 exec, 1\n\tglobal_load_lds_dword %2, %3\n\ts_mov_b64 exec, -1\n\ts_mov_b32 m0, %0\n\ts_nop 0"
               : "=&s"(keep) : "s"(uni(lds_addr(smem + off))), "v"(n * 4), "s"(k_tags) : "memory");
}
struct QFrag { bf16x8 xa[5]; bf16x8 w[3][2]; };
__device__ __forceinline__ void q_read(QFrag& f, const char* sa, const char* sb0, const char* sb1, int ks) {
#pragma unroll
  for (int i = 0; i < 5; ++i) f.xa[i] = lds_b128(sa + i * 2048);
#pragma unroll
  for (int s = 0; s < 3; ++s) {
    f.w[s][0] = lds_tr2(sb0 + s * 8192 + ks * 4096);
    f.w[s][1] = lds_tr2(sb1 + s * 8192 + ks * 4096);
  }
}
__device__ constexpr int acc_no(int HALF, int m, int i, int t) {
  int n = 0;
  for (int ii = 0; ii < 5; ++ii)
    for (int tt = 0; tt < 2; ++tt)
      for (int mm = 0; mm < 3; ++mm) {
        const bool on = mm == 0 ? (HALF == 1 || (ii >= 1 && ii <= 3)) : (HALF == 0 || ii >= 1);
        if (mm == m && ii == i && tt == t) return on ? n : -1;
        if (on) ++n;
      }
  return -1;
}
__device__ constexpr int seq_of(int HALF, int z) {
  int n = 0;
  for (int a = 0; a < 30; ++a) {
    const int m = a / 10, t0 = (a / 5) % 2;
    const int i = m < 2 ? (t0 ? a % 5 : 4 - a % 5) : 4 - (a - 20) / 2, t = m < 2 ? t0 : (a ^ i) & 1;
    if (acc_no(HALF, m, i, t) >= 0) { if (n == z) return m * 100 + i * 10 + t; ++n; }
  }
  return -1;
}
template <int HALF, int Z, int E, bool FIRST>
__device__ __forceinline__ void q_one(const QFrag& f, f32x4 (&acc)[26]) {
  if constexpr (Z < E) {
    constexpr int m = seq_of(HALF, Z) / 100, i = seq_of(HALF, Z) / 10 % 10, t = seq_of(HALF, Z) % 10;
    constexpr int n = acc_no(HALF, m, i, t);
    const f32x4 c = FIRST ? f32x4{0, 0, 0, 0} : acc[n];
    acc[n] = m == 2 ? MFMA16(f.xa[i], f.w[2][t], c) : MFMA16(f.w[m][t], f.xa[i], c);
    q_one<HALF, Z + 1, E, FIRST>(f, acc);
  }
}
template <int HALF, int B, int E, int NRD, bool FIRST = false>
__device__ __forceinline__ void q_mma(const QFrag& f, f32x4 (&acc)[26]) {
  q_one<HALF, B, E, FIRST>(f, acc);
#pragma unroll
  for (int i = 0; i < NRD; ++i) {
    __builtin_amdgcn_sched_group_barrier(0x008, 1, 0);
    __builtin_amdgcn_sched_group_barrier(0x100, 1, 0);
  }
  __builtin_amdgcn_sched_group_barrier(0x008, E - B - NRD, 0);
}

__device__ __forceinline__ void tr_all(u32x4 (&w)[8]) {
  u32x4 t[8];
#pragma unroll
  for (int i = 0; i < 8; ++i) t[i] = __builtin_bit_cast(u32x4, tr_frag(w, i));
#pragma unroll
  for (int i = 0; i < 8; ++i) w[i] = t[i];
  asm volatile("" : "+v"(w[0]), "+v"(w[1]), "+v"(w[2]), "+v"(w[3]), "+v"(w[4]), "+v"(w[5]), "+v"(w[6]), "+v"(w[7]));
}
typedef __amdgpu_buffer_rsrc_t rsrc_t;
__device__ __forceinline__ rsrc_t mk_rsrc(const void* p, unsigned bytes) { return __builtin_amdgcn_make_buffer_rsrc((void*)p, 0, bytes, 0x00020000); }
__device__ __forceinline__ u32x4 ld16(rsrc_t rs, unsigned voff, unsigned soff) { return __builtin_amdgcn_raw_buffer_load_b128(rs, voff, soff, 0); }
__device__ __forceinline__ u32x4 ld16c(rsrc_t rs, unsigned voff, unsigned soff) { return __builtin_amdgcn_raw_buffer_load_b128(rs, voff, soff, 16); }
__device__ __forceinline__ void st16c(rsrc_t rs, unsigned voff, u32x4 v) { __builtin_amdgcn_raw_buffer_store_b128(v, rs, voff, 0, 16); }
__device__ __forceinline__ void st8c(rsrc_t rs, unsigned voff, unsigned v, unsigned tag) { __builtin_amdgcn_raw_buffer_store_b64(u32x2{v, tag}, rs, voff, 0, 16); }
__device__ __forceinline__ unsigned chk(unsigned bad, const u32x4& v, unsigned tag) {
  return __builtin_amdgcn_bitop3_b32(__builtin_amdgcn_bitop3_b32(bad, v[1], tag, 0xF6), v[3], tag, 0xF6);
}
__device__ __forceinline__ float f_of(unsigned u) { return __uint_as_float(u); }
#define LANE_VARS                                                                   \
  int lane = lane_in; asm volatile("" : "+v"(lane));                                \
  const int g = lane >> 4, r16 = lane & 15, q = (lane >> 2) & 3, p = lane & 3;      \
  const int hk = (q >> 1) | ((g & 1) << 1), brow = (8 * g + q) * 128 + 8 * p;       \
  (void)r16; (void)hk; (void)brow;

template <bool MAX>
__device__ __forceinline__ float rows_reduce(const float (&v)[4]) {
  auto op = [](unsigned a, unsigned b) {
    return __float_as_uint(MAX ? __builtin_elementwise_maximum(__uint_as_float(a), __uint_as_float(b)) : __uint_as_float(a) + __uint_as_float(b));
  };
  auto p = __builtin_amdgcn_permlane16_swap(__float_as_uint(v[0]), __float_as_uint(v[1]), false, false);
  auto q = __builtin_amdgcn_permlane16_swap(__float_as_uint(v[2]), __float_as_uint(v[3]), false, false);
  auto t = __builtin_amdgcn_permlane32_swap(op(p[0], p[1]), op(q[0], q[1]), false, false);
  return __uint_as_float(op(t[0], t[1]));
}
__device__ __forceinline__ void rows_spread(float t, float (&v)[4]) {
  auto h = __builtin_amdgcn_permlane32_swap(__float_as_uint(t), __float_as_uint(t), false, false);
  auto p = __builtin_amdgcn_permlane16_swap(h[0], h[0], false, false);
  auto q = __builtin_amdgcn_permlane16_swap(h[1], h[1], false, false);
  v[0] = __uint_as_float(p[0]); v[1] = __uint_as_float(p[1]); v[2] = __uint_as_float(q[0]); v[3] = __uint_as_float(q[1]);
}
typedef __attribute__((ext_vector_type(16))) unsigned u32x16;

template <int CTL>
__device__ __forceinline__ float dpp_f(float v) { return __uint_as_float(__builtin_amdgcn_update_dpp(0, (int)__float_as_uint(v), CTL, 0xf, 0xf, false)); }
__device__ __forceinline__ float sum16(float v) {
  v += dpp_f<0xB1>(v);
  v += dpp_f<0x4E>(v);
  v += dpp_f<0x141>(v);
  return v + dpp_f<0x140>(v);
}
__device__ __forceinline__ float dot2(unsigned a, unsigned b, float c) {
  return __builtin_amdgcn_fdot2_f32_bf16(__builtin_bit_cast(bf16x2_t, a), __builtin_bit_cast(bf16x2_t, b), c, false);
}
__device__ __forceinline__ float sum32(float v) {
  v = sum16(v);
  auto a = __builtin_amdgcn_permlane16_swap(__float_as_uint(v), __float_as_uint(v), false, false);
  return __uint_as_float(a[0]) + __uint_as_float(a[1]);
}
struct RowRegs { u32x4 x[4], w[2]; unsigned ep, keep; };
__device__ __forceinline__ void row_request(RowRegs& r, const u16* __restrict__ x, const u16* __restrict__ wr, char* ws, int row0, int lane, bool on) {
  if (!on) return;
  const rsrc_t xrs = mk_rsrc(x, S_TOK * DM * 2), rrs = mk_rsrc(wr, DM * 2);
  const unsigned xo = (unsigned)(row0 + (lane >> 5)) * 2048u + (lane & 31) * 16, wo = lane * 16;
  asm volatile("buffer_load_dword %0, off, %1, 0 sc1" : "=v"(r.ep) : "s"(mk_rsrc(ws + WS_EP, 4)) : "memory");
#pragma unroll
  for (int j = 0; j < 4; ++j) asm volatile("buffer_load_dwordx4 %0, %1, %2, 0 offen offset:%3" : "=v"(r.x[j]) : "v"(xo), "s"(xrs), "i"(j * 512) : "memory");
#pragma unroll
  for (int j = 0; j < 2; ++j) asm volatile("buffer_load_dwordx4 %0, %1, %2, 0 offen offset:%3" : "=v"(r.w[j]) : "v"(wo), "s"(rrs), "i"(j * 1024) : "memory");
}
__device__ __forceinline__ unsigned row_stats(RowRegs& r, char* ws, int row0, int lane) {
  float s0 = 0.f, s1 = 0.f;
  asm volatile("s_waitcnt vmcnt(2)" : "+v"(r.x[0]), "+v"(r.x[1]), "+v"(r.x[2]), "+v"(r.x[3]), "+v"(r.ep), "+v"(r.keep) :: "memory");
  const unsigned tag = uni(r.ep) + 1u;
#pragma unroll
  for (int j = 0; j < 4; ++j) {
    s0 = dot2(r.x[j][0], 0x3f803f80u, s0); s1 = dot2(r.x[j][1], 0x3f803f80u, s1);
    s0 = dot2(r.x[j][2], 0x3f803f80u, s0); s1 = dot2(r.x[j][3], 0x3f803f80u, s1);
  }
  const float mean = sum32(s0 + s1) * (1.0f / DM), mb = rbf(mean);
  s0 = 0.f; s1 = 0.f;
#pragma unroll
  for (int j = 0; j < 4; ++j)
#pragma unroll
    for (int i = 0; i < 4; ++i) {
      const float a = lo2f(r.x[j][i]) - mb, b = hi2f(r.x[j][i]) - mb;
      s0 = fmaf(a, a, s0); s1 = fmaf(b, b, s1);
      r.x[j][i] = pack2(a, b);
    }
  const float dm = mean - mb;
  const float var = rbf(fmaxf(sum32(s0 + s1) * (1.0f / DM) - dm * dm, 0.f));
  const float den = rbf(__builtin_amdgcn_sqrtf(rbf(var + 1e-5f))), rd = __builtin_amdgcn_rcpf(den);
  s0 = 0.f; s1 = 0.f;
  asm volatile("s_waitcnt vmcnt(0)" : "+v"(r.w[0]), "+v"(r.w[1]) :: "memory");
#pragma unroll
  for (int j = 0; j < 4; ++j)
#pragma unroll
    for (int i = 0; i < 4; ++i) {
      const unsigned n = pack2(lo2f(r.x[j][i]) * rd, hi2f(r.x[j][i]) * rd);
      const unsigned w = __builtin_amdgcn_permlane32_swap(r.w[j >> 1][i], r.w[j >> 1][i], false, false)[j & 1];
      if (i & 1) s1 = dot2(n, w, s1); else s0 = dot2(n, w, s0);
    }
  const float dot = sum32(s0 + s1);
  unsigned k = pack2(dot, dot) & 0xffffu;
  k = (k & 0x8000u) ? (~k & 0xffffu) : (k | 0x8000u);
  const rsrc_t wrs = mk_rsrc(ws, WS_O);
  st16c(wrs, (lane & 31) == 0 ? WS_R + (unsigned)(row0 + (lane >> 5)) * 16u : 0x7fffffffu,
        u32x4{(k << 16) | (__float_as_uint(mb) >> 16), tag, __float_as_uint(den), tag});
  const unsigned k01 = k | ((unsigned)__builtin_amdgcn_readlane((int)k, 32) << 16);
  st8c(wrs, lane == 0 ? WS_K + (unsigned)(((row0 >> 2) & 7) * 1024 + (row0 >> 5) * 16 + (row0 & 2) * 4) : 0x7fffffffu, k01, tag);
  return tag;
}
__device__ __forceinline__ void k_order(int tm, int tn, int& pa, int& pb) {
  pb = tn & 3;
  pa = (tm * 0x1111) ^ ((tn & 4) * 0x51 & 0x144) ^ ((~tn & 2) * 0x2200) ^ ((~tn & 1) << 9) ^ (pb == 3 ? 1 : tm & 1);
}
__device__ __forceinline__ int k_tile(int t, int pa, int pb) { return ((((t + pb) & 12) ? t + pb : t & 3) ^ (pa >> ((t + pb) & 12))) & 15; }
struct EpiRegs { int a0, b0, tx; unsigned xr, go; u32x4 xv[2][2]; unsigned t[16]; };
__device__ __forceinline__ float opq(float v) { asm("" : "+v"(v)); return v; }
typedef __attribute__((ext_vector_type(10))) unsigned u32x10;
#define DV 0
#define PRE 3
#define GRP(N, M, K, C) _Pragma("unroll") for (int z_ = 0; z_ < (N); ++z_) { __builtin_amdgcn_sched_group_barrier(0x008, M, 0); __builtin_amdgcn_sched_group_barrier(K, C, 0); }
__device__ constexpr int tail_of(int HALF, int z) {
  int n = 0;
  for (int a = 0; a < 30; ++a) {
    const int i = HALF ? a / 6 : 4 - a / 6, m = 2 - a % 6 / 2, t = a & 1;
    if (acc_no(HALF, m, i, t) >= 0 && (m < 2 || (HALF == 0 && i == 4))) { if (n == z) return m * 100 + i * 10 + t; ++n; }
  }
  return -1;
}
__device__ __forceinline__ void qkv_setup(char* smem, int wid, int lane_in, int tm, int tn, EpiRegs& e) {
  LANE_VARS
  e.a0 = (wid & 1) * 8192 + r16 * 128 + ((g ^ (r16 >> 1)) << 4);
  e.b0 = XB + brow + (((wid & 2) ^ hk) << 5);
  int pa, pb;
  k_order(tm, tn, pa, pb);
  e.tx = __builtin_ctz((unsigned)__ballot(k_tile(r16, pa, pb) == tn));
  e.xr = lds_addr(smem) + (16 + wid * 32 + r16) * 128 + ((((g & 1) * 2 + (g >> 1)) ^ (r16 >> 1)) << 4);
  asm volatile("" : "+v"(e.a0), "+v"(e.b0), "+v"(e.xr));
  asm volatile("" : "+s"(e.tx));
}
template <int HALF>
__device__ __forceinline__ void qkv_attention(char* smem, int wid, int lane_in, int tm, int tn, char* ws, unsigned tag,
                                              const u16* __restrict__ x, EpiRegs& e) {
  const int h = wid >> 1;
  f32x4 acc[26];
  float ra[5], rb[5], sa[4], sb[4];
  QFrag f0, f1;
  u32x4 fl;
  unsigned aq, ac, aa, ab, xa, xv, xf, ooff, sel[4];
  {
    LANE_VARS
    const int a0 = e.a0, b0 = e.b0, tx = uni(e.tx);
    const unsigned xr = e.xr;
    asm volatile("" : "=v"(e.xv[0][0]), "=v"(e.xv[0][1]), "=v"(e.xv[1][0]), "=v"(e.xv[1][1]));
    auto keep = [&](int t, int slot, int tx) {
      unsigned p0, p1;
      asm volatile("s_cmp_lg_u32 %7, %8\n\ts_cbranch_scc1 1f\n\tv_add_u32 %4, %9, %6\n\tv_xor_b32 %5, 64, %4\n\tds_read_b128 %0, %4\n\tds_read_b128 %1, %5\n\t"
                   "ds_read_b128 %2, %4 offset:2048\n\tds_read_b128 %3, %5 offset:2048\n\t1:"
                   : "+v"(e.xv[0][0]), "+v"(e.xv[0][1]), "+v"(e.xv[1][0]), "+v"(e.xv[1][1]), "=&v"(p0), "=&v"(p1) : "v"(xr), "s"(t), "s"(tx), "s"(slot) : "scc", "memory");
    };
    SCHED0();
    WAITL0();
    BAR();
    STAMP(1);
    q_read(f0, smem + a0, smem + b0, smem + (b0 ^ 32), 0);
    SCHED0();
    q_read(f1, smem + (a0 ^ 64), smem + b0, smem + (b0 ^ 32), 1);
    int qa = SLOT + a0, qc = SLOT + (a0 ^ 64), qb = SLOT + b0, qd = SLOT + (b0 ^ 32), so = SLOT;
    q_mma<HALF, 0, 26, 17, true>(f0, acc);
    asm volatile("" : "+v"(qa), "+v"(qb), "+v"(qc), "+v"(qd));
    SCHED0();
    keep(0, 0, tx);
#pragma unroll 1
    for (int t = 1; t < NBAR - 1; ) {
      SCHED0();
      WAITL0();
      BAR();
      SCHED0();
      q_read(f0, smem + qa, smem + qb, smem + qd, 0);
      q_mma<HALF, 0, 26, 17>(f1, acc);
      SCHED0();
      q_read(f1, smem + qc, smem + qb, smem + qd, 1);
      q_mma<HALF, 0, 19, 17>(f0, acc);
      SCHED0();
      int d = so == 2 * SLOT ? -2 * SLOT : SLOT;
      asm volatile("" : "+s"(d));
      SCHED0();
      q_mma<HALF, 19, 20, 0>(f0, acc);
      SCHED0();
      keep(t, so, tx);
      SCHED0();
      q_mma<HALF, 20, 21, 0>(f0, acc);
      SCHED0(); qa += d; qb += d; asm volatile("" : "+v"(qa), "+v"(qb)); SCHED0();
      q_mma<HALF, 21, 22, 0>(f0, acc);
      SCHED0(); qc += d; qd += d; asm volatile("" : "+v"(qc), "+v"(qd)); SCHED0();
      q_mma<HALF, 22, 23, 0>(f0, acc);
      SCHED0(); so += d; ++t; asm volatile("" : "+s"(so), "+s"(t)); SCHED0();
      q_mma<HALF, 23, 26, 0>(f0, acc);
      SCHED0();
    }
    WAITL0();
    BAR();
    STAMP(2);
#pragma unroll 1
    for (int t = NBAR + 1; t < 17; ) {
      SCHED0();
      q_read(f0, smem + qa, smem + qb, smem + qd, 0);
      q_mma<HALF, 0, 26, 17>(f1, acc);
      SCHED0();
      q_read(f1, smem + qc, smem + qb, smem + qd, 1);
      const u32x4 fl = lds_word4(smem, F_FULL);
      q_mma<HALF, 0, 21, 18>(f0, acc);
      SCHED0();
      const unsigned cur = uni(min(fl[0], fl[1])), need = t;
      SCHED0();
      q_mma<HALF, 21, 22, 0>(f0, acc);
      SCHED0();
      keep(t, so, tx + 2);
      SCHED0();
      qa += SLOT; qb += SLOT; qc += SLOT; qd += SLOT; so += SLOT; ++t;
      q_one<HALF, 22, 25, false>(f0, acc);
      GRP(2, 1, 0x002, 2);
      asm volatile("" : "+v"(qa), "+v"(qb), "+v"(qc), "+v"(qd), "+s"(so), "+s"(t));
      SCHED0();
      q_mma<HALF, 25, 26, 0>(f0, acc);
      SCHED0();
      wait_full(smem, F_FULL, need, cur);
    }
    SCHED0();
    q_read(f0, smem + a0, smem + b0, smem + (b0 ^ 32), 0);
    q_one<HALF, 0, 26, false>(f1, acc);
    {
      const int pa = 4 * g + (r16 & 1);
      const bool odd = r16 & 1, cur_a = pa <= r16, cur_b = pa + 2 <= r16;
      const unsigned qs = lds_addr(smem + L_QST + HALF * 512);
      aq = qs + r16 * 8; ac = lds_addr(smem + L_CSF + h * 256) + r16 * 8;
      aa = qs + pa * 8 + (cur_a ? 128 : 0); ab = qs + pa * 8 + (cur_b ? 128 : 0);
      xa = lds_addr(smem + L_XCH + h * 3072) + lane * 16; xv = xa + 2048 - lane * 8; xf = lds_addr(smem + F_XCH + h * 4);
      const unsigned la = odd ? 0x01000c0cu : 0x0c0c0100u, lb = odd ? 0x03020c0cu : 0x0c0c0302u;
      sel[0] = cur_a ? 0x0c0c0c0cu : la; sel[1] = cur_b ? 0x0c0c0c0cu : lb; sel[2] = cur_a ? la : 0x0c0c0c0cu; sel[3] = cur_b ? lb : 0x0c0c0c0cu;
      ooff = (unsigned)(tm * 8 + HALF * 4) * 32768u + (tn * 2 + h) * 1024 + lane * 16;
#pragma unroll
      for (int z = 0; z < 17; ++z) { GRP(1, 1, 0x100, 1); __builtin_amdgcn_sched_group_barrier(0x002, 1, 0); }
      GRP(9, 1, 0x002, 3);
      asm volatile("" : "+v"(aq), "+v"(ac), "+v"(aa), "+v"(ab), "+v"(xa), "+v"(xv), "+v"(xf), "+v"(ooff));
      asm volatile("" : "+v"(sel[0]), "+v"(sel[1]), "+v"(sel[2]), "+v"(sel[3]));
    }
    SCHED0();
#pragma unroll
    for (int i = 0; i < 5; ++i) f1.xa[i] = lds_b128(smem + (a0 ^ 64) + i * 2048);
#pragma unroll
    for (int s = 0; s < 3; ++s) {
      if (s == 2) fl = lds_word4(smem, F_FULL);
      f1.w[s][0] = lds_tr2(smem + b0 + s * 8192 + 4096);
      f1.w[s][1] = lds_tr2(smem + (b0 ^ 32) + s * 8192 + 4096);
    }
    q_mma<HALF, 0, 26 - DV, 18>(f0, acc);
    SCHED0();
    keep(15, 0, tx);
    SCHED0();
  }
  LANE_VARS
  auto mm = [&](const QFrag& f, int m, int i, int t) {
    const int n = max(acc_no(HALF, m, i, t), 0);
    acc[n] = m == 2 ? MFMA16(f.xa[i], f.w[2][t], acc[n]) : MFMA16(f.w[m][t], f.xa[i], acc[n]);
  };
  auto last = [&](int z0, int z1) {
#pragma unroll
    for (int z = z0; z < z1; ++z) {
      const int c = tail_of(HALF, HALF ? z : z < 6 ? z + 2 : z < 8 ? z - 6 : z);
      mm(f1, c / 100, c / 10 % 10, c % 10);
    }
  };
  last(0, PRE);
  SCHED0();
  const unsigned cur = uni(min(fl[2], fl[3]));
  SCHED0();
  last(PRE, PRE + 1);
  SCHED0();
  wait_full(smem, F_ST, 1, cur);
  SCHED0();
  u32x2 sq[5], cs[3][2];
#pragma unroll
  for (int i = 0; i < 5; ++i) sq[i] = *(const LAS u32x2*)(aq + i * 128);
#pragma unroll
  for (int s = 0; s < 3; ++s)
#pragma unroll
    for (int t = 0; t < 2; ++t) cs[s][t] = *(const LAS u32x2*)(ac + s * 512 + t * 128);
#pragma unroll
  for (int i = 1; i < 5; ++i) { ra[i] = *(const LAS float*)(aa + (i - 1) * 128 + 4); rb[i] = *(const LAS float*)(ab + (i - 1) * 128 + 20); }
  last(PRE + 1, PRE + 5);
  __builtin_amdgcn_sched_group_barrier(0x100, 3, 0); GRP(3, 1, 0x100, 3);
  SCHED0();
  lds_post(smem, F_FREE + wid, 16);
  SCHED0();
  last(PRE + 5, 18);
  const unsigned gm = g == 0 ? ~0u : 0u;
  u32x2 lx[5];
#pragma unroll
  for (int i = 0; i < 5; ++i) lx[i] = u32x2{sq[i][0] & gm, sq[i][0] & gm & 0xffffu};
#pragma unroll
  for (int i = 1; i < 5; ++i) {
    const float c = f_of(sq[i][1]) * (0.17677669529663687f * 1.4426950408889634f);
    sa[i - 1] = opq(c * ra[i]); sb[i - 1] = opq(c * rb[i]);
  }
  __builtin_amdgcn_sched_group_barrier(0x008, 5 - PRE, 0); GRP(8, 1, 0x002, 3);
  asm volatile("" : "+v"(lx[0]), "+v"(lx[1]), "+v"(lx[2]), "+v"(lx[3]), "+v"(lx[4]));
  asm volatile("" : "+v"(sa[0]), "+v"(sa[1]), "+v"(sa[2]), "+v"(sa[3]), "+v"(sb[0]), "+v"(sb[1]), "+v"(sb[2]), "+v"(sb[3]));
  SCHED0();
  auto ln = [&](int m, int i, int t) {
    const int n = max(acc_no(HALF, m, i, t), 0);
    const s16x4 lxi = __builtin_bit_cast(s16x4, lx[i]), lw = __builtin_bit_cast(s16x4, cs[m][t]);
    acc[n] = m == 2 ? __builtin_amdgcn_mfma_f32_16x16x16bf16_1k(lxi, lw, acc[n], 0, 0, 0) : __builtin_amdgcn_mfma_f32_16x16x16bf16_1k(lw, lxi, acc[n], 0, 0, 0);
  };
#pragma unroll
  for (int z = 0; z < 18; ++z) ln(tail_of(HALF, z) / 100, tail_of(HALF, z) / 10 % 10, tail_of(HALF, z) % 10);
  STAMP(3);
  bf16x8 qf[5], kf[5];
  u32x10 vf[2];
  auto pack_qk = [&](int i) {
    if (acc_no(HALF, 0, i, 0) >= 0) qf[i] = pack8(acc[max(acc_no(HALF, 0, i, 0), 0)], acc[max(acc_no(HALF, 0, i, 1), 0)]);
    if (acc_no(HALF, 1, i, 0) >= 0) kf[i] = pack8(acc[max(acc_no(HALF, 1, i, 0), 0)], acc[max(acc_no(HALF, 1, i, 1), 0)]);
  };
  auto pack_v = [&](int i, int t) {
    const f32x4& v = acc[max(acc_no(HALF, 2, i, t), 0)];
    return u32x2{pack2(v[0], v[1]), pack2(v[2], v[3])};
  };
  u32x2 vx[2];
  pack_qk(HALF == 0 ? 4 : 0);
  if (HALF == 0) {
    vx[0] = pack_v(4, 0); vx[1] = pack_v(4, 1);
    asm volatile("ds_write_b128 %0, %2 offset:1024\n\tds_write2st64_b64 %1, %3, %4 offset1:1" :: "v"(xa), "v"(xv), "v"(kf[4]), "v"(vx[0]), "v"(vx[1]) : "memory");
  } else {
    asm volatile("ds_write_b128 %0, %1" :: "v"(xa), "v"(qf[0]) : "memory");
  }
  asm volatile("s_mov_b64 exec, 1\n\tds_write_b8 %0, %0 offset:%1\n\ts_mov_b64 exec, -1" ::"v"(xf), "i"(HALF) : "memory");
  static_assert((F_XCH & 0xf) != 0 && ((F_XCH + 4) & 0xf) != 0, "the low byte of the address is the flag");
#pragma unroll
  for (int i = 0; i < 4; ++i) pack_qk(HALF == 0 ? 3 - i : i + 1);
  const f32x4 z4 = {0.f, 0.f, 0.f, 0.f};
  f32x4 sp[4], sc[4];
#pragma unroll
  for (int j = 0; j < 3; ++j) {
    const int i = HALF == 0 ? 3 - j : j + 2;
    sc[i - 1] = MFMA16(kf[i], qf[i], z4); sp[i - 1] = MFMA16(kf[i - 1], qf[i], z4);
  }
  if (HALF) sc[0] = MFMA16(kf[1], qf[1], z4);
  GRP(1, 6, 0x002, 4); GRP(12, 1, 0x002, 2); GRP(HALF ? 7 : 6, 1, 0x002, 3);
  SCHED0();
  unsigned flag;
  bf16x8 xq;
  if (HALF == 0) asm volatile("ds_read_b32 %0, %2\n\tds_read_b128 %1, %3" : "=&v"(flag), "=&v"(xq) : "v"(xf), "v"(xa) : "memory");
  else asm volatile("ds_read_b32 %0, %4\n\tds_read_b128 %1, %5 offset:1024\n\tds_read_b64 %2, %6\n\tds_read_b64 %3, %6 offset:512"
                    : "=&v"(flag), "=&v"(xq), "=&v"(vx[0]), "=&v"(vx[1]) : "v"(xf), "v"(xa), "v"(xv) : "memory");
  SCHED0();
  const bool odd = r16 & 1, cur_a = sel[0] == 0x0c0c0c0cu, cur_b = sel[1] == 0x0c0c0c0cu;
  float a[4], b[4], m[4];
  auto logits = [&](int j) {
    const f32x4 p4 = sp[j], c4 = sc[j];
    const float p0 = p4[0], p1 = p4[1], p2 = p4[2], p3 = p4[3], c0 = c4[0], c1 = c4[1], c2 = c4[2], c3 = c4[3];
    const float ca = odd ? c1 : c0, cb = odd ? c3 : c2, pa_ = odd ? p1 : p0, pb = odd ? p3 : p2;
    a[j] = opq((cur_a ? ca : pa_) * sa[j]);
    b[j] = opq((cur_b ? cb : pb) * sb[j]);
    if (HALF == 0 && j == 0) {
      a[j] = (!cur_a && tm == 0) ? -INFINITY : a[j];
      b[j] = (!cur_b && tm == 0) ? -INFINITY : b[j];
    }
    m[j] = __builtin_elementwise_maximum(a[j], b[j]);
  };
  auto late = [&](int z0, int z1) {
#pragma unroll
    for (int y = z0; y < z1; ++y) {
      const int c = seq_of(HALF, 18 + ((y + 8 - DV) & 7));
      if (y < 8 + DV) mm(y < DV ? f0 : f1, 2, c / 10 % 10, c % 10);
    }
  };
#pragma unroll
  for (int j = 0; j < 2; ++j) logits(HALF == 0 ? 2 - j : j + 1);
  late(0, 6);
  GRP(6, 1, 0x002, 3);
  asm volatile("" : "+v"(m[1]), "+v"(m[2]));
  SCHED0();
  {
    unsigned t;
#define XWAIT(RD) "s_waitcnt lgkmcnt(0)\n\tv_readfirstlane_b32 %0, %1\n\ts_and_b32 %0, %0, %[mask]\n\ts_cbranch_scc1 2f\n\t1:\n\t" RD \
                  "\n\ts_waitcnt lgkmcnt(0)\n\tv_readfirstlane_b32 %0, %1\n\ts_and_b32 %0, %0, %[mask]\n\ts_cbranch_scc0 1b\n\t2:"
    if (HALF == 0) asm volatile(XWAIT("ds_read_b32 %1, %3\n\tds_read_b128 %2, %4") : "=&s"(t), "+v"(flag), "+v"(xq) : "v"(xf), "v"(xa), [mask] "s"(0xff00u) : "scc", "memory");
    else asm volatile(XWAIT("ds_read_b32 %1, %5\n\tds_read_b128 %2, %6 offset:1024\n\tds_read_b64 %3, %7\n\tds_read_b64 %4, %7 offset:512")
                      : "=&s"(t), "+v"(flag), "+v"(xq), "+v"(vx[0]), "+v"(vx[1]) : "v"(xf), "v"(xa), "v"(xv), [mask] "s"(0xffu) : "scc", "memory");
  }
  SCHED0();
  if (HALF == 0) { sc[3] = MFMA16(kf[4], xq, z4); sp[3] = MFMA16(kf[3], xq, z4); }
  else sp[0] = MFMA16(xq, qf[1], z4);
  logits(HALF == 0 ? 0 : 3);
  late(6, 8);
  GRP(1, HALF ? 1 : 2, 0x002, 3); GRP(2, 1, 0x002, 3);
  asm volatile("" : "+v"(m[HALF ? 3 : 0]));
  SCHED0();
  logits(HALF == 0 ? 3 : 0);
  rows_spread(rows_reduce<true>(m), m);
#pragma unroll
  for (int j = 0; j < 4; ++j) {
    a[j] = __builtin_amdgcn_exp2f(opq(a[j] - m[j]));
    b[j] = __builtin_amdgcn_exp2f(opq(b[j] - m[j]));
    m[j] = opq(a[j] + b[j]);
    a[j] = opq(a[j] * ra[j + 1]); b[j] = opq(b[j] * rb[j + 1]);
  }
  late(8, 16);
#pragma unroll
  for (int z = 0; z < 8; ++z) ln(2, seq_of(HALF, 18 + z) / 10 % 10, seq_of(HALF, 18 + z) % 10);
  GRP(DV, 1, 0x402, 3); GRP(8, 1, 0x402, 2);
  asm volatile("" : "+v"(m[0]), "+v"(m[1]), "+v"(m[2]), "+v"(m[3]));
  SCHED0();
#pragma unroll
  for (int t = 0; t < 2; ++t) {
#pragma unroll
    for (int i = 0; i < 5; ++i) {
      const u32x2 v = i == (HALF ? 0 : 4) ? vx[t] : pack_v(i, t);
      vf[t][2 * i] = v[0]; vf[t][2 * i + 1] = v[1];
    }
    asm("" : "+v"(vf[t]));
  }
  rows_spread(__builtin_amdgcn_rcpf(rows_reduce<false>(m)), m);
  const rsrc_t ors = mk_rsrc(ws + WS_O, S_TOK * DM * 2);
#pragma unroll
  for (int j = 0; j < 4; ++j) {
    const unsigned pk = pack2(a[j] * m[j], b[j] * m[j]);
    const u32x4 pw = {__builtin_amdgcn_perm(0u, pk, sel[0]), __builtin_amdgcn_perm(0u, pk, sel[1]), __builtin_amdgcn_perm(0u, pk, sel[2]), __builtin_amdgcn_perm(0u, pk, sel[3])};
    const u32x4 v0 = {vf[0][2 * j], vf[0][2 * j + 1], vf[0][2 * j + 2], vf[0][2 * j + 3]}, v1 = {vf[1][2 * j], vf[1][2 * j + 1], vf[1][2 * j + 2], vf[1][2 * j + 3]};
    sp[j] = MFMA16(__builtin_bit_cast(bf16x8, v0), __builtin_bit_cast(bf16x8, pw), z4);
    sc[j] = MFMA16(__builtin_bit_cast(bf16x8, v1), __builtin_bit_cast(bf16x8, pw), z4);
  }
#pragma unroll
  for (int j = 0; j < 4; ++j) { qf[j] = pack8(sp[j], sc[j]); st16c(ors, ooff + j * 32768, __builtin_bit_cast(u32x4, qf[j])); }
  RST(4);
  SCHED0();
  asm volatile("ds_read_b32 %0, %1 offset:%2" : "=v"(e.go) : "v"(xf - h * 4), "i"(F_GO - F_XCH) : "memory");
  wait_bytes<0x80808080u>(smem, F_FREE, 16);
  asm volatile("" : "+v"(e.go));
  asm volatile("ds_write_b128 %0, %1\n\tds_write_b128 %0, %2 offset:1024\n\tds_write_b128 %0, %3 offset:4096\n\tds_write_b128 %0, %4 offset:5120"
               :: "v"(lds_addr(smem) + HALF * 8192 + h * 2048 + lane * 16), "v"(qf[0]), "v"(qf[1]), "v"(qf[2]), "v"(qf[3]) : "memory");
  lds_post(smem, F_OL + wid, 1);
}

__device__ __forceinline__ unsigned ln2s(unsigned w, float mb, float r) {
  const unsigned d = pack2(opq(lo2f(w) - mb), opq(hi2f(w) - mb));
  return pack2(opq(lo2f(d) * r), opq(hi2f(d) * r));
}

__device__ __forceinline__ void await_attention(char* smem, int lane, int tm, int tn, char* ws, unsigned tag, unsigned& tp) {
  const rsrc_t wrs = mk_rsrc(ws, WS_O);
  const unsigned off = WS_OF + (tm * 64 + ((lane >> 3) & 1) * 32 + (lane & 7)) * 4;
  const int own = tn >> 3;
  bool first = true;
  unsigned seen = 0, since = 0;
#pragma unroll 1
  for (unsigned n = 0; n < SPIN_CAP; ++n) {
    unsigned want = ~seen & 0xffffu;
    if (since) ++since; else since = uni(lds_word(smem, F_ODONE)) >> 2;
    if (since <= (first ? OK : 0)) want &= own ? 0x00ffu : 0xff00u;
    const unsigned f = __builtin_amdgcn_raw_buffer_load_b32(wrs, ((want >> (lane & 15)) & 1) ? off : 0x7fffffffu, 0, 16);
    seen |= (unsigned)__ballot(f == tag) & want;
    if (seen == 0xffffu) break;
    if (first && ((seen & 0xffu) == 0xffu || (seen >> 8) == 0xffu)) { lds_post(smem, F_GO, (seen & 0xffu) == 0xffu ? 1 : 2); first = false; NAP(ON); }
    NAP(PN);
  }
  lds_post(smem, F_GO + 1, 1);
  if (first) lds_post(smem, F_GO, 1);
}

__device__ __forceinline__ void out_proj(char* smem, int wid, int lane_in, int tm, int tn, char* ws, unsigned tag,
                                         u16* __restrict__ out, EpiRegs& e, unsigned rot2) {
  LANE_VARS
  const rsrc_t ors = mk_rsrc(ws + WS_O, S_TOK * DM * 2), yrs = mk_rsrc(out, S_TOK * DM * 2);
  const int tok = tm * 128 + wid * 32 + r16, co = (g & 1) * 16 + (g >> 1) * 8;
  unsigned eoff = (unsigned)(tok * DM + tn * 64 + co) * 2u;
  const unsigned vo = (unsigned)(tm * 8 + wid * 2) * 32768u + lane * 16;
  bf16x8 wb[3][4], oa[OD][2];
  int bc[4];
#pragma unroll
  for (int c = 0; c < 4; ++c) bc[c] = L_WO + brow + ((c ^ hk) << 5);
  f32x4 acc[2][4];
  SCHED0();
  unsigned go, oth = (tn & 8) * 2 ^ 16;
  asm volatile("" : "+s"(oth));
  auto o_request = [&](int s) {
    asm volatile("buffer_load_dwordx4 %0, %2, %4, %5 offen sc1\n\tbuffer_load_dwordx4 %1, %3, %4, %5 offen sc1" : "+v"(oa[s % OD][0]), "+v"(oa[s % OD][1])
                 : "v"(vo), "v"(vo + 32768), "s"(ors), "s"(((s >> 1 == 7 ? oth : (s & 16) ^ ((go & 2) * 8)) + ((rot2 >> (s & 14) * 2) & 14) + (s & 1)) * 1024) : "memory");
  };
  auto start = [&](unsigned word) {
    go = uni(word) & 3;
    if (go) {
#pragma unroll
      for (int s = 0; s < OD - 1; ++s) o_request(s);
    }
    SCHED0();
  };
#pragma unroll
  for (int s = 0; s < OD; ++s) asm volatile("" : "=v"(oa[s][0]), "=v"(oa[s][1]));
  {
    const unsigned w0 = uni(e.go);
    if (w0 & 3) wait_bytes<0x80808080u>(smem, F_OL, 1);
    start(w0);
  }
  asm volatile("s_waitcnt vmcnt(%1)\n\ts_cmp_lg_u32 %0, 0\n\ts_cbranch_scc1 1f\n\ts_waitcnt vmcnt(0)\n\t1:" :: "s"(go), "i"(2 * OD - 2) : "scc", "memory");
  unsigned old = 0;
  if (lane == 0) old = __hip_atomic_fetch_add((LAS unsigned*)(LAS char*)(smem + F_ODONE), 1u, __ATOMIC_RELAXED, __HIP_MEMORY_SCOPE_WORKGROUP);
  if (uni(old) == 3u) {
    if (lane == 0) __hip_atomic_store((GAS unsigned*)(ws + WS_OF) + tm * 64 + (tn >> 3) * 32 + (tn & 7), tag, RLX_AGENT);
  }
  RST(5);
  SCHED0();
  const unsigned w1 = lds_word(smem, F_GO);
  const unsigned srow = 2047u - (*(const unsigned*)(smem + F_SEL + (lane & 7) * 4) & 0xffffu);
  f32x4 res[2][4];
  const uint2 st[2] = {*(const uint2*)(smem + L_QST + (16 + wid * 32 + r16) * 8), *(const uint2*)(smem + L_QST + (32 + wid * 32 + r16) * 8)};
  auto norm = [&](int u) {
    const float rd = f_of(st[u].y), k = 1.0f + rd, b = lo2f(st[u].x) * rd;
#pragma unroll
    for (int pr = 0; pr < 2; ++pr) {
      auto sa = __builtin_amdgcn_permlane16_swap(e.xv[u][pr][0], e.xv[u][pr][2], false, false);
      auto sb = __builtin_amdgcn_permlane16_swap(e.xv[u][pr][1], e.xv[u][pr][3], false, false);
#pragma unroll
      for (int z = 0; z < 2; ++z) res[u][2 * pr + z] = f32x4{lo2f(sa[z]), hi2f(sa[z]), lo2f(sb[z]), hi2f(sb[z])} * k + b;
    }
    asm volatile("" : "+v"(res[u][0]), "+v"(res[u][1]), "+v"(res[u][2]), "+v"(res[u][3]));
  };
  f32x4 ac2[2][4], (&ac3)[2][4] = res;
  auto w_frag = [&](int slot, const int (&b)[4], int z) {
#pragma unroll
    for (int c = 0; c < 4; ++c) wb[slot][c] = lds_tr2(smem + b[c] + z * 4096);
  };
  auto step = [&](f32x4 (&d)[2][4], int slot, const bf16x8 (&o)[2], bool fresh) {
#pragma unroll
    for (int c = 0; c < 4; ++c)
#pragma unroll
      for (int uu = 0; uu < 2; ++uu) {
        const int u = uu ^ (c & 1);
        d[u][c] = MFMA16(wb[slot][c], o[u], fresh ? (f32x4{0, 0, 0, 0}) : d[u][c]);
      }
  };
  int b3[4];
#pragma unroll
  for (int c = 0; c < 4; ++c) b3[c] = bc[c] + 28 * 4096;
  auto own_heads = [&]() {
    w_frag(0, b3, 2); w_frag(1, b3, 3);
    const unsigned w2 = lds_word(smem, F_GO);
    SCHED0();
    if (wid < 2) wait_bytes<0x00800080u>(smem, F_OL, 1); else wait_bytes<0x80008000u>(smem, F_OL, 1);
    if (!go) start(w2);
    bf16x8 own[2][2];
#pragma unroll
    for (int z = 0; z < 4; ++z) own[z >> 1][z & 1] = lds_b128(smem + wid * 4096 + lane * 16 + z * 1024);
    step(ac3, 0, own[0], false); step(ac3, 1, own[1], false);
    asm volatile("" : "+v"(ac3[0][0]), "+v"(ac3[0][1]), "+v"(ac3[0][2]), "+v"(ac3[0][3]), "+v"(ac3[1][0]), "+v"(ac3[1][1]), "+v"(ac3[1][2]), "+v"(ac3[1][3]));
    SCHED0();
  };
  unsigned mine = (unsigned)__ballot((srow >> 5) == (unsigned)(tm * 4 + wid)) & 0xffu;
  asm volatile("" : "+s"(mine));
  asm volatile("" : "+v"(eoff));
  if (!go) start(w1);
  norm(0);
  norm(1);
  SCHED0();
  own_heads();
  if (!go) start(wait_bytes<0x80u>(smem, F_GO, 1));
  go = uni(go);
  RST(6);
  int b2[4];
  const int mine1st = ((go >> 1) ^ (tn >> 3) ^ 1) & 1;
#pragma unroll
  for (int c = 0; c < 4; ++c) { b2[c] = bc[c] + mine1st * 57344; bc[c] += (mine1st ^ 1) * 57344; }
  unsigned so[15];
#pragma unroll
  for (int z = (OD - 1) / 2; z < 15; ++z) { so[z] = ((z == 7 ? oth : (2 * z & 16) ^ ((go & 2) * 8)) + ((rot2 >> (z & 7) * 4) & 14)) * 1024; asm volatile("" : "+s"(so[z])); }
  s16x4 wh[3][8];
  auto w_half = [&](int s, int z0, int z1) {
#pragma unroll
    for (int z = z0; z < z1; ++z)
      if (s < 30) wh[s % 3][z] = __builtin_amdgcn_ds_read_tr16_b64_v4i16((LAS s16x4*)(smem + (s < 14 ? bc[z >> 1] + s * 4096 : s < 16 ? b3[z >> 1] + (s - 14) * 4096 : b2[z >> 1] + (s - 16) * 4096) + (z & 1) * 512));
  };
  wait_full(smem, F_WO, 1);
  RST(7);
  RSC(0);
  w_half(0, 0, 8); w_half(1, 0, 8);
  SCHED0();
  asm volatile("s_waitcnt vmcnt(%2)" : "+v"(oa[0][0]), "+v"(oa[0][1]) : "i"(2 * OD - 4) : "memory");
  auto one = [&](int s) __attribute__((always_inline)) {
    f32x4 (&d)[2][4] = s < 14 ? ac2 : s < 16 ? ac3 : acc;
    auto part = [&](int z0, int z1) {
      SCHED0();
      w_half(s + 2, z0, z1);
#pragma unroll
      for (int z = z0; z < z1; ++z) {
        const int c = z >> 1, u = (z ^ c) & 1;
        d[u][c] = MFMA16(__builtin_shufflevector(wh[s % 3][2 * c], wh[s % 3][2 * c + 1], 0, 1, 2, 3, 4, 5, 6, 7), oa[s % OD][u], s == 0 || s == 16 ? (f32x4{0, 0, 0, 0}) : d[u][c]);
        __builtin_amdgcn_sched_group_barrier(0x008, 1, 0);
        __builtin_amdgcn_sched_group_barrier(0x100, 1, 0);
      }
      SCHED0();
    };
    const int n = s + OD - 1;
    auto ask = [&](int u) {
      if (n < 30) asm volatile("buffer_load_dwordx4 %0, %1, %2, %3 offen offset:%4 sc1" : "+v"(oa[n % OD][u]) : "v"(vo + u * 32768), "s"(ors), "s"(so[n >> 1]), "i"((n & 1) * 1024) : "memory");
    };
    part(0, 1);
    if (n == 14 || n == 16) wait_bytes<0x8000u, true>(smem, F_GO, 1, n == 14 ? mine1st ^ 1 : mine1st);
    ask(0);
    part(1, 3);
    ask(1);
    part(3, 5);
    if (s < 29) asm volatile("s_waitcnt vmcnt(%2)" : "+v"(oa[(s + 1) % OD][0]), "+v"(oa[(s + 1) % OD][1]) : "i"(2 * ((n < 29 ? n : 29) - s - 1)) : "memory");
    part(5, 8);
    asm volatile("" : "+v"(d[0][0]), "+v"(d[0][1]), "+v"(d[0][2]), "+v"(d[0][3]), "+v"(d[1][0]), "+v"(d[1][1]), "+v"(d[1][2]), "+v"(d[1][3]));
    if (s == 0) { RST(14); RSC(1); }
    if (s == 14) { RST(15); RSC(2); }
  };
#pragma unroll
  for (int s = 0; s < 10; ++s) one(s);
#pragma unroll
  for (int s = 10; s < 20; ++s) one(s);
#pragma unroll
  for (int s = 20; s < 28; ++s) one(s);
  asm volatile("s_waitcnt vmcnt(0)" : "+v"(oa[29 % OD][0]), "+v"(oa[29 % OD][1]) :: "memory");
  if (mine != 0) {
    wait_bytes<0x80808080u>(smem, F_UP, 1);
#pragma unroll 1
    for (; mine != 0; mine &= mine - 1) {
      const int c = __builtin_ctz(mine), row = __builtin_amdgcn_readlane((int)srow, c) & 31;
#pragma unroll
      for (int u = 0; u < 2; ++u)
        if (row == u * 16 + r16) {
#pragma unroll
          for (int z = 0; z < 4; ++z) res[u][z] += *(const f32x4*)(smem + L_UP + c * 256 + (z * 16 + g * 4) * 4);
        }
    }
  }
  SCHED0();
#pragma unroll
  for (int u = 0; u < 2; ++u)
#pragma unroll
    for (int z = 0; z < 8; ++z)
      acc[u][z & 3] = MFMA16(__builtin_shufflevector(wh[(28 + (z >> 2)) % 3][2 * (z & 3)], wh[(28 + (z >> 2)) % 3][2 * (z & 3) + 1], 0, 1, 2, 3, 4, 5, 6, 7), oa[(28 + (z >> 2)) % OD][u], acc[u][z & 3]);
#pragma unroll
  for (int u = 0; u < 2; ++u)
#pragma unroll
    for (int pr = 0; pr < 2; ++pr) {
      const f32x4 c0 = (acc[u][2 * pr] + ac2[u][2 * pr]) + ac3[u][2 * pr], c1 = (acc[u][2 * pr + 1] + ac2[u][2 * pr + 1]) + ac3[u][2 * pr + 1];
      auto sa = __builtin_amdgcn_permlane16_swap(pack2(c0[0], c0[1]), pack2(c1[0], c1[1]), false, false);
      auto sb = __builtin_amdgcn_permlane16_swap(pack2(c0[2], c0[3]), pack2(c1[2], c1[3]), false, false);
      __builtin_amdgcn_raw_buffer_store_b128(u32x4{sa[0], sb[0], sa[1], sb[1]}, yrs, eoff + pr * 64 + u * 16 * DM * 2, 0, 16);
    }
  __builtin_amdgcn_sched_group_barrier(0x008, 9, 0);
  GRP(7, 1, 0x002, EV);
  SCHED0();
  RST(8);
  STAMP(9);
}

__device__ __forceinline__ unsigned med3(unsigned a, unsigned b, unsigned c) { return max(min(a, b), min(max(a, b), c)); }
#define PIN8(a) asm volatile("" : "+v"(a[0]), "+v"(a[1]), "+v"(a[2]), "+v"(a[3]), "+v"(a[4]), "+v"(a[5]), "+v"(a[6]), "+v"(a[7]))
__device__ __forceinline__ void tick(int& nb) {
  SCHED0();
  if (nb < NBAR) { BAR(); ++nb; if (HN) NAP(HN); }
  asm volatile("" ::: "memory");
  SCHED0();
}
__device__ __forceinline__ void hidden_half(char* smem, int lane, int l, int cg, char* ws, unsigned tag, u32x4 (&fd)[2][8]) {
  const rsrc_t wrs = mk_rsrc(ws, WS_O);
  const unsigned off = WS_GU + (unsigned)(cg * 32768 + l * 2048 + lane * 16);
  u32x4 ps[2][8];
  auto request = [&]() {
#pragma unroll
    for (int p = 0; p < 16; ++p) ps[p & 1][p >> 1] = ld16c(wrs, off + (p & 1) * 1024, (p >> 1) * 4096);
    SCHED0();
  };
  auto good = [&]() {
    unsigned bad = 0;
#pragma unroll
    for (int p = 0; p < 16; ++p) bad = chk(bad, ps[p & 1][p >> 1], tag);
    return __all(bad == 0);
  };
  request();
  tr_all(fd[0]); tr_all(fd[1]);
  SCHED0();
#pragma unroll 1
  for (unsigned n = 0; n < SPIN_CAP && !good(); ++n) { NAP(4); request(); }
  unsigned hp[2];
#pragma unroll
  for (int j = 0; j < 2; ++j) {
    float hv[2];
#pragma unroll
    for (int z = 0; z < 2; ++z) {
      const float gb = rbf((f_of(ps[j][0][2 * z]) + f_of(ps[j][2][2 * z])) + (f_of(ps[j][4][2 * z]) + f_of(ps[j][6][2 * z])));
      const float ub = rbf((f_of(ps[j][1][2 * z]) + f_of(ps[j][3][2 * z])) + (f_of(ps[j][5][2 * z]) + f_of(ps[j][7][2 * z])));
      hv[z] = rbf(gb / (1.0f + __expf(-gb))) * ub;
    }
    hp[j] = pack2(hv[0], hv[1]);
  }
#pragma unroll
  for (int j = 0; j < 2; ++j)
    *(unsigned*)(smem + L_HF + (lane & 7) * 128 + (((lane >> 4) & 1) * 4 + 2 * l + j) * 16 + ((lane >> 3) & 1) * 4 + (lane >> 5) * 8) = hp[j];
  lds_post(smem, F_H + l, 1);
}

struct Stager {
  rsrc_t xrs, qrs;
  LAS char* lds;
  unsigned va, vb;
  int arow, pa, pb;
  __device__ __forceinline__ Stager(char* smem, int l, int lane, int tm, int tn, const u16* x, const u16* w) {
    asm volatile("" : "+v"(lane));
    const int kr = lane >> 3;
    xrs = mk_rsrc(x, S_TOK * DM * 2); qrs = mk_rsrc(w, DM * NQKV * 2);
    lds = (LAS char*)smem + l * 1024;
    va = (unsigned)(kr + 8 * l) * 2048u + (((lane & 7) ^ (kr >> 1) ^ (l << 2)) << 4);
    vb = (unsigned)(kr + 8 * l) * 6144u + tn * 128 + (((lane & 7) ^ (((kr >> 1) & 1) << 1) ^ (l << 2)) << 4);
    arow = tm * 128 - 16;
    k_order(tm, tn, pa, pb);
  }
};
#define DM1(l, o) "s_add_u32 m0, %2, " #l "\n\ts_add_u32 %0, %5, " #o "\n\tbuffer_load_dwordx4 %3, %4, %0 offen lds\n\t"
#define DMA_(str, v_, rs_, so_, s0_) asm volatile("s_mov_b32 %1, m0\n\t" str "s_mov_b32 m0, %1" : "=&s"(tmp), "=&s"(keep) : "s"(base), "v"(v_), "s"(rs_), "s"(so_), "s"(s0_) : "scc", "memory")
__device__ __forceinline__ void stage_asm(const Stager& g, int t, int so) {
  const int kt = k_tile(t, g.pa, g.pb);
  const unsigned base = (unsigned)(size_t)g.lds + so;
  unsigned keep, tmp;
  DMA_("s_mov_b32 m0, %2\n\ts_nop 0\n\tbuffer_load_dwordx4 %3, %4, %6 offen lds\n\t" DM1(2048, 32768) DM1(4096, 65536) DM1(6144, 98304) DM1(8192, 131072)
       DM1(10240, 163840) DM1(12288, 196608) DM1(14336, 229376) DM1(16384, 262144), g.va, g.xrs, g.arow * 2048 + kt * 128, max(g.arow, 0) * 2048 + kt * 128);
  DMA_(DM1(18432, 0) DM1(20480, 98304) DM1(22528, 196608) DM1(24576, 294912) DM1(26624, 2048) DM1(28672, 100352) DM1(30720, 198656) DM1(32768, 296960)
       DM1(34816, 4096) DM1(36864, 102400) DM1(38912, 200704) DM1(40960, 299008), g.vb, g.qrs, kt * 64 * 6144, 0);
}
__device__ __forceinline__ int unit_of(int bid) {
  const int off = bid >> 3;
  return (off >> 2) == 3 ? off + 16 : (off >> 2) == 7 ? off - 16 : off;
}
__device__ constexpr int wo_early(int t) { return t < TW0 ? 0 : ((t - TW0) * NW < 64 - WR + 2 * LF ? (t - TW0) * NW : 64 - WR + 2 * LF); }
__device__ __forceinline__ void loader(char* smem, int l, int lane, int tm, int tn, unsigned rot2, const u16* __restrict__ x,
                                       const u16* __restrict__ w, const u16* __restrict__ wo, char* ws) {
  const Stager stage(smem, l, lane, tm, tn, x, w);
  dma_tag(smem, F_FULL + l * 4, 0);
  const rsrc_t prs = mk_rsrc(wo, DM * DM * 2);
  const int kr = lane >> 3;
  const unsigned vo = (unsigned)((kr < 4 ? kr : 12 + kr) + 4 * l) * 2048u + tn * 128 + (((lane & 7) ^ (((kr >> 1) & 1) << 1) ^ (l << 2)) << 4);
  u32x4 pc[2 * LF];
  int oth = (tn & 8) * 2 ^ 16;
  asm volatile("" : "+s"(oth));
  auto source = [&](int n) {
    const int k = n >> 1, half = k < 14 || k >= 30 ? (tn & 8) * 2 : oth;
    return (unsigned)((half + ((rot2 >> ((k < 14 ? k : k < 28 ? k - 14 : 14) & 14) * 2) & 14) + (k & 1)) * 32 + 8 * (n & 1)) * 2048;
  };
  auto request = [&](int z) {
    unsigned keep;
    if (z >= 64 - WR) asm volatile("s_nop 4\n\tbuffer_load_dwordx4 %0, %1, %2, %3 offen" : "=v"(pc[(z + 2 * LF - 64 + WR) % (2 * LF)])
                                   : "v"(vo), "s"(prs), "s"(source(z - 64 + WR + (z - 64 + WR < LF ? 0 : 28 - LF))) : "memory");
    else asm volatile("s_mov_b32 %0, m0\n\ts_add_u32 m0, %4, %5\n\ts_nop 2\n\tbuffer_load_dwordx4 %1, %2, %3 offen lds\n\ts_mov_b32 m0, %0"
                      : "=&s"(keep) : "v"(vo), "s"(prs), "s"(source(z + WR)), "s"((unsigned)(size_t)stage.lds), "i"(L_WO + (z + WR) * 2048) : "scc", "memory");
  };
  static_assert(L_WO + WR * 2048 >= 3 * SLOT && WR <= 60 && wo_early(NBAR - 4) >= 64 - WR, "beyond the ring, and early");
#pragma unroll
  for (int t = -1; t < NBAR - 2; ++t) {
    if (t >= 0) stage_asm(stage, t + 3, ((t + 3) % 3) * SLOT);
    SCHED0();
#pragma unroll
    for (int i = wo_early(t); i < wo_early(t + 1); ++i) request(i);
    WAITV(21 + (t < 0) + wo_early(t + 1) - wo_early(t - 1));
    BAR();
  }
  dma_tag(smem, F_FULL + l * 4, 15);
  stage_asm(stage, 15, 0);
  dma_tag(smem, F_FULL + l * 4, 16);
  SCHED0();
  {
    const unsigned d0 = lds_addr(smem + L_WO + l * 1024 + lane * 16), d1 = d0 + 65536;
#pragma unroll
    for (int i = wo_early(NBAR - 2); i < 64 - WR + 2 * LF; ++i) request(i);
    wait_bytes<0x80808080u>(smem, F_FREE, 16);
    WAITV(0);
#pragma unroll
    for (int i = 0; i < 2 * LF; ++i) {
      const int n = i < LF ? i : i - LF + 28;
      asm volatile("ds_write_b128 %1, %0 offset:%2" : "+v"(pc[i]) : "v"(n < 32 ? d0 : d1), "i"((n & 31) * 2048) : "memory");
    }
    if (l == 0) {
      const unsigned v1 = (vo + 4 * 2048) ^ 64;
      wait_bytes<0x80808080u>(smem, F_OL, 1);
#pragma unroll
      for (int y = 0; y < WR - 2 * LF; ++y) {
        const int n = y < 28 - LF ? LF + y : y + 2 * LF;
        unsigned keep;
        asm volatile("s_mov_b32 %0, m0\n\ts_add_u32 m0, %4, %5\n\ts_nop 0\n\tbuffer_load_dwordx4 %1, %2, %3 offen lds\n\ts_add_u32 m0, m0, 1024\n\ts_nop 0\n\tbuffer_load_dwordx4 %6, %2, %3 offen lds\n\ts_mov_b32 m0, %0"
                     : "=&s"(keep) : "v"(vo), "s"(prs), "s"(source(n)), "s"((unsigned)(size_t)stage.lds), "i"(L_WO + n * 2048), "v"(v1) : "scc", "memory");
      }
      dma_tag(smem, F_WO, 1);
    } else lds_post(smem, F_WO + 4, 1);
  }
  unsigned tp = 0;
  __builtin_amdgcn_s_setprio(1);
  if (l) await_attention(smem, lane, tm, tn, ws, uni(lds_word(smem, F_TAG)), tp);
}

__device__ __forceinline__ void updates(char* smem, int w, int lane, int tm, int tn, char* ws) {
  const rsrc_t wrs = mk_rsrc(ws, WS_O);
  const unsigned tag = uni(lds_word(smem, F_TAG));
  const int g = lane >> 4, li = lane & 15;
  wait_bytes<0x80u>(smem, F_SELOK, 1);
  const unsigned selkey = *(const unsigned*)(smem + F_SEL + (lane & 7) * 4);
  unsigned mine = (unsigned)__ballot(((2047u - (selkey & 0xffffu)) >> 7) == (unsigned)tm) & 0xffu;
#pragma unroll
  for (int i = 0; i < 3; ++i) mine &= i < w ? mine - 1 : ~0u;
  if (mine != 0 && w < 3) {
    wait_bytes<0x80u>(smem, F_GO, 1);
    NAP(YD);
  }
  for (; mine != 0; mine &= mine - 1, mine &= mine - 1, mine &= mine - 1, mine &= mine - 1) {
    const int c = __builtin_ctz(mine);
    const unsigned yoff = WS_Y + (unsigned)((g * 14 * 8 + c) * DM + tn * 64 + li * 4) * 4u;
    u32x4 yv[14];
    const unsigned key = (unsigned)__builtin_amdgcn_readlane((int)selkey, c), kb = key >> 16;
#pragma unroll 1
    for (unsigned n = 0; n < SPIN_CAP; ++n) {
      unsigned bad = 0;
#pragma unroll
      for (int j = 0; j < 14; ++j) yv[j] = ld16c(wrs, yoff, j * (8 * DM * 4));
#pragma unroll
      for (int j = 0; j < 14; ++j) bad = chk(bad, yv[j], tag);
      if (__all(bad == 0)) break;
      NAP(8);
    }
    float y[4] = {0.f, 0.f, 0.f, 0.f};
#pragma unroll
    for (int j = 0; j < 14; ++j) { y[0] += lo2f(yv[j][0]); y[1] += hi2f(yv[j][0]); y[2] += lo2f(yv[j][2]); y[3] += hi2f(yv[j][2]); }
    const float r = __uint_as_float(((kb & 0x8000u) ? (kb & 0x7fffu) : (~kb & 0xffffu)) << 16);
    const float rs = rbf(1.0f / (1.0f + __expf(-r)));
#pragma unroll
    for (int e = 0; e < 4; ++e) y[e] = rbf(rbf(xsum16_32(y[e])) * rs);
    *(f32x4*)(smem + L_UP + c * 256 + li * 16) = f32x4{y[0], y[1], y[2], y[3]};
  }
  lds_post(smem, F_UP + w, 1);
  STAMP(7 + (w == 0 || w == 3 ? -5 : 0));
}

__device__ __forceinline__ void ld8k(u32x4 (&d)[8], rsrc_t rs, unsigned off) {
#define L1K(i, so, o) "buffer_load_dwordx4 %" #i ", %8, %9, " so " offen offset:" #o " sc1\n\t"
  asm volatile("s_nop 4\n\t" L1K(0, "0", 0) L1K(1, "0", 1024) L1K(2, "0", 2048) L1K(3, "0", 3072) L1K(4, "%10", 0) L1K(5, "%10", 1024) L1K(6, "%10", 2048) L1K(7, "%10", 3072)
               : "=&v"(d[0]), "=&v"(d[1]), "=&v"(d[2]), "=&v"(d[3]), "=&v"(d[4]), "=&v"(d[5]), "=&v"(d[6]), "=&v"(d[7]) : "v"(off), "s"(rs), "s"(4096) : "memory");
  SCHED0();
}
template <int V> struct IC { static constexpr int v = V; };
__device__ __forceinline__ void side_wave(char* smem, int F, int lane, int bid, int tm, int tn, const u16* __restrict__ x,
                                          const u16* __restrict__ wgu, const u16* __restrict__ wd, char* ws) {
  const rsrc_t wrs = mk_rsrc(ws, WS_O), xrs = mk_rsrc(x, S_TOK * DM * 2);
  const int u = unit_of(bid), cg = (bid & 7) * 7 + ((u >> 2) % 7), q = u & 3;
  const int g = lane >> 4, li = lane & 15;
  u32x4 fw[4][8];
  const rsrc_t grs = mk_rsrc(wgu, u < 28 ? DM * FF * 2 : 0);
  const int cu = (bid & 7) * 7 + (((u >> 2) + F) % 7);
  const unsigned fo = (unsigned)(q * 256 + (li >> 3) * 128 + 8 * g) * (FF * 2) + cu * 128 + (li & 7) * 16;
#pragma unroll
  for (int s = 0; s < 4; ++s) asm volatile("" : "=v"(fw[s][0]), "=v"(fw[s][1]), "=v"(fw[s][2]), "=v"(fw[s][3]), "=v"(fw[s][4]), "=v"(fw[s][5]), "=v"(fw[s][6]), "=v"(fw[s][7]));
  const int csb = WS_CS + ((tn * 2 + F) * 16 + (tm & 1) * 8) * 1024;
  u32x4 cs[8], rec[2];
  unsigned ca = csb + min(lane, 47) * 16, cb = ca + 4096, ra = WS_R + (unsigned)max(tm * 128 - 16 + F * 72 + lane, 0) * 16, rb = WS_R + (unsigned)max(tm * 128 - 8 + F * 72 + lane, 0) * 16;
  asm volatile("" : "+v"(ca), "+v"(cb), "+v"(ra), "+v"(rb));
  unsigned due = (0x3012 >> ((tn & 3) * 4)) & 15, iss = 0;
#define FW(n) fw[(n) >> 3][(n) & 7]
#define LD(i) "s_mov_b32 %[t], (((%[n0]+" #i ")>>3)*32+((%[n0]+" #i ")&7))*7168\n\tbuffer_load_dwordx4 %" #i ", %[fo], %[rs], %[t] offen nt\n\t"
#define LD8 LD(0) LD(1) LD(2) LD(3) LD(4) LD(5) LD(6) LD(7)
#define R8(n) "+v"(FW(n)), "+v"(FW(n + 1)), "+v"(FW(n + 2)), "+v"(FW(n + 3)), "+v"(FW(n + 4)), "+v"(FW(n + 5)), "+v"(FW(n + 6)), "+v"(FW(n + 7))
#define SITE(lds_, ...) asm volatile("s_cmp_lg_u32 %[iss], %[k]\n\ts_cbranch_scc1 1f\n\ts_cmp_le_u32 %[nb], %[due]\n\ts_cbranch_scc1 1f\n\ts_sleep %[dly]\n\t" lds_ \
    "s_add_u32 %[iss], %[iss], 1\n\ts_add_u32 %[due], %[due], 4\n\t1:" : __VA_ARGS__, [iss] "+s"(iss), [due] "+s"(due), [t] "=&s"(t) \
    : [fo] "v"(fo), [rs] "s"(grs), [nb] "s"(nb), [k] "i"(K), [n0] "i"(n0), [dly] "i"(D), [ca] "v"(ca), [cb] "v"(cb), [ra] "v"(ra), [rb] "v"(rb), [ws] "s"(wrs) : "scc", "memory")
#define C1K(i, a, o) "buffer_load_dwordx4 %" #i ", %[" #a "], %[ws], 0 offen offset:" #o " sc1\n\t"
#define G2N C1K(0, ca, 0) C1K(1, ca, 1024) C1K(2, ca, 2048) C1K(3, ca, 3072) C1K(4, cb, 0) C1K(5, cb, 1024) C1K(6, cb, 2048) C1K(7, cb, 3072) C1K(8, ra, 0) C1K(9, rb, 0)
#define G2 C1K(8, ca, 0) C1K(9, ca, 1024) C1K(10, ca, 2048) C1K(11, ca, 3072) C1K(12, cb, 0) C1K(13, cb, 1024) C1K(14, cb, 2048) C1K(15, cb, 3072) C1K(16, ra, 0) C1K(17, rb, 0)
  auto site = [&](auto kk, auto dd, unsigned nb) {
    constexpr int K = decltype(kk)::v, D = decltype(dd)::v, n0 = K == 0 ? 0 : K == 3 ? 16 : 8 + K * 8;
    unsigned t;
    SCHED0();
    if constexpr (K == 1 && LC == 3) SITE(G2N, "+v"(cs[0]), "+v"(cs[1]), "+v"(cs[2]), "+v"(cs[3]), "+v"(cs[4]), "+v"(cs[5]), "+v"(cs[6]), "+v"(cs[7]), "+v"(rec[0]), "+v"(rec[1]));
    else if constexpr (K == 3) SITE(LD8, R8(16));
    else if constexpr (K == 1) SITE(G2 LD8, R8(n0), "+v"(cs[0]), "+v"(cs[1]), "+v"(cs[2]), "+v"(cs[3]), "+v"(cs[4]), "+v"(cs[5]), "+v"(cs[6]), "+v"(cs[7]), "+v"(rec[0]), "+v"(rec[1]));
    else if constexpr (K == 2) SITE(LD8, R8(n0));
    else SITE(LD8 LD(8) LD(9) LD(10) LD(11) LD(12) LD(13) LD(14) LD(15), R8(n0), R8(n0 + 8));
    SCHED0();
  };
#define CH(k, d, n) site(IC<k>{}, IC<d>{}, n)
#define WAITD(K, d) asm volatile("s_waitcnt vmcnt(%1)\n\ts_cmp_lg_u32 %0, %3\n\ts_cbranch_scc1 1f\n\ts_waitcnt vmcnt(%2)\n\t1:" : "+s"(iss) : "i"((K) + 8), "i"(K), "i"(d) : "scc", "memory")
  unsigned tag;
  {
    const int qq = (lane >> 2) & 3, hk = (qq >> 1) | ((g & 1) << 1);
    const char* pb = smem + XB + (8 * g + qq) * 128 + 8 * (lane & 3);
    const bf16x8 ones = {0x3f80, 0x3f80, 0x3f80, 0x3f80, 0x3f80, 0x3f80, 0x3f80, 0x3f80};
    f32x4 a[6];
#pragma unroll
    for (int t = 0; t < 2; ++t) {
      SCHED0(); BAR(); SCHED0();
      __builtin_amdgcn_s_setprio(3);
#pragma unroll
      for (int c = 0; c < 6; ++c) {
        const char* p = pb + t * SLOT + (c >> 1) * 8192 + (((2 * F + (c & 1)) ^ hk) << 5);
        a[c] = MFMA16(ones, lds_tr2(p), t ? a[c] : (f32x4{0, 0, 0, 0}));
        a[c] = MFMA16(ones, lds_tr2(p + 4096), a[c]);
      }
      asm volatile("" : "+v"(a[0]), "+v"(a[1]), "+v"(a[2]), "+v"(a[3]), "+v"(a[4]), "+v"(a[5]));
      if (t == 0) { tag = uni(lds_word(smem, F_TAG)); CH(0, CD, 1); }
    }
    const unsigned off = csb + (tm >> 1) * 1024 + lane * 8;
    st8c(wrs, off, __float_as_uint(g == 0 ? a[0][0] : g == 1 ? a[1][0] : g == 2 ? a[2][0] : a[3][0]), tag);
    st8c(wrs, lane < 32 ? off + 512 : 0x7fffffffu, __float_as_uint(g == 0 ? a[4][0] : a[5][0]), tag);
    __builtin_amdgcn_s_setprio(0);
    CH(0, CD, 2);
  }
#define SST(i)
  int nb = 2;
  tick(nb);
  const unsigned xs_off = WS_S + (unsigned)(((bid & 7) * 4 + q) * 6144 + lane * 16);
  u32x4 tc;
  uint2 tq[2];
  unsigned ta[3], tv, todo = 1;
  auto gather2 = [&]() {
    asm volatile(G2N : "=&v"(cs[0]), "=&v"(cs[1]), "=&v"(cs[2]), "=&v"(cs[3]), "=&v"(cs[4]), "=&v"(cs[5]), "=&v"(cs[6]), "=&v"(cs[7]), "=&v"(rec[0]), "=&v"(rec[1])
                 : [ca] "v"(ca), [cb] "v"(cb), [ra] "v"(ra), [rb] "v"(rb), [ws] "s"(wrs) : "memory");
    SCHED0();
  };
  auto prepare = [&]() {
#pragma unroll 1
    for (unsigned n = 0; n < SPIN_CAP; ++n) {
      unsigned bad = chk(chk(0u, rec[0], tag), rec[1], tag);
#pragma unroll
      for (int j = 0; j < 8; ++j) bad = chk(bad, cs[j], tag);
      if (__all(bad == 0)) break;
      NAP(8);
      gather2();
      tick(nb); tick(nb); WAITV(0); asm volatile("" : "+v"(rec[0]), "+v"(rec[1])); PIN8(cs);
    }
    float csum[2] = {f_of(cs[0][0]), f_of(cs[0][2])};
#pragma unroll
    for (int r = 1; r < 8; ++r) { csum[0] = opq(csum[0] + f_of(cs[r][0])); csum[1] = opq(csum[1] + f_of(cs[r][2])); }
    const float h0 = rbf(csum[0]), d0 = opq(csum[0] - h0), c0 = rbf(d0), h1 = rbf(csum[1]), d1 = opq(csum[1] - h1), c1 = rbf(d1);
    tc = u32x4{pack2(h0, c0), pack2(opq(d0 - c0), 0.f), pack2(h1, c1), pack2(opq(d1 - c1), 0.f)};
    asm volatile("" : "+v"(tc));
#pragma unroll
    for (int j = 0; j < 2; ++j) {
      const unsigned nm = (rec[j][0] & 0xffffu) ^ 0x8000u;
      tq[j] = make_uint2(nm | (nm << 16), __float_as_uint(__builtin_amdgcn_rcpf(f_of(rec[j][2]))));
      asm volatile("" : "+v"(tq[j].x), "+v"(tq[j].y));
    }
    ta[0] = lds_addr(smem + L_CSF + (min(lane >> 4, 2) * 64 + F * 32 + (lane < 48 ? li : 15) * 2) * 8);
    ta[1] = lds_addr(smem + L_QST + (F * 72 + lane) * 8);
    ta[2] = lds_addr(smem + (lane < 8 ? F_SEL + lane * 4 : lane == 8 ? F_SELOK : F_ST + F * 4));
    asm volatile("" : "+v"(ta[0]), "+v"(ta[1]), "+v"(ta[2]));
    SCHED0();
  };
  auto tables = [&](unsigned now) {
    asm volatile("s_and_b32 %0, %0, %8\n\ts_cbranch_scc0 1f\n\tds_write_b128 %1, %2\n\tds_write2_b64 %3, %4, %5 offset1:8\n\ts_mov_b64 exec, 0x3ff\n\tds_write_b32 %6, %7\n\ts_mov_b64 exec, -1\n\t1:"
                 : "+s"(now) : "v"(ta[0]), "v"(tc), "v"(ta[1]), "v"(tq[0]), "v"(tq[1]), "v"(ta[2]), "v"(tv), "s"(todo) : "scc", "memory");
    todo &= ~now;
  };
  auto slice = [&]() {
    if (nb < NBAR) tick(nb);
    else { const u32x2 fl = *(const volatile LAS u32x2*)(LAS const char*)(smem + F_FULL); tables(uni(min(fl[0], fl[1])) >> 4); SCHED0(); }
  };
  if (u >= 28) {
    u32x4 kd[4];
#pragma unroll 1
    for (unsigned n = 0; n < SPIN_CAP; ++n) {
#define L1K(i, o) "buffer_load_dwordx4 %" #i ", %4, %5, 0 offen offset:" #o " sc1\n\t"
      asm volatile("s_nop 4\n\t" L1K(0, 0) L1K(1, 1024) L1K(2, 2048) L1K(3, 3072)
                   : "=&v"(kd[0]), "=&v"(kd[1]), "=&v"(kd[2]), "=&v"(kd[3]) : "v"(WS_K + F * 4096 + lane * 16), "s"(wrs) : "memory");
#undef L1K
      tick(nb);
      asm volatile("s_waitcnt vmcnt(0)" : "+v"(kd[0]), "+v"(kd[1]), "+v"(kd[2]), "+v"(kd[3]) :: "memory");
      if (__all(chk(chk(chk(chk(0u, kd[0], tag), kd[1], tag), kd[2], tag), kd[3], tag) == 0)) break;
      NAP(4);
    }
    SST(8);
    unsigned m1 = 0, m2 = 0, m3 = 0, m4 = 0;
#pragma unroll
    for (int j = 0; j < 4; ++j) {
      kd[j] = u32x4{(kd[j][0] << 16) | (15 - 4 * j), (kd[j][0] & 0xffff0000u) | (14 - 4 * j), (kd[j][2] << 16) | (13 - 4 * j), (kd[j][2] & 0xffff0000u) | (12 - 4 * j)};
#pragma unroll
      for (int i = 0; i < 4; ++i) {
        const unsigned v = kd[j][i];
        m4 = med3(m3, m4, v); m3 = med3(m2, m3, v); m2 = med3(m1, m2, v); m1 = max(m1, v);
      }
    }
    const unsigned lb = ((63 - lane) << 5) | (F ? 0 : 16);
    m1 |= lb; m2 |= lb; m3 |= lb; m4 |= lb;
    asm volatile("" : "+v"(m1), "+v"(m2), "+v"(m3), "+v"(m4));
    tick(nb);
    unsigned best = 0;
#pragma unroll
    for (int r = 0; r < 8; ++r) {
      const unsigned wm = wave_max_u32(m1);
      best = (lane & 7) == r ? wm : best;
      const bool won = m1 == wm;
      m1 = won ? m2 : m1; m2 = won ? m3 : m2; m3 = won ? m4 : m3; m4 = won ? 0u : m4;
      if (r == 3) { asm volatile("" : "+v"(m1), "+v"(m2), "+v"(m3), "+v"(m4), "+v"(best)); tick(nb); }
    }
    if (__any(m1 == 0)) {
      unsigned wm = 0;
#pragma unroll 1
      for (int r = 0; r < 8; ++r) {
        if (r & 1) tick(nb);
        unsigned m = 0;
#pragma unroll
        for (int j = 0; j < 4; ++j)
#pragma unroll
          for (int i = 0; i < 4; ++i) m = max(m, (kd[j][i] | lb) - wm);
        wm += wave_max_u32(m);
        best = (lane & 7) == r ? wm : best;
      }
    }
    *(volatile LAS unsigned*)(LAS char*)(smem + F_CND + (F ? 60 - (lane & 7) * 4 : (lane & 7) * 4)) = best + 1;
    unsigned d = 0;
    if (TCH & 1) {
      const unsigned ct = 2047u - (best & 0xffffu);
      asm volatile("buffer_load_dword %0, %1, %2, 0 offen\n\tbuffer_load_dword %0, %3, %4, 0 offen sc1" : "=&v"(d) : "v"(lane < 32 ? ct * 2048u + q * 512 + (lane >> 3) * 128 : 0x7fffffffu), "s"(xrs), "v"(lane < 8 ? WS_R + ct * 16 : 0x7fffffffu), "s"(wrs) : "memory");
    }
    SST(9);
    gather2();
    unsigned ka, kb;
#pragma unroll 1
    for (unsigned n = 0; n < SPIN_CAP; ++n) {
      tick(nb);
      ka = *(const volatile LAS unsigned*)(LAS const char*)(smem + F_CND + (lane & 7) * 4);
      kb = *(const volatile LAS unsigned*)(LAS const char*)(smem + F_CND + 32 + (lane & 7) * 4);
      if (__all(min(ka, kb) != 0)) break;
      NAP(1);
    }
    const unsigned selkey = max(ka, kb) - 1, tok = 2047u - (selkey & 0xffffu);
    u32x4 xf[2], trec;
#pragma unroll 1
    for (unsigned n = 0; n < SPIN_CAP; ++n) {
      asm volatile("buffer_load_dwordx4 %0, %3, %4, 0 offen\n\tbuffer_load_dwordx4 %1, %3, %4, 0 offen offset:64\n\tbuffer_load_dwordx4 %2, %5, %6, 0 offen sc1"
                   : "=&v"(xf[0]), "=&v"(xf[1]), "=&v"(trec) : "v"(tok * 2048u + q * 512 + (li >> 3) * 256 + g * 16 + F * 128), "s"(xrs), "v"(WS_R + tok * 16), "s"(wrs) : "memory");
      if (!KQ || n) tick(nb);
      asm volatile("s_waitcnt vmcnt(0)" : "+v"(xf[0]), "+v"(xf[1]), "+v"(trec), "+v"(rec[0]), "+v"(rec[1]), "+v"(d) :: "memory"); PIN8(cs);
      if (__all(chk(0u, trec, tag) == 0)) break;
      NAP(1);
    }
    const float mb = lo2f(trec[0]), rd = 1.0f / f_of(trec[2]);
#pragma unroll
    for (int s = 0; s < 2; ++s)
#pragma unroll
      for (int j = 0; j < 4; ++j) xf[s][j] = ln2s(xf[s][j], mb, rd);
    st16c(wrs, xs_off + F * 3072, u32x4{xf[0][0], xf[0][1], xf[0][2], tag});
    st16c(wrs, xs_off + F * 3072 + 1024, u32x4{xf[1][0], xf[1][1], xf[1][2], tag});
    st16c(wrs, xs_off + F * 3072 + 2048, u32x4{xf[0][3], xf[1][3], selkey, tag});
    tv = lane < 8 ? selkey : 1u;
    SST(2);
    tick(nb);
    if (TCH & 2) {
      asm volatile("s_waitcnt vmcnt(0)\n\tbuffer_load_dword %0, %1, %2, 0 offen sc1" : "=&v"(d) : "v"(lane < 24 ? xs_off + F * 3072 + lane * 112 : 0x7fffffffu), "s"(wrs) : "memory");
    }
    prepare();
    asm volatile("s_waitcnt vmcnt(0)" : "+v"(d) :: "memory");
    while (nb < NBAR) tick(nb);
    __builtin_amdgcn_s_setprio(1);
    wait_full(smem, F_FULL, 16);
    tables(1);
  } else {
    u32x4 xs[6];
    auto ask = [&]() {
#define X1K(i, o) "buffer_load_dwordx4 %" #i ", %6, %7, 0 offen offset:" #o " sc1\n\t"
#define X2K(i, o) "buffer_load_dwordx4 %" #i ", %6, %7, %8 offen offset:" #o " sc1\n\t"
      asm volatile(X1K(0, 0) X1K(1, 1024) X1K(2, 2048) X1K(3, 3072) X2K(4, 0) X2K(5, 1024)
                   : "=&v"(xs[0]), "=&v"(xs[1]), "=&v"(xs[2]), "=&v"(xs[3]), "=&v"(xs[4]), "=&v"(xs[5]) : "v"(xs_off), "s"(wrs), "s"(4096) : "memory");
      SCHED0();
    };
    auto good = [&]() {
      unsigned bad = xs[0][3] ^ tag;
#pragma unroll
      for (int j = 1; j < 6; ++j) bad = __builtin_amdgcn_bitop3_b32(bad, xs[j][3], tag, 0xF6);
      return __all(bad == 0);
    };
#define PIN6(a) asm volatile("" : "+v"(a[0]), "+v"(a[1]), "+v"(a[2]), "+v"(a[3]), "+v"(a[4]), "+v"(a[5]))
#define WAITS(snap, n) asm volatile("s_waitcnt vmcnt(%2)\n\ts_cmp_lg_u32 %0, %1\n\ts_cbranch_scc1 1f\n\ts_waitcnt vmcnt(0)\n\t1:" : "+s"(iss) : "s"(snap), "i"(n) : "scc", "memory")
    asm volatile("" : "=v"(cs[0]), "=v"(cs[1]), "=v"(cs[2]), "=v"(cs[3]), "=v"(cs[4]), "=v"(cs[5]), "=v"(cs[6]), "=v"(cs[7]), "=v"(rec[0]), "=v"(rec[1]));
    CH(0, 8 - HN, 3);
    tick(nb);
    CH(0, 8 - HN, 99);
    tick(nb);
    CH(1, 8 - HN, nb);
    if (MG) CH(2, 0, 99);
    tick(nb);
    WAITS(1, MG ? 26 : LC == 3 ? 10 : 18); PIN8(fw[0]); PIN8(fw[1]);
    tr_all(fw[0]);
    CH(1, 3, nb);
    if (MG) CH(2, 0, 99);
    tick(nb);
    tr_all(fw[1]);
    CH(1, 3, nb);
    if (MG) CH(2, 0, 99);
    tick(nb);
    CH(1, 8 - HN, 99);
    if (MG) CH(2, 0, 99);
    tick(nb);
    if (LC == 0) CH(2, 8 - HN, nb);
    tick(nb);
    WAITS(2, 8);
    asm volatile("" : "+v"(rec[0]), "+v"(rec[1])); PIN8(cs); if (LC < 3) PIN8(fw[2]);
    if (!SD) ask();
    prepare();
    if (SD) ask();
    unsigned snap = iss;
    asm volatile("" : "+s"(snap));
    if (LC == 0) CH(2, 0, nb);
    tick(nb);
    WAITS(snap, 8); PIN6(xs);
    SST(1);
    if (!good()) {
      if (LC == 0) CH(2, 0, 99);
#pragma unroll 1
      for (unsigned n = 0; n < SPIN_CAP; ++n) {
        ask(); tick(nb); WAITV(0); PIN6(xs);
        if (good()) break;
        NAP(4);
      }
    }
    SST(2);
    tv = lane < 8 ? xs[2][2] : 1u;
    xs[0][3] = xs[2][0]; xs[1][3] = xs[2][1]; xs[3][3] = xs[5][0]; xs[4][3] = xs[5][1];
    PIN6(xs); asm volatile("" : "+v"(tv));
    if (nb == NBAR) { __builtin_amdgcn_s_setprio(1); wait_full(smem, F_FULL, 16); tables(1); }
    f32x4 acc[8];
    auto mma = [&](int s) {
      SCHED0(); __builtin_amdgcn_s_setprio(3); SCHED0();
#pragma unroll
      for (int i = 0; i < 8; ++i) acc[i] = MFMA16(__builtin_bit_cast(bf16x8, fw[s][i]), __builtin_bit_cast(bf16x8, xs[s + (s >> 1)]), s ? acc[i] : (f32x4{0, 0, 0, 0}));
      SCHED0(); __builtin_amdgcn_s_setprio(0); SCHED0();
      PIN8(acc);
    };
    mma(0);
    if (LC < 3) tr_all(fw[2]);
    if (LC == 0) CH(2, 3, 99);
    slice();
    mma(1);
    if (LC < 2) mma(2);
    if (LC == 1) { PIN8(fw[3]); tr_all(fw[3]); }
    slice();
    if (LC == 0) { WAITV(0); PIN8(fw[3]); tr_all(fw[3]); }
    if (LC < 2) mma(3);
    if (LC == 2) mma(2);
    auto finish = [&]() {
#pragma unroll
      for (int r = 0; r < 4; ++r) {
        float v[2];
#pragma unroll
        for (int z = 0; z < 2; ++z) {
          auto a = __builtin_amdgcn_permlane32_swap(__float_as_uint(acc[z][r]), __float_as_uint(acc[z + 4][r]), false, false);
          auto b = __builtin_amdgcn_permlane32_swap(__float_as_uint(acc[z + 2][r]), __float_as_uint(acc[z + 6][r]), false, false);
          v[z] = f_of(li < 8 ? a[0] : b[1]) + dpp_f<0x128>(f_of(li < 8 ? b[0] : a[1]));
        }
        st16c(wrs, WS_GU + (unsigned)(((cu * 4 + q) * 2 + F) * 4096 + r * 1024 + lane * 16), u32x4{__float_as_uint(v[0]), tag, __float_as_uint(v[1]), tag});
      }
      SCHED0();
      SST(3);
    };
    if (LC < 2 && EP == 1) finish();
    while (nb < NBAR) tick(nb);
    if (LC < 2 && EP == 0) { slice(); finish(); }
    __builtin_amdgcn_s_setprio(1);
    u32x4 fd[2][8];
    {
      const rsrc_t drs = mk_rsrc(wd, DM * FF * 2);
      const unsigned dof = (unsigned)(cg * 64 + 8 * g) * (DM * 2) + q * 512 + F * 256 + li * 16;
      wait_full(smem, F_FULL, 15);
      if (LC == 3) { iss = 3; CH(3, 0, 99); iss = 2; }
      if (LC >= 2) CH(2, 0, 99);
#pragma unroll
      for (int s = 0; s < 2; ++s)
#pragma unroll
        for (int j = 0; j < 8; ++j) fd[s][j] = __builtin_amdgcn_raw_buffer_load_b128(drs, dof + (j & 1) * (DM * 2), (s * 32 + (j & 6)) * (DM * 2), 2);
      SCHED0();
    }
    wait_full(smem, F_FULL, 16);
    tables(1);
    SST(4);
    if (LC == 3) { WAITV(24); PIN8(fw[2]); tr_all(fw[2]); mma(2); }
    if (LC >= 2) { WAITV(16); PIN8(fw[3]); tr_all(fw[3]); mma(3); __builtin_amdgcn_s_setprio(1); finish(); }
    if (bid == 0 && F == 0 && lane == 0) __hip_atomic_store((GAS unsigned*)(ws + WS_EP), tag, RLX_AGENT);
    hidden_half(smem, lane, F, cg, ws, tag, fd);
    SST(5);
    wait_bytes<0x8080u>(smem, F_H, 1);
    const bf16x8 h0 = lds_b128(smem + L_HF + (li & 7) * 128 + g * 16), h1 = lds_b128(smem + L_HF + (li & 7) * 128 + 64 + g * 16);
#pragma unroll
    for (int i = 0; i < 8; ++i) {
      acc[i] = MFMA16(h0, __builtin_bit_cast(bf16x8, fd[0][i]), (f32x4{0, 0, 0, 0}));
      acc[i] = MFMA16(h1, __builtin_bit_cast(bf16x8, fd[1][i]), acc[i]);
    }
    const unsigned off = WS_Y + (unsigned)((cg * 8 + (g & 1) * 4) * DM + q * 256 + F * 128 + li * 8 + (g >> 1) * 4) * 4u;
#pragma unroll
    for (int r = 0; r < 4; ++r) {
      const unsigned lo = pack2(acc[0][r], acc[1][r]), hi = pack2(acc[2][r], acc[3][r]), lo2 = pack2(acc[4][r], acc[5][r]), hi2 = pack2(acc[6][r], acc[7][r]);
      st16c(wrs, off + r * (DM * 4), u32x4{g < 2 ? lo : lo2, tag, g < 2 ? hi : hi2, tag});
    }
    SST(6);
  }
  __builtin_amdgcn_s_waitcnt(0x0F70);
}

__global__ __launch_bounds__(512, 2) void fused_block(const u16* __restrict__ x, const u16* __restrict__ wqkv, const u16* __restrict__ wo,
                                                      const u16* __restrict__ wr, const u16* __restrict__ wg, const u16* __restrict__ wu,
                                                      const u16* __restrict__ wd, char* ws, u16* __restrict__ out) {
  __shared__ __attribute__((aligned(128))) char smem[L_SIZE];
  unsigned long long pc;
  unsigned first;
  asm volatile("v_cmp_gt_u32 vcc, 24, %2\n\ts_getpc_b64 %0\n\ts_cbranch_vccz 1f\n\ts_mov_b64 exec, vcc\n\tv_lshlrev_b32 %1, 7, %2\n\tglobal_load_dword %1, %1, %0\n\ts_mov_b64 exec, -1\n\t1:"
               : "=s"(pc), "=&v"(first) : "v"(threadIdx.x) : "vcc", "memory");
  SCHED0();
  unsigned long long t00; asm volatile("s_memrealtime %0\n\ts_waitcnt lgkmcnt(0)" : "=s"(t00) :: "memory");
  const int lane = threadIdx.x & 63;
  const int wid = __builtin_amdgcn_readfirstlane(threadIdx.x >> 6);
  const int bid = blockIdx.x, xcd = bid & 7, off = bid >> 3;
  const int tm = (xcd & 1) * 8 + (xcd & 2) * 2 + (off & 3), tn = (xcd >> 2) * 8 + (off >> 2);
  STAMP(0);
  if (wid == 7) {
    unsigned d;
    asm volatile("s_getpc_b64 %0" : "=s"(pc));
    asm volatile("buffer_load_dword %0, %1, %2, 0 offen\n\ts_waitcnt vmcnt(0)" : "=&v"(d)
                 : "v"((unsigned)(lane * 32 + off) * 128u), "s"(mk_rsrc((const void*)(pc & ~255ull), CODE_BYTES)) : "memory");
  }
  asm volatile("" : "+s"(x), "+s"(wqkv), "+s"(wr), "+s"(ws), "+s"(out), "+s"(wo), "+s"(wd), "+s"(wg), "+s"(wu));
  unsigned rot2 = ((((unsigned)(0x65432106543210ull >> (off & 3) * 8) & 0xfffffffu) + ((tn & 7) + 1) * 0x1111111u) & 0x7777777u) * 2 | (unsigned)tn << 29;
  unsigned tag = 0;
  RowRegs rr;
  EpiRegs e;
  row_request(rr, x, wr, ws, tm * 128 + tn * 8 + wid * 2, lane, wid < 4);
  rr.keep = first;
  SCHED0();
  if ((wid >> 1) == 2) {
    __builtin_amdgcn_s_setprio(2);
    const Stager stage(smem, wid - 4, lane, tm, tn, x, wqkv);
    NAP(4);
    stage_asm(stage, 0, 0); stage_asm(stage, 1, SLOT);
    WAITV(21);
    BAR();
    stage_asm(stage, 2, 2 * SLOT);
  }
  asm volatile("" ::: "memory");
  if (wid < 4) {
    __builtin_amdgcn_s_setprio(2);
    qkv_setup(smem, wid, lane, tm, tn, e);
    asm volatile("" : "+s"(rot2));
    SCHED0();
    tag = row_stats(rr, ws, tm * 128 + tn * 8 + wid * 2, lane);
    if (wid == 0) *(unsigned*)(smem + L_FLG + lane * 4) = lane * 4 == F_TAG - L_FLG ? tag : 0u;
    STAMP(10);
  }
  asm volatile("" ::: "memory");
  if (wid < 4) {
    if (wid & 1) qkv_attention<1>(smem, wid, lane, tm, tn, ws, tag, x, e);
    else qkv_attention<0>(smem, wid, lane, tm, tn, ws, tag, x, e);
    out_proj(smem, wid, lane, tm, tn, ws, tag, out, e, rot2);
    {
      unsigned t0 = (unsigned)t00, xc;
      asm volatile("s_getreg_b32 %0, hwreg(HW_REG_XCC_ID, 0, 4)" : "=s"(xc));
      const rsrc_t prs = mk_rsrc(ws + (32u << 20), 4096);
      if (wid == 0) __builtin_amdgcn_raw_buffer_store_b32((t0 << 3) | ((xc & 7)), prs, lane == 0 ? bid * 4 : 0x7fffffff, 0, 16);
      for (int n = 0; n < 100000 && (unsigned)__builtin_amdgcn_s_memrealtime() - t0 < 3200u; ++n) NAP(2);
      const u32x4 v = ld16c(prs, lane * 16, 0);
      int d = 100000; float sm = 0.f;
      for (int i = 0; i < 4; ++i) { const int z = (int)((v[i] & ~7u) - (t0 << 3)) >> 3; d = min(d, z); sm += (v[i] & 4) ? (float)z : -(float)z; }
      const int mn = 100000 - (int)wave_max_u32((unsigned)(100000 - d));
      sm = xsum16_32(sum16(sm)) * (1.0f / 128);
      t0 += mn;
      const unsigned until = 4000u + 100u * (uni(v[0]) & 7);
      for (int n = 0; n < 100000 && (unsigned)__builtin_amdgcn_s_memrealtime() - t0 < until; ++n) NAP(2);
    }
  } else {
    if (wid < 6) loader(smem, wid - 4, lane, tm, tn, rot2, x, wqkv, wo, ws);
    else side_wave(smem, wid - 6, lane, bid, tm, tn, x, wid == 6 ? wg : wu, wd, ws);
    updates(smem, (0x2130 >> ((wid - 4) * 4)) & 3, lane, tm, tn, ws);
    __builtin_amdgcn_s_waitcnt(0x0F70);
  }
}

extern "C" void kernel_launch(void* const* d_in, const int* in_sizes, int n_in, void* d_out, int out_size, void* d_ws,
                              size_t ws_size, hipStream_t stream) {
  fused_block<<<256, 512, 0, stream>>>((const u16*)d_in[0], (const u16*)d_in[1], (const u16*)d_in[2], (const u16*)d_in[4],
                                       (const u16*)d_in[5], (const u16*)d_in[6], (const u16*)d_in[7], (char*)d_ws, (u16*)d_out);
}
```

```cpp
#define GS 28
#define NB1 6
#define L1 0
#define DL 12
#define EPOS 2
#include <hip/hip_runtime.h>
#include <hip/hip_bf16.h>
#include <stdint.h>

typedef __attribute__((ext_vector_type(8))) short bf16x8;
typedef __attribute__((ext_vector_type(4))) short s16x4;
typedef __attribute__((ext_vector_type(4))) float f32x4;
typedef __attribute__((ext_vector_type(2))) float f32x2;
typedef __attribute__((ext_vector_type(2))) __bf16 bf16x2_t;
typedef __attribute__((ext_vector_type(4))) unsigned u32x4;
typedef unsigned short u16;
#define LAS __attribute__((address_space(3)))
#define GAS __attribute__((address_space(1)))
#define RLX_AGENT __ATOMIC_RELAXED, __HIP_MEMORY_SCOPE_AGENT

#define S_TOK 2048
#define DM 1024
#define NQKV 3072
#define FF 3584

typedef __attribute__((ext_vector_type(2))) unsigned u32x2;

#define WS_EP   0
#define WS_R    4096
#define WS_K    (WS_R + 32768)
#define WS_OF   (WS_K + 8192)
#define WS_CS   (WS_OF + 4096)
#define WS_GU   (WS_CS + 16 * 2 * 16 * 1024)
#define WS_Y    (WS_GU + 56 * 4 * 8192)
#define WS_S    (WS_Y + 56 * 8 * DM * 4)
#define WS_O    (8u << 20)

__device__ const unsigned k_tags[32] = {0,  1,  2,  3,  4,  5,  6,  7,  8,  9,  10, 11, 12, 13, 14, 15,
                                        16, 17, 18, 19, 20, 21, 22, 23, 24, 25, 26, 27, 28, 29, 30, 31};

__device__ __forceinline__ unsigned pack2(float a, float b) {
  f32x2 v = {a, b};
  bf16x2_t r = __builtin_convertvector(v, bf16x2_t);
  return __builtin_bit_cast(unsigned, r);
}
__device__ __forceinline__ float rbf(float f) { return __uint_as_float(pack2(f, f) << 16); }
__device__ __forceinline__ float lo2f(unsigned w) { return __uint_as_float(w << 16); }
__device__ __forceinline__ float hi2f(unsigned w) { return __uint_as_float(w & 0xffff0000u); }

__device__ __forceinline__ bf16x8 lds_b128(const char* p) { return *(const bf16x8*)p; }
__device__ __forceinline__ bf16x8 lds_tr2(const char* p) {
  s16x4 lo = __builtin_amdgcn_ds_read_tr16_b64_v4i16((LAS s16x4*)p);
  s16x4 hi = __builtin_amdgcn_ds_read_tr16_b64_v4i16((LAS s16x4*)(p + 512));
  return __builtin_shufflevector(lo, hi, 0, 1, 2, 3, 4, 5, 6, 7);
}
#define MFMA16(a, b, c) __builtin_amdgcn_mfma_f32_16x16x32_bf16(a, b, c, 0, 0, 0)
#define WAITV(n) asm volatile("s_waitcnt vmcnt(%0)" ::"i"(n) : "memory")
#define WAITL0() __builtin_amdgcn_s_waitcnt(0xC07F)
#define BAR() __builtin_amdgcn_s_barrier()
#define SCHED0() __builtin_amdgcn_sched_barrier(0)
#define NAP(n) asm volatile("s_sleep %0" ::"i"(n) : "memory")
__device__ __forceinline__ float xsum16_32(float v) {
  unsigned u = __float_as_uint(v);
  auto a = __builtin_amdgcn_permlane16_swap(u, u, false, false);
  v = __uint_as_float(a[0]) + __uint_as_float(a[1]);
  u = __float_as_uint(v);
  auto b = __builtin_amdgcn_permlane32_swap(u, u, false, false);
  return __uint_as_float(b[0]) + __uint_as_float(b[1]);
}
__device__ __forceinline__ bf16x8 pack8(const f32x4& a, const f32x4& b) {
  u32x4 r = {pack2(a[0], a[1]), pack2(a[2], a[3]), pack2(b[0], b[1]), pack2(b[2], b[3])};
  return __builtin_bit_cast(bf16x8, r);
}
__device__ __forceinline__ unsigned wave_max_u32(unsigned v) {
  v = max(v, (unsigned)__builtin_amdgcn_update_dpp(0, (int)v, 0x111, 0xf, 0xf, false));
  v = max(v, (unsigned)__builtin_amdgcn_update_dpp(0, (int)v, 0x112, 0xf, 0xf, false));
  v = max(v, (unsigned)__builtin_amdgcn_update_dpp(0, (int)v, 0x114, 0xf, 0xf, false));
  v = max(v, (unsigned)__builtin_amdgcn_update_dpp(0, (int)v, 0x118, 0xf, 0xf, false));
  v = max(v, (unsigned)__builtin_amdgcn_update_dpp(0, (int)v, 0x142, 0xa, 0xf, false));
  v = max(v, (unsigned)__builtin_amdgcn_update_dpp(0, (int)v, 0x143, 0xc, 0xf, false));
  return (unsigned)__builtin_amdgcn_readlane((int)v, 63);
}
__device__ __forceinline__ bf16x8 tr_frag(const u32x4 (&w)[8], int i) {
  const unsigned sel = (i & 1) ? 0x07060302u : 0x05040100u;
  u32x4 r = {__builtin_amdgcn_perm(w[1][i >> 1], w[0][i >> 1], sel), __builtin_amdgcn_perm(w[3][i >> 1], w[2][i >> 1], sel),
             __builtin_amdgcn_perm(w[5][i >> 1], w[4][i >> 1], sel), __builtin_amdgcn_perm(w[7][i >> 1], w[6][i >> 1], sel)};
  return __builtin_bit_cast(bf16x8, r);
}

#define XSL_ 18432
#define WSL 24576
#define XB (3 * XSL_)
#define NBAR 14
#define L_UP 0
#define L_WA 6144
#define L_W3 (L_WA + 57344)
#define L_WB (L_W3 + 16384)
#define L_OWN (L_WB + 57344)
#define L_XCH (XB + 4 * WSL)
#define L_HF (L_XCH + 6144)
#define L_CSF (L_HF + 1024)
#define L_QST (L_CSF + 1536)
#define L_FLG (L_QST + 1152)
#define L_SIZE (L_FLG + 256)
#define F_FULL (L_FLG + 0)
#define F_ST (L_FLG + 8)
#define F_WO (L_FLG + 16)
#define F_FREE (L_FLG + 32)
#define F_XCH (L_FLG + 40)
#define F_ODONE (L_FLG + 48)
#define F_SELOK (L_FLG + 52)
#define F_GO (L_FLG + 56)
#define F_OL (L_FLG + 60)
#define F_H (L_FLG + 64)
#define F_TAG (L_FLG + 68)
#define F_UP (L_FLG + 72)
#define F_SEL (L_FLG + 96)
#define F_CND (L_FLG + 128)
#define OD 7
#define SPIN_CAP (1u << 18)
#define LA 0
#define LB 0
#define L0 3
#define GW 0
#define WD 4
#define LF 0
#define DUE 0x3012
#define NW 3
#define TW0 3
#define CD 4
#define EV 4
#define PN 2
#define YD 24
#define OK 2
#define ON 8
#define HN 2
#define SD 0
#define PW 10
#define W2 1
#define LC 0
#define EP 1
#define DN 0
#define PR 8
#define MG (LC == 1)
#define XSL 10
#define KQ 1
#define TCH 3
#define TS 0
#define SX 0
#define CODE_BYTES 55424

#define RSC(i)
#define STAMP(i)
#define RST(i)

__device__ __forceinline__ unsigned lds_addr(const char* p) { return (unsigned)(size_t)(LAS const char*)p; }
__device__ __forceinline__ void lds_post(char* smem, int off, unsigned v) {
  asm volatile("s_mov_b64 exec, 1\n\tds_write_b8 %0, %1\n\ts_mov_b64 exec, -1" ::"v"(lds_addr(smem + off)), "v"(v) : "memory");
}
__device__ __forceinline__ unsigned lds_word(const char* smem, int off) { return *(const volatile LAS unsigned*)(LAS const char*)(smem + off); }
__device__ __forceinline__ u32x4 lds_word4(const char* smem, int off) { return *(const volatile LAS u32x4*)(LAS const char*)(smem + off); }
template <unsigned MASK, bool COND = false>
__device__ __forceinline__ unsigned wait_bytes(const char* smem, int off, unsigned need, unsigned skip = 0) {
  unsigned t, f;
  asm volatile(
      ".if %6\n\ts_cmp_lg_u32 %5, 0\n\ts_cbranch_scc1 2f\n\t.endif\n\t"
      "1:\n\t"
      "ds_read_b32 %1, %2\n\t"
      "s_waitcnt lgkmcnt(0)\n\t"
      "v_readfirstlane_b32 %0, %1\n\t"
      "s_or_b32 %0, %0, 0x80808080\n\t"
      "s_sub_u32 %0, %0, %3\n\t"
      "s_and_b32 %0, %0, %4\n\t"
      "s_cmp_eq_u32 %0, %4\n\t"
      "s_cbranch_scc1 2f\n\t"
      "s_sleep 1\n\t"
      "s_branch 1b\n\t"
      "2:"
      : "=&s"(t), "=&v"(f)
      : "v"(lds_addr(smem + off)), "s"(need * 0x01010101u), "s"(MASK), "s"(COND ? skip : MASK), "i"((int)COND)
      : "scc", "memory");
  return f;
}
__device__ __forceinline__ void wait_full(const char* smem, int off, unsigned need, unsigned cur = 0) {
  unsigned a, b;
  asm volatile(
      "s_cmp_ge_u32 %0, %4\n\t"
      "s_cbranch_scc1 3f\n\t"
      "s_branch 2f\n\t"
      "1:\n\t"
      "s_sleep 1\n\t"
      "2:\n\t"
      "ds_read_b32 %1, %3\n\t"
      "ds_read_b32 %2, %3 offset:4\n\t"
      "s_waitcnt lgkmcnt(0)\n\t"
      "v_min_u32 %1, %1, %2\n\t"
      "s_nop 0\n\t"
      "v_readfirstlane_b32 %0, %1\n\t"
      "s_cmp_lt_u32 %0, %4\n\t"
      "s_cbranch_scc1 1b\n\t"
      "3:"
      : "+s"(cur), "=&v"(a), "=&v"(b)
      : "v"(lds_addr(smem + off)), "s"(need)
      : "scc", "memory");
}
__device__ __forceinline__ unsigned uni(unsigned v) { return (unsigned)__builtin_amdgcn_readfirstlane((int)v); }
__device__ __forceinline__ void dma_tag(char* smem, int off, int n) {
  unsigned keep;
  asm volatile("s_nop 2\n\ts_mov_b32 %0, m0\n\ts_mov_b32 m0, %1\n\ts_mov_b64 exec, 1\n\tglobal_load_lds_dword %2, %3\n\ts_mov_b64 exec, -1\n\ts_mov_b32 m0, %0\n\ts_nop 0"
               : "=&s"(keep) : "s"(uni(lds_addr(smem + off))), "v"(n * 4), "s"(k_tags) : "memory");
}
template <int V> struct IC { static constexpr int v = V; };
struct QFrag { bf16x8 xa[5]; bf16x8 w[3][2]; };
__device__ __forceinline__ void q_read(QFrag& f, const char* sa, const char* sb0, const char* sb1, int ks) {
#pragma unroll
  for (int i = 0; i < 5; ++i) f.xa[i] = lds_b128(sa + i * 2048);
#pragma unroll
  for (int s = 0; s < 3; ++s) {
    f.w[s][0] = lds_tr2(sb0 + s * 8192 + ks * 4096);
    f.w[s][1] = lds_tr2(sb1 + s * 8192 + ks * 4096);
  }
}
__device__ constexpr int acc_no(int HALF, int m, int i, int t) {
  int n = 0;
  for (int ii = 0; ii < 5; ++ii)
    for (int tt = 0; tt < 2; ++tt)
      for (int mm = 0; mm < 3; ++mm) {
        const bool on = mm == 0 ? (HALF == 1 || (ii >= 1 && ii <= 3)) : (HALF == 0 || ii >= 1);
        if (mm == m && ii == i && tt == t) return on ? n : -1;
        if (on) ++n;
      }
  return -1;
}
__device__ constexpr int seq_of(int HALF, int z) {
  int n = 0;
  for (int a = 0; a < 30; ++a) {
    const int m = a / 10, t0 = (a / 5) % 2;
    const int i = m < 2 ? (t0 ? a % 5 : 4 - a % 5) : 4 - (a - 20) / 2, t = m < 2 ? t0 : (a ^ i) & 1;
    if (acc_no(HALF, m, i, t) >= 0) { if (n == z) return m * 100 + i * 10 + t; ++n; }
  }
  return -1;
}
template <int HALF, int Z, int E, bool FIRST>
__device__ __forceinline__ void q_one(const QFrag& f, f32x4 (&acc)[26]) {
  if constexpr (Z < E) {
    constexpr int m = seq_of(HALF, Z) / 100, i = seq_of(HALF, Z) / 10 % 10, t = seq_of(HALF, Z) % 10;
    constexpr int n = acc_no(HALF, m, i, t);
    const f32x4 c = FIRST ? f32x4{0, 0, 0, 0} : acc[n];
    acc[n] = m == 2 ? MFMA16(f.xa[i], f.w[2][t], c) : MFMA16(f.w[m][t], f.xa[i], c);
    q_one<HALF, Z + 1, E, FIRST>(f, acc);
  }
}
template <int HALF, int B, int E, int NRD, bool FIRST = false>
__device__ __forceinline__ void q_mma(const QFrag& f, f32x4 (&acc)[26]) {
  q_one<HALF, B, E, FIRST>(f, acc);
#pragma unroll
  for (int i = 0; i < NRD; ++i) {
    __builtin_amdgcn_sched_group_barrier(0x008, 1, 0);
    __builtin_amdgcn_sched_group_barrier(0x100, 1, 0);
  }
  __builtin_amdgcn_sched_group_barrier(0x008, E - B - NRD, 0);
}

__device__ __forceinline__ void tr_all(u32x4 (&w)[8]) {
  u32x4 t[8];
#pragma unroll
  for (int i = 0; i < 8; ++i) t[i] = __builtin_bit_cast(u32x4, tr_frag(w, i));
#pragma unroll
  for (int i = 0; i < 8; ++i) w[i] = t[i];
  asm volatile("" : "+v"(w[0]), "+v"(w[1]), "+v"(w[2]), "+v"(w[3]), "+v"(w[4]), "+v"(w[5]), "+v"(w[6]), "+v"(w[7]));
}
typedef __amdgpu_buffer_rsrc_t rsrc_t;
__device__ __forceinline__ rsrc_t mk_rsrc(const void* p, unsigned bytes) { return __builtin_amdgcn_make_buffer_rsrc((void*)p, 0, bytes, 0x00020000); }
__device__ __forceinline__ u32x4 ld16(rsrc_t rs, unsigned voff, unsigned soff) { return __builtin_amdgcn_raw_buffer_load_b128(rs, voff, soff, 0); }
__device__ __forceinline__ u32x4 ld16c(rsrc_t rs, unsigned voff, unsigned soff) { return __builtin_amdgcn_raw_buffer_load_b128(rs, voff, soff, 16); }
__device__ __forceinline__ void st16c(rsrc_t rs, unsigned voff, u32x4 v) { __builtin_amdgcn_raw_buffer_store_b128(v, rs, voff, 0, 16); }
__device__ __forceinline__ void st8c(rsrc_t rs, unsigned voff, unsigned v, unsigned tag) { __builtin_amdgcn_raw_buffer_store_b64(u32x2{v, tag}, rs, voff, 0, 16); }
__device__ __forceinline__ unsigned chk(unsigned bad, const u32x4& v, unsigned tag) {
  return __builtin_amdgcn_bitop3_b32(__builtin_amdgcn_bitop3_b32(bad, v[1], tag, 0xF6), v[3], tag, 0xF6);
}
__device__ __forceinline__ float f_of(unsigned u) { return __uint_as_float(u); }
#define LANE_VARS                                                                   \
  int lane = lane_in; asm volatile("" : "+v"(lane));                                \
  const int g = lane >> 4, r16 = lane & 15, q = (lane >> 2) & 3, p = lane & 3;      \
  const int hk = (q >> 1) | ((g & 1) << 1), brow = (8 * g + q) * 128 + 8 * p;       \
  (void)r16; (void)hk; (void)brow;

template <bool MAX>
__device__ __forceinline__ float rows_reduce(const float (&v)[4]) {
  auto op = [](unsigned a, unsigned b) {
    return __float_as_uint(MAX ? __builtin_elementwise_maximum(__uint_as_float(a), __uint_as_float(b)) : __uint_as_float(a) + __uint_as_float(b));
  };
  auto p = __builtin_amdgcn_permlane16_swap(__float_as_uint(v[0]), __float_as_uint(v[1]), false, false);
  auto q = __builtin_amdgcn_permlane16_swap(__float_as_uint(v[2]), __float_as_uint(v[3]), false, false);
  auto t = __builtin_amdgcn_permlane32_swap(op(p[0], p[1]), op(q[0], q[1]), false, false);
  return __uint_as_float(op(t[0], t[1]));
}
__device__ __forceinline__ void rows_spread(float t, float (&v)[4]) {
  auto h = __builtin_amdgcn_permlane32_swap(__float_as_uint(t), __float_as_uint(t), false, false);
  auto p = __builtin_amdgcn_permlane16_swap(h[0], h[0], false, false);
  auto q = __builtin_amdgcn_permlane16_swap(h[1], h[1], false, false);
  v[0] = __uint_as_float(p[0]); v[1] = __uint_as_float(p[1]); v[2] = __uint_as_float(q[0]); v[3] = __uint_as_float(q[1]);
}
typedef __attribute__((ext_vector_type(16))) unsigned u32x16;

template <int CTL>
__device__ __forceinline__ float dpp_f(float v) { return __uint_as_float(__builtin_amdgcn_update_dpp(0, (int)__float_as_uint(v), CTL, 0xf, 0xf, false)); }
__device__ __forceinline__ float sum16(float v) {
  v += dpp_f<0xB1>(v);
  v += dpp_f<0x4E>(v);
  v += dpp_f<0x141>(v);
  return v + dpp_f<0x140>(v);
}
__device__ __forceinline__ float dot2(unsigned a, unsigned b, float c) {
  return __builtin_amdgcn_fdot2_f32_bf16(__builtin_bit_cast(bf16x2_t, a), __builtin_bit_cast(bf16x2_t, b), c, false);
}
__device__ __forceinline__ float sum32(float v) {
  v = sum16(v);
  auto a = __builtin_amdgcn_permlane16_swap(__float_as_uint(v), __float_as_uint(v), false, false);
  return __uint_as_float(a[0]) + __uint_as_float(a[1]);
}
struct RowRegs { u32x4 x[4], w[2]; unsigned ep, keep; };
__device__ __forceinline__ void row_request(RowRegs& r, const u16* __restrict__ x, const u16* __restrict__ wr, char* ws, int row0, int lane, bool on) {
  if (!on) return;
  const rsrc_t xrs = mk_rsrc(x, S_TOK * DM * 2), rrs = mk_rsrc(wr, DM * 2);
  const unsigned xo = (unsigned)(row0 + (lane >> 5)) * 2048u + (lane & 31) * 16, wo = lane * 16;
  asm volatile("buffer_load_dword %0, off, %1, 0 sc1" : "=v"(r.ep) : "s"(mk_rsrc(ws + WS_EP, 4)) : "memory");
#pragma unroll
  for (int j = 0; j < 4; ++j) asm volatile("buffer_load_dwordx4 %0, %1, %2, 0 offen offset:%3" : "=v"(r.x[j]) : "v"(xo), "s"(xrs), "i"(j * 512) : "memory");
#pragma unroll
  for (int j = 0; j < 2; ++j) asm volatile("buffer_load_dwordx4 %0, %1, %2, 0 offen offset:%3" : "=v"(r.w[j]) : "v"(wo), "s"(rrs), "i"(j * 1024) : "memory");
}
__device__ __forceinline__ unsigned row_stats(RowRegs& r, char* ws, int row0, int lane) {
  float s0 = 0.f, s1 = 0.f;
  asm volatile("s_waitcnt vmcnt(2)" : "+v"(r.x[0]), "+v"(r.x[1]), "+v"(r.x[2]), "+v"(r.x[3]), "+v"(r.ep), "+v"(r.keep) :: "memory");
  const unsigned tag = uni(r.ep) + 1u;
#pragma unroll
  for (int j = 0; j < 4; ++j) {
    s0 = dot2(r.x[j][0], 0x3f803f80u, s0); s1 = dot2(r.x[j][1], 0x3f803f80u, s1);
    s0 = dot2(r.x[j][2], 0x3f803f80u, s0); s1 = dot2(r.x[j][3], 0x3f803f80u, s1);
  }
  const float mean = sum32(s0 + s1) * (1.0f / DM), mb = rbf(mean);
  s0 = 0.f; s1 = 0.f;
#pragma unroll
  for (int j = 0; j < 4; ++j)
#pragma unroll
    for (int i = 0; i < 4; ++i) {
      const float a = lo2f(r.x[j][i]) - mb, b = hi2f(r.x[j][i]) - mb;
      s0 = fmaf(a, a, s0); s1 = fmaf(b, b, s1);
      r.x[j][i] = pack2(a, b);
    }
  const float dm = mean - mb;
  const float var = rbf(fmaxf(sum32(s0 + s1) * (1.0f / DM) - dm * dm, 0.f));
  const float den = rbf(__builtin_amdgcn_sqrtf(rbf(var + 1e-5f))), rd = __builtin_amdgcn_rcpf(den);
  s0 = 0.f; s1 = 0.f;
  asm volatile("s_waitcnt vmcnt(0)" : "+v"(r.w[0]), "+v"(r.w[1]) :: "memory");
#pragma unroll
  for (int j = 0; j < 4; ++j)
#pragma unroll
    for (int i = 0; i < 4; ++i) {
      const unsigned n = pack2(lo2f(r.x[j][i]) * rd, hi2f(r.x[j][i]) * rd);
      const unsigned w = __builtin_amdgcn_permlane32_swap(r.w[j >> 1][i], r.w[j >> 1][i], false, false)[j & 1];
      if (i & 1) s1 = dot2(n, w, s1); else s0 = dot2(n, w, s0);
    }
  const float dot = sum32(s0 + s1);
  unsigned k = pack2(dot, dot) & 0xffffu;
  k = (k & 0x8000u) ? (~k & 0xffffu) : (k | 0x8000u);
  const rsrc_t wrs = mk_rsrc(ws, WS_O);
  st16c(wrs, (lane & 31) == 0 ? WS_R + (unsigned)(row0 + (lane >> 5)) * 16u : 0x7fffffffu,
        u32x4{(k << 16) | (__float_as_uint(mb) >> 16), tag, __float_as_uint(den), tag});
  const unsigned k01 = k | ((unsigned)__builtin_amdgcn_readlane((int)k, 32) << 16);
  st8c(wrs, lane == 0 ? WS_K + (unsigned)(((row0 >> 2) & 7) * 1024 + (row0 >> 5) * 16 + (row0 & 2) * 4) : 0x7fffffffu, k01, tag);
  return tag;
}
__device__ __forceinline__ void k_order(int tm, int tn, int& pa, int& pb) {
  pb = tn & 3;
  pa = (tm * 0x1111) ^ ((tn & 4) * 0x51 & 0x144) ^ ((~tn & 2) * 0x2200) ^ ((~tn & 1) << 9) ^ (pb == 3 ? 1 : tm & 1);
}
__device__ __forceinline__ int k_tile(int t, int pa, int pb) { return ((((t + pb) & 12) ? t + pb : t & 3) ^ (pa >> ((t + pb) & 12))) & 15; }
struct EpiRegs { int a0, b0, tx; unsigned xr, go; u32x4 xv[2][2]; unsigned t[16]; };
__device__ __forceinline__ float opq(float v) { asm("" : "+v"(v)); return v; }
typedef __attribute__((ext_vector_type(10))) unsigned u32x10;
#define DV 0
#define PRE 3
#define GRP(N, M, K, C) _Pragma("unroll") for (int z_ = 0; z_ < (N); ++z_) { __builtin_amdgcn_sched_group_barrier(0x008, M, 0); __builtin_amdgcn_sched_group_barrier(K, C, 0); }
__device__ constexpr int tail_of(int HALF, int z) {
  int n = 0;
  for (int a = 0; a < 30; ++a) {
    const int i = HALF ? a / 6 : 4 - a / 6, m = 2 - a % 6 / 2, t = a & 1;
    if (acc_no(HALF, m, i, t) >= 0 && (m < 2 || (HALF == 0 && i == 4))) { if (n == z) return m * 100 + i * 10 + t; ++n; }
  }
  return -1;
}
__device__ __forceinline__ void qkv_setup(char* smem, int wid, int lane_in, int tm, int tn, EpiRegs& e) {
  LANE_VARS
  e.a0 = (wid & 1) * 8192 + r16 * 128 + ((g ^ (r16 >> 1)) << 4);
  e.b0 = XB + brow + (((wid & 2) ^ hk) << 5);
  int pa, pb;
  k_order(tm, tn, pa, pb);
  e.tx = __builtin_ctz((unsigned)__ballot(k_tile(r16, pa, pb) == tn));
  e.xr = lds_addr(smem) + (16 + wid * 32 + r16) * 128 + ((((g & 1) * 2 + (g >> 1)) ^ (r16 >> 1)) << 4);
  asm volatile("" : "+v"(e.a0), "+v"(e.b0), "+v"(e.xr));
  asm volatile("" : "+s"(e.tx));
}
template <int HALF>
__device__ __forceinline__ void qkv_attention(char* smem, int wid, int lane_in, int tm, int tn, char* ws, unsigned tag,
                                              const u16* __restrict__ x, EpiRegs& e) {
  const int h = wid >> 1;
  f32x4 acc[26];
  float ra[5], rb[5], sa[4], sb[4];
  QFrag f0, f1;
  u32x4 fl;
  unsigned aq, ac, aa, ab, xa, xv, xf, ooff, sel[4];
  {
    LANE_VARS
    const int a0 = e.a0, b0 = e.b0, tx = uni(e.tx);
    const unsigned xr = e.xr;
    asm volatile("" : "=v"(e.xv[0][0]), "=v"(e.xv[0][1]), "=v"(e.xv[1][0]), "=v"(e.xv[1][1]));
    auto keep = [&](int t, int slot, int tx) {
      unsigned p0, p1;
      asm volatile("s_cmp_lg_u32 %7, %8\n\ts_cbranch_scc1 1f\n\tv_add_u32 %4, %9, %6\n\tv_xor_b32 %5, 64, %4\n\tds_read_b128 %0, %4\n\tds_read_b128 %1, %5\n\t"
                   "ds_read_b128 %2, %4 offset:2048\n\tds_read_b128 %3, %5 offset:2048\n\t1:"
                   : "+v"(e.xv[0][0]), "+v"(e.xv[0][1]), "+v"(e.xv[1][0]), "+v"(e.xv[1][1]), "=&v"(p0), "=&v"(p1) : "v"(xr), "s"(t), "s"(tx), "s"(slot) : "scc", "memory");
    };
    SCHED0();
    WAITL0();
    BAR();
    STAMP(1);
    q_read(f0, smem + a0, smem + b0, smem + (b0 ^ 32), 0);
    SCHED0();
    q_read(f1, smem + (a0 ^ 64), smem + b0, smem + (b0 ^ 32), 1);
    int qa = XSL_ + a0, qc = XSL_ + (a0 ^ 64), qb = WSL + b0, qd = WSL + (b0 ^ 32), so = XSL_;
    q_mma<HALF, 0, 26, 17, true>(f0, acc);
    asm volatile("" : "+v"(qa), "+v"(qb), "+v"(qc), "+v"(qd));
    SCHED0();
    keep(0, 0, tx);
#pragma unroll 1
    for (int t = 1; t < NBAR - 1; ) {
      SCHED0();
      WAITL0();
      BAR();
      SCHED0();
      q_read(f0, smem + qa, smem + qb, smem + qd, 0);
      q_mma<HALF, 0, 26, 17>(f1, acc);
      SCHED0();
      q_read(f1, smem + qc, smem + qb, smem + qd, 1);
      q_mma<HALF, 0, 19, 17>(f0, acc);
      SCHED0();
      int d = so == 2 * XSL_ ? -2 * XSL_ : XSL_;
      asm volatile("" : "+s"(d));
      SCHED0();
      q_mma<HALF, 19, 20, 0>(f0, acc);
      SCHED0();
      keep(t, so, tx);
      SCHED0();
      q_mma<HALF, 20, 21, 0>(f0, acc);
      SCHED0(); qa += d; qc += d; asm volatile("" : "+v"(qa), "+v"(qc)); SCHED0();
      q_mma<HALF, 21, 22, 0>(f0, acc);
      SCHED0(); so += d; ++t; asm volatile("" : "+s"(so), "+s"(t)); SCHED0();
      q_mma<HALF, 22, 23, 0>(f0, acc);
      SCHED0(); int e = (t & 3) ? WSL : -3 * WSL; asm volatile("" : "+s"(e)); SCHED0();
      q_mma<HALF, 23, 24, 0>(f0, acc);
      SCHED0(); qb += e; qd += e; asm volatile("" : "+v"(qb), "+v"(qd)); SCHED0();
      q_mma<HALF, 24, 26, 0>(f0, acc);
      SCHED0();
    }
    WAITL0();
    BAR();
    STAMP(2);
#pragma unroll 1
    for (int t = NBAR + 1; t < 17; ) {
      SCHED0();
      q_read(f0, smem + qa, smem + qb, smem + qd, 0);
      q_mma<HALF, 0, 26, 17>(f1, acc);
      SCHED0();
      q_read(f1, smem + qc, smem + qb, smem + qd, 1);
      const u32x4 fl = lds_word4(smem, F_FULL);
      q_mma<HALF, 0, 21, 18>(f0, acc);
      SCHED0();
      const unsigned cur = uni(min(fl[0], fl[1])), need = t;
      SCHED0();
      q_mma<HALF, 21, 22, 0>(f0, acc);
      SCHED0();
      keep(t, so, tx + 2);
      SCHED0();
      qa += XSL_; qb += WSL; qc += XSL_; qd += WSL; so += XSL_; ++t;
      q_one<HALF, 22, 25, false>(f0, acc);
      GRP(2, 1, 0x002, 2);
      asm volatile("" : "+v"(qa), "+v"(qb), "+v"(qc), "+v"(qd), "+s"(so), "+s"(t));
      SCHED0();
      q_mma<HALF, 25, 26, 0>(f0, acc);
      SCHED0();
      wait_full(smem, F_FULL, need, cur);
    }
    SCHED0();
    q_read(f0, smem + a0, smem + qb, smem + qd, 0);
    q_one<HALF, 0, 26, false>(f1, acc);
    {
      const int pa = 4 * g + (r16 & 1);
      const bool odd = r16 & 1, cur_a = pa <= r16, cur_b = pa + 2 <= r16;
      const unsigned qs = lds_addr(smem + L_QST + HALF * 512);
      aq = qs + r16 * 8; ac = lds_addr(smem + L_CSF + h * 256) + r16 * 8;
      aa = qs + pa * 8 + (cur_a ? 128 : 0); ab = qs + pa * 8 + (cur_b ? 128 : 0);
      xa = lds_addr(smem + L_XCH + h * 3072) + lane * 16; xv = xa + 2048 - lane * 8; xf = lds_addr(smem + F_XCH + h * 4);
      const unsigned la = odd ? 0x01000c0cu : 0x0c0c0100u, lb = odd ? 0x03020c0cu : 0x0c0c0302u;
      sel[0] = cur_a ? 0x0c0c0c0cu : la; sel[1] = cur_b ? 0x0c0c0c0cu : lb; sel[2] = cur_a ? la : 0x0c0c0c0cu; sel[3] = cur_b ? lb : 0x0c0c0c0cu;
      ooff = (unsigned)(tm * 8 + HALF * 4) * 32768u + (tn * 2 + h) * 1024 + lane * 16;
#pragma unroll
      for (int z = 0; z < 17; ++z) { GRP(1, 1, 0x100, 1); __builtin_amdgcn_sched_group_barrier(0x002, 1, 0); }
      GRP(9, 1, 0x002, 3);
      asm volatile("" : "+v"(aq), "+v"(ac), "+v"(aa), "+v"(ab), "+v"(xa), "+v"(xv), "+v"(xf), "+v"(ooff));
      asm volatile("" : "+v"(sel[0]), "+v"(sel[1]), "+v"(sel[2]), "+v"(sel[3]));
    }
    SCHED0();
#pragma unroll
    for (int i = 0; i < 5; ++i) f1.xa[i] = lds_b128(smem + (a0 ^ 64) + i * 2048);
#pragma unroll
    for (int s = 0; s < 3; ++s) {
      if (s == 2) fl = lds_word4(smem, F_FULL);
      f1.w[s][0] = lds_tr2(smem + qb + s * 8192 + 4096);
      f1.w[s][1] = lds_tr2(smem + qd + s * 8192 + 4096);
    }
    q_mma<HALF, 0, 26 - DV, 18>(f0, acc);
    SCHED0();
    keep(15, 0, tx);
    SCHED0();
  }
  LANE_VARS
  auto mm = [&](const QFrag& f, int m, int i, int t) {
    const int n = max(acc_no(HALF, m, i, t), 0);
    acc[n] = m == 2 ? MFMA16(f.xa[i], f.w[2][t], acc[n]) : MFMA16(f.w[m][t], f.xa[i], acc[n]);
  };
  auto last = [&](int z0, int z1) {
#pragma unroll
    for (int z = z0; z < z1; ++z) {
      const int c = tail_of(HALF, HALF ? z : z < 6 ? z + 2 : z < 8 ? z - 6 : z);
      mm(f1, c / 100, c / 10 % 10, c % 10);
    }
  };
  last(0, PRE);
  SCHED0();
  const unsigned cur = uni(min(fl[2], fl[3]));
  SCHED0();
  last(PRE, PRE + 1);
  SCHED0();
  wait_full(smem, F_ST, 1, cur);
  SCHED0();
  u32x2 sq[5], cs[3][2];
#pragma unroll
  for (int i = 0; i < 5; ++i) sq[i] = *(const LAS u32x2*)(aq + i * 128);
#pragma unroll
  for (int s = 0; s < 3; ++s)
#pragma unroll
    for (int t = 0; t < 2; ++t) cs[s][t] = *(const LAS u32x2*)(ac + s * 512 + t * 128);
#pragma unroll
  for (int i = 1; i < 5; ++i) { ra[i] = *(const LAS float*)(aa + (i - 1) * 128 + 4); rb[i] = *(const LAS float*)(ab + (i - 1) * 128 + 20); }
  last(PRE + 1, PRE + 5);
  __builtin_amdgcn_sched_group_barrier(0x100, 3, 0); GRP(3, 1, 0x100, 3);
  SCHED0();
  lds_post(smem, F_FREE + wid, 16);
  SCHED0();
  last(PRE + 5, 18);
  const unsigned gm = g == 0 ? ~0u : 0u;
  u32x2 lx[5];
#pragma unroll
  for (int i = 0; i < 5; ++i) lx[i] = u32x2{sq[i][0] & gm, sq[i][0] & gm & 0xffffu};
#pragma unroll
  for (int i = 1; i < 5; ++i) {
    const float c = f_of(sq[i][1]) * (0.17677669529663687f * 1.4426950408889634f);
    sa[i - 1] = opq(c * ra[i]); sb[i - 1] = opq(c * rb[i]);
  }
  __builtin_amdgcn_sched_group_barrier(0x008, 5 - PRE, 0); GRP(8, 1, 0x002, 3);
  asm volatile("" : "+v"(lx[0]), "+v"(lx[1]), "+v"(lx[2]), "+v"(lx[3]), "+v"(lx[4]));
  asm volatile("" : "+v"(sa[0]), "+v"(sa[1]), "+v"(sa[2]), "+v"(sa[3]), "+v"(sb[0]), "+v"(sb[1]), "+v"(sb[2]), "+v"(sb[3]));
  SCHED0();
  auto ln = [&](int m, int i, int t) {
    const int n = max(acc_no(HALF, m, i, t), 0);
    const s16x4 lxi = __builtin_bit_cast(s16x4, lx[i]), lw = __builtin_bit_cast(s16x4, cs[m][t]);
    acc[n] = m == 2 ? __builtin_amdgcn_mfma_f32_16x16x16bf16_1k(lxi, lw, acc[n], 0, 0, 0) : __builtin_amdgcn_mfma_f32_16x16x16bf16_1k(lw, lxi, acc[n], 0, 0, 0);
  };
#pragma unroll
  for (int z = 0; z < 18; ++z) ln(tail_of(HALF, z) / 100, tail_of(HALF, z) / 10 % 10, tail_of(HALF, z) % 10);
  STAMP(3);
  bf16x8 qf[5], kf[5];
  u32x10 vf[2];
  auto pack_qk = [&](int i) {
    if (acc_no(HALF, 0, i, 0) >= 0) qf[i] = pack8(acc[max(acc_no(HALF, 0, i, 0), 0)], acc[max(acc_no(HALF, 0, i, 1), 0)]);
    if (acc_no(HALF, 1, i, 0) >= 0) kf[i] = pack8(acc[max(acc_no(HALF, 1, i, 0), 0)], acc[max(acc_no(HALF, 1, i, 1), 0)]);
  };
  auto pack_v = [&](int i, int t) {
    const f32x4& v = acc[max(acc_no(HALF, 2, i, t), 0)];
    return u32x2{pack2(v[0], v[1]), pack2(v[2], v[3])};
  };
  u32x2 vx[2];
  pack_qk(HALF == 0 ? 4 : 0);
  if (HALF == 0) {
    vx[0] = pack_v(4, 0); vx[1] = pack_v(4, 1);
    asm volatile("ds_write_b128 %0, %2 offset:1024\n\tds_write2st64_b64 %1, %3, %4 offset1:1" :: "v"(xa), "v"(xv), "v"(kf[4]), "v"(vx[0]), "v"(vx[1]) : "memory");
  } else {
    asm volatile("ds_write_b128 %0, %1" :: "v"(xa), "v"(qf[0]) : "memory");
  }
  asm volatile("s_mov_b64 exec, 1\n\tds_write_b8 %0, %0 offset:%1\n\ts_mov_b64 exec, -1" ::"v"(xf), "i"(HALF) : "memory");
  static_assert((F_XCH & 0xf) != 0 && ((F_XCH + 4) & 0xf) != 0, "the low byte of the address is the flag");
#pragma unroll
  for (int i = 0; i < 4; ++i) pack_qk(HALF == 0 ? 3 - i : i + 1);
  const f32x4 z4 = {0.f, 0.f, 0.f, 0.f};
  f32x4 sp[4], sc[4];
#pragma unroll
  for (int j = 0; j < 3; ++j) {
    const int i = HALF == 0 ? 3 - j : j + 2;
    sc[i - 1] = MFMA16(kf[i], qf[i], z4); sp[i - 1] = MFMA16(kf[i - 1], qf[i], z4);
  }
  if (HALF) sc[0] = MFMA16(kf[1], qf[1], z4);
  GRP(1, 6, 0x002, 4); GRP(12, 1, 0x002, 2); GRP(HALF ? 7 : 6, 1, 0x002, 3);
  SCHED0();
  unsigned flag;
  bf16x8 xq;
  if (HALF == 0) asm volatile("ds_read_b32 %0, %2\n\tds_read_b128 %1, %3" : "=&v"(flag), "=&v"(xq) : "v"(xf), "v"(xa) : "memory");
  else asm volatile("ds_read_b32 %0, %4\n\tds_read_b128 %1, %5 offset:1024\n\tds_read_b64 %2, %6\n\tds_read_b64 %3, %6 offset:512"
                    : "=&v"(flag), "=&v"(xq), "=&v"(vx[0]), "=&v"(vx[1]) : "v"(xf), "v"(xa), "v"(xv) : "memory");
  SCHED0();
  const bool odd = r16 & 1, cur_a = sel[0] == 0x0c0c0c0cu, cur_b = sel[1] == 0x0c0c0c0cu;
  float a[4], b[4], m[4];
  auto logits = [&](int j) {
    const f32x4 p4 = sp[j], c4 = sc[j];
    const float p0 = p4[0], p1 = p4[1], p2 = p4[2], p3 = p4[3], c0 = c4[0], c1 = c4[1], c2 = c4[2], c3 = c4[3];
    const float ca = odd ? c1 : c0, cb = odd ? c3 : c2, pa_ = odd ? p1 : p0, pb = odd ? p3 : p2;
    a[j] = opq((cur_a ? ca : pa_) * sa[j]);
    b[j] = opq((cur_b ? cb : pb) * sb[j]);
    if (HALF == 0 && j == 0) {
      a[j] = (!cur_a && tm == 0) ? -INFINITY : a[j];
      b[j] = (!cur_b && tm == 0) ? -INFINITY : b[j];
    }
    m[j] = __builtin_elementwise_maximum(a[j], b[j]);
  };
  auto late = [&](int z0, int z1) {
#pragma unroll
    for (int y = z0; y < z1; ++y) {
      const int c = seq_of(HALF, 18 + ((y + 8 - DV) & 7));
      if (y < 8 + DV) mm(y < DV ? f0 : f1, 2, c / 10 % 10, c % 10);
    }
  };
#pragma unroll
  for (int j = 0; j < 2; ++j) logits(HALF == 0 ? 2 - j : j + 1);
  late(0, 6);
  GRP(6, 1, 0x002, 3);
  asm volatile("" : "+v"(m[1]), "+v"(m[2]));
  SCHED0();
  {
    unsigned t;
#define XWAIT(RD) "s_waitcnt lgkmcnt(0)\n\tv_readfirstlane_b32 %0, %1\n\ts_and_b32 %0, %0, %[mask]\n\ts_cbranch_scc1 2f\n\t1:\n\t" RD \
                  "\n\ts_waitcnt lgkmcnt(0)\n\tv_readfirstlane_b32 %0, %1\n\ts_and_b32 %0, %0, %[mask]\n\ts_cbranch_scc0 1b\n\t2:"
    if (HALF == 0) asm volatile(XWAIT("ds_read_b32 %1, %3\n\tds_read_b128 %2, %4") : "=&s"(t), "+v"(flag), "+v"(xq) : "v"(xf), "v"(xa), [mask] "s"(0xff00u) : "scc", "memory");
    else asm volatile(XWAIT("ds_read_b32 %1, %5\n\tds_read_b128 %2, %6 offset:1024\n\tds_read_b64 %3, %7\n\tds_read_b64 %4, %7 offset:512")
                      : "=&s"(t), "+v"(flag), "+v"(xq), "+v"(vx[0]), "+v"(vx[1]) : "v"(xf), "v"(xa), "v"(xv), [mask] "s"(0xffu) : "scc", "memory");
  }
  SCHED0();
  if (HALF == 0) { sc[3] = MFMA16(kf[4], xq, z4); sp[3] = MFMA16(kf[3], xq, z4); }
  else sp[0] = MFMA16(xq, qf[1], z4);
  logits(HALF == 0 ? 0 : 3);
  late(6, 8);
  GRP(1, HALF ? 1 : 2, 0x002, 3); GRP(2, 1, 0x002, 3);
  asm volatile("" : "+v"(m[HALF ? 3 : 0]));
  SCHED0();
  logits(HALF == 0 ? 3 : 0);
  rows_spread(rows_reduce<true>(m), m);
#pragma unroll
  for (int j = 0; j < 4; ++j) {
    a[j] = __builtin_amdgcn_exp2f(opq(a[j] - m[j]));
    b[j] = __builtin_amdgcn_exp2f(opq(b[j] - m[j]));
    m[j] = opq(a[j] + b[j]);
    a[j] = opq(a[j] * ra[j + 1]); b[j] = opq(b[j] * rb[j + 1]);
  }
  late(8, 16);
#pragma unroll
  for (int z = 0; z < 8; ++z) ln(2, seq_of(HALF, 18 + z) / 10 % 10, seq_of(HALF, 18 + z) % 10);
  GRP(DV, 1, 0x402, 3); GRP(8, 1, 0x402, 2);
  asm volatile("" : "+v"(m[0]), "+v"(m[1]), "+v"(m[2]), "+v"(m[3]));
  SCHED0();
#pragma unroll
  for (int t = 0; t < 2; ++t) {
#pragma unroll
    for (int i = 0; i < 5; ++i) {
      const u32x2 v = i == (HALF ? 0 : 4) ? vx[t] : pack_v(i, t);
      vf[t][2 * i] = v[0]; vf[t][2 * i + 1] = v[1];
    }
    asm("" : "+v"(vf[t]));
  }
  rows_spread(__builtin_amdgcn_rcpf(rows_reduce<false>(m)), m);
  const rsrc_t ors = mk_rsrc(ws + WS_O, S_TOK * DM * 2);
#pragma unroll
  for (int j = 0; j < 4; ++j) {
    const unsigned pk = pack2(a[j] * m[j], b[j] * m[j]);
    const u32x4 pw = {__builtin_amdgcn_perm(0u, pk, sel[0]), __builtin_amdgcn_perm(0u, pk, sel[1]), __builtin_amdgcn_perm(0u, pk, sel[2]), __builtin_amdgcn_perm(0u, pk, sel[3])};
    const u32x4 v0 = {vf[0][2 * j], vf[0][2 * j + 1], vf[0][2 * j + 2], vf[0][2 * j + 3]}, v1 = {vf[1][2 * j], vf[1][2 * j + 1], vf[1][2 * j + 2], vf[1][2 * j + 3]};
    sp[j] = MFMA16(__builtin_bit_cast(bf16x8, v0), __builtin_bit_cast(bf16x8, pw), z4);
    sc[j] = MFMA16(__builtin_bit_cast(bf16x8, v1), __builtin_bit_cast(bf16x8, pw), z4);
  }
#pragma unroll
  for (int j = 0; j < 4; ++j) { qf[j] = pack8(sp[j], sc[j]); st16c(ors, ooff + j * 32768, __builtin_bit_cast(u32x4, qf[j])); }
  RST(4);
  SCHED0();
  asm volatile("ds_read_b32 %0, %1 offset:%2" : "=v"(e.go) : "v"(xf - h * 4), "i"(F_GO - F_XCH) : "memory");
  wait_bytes<0x80808080u>(smem, F_FREE, 16);
  asm volatile("" : "+v"(e.go));
  asm volatile("ds_write_b128 %0, %1\n\tds_write_b128 %0, %2 offset:1024\n\tds_write_b128 %0, %3 offset:4096\n\tds_write_b128 %0, %4 offset:5120"
               :: "v"(lds_addr(smem + L_OWN) + HALF * 8192 + h * 2048 + lane * 16), "v"(qf[0]), "v"(qf[1]), "v"(qf[2]), "v"(qf[3]) : "memory");
  lds_post(smem, F_OL + wid, 1);
}

__device__ __forceinline__ unsigned ln2s(unsigned w, float mb, float r) {
  const unsigned d = pack2(opq(lo2f(w) - mb), opq(hi2f(w) - mb));
  return pack2(opq(lo2f(d) * r), opq(hi2f(d) * r));
}

__device__ __forceinline__ void await_attention(char* smem, int lane, int tm, int tn, char* ws, unsigned tag, unsigned seen, unsigned& tp) {
  const rsrc_t wrs = mk_rsrc(ws, WS_O);
  const unsigned off = WS_OF + (tm * 64 + ((lane >> 3) & 1) * 32 + (lane & 7)) * 4;
  const int own = tn >> 3;
  bool first = seen == 0;
  unsigned since = 0;
#pragma unroll 1
  for (unsigned n = 0; n < SPIN_CAP; ++n) {
    unsigned want = ~seen & 0xffffu;
    if (since) ++since; else since = uni(lds_word(smem, F_ODONE)) >> 2;
    if (since <= (first ? OK : 0)) want &= own ? 0x00ffu : 0xff00u;
    const unsigned f = __builtin_amdgcn_raw_buffer_load_b32(wrs, ((want >> (lane & 15)) & 1) ? off : 0x7fffffffu, 0, 16);
    seen |= (unsigned)__ballot(f == tag) & want;
    if (seen == 0xffffu) break;
    if (first && ((seen & 0xffu) == 0xffu || (seen >> 8) == 0xffu)) { lds_post(smem, F_GO, (seen & 0xffu) == 0xffu ? 1 : 2); first = false; NAP(ON); }
    NAP(PN);
  }
  lds_post(smem, F_GO + 1, 1);
  if (first) lds_post(smem, F_GO, 1);
}

__device__ __forceinline__ void out_proj(char* smem, int wid, int lane_in, int tm, int tn, char* ws, unsigned tag,
                                         u16* __restrict__ out, EpiRegs& e, unsigned rot2) {
  LANE_VARS
  const rsrc_t ors = mk_rsrc(ws + WS_O, S_TOK * DM * 2), yrs = mk_rsrc(out, S_TOK * DM * 2);
  const int tok = tm * 128 + wid * 32 + r16, co = (g & 1) * 16 + (g >> 1) * 8;
  unsigned eoff = (unsigned)(tok * DM + tn * 64 + co) * 2u;
  const unsigned vo = (unsigned)(tm * 8 + wid * 2) * 32768u + lane * 16;
  bf16x8 wb[3][4], oa[OD][2];
  int bc[4];
#pragma unroll
  for (int c = 0; c < 4; ++c) bc[c] = L_WA + brow + ((c ^ hk) << 5);
  f32x4 acc[2][4];
  SCHED0();
  unsigned go, oth = (tn & 8) * 2 ^ 16;
  asm volatile("" : "+s"(oth));
  auto o_request = [&](int s) {
    asm volatile("buffer_load_dwordx4 %0, %2, %4, %5 offen sc1\n\tbuffer_load_dwordx4 %1, %3, %4, %5 offen sc1" : "+v"(oa[s % OD][0]), "+v"(oa[s % OD][1])
                 : "v"(vo), "v"(vo + 32768), "s"(ors), "s"(((s >> 1 == 7 ? oth : (s & 16) ^ ((go & 2) * 8)) + ((rot2 >> (s & 14) * 2) & 14) + (s & 1)) * 1024) : "memory");
  };
  auto start = [&](unsigned word) {
    go = uni(word) & 3;
    if (go) {
#pragma unroll
      for (int s = 0; s < OD - 1; ++s) o_request(s);
    }
    SCHED0();
  };
#pragma unroll
  for (int s = 0; s < OD; ++s) asm volatile("" : "=v"(oa[s][0]), "=v"(oa[s][1]));
  {
    const unsigned w0 = uni(e.go);
    if (w0 & 3) wait_bytes<0x80808080u>(smem, F_OL, 1);
    start(w0);
  }
  const unsigned early = go;
  RST(11);
  auto publish = [&](auto nn) {
    WAITV(decltype(nn)::v);
    unsigned old = 0;
    if (lane == 0) old = __hip_atomic_fetch_add((LAS unsigned*)(LAS char*)(smem + F_ODONE), 1u, __ATOMIC_RELAXED, __HIP_MEMORY_SCOPE_WORKGROUP);
    if (uni(old) == 3u) {
      if (lane == 0) __hip_atomic_store((GAS unsigned*)(ws + WS_OF) + tm * 64 + (tn >> 3) * 32 + (tn & 7), tag, RLX_AGENT);
    }
    RST(5);
    SCHED0();
  };
  if (!early) publish(IC<0>{});
  else if (EPOS == 0) publish(IC<2 * OD - 2>{});
  const unsigned w1 = lds_word(smem, F_GO);
  const u32x2 fl = *(const volatile LAS u32x2*)(LAS const char*)(smem + F_FULL);
  const unsigned srow = 2047u - (*(const unsigned*)(smem + F_SEL + (lane & 7) * 4) & 0xffffu);
  f32x4 res[2][4];
  const uint2 st[2] = {*(const uint2*)(smem + L_QST + (16 + wid * 32 + r16) * 8), *(const uint2*)(smem + L_QST + (32 + wid * 32 + r16) * 8)};
  auto norm = [&](int u) {
    const float rd = f_of(st[u].y), k = 1.0f + rd, b = lo2f(st[u].x) * rd;
#pragma unroll
    for (int pr = 0; pr < 2; ++pr) {
      auto sa = __builtin_amdgcn_permlane16_swap(e.xv[u][pr][0], e.xv[u][pr][2], false, false);
      auto sb = __builtin_amdgcn_permlane16_swap(e.xv[u][pr][1], e.xv[u][pr][3], false, false);
#pragma unroll
      for (int z = 0; z < 2; ++z) res[u][2 * pr + z] = f32x4{lo2f(sa[z]), hi2f(sa[z]), lo2f(sb[z]), hi2f(sb[z])} * k + b;
    }
    asm volatile("" : "+v"(res[u][0]), "+v"(res[u][1]), "+v"(res[u][2]), "+v"(res[u][3]));
  };
  f32x4 ac2[2][4], (&ac3)[2][4] = res;
  auto w_frag = [&](int slot, const int (&b)[4], int z) {
#pragma unroll
    for (int c = 0; c < 4; ++c) wb[slot][c] = lds_tr2(smem + b[c] + z * 4096);
  };
  auto step = [&](f32x4 (&d)[2][4], int slot, const bf16x8 (&o)[2], bool fresh) {
#pragma unroll
    for (int c = 0; c < 4; ++c)
#pragma unroll
      for (int uu = 0; uu < 2; ++uu) {
        const int u = uu ^ (c & 1);
        d[u][c] = MFMA16(wb[slot][c], o[u], fresh ? (f32x4{0, 0, 0, 0}) : d[u][c]);
      }
  };
  int b3[4];
#pragma unroll
  for (int c = 0; c < 4; ++c) b3[c] = bc[c] + (L_W3 - L_WA);
  auto own_heads = [&](unsigned fu) {
    wait_full(smem, F_FULL, 17, fu);
    w_frag(0, b3, 2); w_frag(1, b3, 3);
    const unsigned w2 = lds_word(smem, F_GO);
    SCHED0();
    if (wid < 2) wait_bytes<0x00800080u>(smem, F_OL, 1); else wait_bytes<0x80008000u>(smem, F_OL, 1);
    if (!go) start(w2);
    bf16x8 own[2][2];
#pragma unroll
    for (int z = 0; z < 4; ++z) own[z >> 1][z & 1] = lds_b128(smem + L_OWN + wid * 4096 + lane * 16 + z * 1024);
    step(ac3, 0, own[0], false); step(ac3, 1, own[1], false);
    asm volatile("" : "+v"(ac3[0][0]), "+v"(ac3[0][1]), "+v"(ac3[0][2]), "+v"(ac3[0][3]), "+v"(ac3[1][0]), "+v"(ac3[1][1]), "+v"(ac3[1][2]), "+v"(ac3[1][3]));
    SCHED0();
  };
  unsigned mine = (unsigned)__ballot((srow >> 5) == (unsigned)(tm * 4 + wid)) & 0xffu;
  asm volatile("" : "+s"(mine));
  asm volatile("" : "+v"(eoff));
  if (!go) start(w1);
  norm(0);
  norm(1);
  SCHED0();
  RST(12);
  if (EPOS == 1 && early) publish(IC<2 * OD - 2>{});
  own_heads(uni(min(fl[0], fl[1])));
  RST(10);
  if (EPOS == 2 && early) publish(IC<2 * OD - 2>{});
  if (!go) start(wait_bytes<0x80u>(smem, F_GO, 1));
  go = uni(go);
  RST(6);
  int b2[4];
  const int mine1st = ((go >> 1) ^ (tn >> 3) ^ 1) & 1;
#pragma unroll
  for (int c = 0; c < 4; ++c) { b2[c] = bc[c] + (go & 1) * (L_WB - L_WA); bc[c] += (go >> 1) * (L_WB - L_WA); }
  unsigned so[15];
#pragma unroll
  for (int z = (OD - 1) / 2; z < 15; ++z) { so[z] = ((z == 7 ? oth : (2 * z & 16) ^ ((go & 2) * 8)) + ((rot2 >> (z & 7) * 4) & 14)) * 1024; asm volatile("" : "+s"(so[z])); }
  s16x4 wh[3][8];
  auto w_half = [&](int s, int z0, int z1) {
#pragma unroll
    for (int z = z0; z < z1; ++z)
      if (s < 30) wh[s % 3][z] = __builtin_amdgcn_ds_read_tr16_b64_v4i16((LAS s16x4*)(smem + (s < 14 ? bc[z >> 1] + s * 4096 : s < 16 ? b3[z >> 1] + (s - 14) * 4096 : b2[z >> 1] + (s - 16) * 4096) + (z & 1) * 512));
  };
  wait_full(smem, F_WO, 1);
  wait_bytes<0x80u, true>(smem, F_WO + 8, 1, go & 1);
  RST(7);
  RSC(0);
  w_half(0, 0, 8); w_half(1, 0, 8);
  SCHED0();
  if (EPOS == 3 && early) publish(IC<2 * OD - 2>{});
  asm volatile("s_waitcnt vmcnt(%2)" : "+v"(oa[0][0]), "+v"(oa[0][1]) : "i"(2 * OD - 4) : "memory");
  auto one = [&](int s) __attribute__((always_inline)) {
    f32x4 (&d)[2][4] = s < 14 ? ac2 : s < 16 ? ac3 : acc;
    auto part = [&](int z0, int z1) {
      SCHED0();
      w_half(s + 2, z0, z1);
#pragma unroll
      for (int z = z0; z < z1; ++z) {
        const int c = z >> 1, u = (z ^ c) & 1;
        d[u][c] = MFMA16(__builtin_shufflevector(wh[s % 3][2 * c], wh[s % 3][2 * c + 1], 0, 1, 2, 3, 4, 5, 6, 7), oa[s % OD][u], s == 0 || s == 16 ? (f32x4{0, 0, 0, 0}) : d[u][c]);
        __builtin_amdgcn_sched_group_barrier(0x008, 1, 0);
        __builtin_amdgcn_sched_group_barrier(0x100, 1, 0);
      }
      SCHED0();
    };
    const int n = s + OD - 1;
    auto ask = [&](int u) {
      if (n < 30) asm volatile("buffer_load_dwordx4 %0, %1, %2, %3 offen offset:%4 sc1" : "+v"(oa[n % OD][u]) : "v"(vo + u * 32768), "s"(ors), "s"(so[n >> 1]), "i"((n & 1) * 1024) : "memory");
    };
    part(0, 1);
    if (n == 14 || n == 16) wait_bytes<0x8000u, true>(smem, F_GO, 1, n == 14 ? mine1st ^ 1 : mine1st);
    ask(0);
    part(1, 3);
    ask(1);
    if (s == 13) wait_bytes<0x80u>(smem, F_WO + 8, 1);
    part(3, 5);
    if (s < 29) asm volatile("s_waitcnt vmcnt(%2)" : "+v"(oa[(s + 1) % OD][0]), "+v"(oa[(s + 1) % OD][1]) : "i"(2 * ((n < 29 ? n : 29) - s - 1)) : "memory");
    part(5, 8);
    asm volatile("" : "+v"(d[0][0]), "+v"(d[0][1]), "+v"(d[0][2]), "+v"(d[0][3]), "+v"(d[1][0]), "+v"(d[1][1]), "+v"(d[1][2]), "+v"(d[1][3]));
    if (s == 0) { RST(14); RSC(1); }
    if (s == 14) { RST(15); RSC(2); }
  };
#pragma unroll
  for (int s = 0; s < 10; ++s) one(s);
#pragma unroll
  for (int s = 10; s < 20; ++s) one(s);
#pragma unroll
  for (int s = 20; s < 28; ++s) one(s);
  asm volatile("s_waitcnt vmcnt(0)" : "+v"(oa[29 % OD][0]), "+v"(oa[29 % OD][1]) :: "memory");
  if (mine != 0) {
    wait_bytes<0x80808080u>(smem, F_UP, 1);
#pragma unroll 1
    for (; mine != 0; mine &= mine - 1) {
      const int c = __builtin_ctz(mine), row = __builtin_amdgcn_readlane((int)srow, c) & 31;
#pragma unroll
      for (int u = 0; u < 2; ++u)
        if (row == u * 16 + r16) {
#pragma unroll
          for (int z = 0; z < 4; ++z) res[u][z] += *(const f32x4*)(smem + L_UP + c * 256 + (z * 16 + g * 4) * 4);
        }
    }
  }
  SCHED0();
#pragma unroll
  for (int u = 0; u < 2; ++u)
#pragma unroll
    for (int z = 0; z < 8; ++z)
      acc[u][z & 3] = MFMA16(__builtin_shufflevector(wh[(28 + (z >> 2)) % 3][2 * (z & 3)], wh[(28 + (z >> 2)) % 3][2 * (z & 3) + 1], 0, 1, 2, 3, 4, 5, 6, 7), oa[(28 + (z >> 2)) % OD][u], acc[u][z & 3]);
#pragma unroll
  for (int u = 0; u < 2; ++u)
#pragma unroll
    for (int pr = 0; pr < 2; ++pr) {
      const f32x4 c0 = (acc[u][2 * pr] + ac2[u][2 * pr]) + ac3[u][2 * pr], c1 = (acc[u][2 * pr + 1] + ac2[u][2 * pr + 1]) + ac3[u][2 * pr + 1];
      auto sa = __builtin_amdgcn_permlane16_swap(pack2(c0[0], c0[1]), pack2(c1[0], c1[1]), false, false);
      auto sb = __builtin_amdgcn_permlane16_swap(pack2(c0[2], c0[3]), pack2(c1[2], c1[3]), false, false);
      __builtin_amdgcn_raw_buffer_store_b128(u32x4{sa[0], sb[0], sa[1], sb[1]}, yrs, eoff + pr * 64 + u * 16 * DM * 2, 0, 16);
    }
  __builtin_amdgcn_sched_group_barrier(0x008, 9, 0);
  GRP(7, 1, 0x002, EV);
  SCHED0();
  RST(8);
  STAMP(9);
}

__device__ __forceinline__ unsigned med3(unsigned a, unsigned b, unsigned c) { return max(min(a, b), min(max(a, b), c)); }
#define PIN8(a) asm volatile("" : "+v"(a[0]), "+v"(a[1]), "+v"(a[2]), "+v"(a[3]), "+v"(a[4]), "+v"(a[5]), "+v"(a[6]), "+v"(a[7]))
__device__ __forceinline__ void tick(int& nb) {
  SCHED0();
  if (nb < NBAR) { BAR(); ++nb; if (HN) NAP(HN); }
  asm volatile("" ::: "memory");
  SCHED0();
}
__device__ __forceinline__ void hidden_half(char* smem, int lane, int l, int cg, char* ws, unsigned tag, u32x4 (&fd)[2][8]) {
  const rsrc_t wrs = mk_rsrc(ws, WS_O);
  const unsigned off = WS_GU + (unsigned)(cg * 32768 + l * 2048 + lane * 16);
  u32x4 ps[2][8];
  auto request = [&]() {
#pragma unroll
    for (int p = 0; p < 16; ++p) ps[p & 1][p >> 1] = ld16c(wrs, off + (p & 1) * 1024, (p >> 1) * 4096);
    SCHED0();
  };
  auto good = [&]() {
    unsigned bad = 0;
#pragma unroll
    for (int p = 0; p < 16; ++p) bad = chk(bad, ps[p & 1][p >> 1], tag);
    return __all(bad == 0);
  };
  if (PR) {
#pragma unroll 1
    for (unsigned n = 0; n < SPIN_CAP; ++n) {
      const unsigned t = __builtin_amdgcn_raw_buffer_load_b32(wrs, WS_GU + (unsigned)(cg * 32768 + (lane >> 3) * 4096 + l * 2048 + (lane & 7) * 256 + 132), 0, 16);
      if (__all(t == tag)) break;
      NAP(PR);
    }
  }
  request();
  if (!DN) { tr_all(fd[0]); tr_all(fd[1]); }
  SCHED0();
#pragma unroll 1
  for (unsigned n = 0; n < SPIN_CAP && !good(); ++n) { NAP(4); request(); }
  unsigned hp[2];
#pragma unroll
  for (int j = 0; j < 2; ++j) {
    float hv[2];
#pragma unroll
    for (int z = 0; z < 2; ++z) {
      const float gb = rbf((f_of(ps[j][0][2 * z]) + f_of(ps[j][2][2 * z])) + (f_of(ps[j][4][2 * z]) + f_of(ps[j][6][2 * z])));
      const float ub = rbf((f_of(ps[j][1][2 * z]) + f_of(ps[j][3][2 * z])) + (f_of(ps[j][5][2 * z]) + f_of(ps[j][7][2 * z])));
      hv[z] = rbf(gb / (1.0f + __expf(-gb))) * ub;
    }
    hp[j] = pack2(hv[0], hv[1]);
  }
#pragma unroll
  for (int j = 0; j < 2; ++j)
    *(unsigned*)(smem + L_HF + (lane & 7) * 128 + (((lane >> 4) & 1) * 4 + 2 * l + j) * 16 + ((lane >> 3) & 1) * 4 + (lane >> 5) * 8) = hp[j];
  lds_post(smem, F_H + l, 1);
}

struct Stager {
  rsrc_t xrs, qrs;
  LAS char* lds;
  unsigned va, vb;
  int arow, pa, pb;
  __device__ __forceinline__ Stager(char* smem, int l, int lane, int tm, int tn, const u16* x, const u16* w) {
    asm volatile("" : "+v"(lane));
    const int kr = lane >> 3;
    xrs = mk_rsrc(x, S_TOK * DM * 2); qrs = mk_rsrc(w, DM * NQKV * 2);
    lds = (LAS char*)smem + l * 1024;
    va = (unsigned)(kr + 8 * l) * 2048u + (((lane & 7) ^ (kr >> 1) ^ (l << 2)) << 4);
    vb = (unsigned)(kr + 8 * l) * 6144u + tn * 128 + (((lane & 7) ^ (((kr >> 1) & 1) << 1) ^ (l << 2)) << 4);
    arow = tm * 128 - 16;
    k_order(tm, tn, pa, pb);
  }
};
#define DM1(l, o) "s_add_u32 m0, %2, " #l "\n\ts_add_u32 %0, %5, " #o "\n\tbuffer_load_dwordx4 %3, %4, %0 offen lds\n\t"
#define DMA_(str, v_, rs_, so_, s0_) asm volatile("s_mov_b32 %1, m0\n\t" str "s_mov_b32 m0, %1" : "=&s"(tmp), "=&s"(keep) : "s"(base), "v"(v_), "s"(rs_), "s"(so_), "s"(s0_) : "scc", "memory")
__device__ __forceinline__ void stage_x(const Stager& g, int t) {
  const int kt = k_tile(t, g.pa, g.pb);
  const unsigned base = (unsigned)(size_t)g.lds + (t % 3) * XSL_;
  unsigned keep, tmp;
  DMA_("s_mov_b32 m0, %2\n\ts_nop 0\n\tbuffer_load_dwordx4 %3, %4, %6 offen lds\n\t" DM1(2048, 32768) DM1(4096, 65536) DM1(6144, 98304) DM1(8192, 131072)
       DM1(10240, 163840) DM1(12288, 196608) DM1(14336, 229376) DM1(16384, 262144), g.va, g.xrs, g.arow * 2048 + kt * 128, max(g.arow, 0) * 2048 + kt * 128);
}
__device__ __forceinline__ void stage_w(const Stager& g, int t) {
  const int kt = k_tile(t, g.pa, g.pb);
  const unsigned base = (unsigned)(size_t)g.lds + XB + (t & 3) * WSL;
  unsigned keep, tmp;
  DMA_(DM1(0, 0) DM1(2048, 98304) DM1(4096, 196608) DM1(6144, 294912) DM1(8192, 2048) DM1(10240, 100352) DM1(12288, 198656) DM1(14336, 296960)
       DM1(16384, 4096) DM1(18432, 102400) DM1(20480, 200704) DM1(22528, 299008), g.vb, g.qrs, kt * 64 * 6144, 0);
}
__device__ __forceinline__ void stage_w3(const Stager& g, int t, int z) {
  const int kt = k_tile(t, g.pa, g.pb);
  const unsigned base = (unsigned)(size_t)g.lds + XB + (t & 3) * WSL + z * 8192;
  unsigned keep, tmp;
  DMA_(DM1(0, 0) DM1(2048, 98304) DM1(4096, 196608) DM1(6144, 294912), g.vb, g.qrs, kt * 64 * 6144 + z * 2048, 0);
}
__device__ __forceinline__ int unit_of(int bid) {
  const int off = bid >> 3;
  return (off >> 2) == 3 ? off + 16 : (off >> 2) == 7 ? off - 16 : off;
}
__device__ constexpr int wo_early(int t) { return t < TW0 ? 0 : ((t - TW0) * NW < LF ? (t - TW0) * NW : LF); }
__device__ constexpr int wo_at(int n) { return (n < 28 ? L_WA + n * 2048 : n < 56 ? L_WB + (n - 28) * 2048 : L_W3 + (n - 56) * 2048); }
__device__ __forceinline__ void loader(char* smem, int l, int lane, int tm, int tn, bool unit, unsigned rot2, const u16* __restrict__ x,
                                       const u16* __restrict__ w, const u16* __restrict__ wo, char* ws) {
  const Stager stage(smem, l, lane, tm, tn, x, w);
  dma_tag(smem, F_FULL + l * 4, 0);
  const rsrc_t prs = mk_rsrc(wo, DM * DM * 2);
  const int kr = lane >> 3;
  const unsigned vo = (unsigned)((kr < 4 ? kr : 12 + kr) + 4 * l) * 2048u + tn * 128 + (((lane & 7) ^ (((kr >> 1) & 1) << 1) ^ (l << 2)) << 4);
  u32x4 pc[LF + 1];
  int oth = (tn & 8) * 2 ^ 16;
  asm volatile("" : "+s"(oth));
  auto source = [&](int n) {
    const int k = n >> 1, half = k < 14 ? 0 : k < 28 ? 16 : k >= 30 ? (tn & 8) * 2 : oth;
    return (unsigned)((half + ((rot2 >> ((k < 14 ? k : k < 28 ? k - 14 : 14) & 14) * 2) & 14) + (k & 1)) * 32 + 8 * (n & 1)) * 2048;
  };
  auto request = [&](int i) {
    asm volatile("s_nop 4\n\tbuffer_load_dwordx4 %0, %1, %2, %3 offen" : "=v"(pc[i]) : "v"(vo), "s"(prs), "s"(source(i)) : "memory");
  };
  auto fetch = [&](int n) {
    unsigned keep;
    asm volatile("s_mov_b32 %0, m0\n\ts_add_u32 m0, %4, %5\n\ts_nop 2\n\tbuffer_load_dwordx4 %1, %2, %3 offen lds\n\ts_mov_b32 m0, %0"
                 : "=&s"(keep) : "v"(vo), "s"(prs), "s"(source(n)), "s"((unsigned)(size_t)stage.lds), "i"(wo_at(n)) : "scc", "memory");
  };
  static_assert(L_WA + 24 * 2048 == XB && L_WB == XB + WSL && L_OWN + 16384 <= L_XCH && LF <= 24, "layout");
  unsigned plan = L0 | L1 << 2;
  if (unit) {
    const unsigned d0 = (DUE >> ((tn & 3) * 4)) & 15;
    plan |= ((LA | LB << 2) * 0x101u) << (2 * d0 + 2);
    if (LC == 0) plan |= (LA | LB << 2) << (2 * min(d0, 2u) + 18);
  }
#pragma unroll
  for (int t = -1; t < NBAR - 2; ++t) {
    const unsigned lag = (plan >> (2 * t + 2)) & 3;
    if (t >= 0) stage_x(stage, t + 3);
    if (t == NBAR - 3) dma_tag(smem, F_FULL + l * 4, 15);
    SCHED0();
#pragma unroll
    for (int i = wo_early(t); i < wo_early(t + 1); ++i) request(i);
#pragma unroll
    for (int z = 3; z >= 0; --z) {
      if (z < 3) stage_w3(stage, t + 4, 2 - z);
      SCHED0();
      if (lag == (unsigned)z) {
        WAITV(33 - 4 * z + (t <= 0) + (t == NBAR - 3) + wo_early(t + 1) - wo_early(t - 1));
        BAR();
      }
      SCHED0();
    }
  }
  unsigned tp = 0, seen = 0;
  stage_x(stage, 15);
  dma_tag(smem, F_FULL + l * 4, 16);
  SCHED0();
#pragma unroll
  for (int y = 0; y < 12; ++y) fetch(y < 8 ? 63 - y : 16 + y);
  dma_tag(smem, F_FULL + l * 4, 17);
#pragma unroll
  for (int i = wo_early(NBAR - 2); i < LF; ++i) request(i);
  const unsigned tag = uni(lds_word(smem, F_TAG));
  wait_bytes<0x80808080u>(smem, F_FREE, 16);
#pragma unroll
  for (int n = LF; n < 24; ++n) fetch(n);
  if (LF) {
    const unsigned d0 = lds_addr(smem + L_WA + l * 1024 + lane * 16);
    WAITV(24 - LF);
#pragma unroll
    for (int i = 0; i < LF; ++i) asm volatile("ds_write_b128 %1, %0 offset:%2" : "+v"(pc[i]) : "v"(d0), "i"(i * 2048) : "memory");
    WAITL0();
  }
  dma_tag(smem, F_WO + l * 4, 1);
#pragma unroll
  for (int n = 28; n < 28 + NB1; ++n) fetch(n);
  if (l == 0) {
    const unsigned v1 = (vo + 4 * 2048) ^ 64;
    wait_bytes<0x80808080u>(smem, F_OL, 1);
#pragma unroll
    for (int n = 28 + NB1; n < 56; ++n) {
      unsigned keep;
      asm volatile("s_mov_b32 %0, m0\n\ts_add_u32 m0, %4, %5\n\ts_nop 0\n\tbuffer_load_dwordx4 %1, %2, %3 offen lds\n\ts_add_u32 m0, m0, 1024\n\ts_nop 0\n\tbuffer_load_dwordx4 %6, %2, %3 offen lds\n\ts_mov_b32 m0, %0"
                   : "=&s"(keep) : "v"(vo), "s"(prs), "s"(source(n)), "s"((unsigned)(size_t)stage.lds), "i"(wo_at(n)), "v"(v1) : "scc", "memory");
      if ((n - 27 - NB1) % GS == 0 && n < 55) WAITV(GW);
    }
    if (NB1) wait_bytes<0x80u>(smem, F_WO + 12, 1);
    dma_tag(smem, F_WO + 8, 1);
  } else if (NB1) dma_tag(smem, F_WO + 12, 1);
  __builtin_amdgcn_s_setprio(1);
  if (l) await_attention(smem, lane, tm, tn, ws, tag, seen, tp);
}

__device__ __forceinline__ void updates(char* smem, int w, int lane, int tm, int tn, char* ws) {
  const rsrc_t wrs = mk_rsrc(ws, WS_O);
  const unsigned tag = uni(lds_word(smem, F_TAG));
  const int g = lane >> 4, li = lane & 15;
  wait_bytes<0x80u>(smem, F_SELOK, 1);
  const unsigned selkey = *(const unsigned*)(smem + F_SEL + (lane & 7) * 4);
  unsigned mine = (unsigned)__ballot(((2047u - (selkey & 0xffffu)) >> 7) == (unsigned)tm) & 0xffu;
#pragma unroll
  for (int i = 0; i < 3; ++i) mine &= i < w ? mine - 1 : ~0u;
  if (mine != 0 && w < 3) {
    wait_bytes<0x80u>(smem, F_GO, 1);
    NAP(YD);
  }
  for (; mine != 0; mine &= mine - 1, mine &= mine - 1, mine &= mine - 1, mine &= mine - 1) {
    const int c = __builtin_ctz(mine);
    const unsigned yoff = WS_Y + (unsigned)((g * 14 * 8 + c) * DM + tn * 64 + li * 4) * 4u;
    u32x4 yv[14];
    const unsigned key = (unsigned)__builtin_amdgcn_readlane((int)selkey, c), kb = key >> 16;
#pragma unroll 1
    for (unsigned n = 0; n < SPIN_CAP; ++n) {
      unsigned bad = 0;
#pragma unroll
      for (int j = 0; j < 14; ++j) yv[j] = ld16c(wrs, yoff, j * (8 * DM * 4));
#pragma unroll
      for (int j = 0; j < 14; ++j) bad = chk(bad, yv[j], tag);
      if (__all(bad == 0)) break;
      NAP(8);
    }
    float y[4] = {0.f, 0.f, 0.f, 0.f};
#pragma unroll
    for (int j = 0; j < 14; ++j) { y[0] += lo2f(yv[j][0]); y[1] += hi2f(yv[j][0]); y[2] += lo2f(yv[j][2]); y[3] += hi2f(yv[j][2]); }
    const float r = __uint_as_float(((kb & 0x8000u) ? (kb & 0x7fffu) : (~kb & 0xffffu)) << 16);
    const float rs = rbf(1.0f / (1.0f + __expf(-r)));
#pragma unroll
    for (int e = 0; e < 4; ++e) y[e] = rbf(rbf(xsum16_32(y[e])) * rs);
    wait_bytes<0x80808080u>(smem, F_FREE, 16);
    *(f32x4*)(smem + L_UP + c * 256 + li * 16) = f32x4{y[0], y[1], y[2], y[3]};
  }
  lds_post(smem, F_UP + w, 1);
  STAMP(7 + (w == 0 || w == 3 ? -5 : 0));
}

__device__ __forceinline__ void ld8k(u32x4 (&d)[8], rsrc_t rs, unsigned off) {
#define L1K(i, so, o) "buffer_load_dwordx4 %" #i ", %8, %9, " so " offen offset:" #o " sc1\n\t"
  asm volatile("s_nop 4\n\t" L1K(0, "0", 0) L1K(1, "0", 1024) L1K(2, "0", 2048) L1K(3, "0", 3072) L1K(4, "%10", 0) L1K(5, "%10", 1024) L1K(6, "%10", 2048) L1K(7, "%10", 3072)
               : "=&v"(d[0]), "=&v"(d[1]), "=&v"(d[2]), "=&v"(d[3]), "=&v"(d[4]), "=&v"(d[5]), "=&v"(d[6]), "=&v"(d[7]) : "v"(off), "s"(rs), "s"(4096) : "memory");
  SCHED0();
}
__device__ __forceinline__ void side_wave(char* smem, int F, int lane, int bid, int tm, int tn, const u16* __restrict__ x,
                                          const u16* __restrict__ wgu, const u16* __restrict__ wd, char* ws) {
  const rsrc_t wrs = mk_rsrc(ws, WS_O), xrs = mk_rsrc(x, S_TOK * DM * 2);
  const int u = unit_of(bid), cg = (bid & 7) * 7 + ((u >> 2) % 7), q = u & 3;
  const int g = lane >> 4, li = lane & 15;
  u32x4 fw[4][8];
  const rsrc_t grs = mk_rsrc(wgu, u < 28 ? DM * FF * 2 : 0);
  const int cu = (bid & 7) * 7 + (((u >> 2) + F) % 7);
  const unsigned fo = (unsigned)(q * 256 + (li >> 3) * 128 + 8 * g) * (FF * 2) + cu * 128 + (li & 7) * 16;
#pragma unroll
  for (int s = 0; s < 4; ++s) asm volatile("" : "=v"(fw[s][0]), "=v"(fw[s][1]), "=v"(fw[s][2]), "=v"(fw[s][3]), "=v"(fw[s][4]), "=v"(fw[s][5]), "=v"(fw[s][6]), "=v"(fw[s][7]));
  const int csb = WS_CS + ((tn * 2 + F) * 16 + (tm & 1) * 8) * 1024;
  u32x4 cs[8], rec[2];
  unsigned ca = csb + min(lane, 47) * 16, cb = ca + 4096, ra = WS_R + (unsigned)max(tm * 128 - 16 + F * 72 + lane, 0) * 16, rb = WS_R + (unsigned)max(tm * 128 - 8 + F * 72 + lane, 0) * 16;
  asm volatile("" : "+v"(ca), "+v"(cb), "+v"(ra), "+v"(rb));
  unsigned due = (DUE >> ((tn & 3) * 4)) & 15, iss = 0;
#define FW(n) fw[(n) >> 3][(n) & 7]
#define LD(i) "s_mov_b32 %[t], (((%[n0]+" #i ")>>3)*32+((%[n0]+" #i ")&7))*7168\n\tbuffer_load_dwordx4 %" #i ", %[fo], %[rs], %[t] offen nt\n\t"
#define LD8 LD(0) LD(1) LD(2) LD(3) LD(4) LD(5) LD(6) LD(7)
#define R8(n) "+v"(FW(n)), "+v"(FW(n + 1)), "+v"(FW(n + 2)), "+v"(FW(n + 3)), "+v"(FW(n + 4)), "+v"(FW(n + 5)), "+v"(FW(n + 6)), "+v"(FW(n + 7))
#define SITE(lds_, ...) asm volatile("s_cmp_lg_u32 %[iss], %[k]\n\ts_cbranch_scc1 1f\n\ts_cmp_le_u32 %[nb], %[due]\n\ts_cbranch_scc1 1f\n\ts_sleep %[dly]\n\t" lds_ \
    "s_add_u32 %[iss], %[iss], 1\n\ts_add_u32 %[due], %[due], 4\n\t1:" : __VA_ARGS__, [iss] "+s"(iss), [due] "+s"(due), [t] "=&s"(t) \
    : [fo] "v"(fo), [rs] "s"(grs), [nb] "s"(nb), [k] "i"(K), [n0] "i"(n0), [dly] "i"(D), [ca] "v"(ca), [cb] "v"(cb), [ra] "v"(ra), [rb] "v"(rb), [ws] "s"(wrs) : "scc", "memory")
#define C1K(i, a, o) "buffer_load_dwordx4 %" #i ", %[" #a "], %[ws], 0 offen offset:" #o " sc1\n\t"
#define G2N C1K(0, ca, 0) C1K(1, ca, 1024) C1K(2, ca, 2048) C1K(3, ca, 3072) C1K(4, cb, 0) C1K(5, cb, 1024) C1K(6, cb, 2048) C1K(7, cb, 3072) C1K(8, ra, 0) C1K(9, rb, 0)
#define G2 C1K(8, ca, 0) C1K(9, ca, 1024) C1K(10, ca, 2048) C1K(11, ca, 3072) C1K(12, cb, 0) C1K(13, cb, 1024) C1K(14, cb, 2048) C1K(15, cb, 3072) C1K(16, ra, 0) C1K(17, rb, 0)
  auto site = [&](auto kk, auto dd, unsigned nb) {
    constexpr int K = decltype(kk)::v, D = decltype(dd)::v, n0 = K == 0 ? 0 : K == 3 ? 16 : 8 + K * 8;
    unsigned t;
    SCHED0();
    if constexpr (K == 1 && LC == 3) SITE(G2N, "+v"(cs[0]), "+v"(cs[1]), "+v"(cs[2]), "+v"(cs[3]), "+v"(cs[4]), "+v"(cs[5]), "+v"(cs[6]), "+v"(cs[7]), "+v"(rec[0]), "+v"(rec[1]));
    else if constexpr (K == 3) SITE(LD8, R8(16));
    else if constexpr (K == 1) SITE(G2 LD8, R8(n0), "+v"(cs[0]), "+v"(cs[1]), "+v"(cs[2]), "+v"(cs[3]), "+v"(cs[4]), "+v"(cs[5]), "+v"(cs[6]), "+v"(cs[7]), "+v"(rec[0]), "+v"(rec[1]));
    else if constexpr (K == 2) SITE(LD8, R8(n0));
    else SITE(LD8 LD(8) LD(9) LD(10) LD(11) LD(12) LD(13) LD(14) LD(15), R8(n0), R8(n0 + 8));
    SCHED0();
  };
#define CH(k, d, n) site(IC<k>{}, IC<d>{}, n)
#define WAITD(K, d) asm volatile("s_waitcnt vmcnt(%1)\n\ts_cmp_lg_u32 %0, %3\n\ts_cbranch_scc1 1f\n\ts_waitcnt vmcnt(%2)\n\t1:" : "+s"(iss) : "i"((K) + 8), "i"(K), "i"(d) : "scc", "memory")
  unsigned tag;
  {
    const int qq = (lane >> 2) & 3, hk = (qq >> 1) | ((g & 1) << 1);
    const char* pb = smem + XB + (8 * g + qq) * 128 + 8 * (lane & 3);
    const bf16x8 ones = {0x3f80, 0x3f80, 0x3f80, 0x3f80, 0x3f80, 0x3f80, 0x3f80, 0x3f80};
    f32x4 a[6];
#pragma unroll
    for (int t = 0; t < 2; ++t) {
      SCHED0(); BAR(); SCHED0();
      __builtin_amdgcn_s_setprio(3);
#pragma unroll
      for (int c = 0; c < 6; ++c) {
        const char* p = pb + t * WSL + (c >> 1) * 8192 + (((2 * F + (c & 1)) ^ hk) << 5);
        a[c] = MFMA16(ones, lds_tr2(p), t ? a[c] : (f32x4{0, 0, 0, 0}));
        a[c] = MFMA16(ones, lds_tr2(p + 4096), a[c]);
      }
      asm volatile("" : "+v"(a[0]), "+v"(a[1]), "+v"(a[2]), "+v"(a[3]), "+v"(a[4]), "+v"(a[5]));
      if (t == 0) { tag = uni(lds_word(smem, F_TAG)); CH(0, CD, 1); }
    }
    const unsigned off = csb + (tm >> 1) * 1024 + lane * 8;
    st8c(wrs, off, __float_as_uint(g == 0 ? a[0][0] : g == 1 ? a[1][0] : g == 2 ? a[2][0] : a[3][0]), tag);
    st8c(wrs, lane < 32 ? off + 512 : 0x7fffffffu, __float_as_uint(g == 0 ? a[4][0] : a[5][0]), tag);
    __builtin_amdgcn_s_setprio(0);
    CH(0, CD, 2);
  }
#define SST(i)
  int nb = 2;
  tick(nb);
  const unsigned xs_off = WS_S + (unsigned)(((bid & 7) * 4 + q) * 6144 + lane * 16);
  u32x4 tc;
  uint2 tq[2];
  unsigned ta[3], tv, todo = 1;
  auto gather2 = [&]() {
    asm volatile(G2N : "=&v"(cs[0]), "=&v"(cs[1]), "=&v"(cs[2]), "=&v"(cs[3]), "=&v"(cs[4]), "=&v"(cs[5]), "=&v"(cs[6]), "=&v"(cs[7]), "=&v"(rec[0]), "=&v"(rec[1])
                 : [ca] "v"(ca), [cb] "v"(cb), [ra] "v"(ra), [rb] "v"(rb), [ws] "s"(wrs) : "memory");
    SCHED0();
  };
  auto prepare = [&]() {
#pragma unroll 1
    for (unsigned n = 0; n < SPIN_CAP; ++n) {
      unsigned bad = chk(chk(0u, rec[0], tag), rec[1], tag);
#pragma unroll
      for (int j = 0; j < 8; ++j) bad = chk(bad, cs[j], tag);
      if (__all(bad == 0)) break;
      NAP(8);
      gather2();
      tick(nb); tick(nb); WAITV(0); asm volatile("" : "+v"(rec[0]), "+v"(rec[1])); PIN8(cs);
    }
    float csum[2] = {f_of(cs[0][0]), f_of(cs[0][2])};
#pragma unroll
    for (int r = 1; r < 8; ++r) { csum[0] = opq(csum[0] + f_of(cs[r][0])); csum[1] = opq(csum[1] + f_of(cs[r][2])); }
    const float h0 = rbf(csum[0]), d0 = opq(csum[0] - h0), c0 = rbf(d0), h1 = rbf(csum[1]), d1 = opq(csum[1] - h1), c1 = rbf(d1);
    tc = u32x4{pack2(h0, c0), pack2(opq(d0 - c0), 0.f), pack2(h1, c1), pack2(opq(d1 - c1), 0.f)};
    asm volatile("" : "+v"(tc));
#pragma unroll
    for (int j = 0; j < 2; ++j) {
      const unsigned nm = (rec[j][0] & 0xffffu) ^ 0x8000u;
      tq[j] = make_uint2(nm | (nm << 16), __float_as_uint(__builtin_amdgcn_rcpf(f_of(rec[j][2]))));
      asm volatile("" : "+v"(tq[j].x), "+v"(tq[j].y));
    }
    ta[0] = lds_addr(smem + L_CSF + (min(lane >> 4, 2) * 64 + F * 32 + (lane < 48 ? li : 15) * 2) * 8);
    ta[1] = lds_addr(smem + L_QST + (F * 72 + lane) * 8);
    ta[2] = lds_addr(smem + (lane < 8 ? F_SEL + lane * 4 : lane == 8 ? F_SELOK : F_ST + F * 4));
    asm volatile("" : "+v"(ta[0]), "+v"(ta[1]), "+v"(ta[2]));
    SCHED0();
  };
  auto tables = [&](unsigned now) {
    asm volatile("s_and_b32 %0, %0, %8\n\ts_cbranch_scc0 1f\n\tds_write_b128 %1, %2\n\tds_write2_b64 %3, %4, %5 offset1:8\n\ts_mov_b64 exec, 0x3ff\n\tds_write_b32 %6, %7\n\ts_mov_b64 exec, -1\n\t1:"
                 : "+s"(now) : "v"(ta[0]), "v"(tc), "v"(ta[1]), "v"(tq[0]), "v"(tq[1]), "v"(ta[2]), "v"(tv), "s"(todo) : "scc", "memory");
    todo &= ~now;
  };
  auto slice = [&]() {
    if (nb < NBAR) tick(nb);
    else { const u32x2 fl = *(const volatile LAS u32x2*)(LAS const char*)(smem + F_FULL); tables(uni(min(fl[0], fl[1])) >> 4); SCHED0(); }
  };
  if (u >= 28) {
    u32x4 kd[4];
#pragma unroll 1
    for (unsigned n = 0; n < SPIN_CAP; ++n) {
#define L1K(i, o) "buffer_load_dwordx4 %" #i ", %4, %5, 0 offen offset:" #o " sc1\n\t"
      asm volatile("s_nop 4\n\t" L1K(0, 0) L1K(1, 1024) L1K(2, 2048) L1K(3, 3072)
                   : "=&v"(kd[0]), "=&v"(kd[1]), "=&v"(kd[2]), "=&v"(kd[3]) : "v"(WS_K + F * 4096 + lane * 16), "s"(wrs) : "memory");
#undef L1K
      tick(nb);
      asm volatile("s_waitcnt vmcnt(0)" : "+v"(kd[0]), "+v"(kd[1]), "+v"(kd[2]), "+v"(kd[3]) :: "memory");
      if (__all(chk(chk(chk(chk(0u, kd[0], tag), kd[1], tag), kd[2], tag), kd[3], tag) == 0)) break;
      NAP(4);
    }
    SST(8);
    unsigned m1 = 0, m2 = 0, m3 = 0, m4 = 0;
#pragma unroll
    for (int j = 0; j < 4; ++j) {
      kd[j] = u32x4{(kd[j][0] << 16) | (15 - 4 * j), (kd[j][0] & 0xffff0000u) | (14 - 4 * j), (kd[j][2] << 16) | (13 - 4 * j), (kd[j][2] & 0xffff0000u) | (12 - 4 * j)};
#pragma unroll
      for (int i = 0; i < 4; ++i) {
        const unsigned v = kd[j][i];
        m4 = med3(m3, m4, v); m3 = med3(m2, m3, v); m2 = med3(m1, m2, v); m1 = max(m1, v);
      }
    }
    const unsigned lb = ((63 - lane) << 5) | (F ? 0 : 16);
    m1 |= lb; m2 |= lb; m3 |= lb; m4 |= lb;
    asm volatile("" : "+v"(m1), "+v"(m2), "+v"(m3), "+v"(m4));
    tick(nb);
    unsigned best = 0;
#pragma unroll
    for (int r = 0; r < 8; ++r) {
      const unsigned wm = wave_max_u32(m1);
      best = (lane & 7) == r ? wm : best;
      const bool won = m1 == wm;
      m1 = won ? m2 : m1; m2 = won ? m3 : m2; m3 = won ? m4 : m3; m4 = won ? 0u : m4;
      if (r == 3) { asm volatile("" : "+v"(m1), "+v"(m2), "+v"(m3), "+v"(m4), "+v"(best)); tick(nb); }
    }
    if (__any(m1 == 0)) {
      unsigned wm = 0;
#pragma unroll 1
      for (int r = 0; r < 8; ++r) {
        if (r & 1) tick(nb);
        unsigned m = 0;
#pragma unroll
        for (int j = 0; j < 4; ++j)
#pragma unroll
          for (int i = 0; i < 4; ++i) m = max(m, (kd[j][i] | lb) - wm);
        wm += wave_max_u32(m);
        best = (lane & 7) == r ? wm : best;
      }
    }
    *(volatile LAS unsigned*)(LAS char*)(smem + F_CND + (F ? 60 - (lane & 7) * 4 : (lane & 7) * 4)) = best + 1;
    unsigned d = 0;
    if (TCH & 1) {
      const unsigned ct = 2047u - (best & 0xffffu);
      asm volatile("buffer_load_dword %0, %1, %2, 0 offen\n\tbuffer_load_dword %0, %3, %4, 0 offen sc1" : "=&v"(d) : "v"(lane < 32 ? ct * 2048u + q * 512 + (lane >> 3) * 128 : 0x7fffffffu), "s"(xrs), "v"(lane < 8 ? WS_R + ct * 16 : 0x7fffffffu), "s"(wrs) : "memory");
    }
    SST(9);
    gather2();
    unsigned ka, kb;
#pragma unroll 1
    for (unsigned n = 0; n < SPIN_CAP; ++n) {
      tick(nb);
      ka = *(const volatile LAS unsigned*)(LAS const char*)(smem + F_CND + (lane & 7) * 4);
      kb = *(const volatile LAS unsigned*)(LAS const char*)(smem + F_CND + 32 + (lane & 7) * 4);
      if (__all(min(ka, kb) != 0)) break;
      NAP(1);
    }
    const unsigned selkey = max(ka, kb) - 1, tok = 2047u - (selkey & 0xffffu);
    u32x4 xf[2], trec;
#pragma unroll 1
    for (unsigned n = 0; n < SPIN_CAP; ++n) {
      asm volatile("buffer_load_dwordx4 %0, %3, %4, 0 offen\n\tbuffer_load_dwordx4 %1, %3, %4, 0 offen offset:64\n\tbuffer_load_dwordx4 %2, %5, %6, 0 offen sc1"
                   : "=&v"(xf[0]), "=&v"(xf[1]), "=&v"(trec) : "v"(tok * 2048u + q * 512 + (li >> 3) * 256 + g * 16 + F * 128), "s"(xrs), "v"(WS_R + tok * 16), "s"(wrs) : "memory");
      if (!KQ || n) tick(nb);
      asm volatile("s_waitcnt vmcnt(0)" : "+v"(xf[0]), "+v"(xf[1]), "+v"(trec), "+v"(rec[0]), "+v"(rec[1]), "+v"(d) :: "memory"); PIN8(cs);
      if (__all(chk(0u, trec, tag) == 0)) break;
      NAP(1);
    }
    const float mb = lo2f(trec[0]), rd = 1.0f / f_of(trec[2]);
#pragma unroll
    for (int s = 0; s < 2; ++s)
#pragma unroll
      for (int j = 0; j < 4; ++j) xf[s][j] = ln2s(xf[s][j], mb, rd);
    st16c(wrs, xs_off + F * 3072, u32x4{xf[0][0], xf[0][1], xf[0][2], tag});
    st16c(wrs, xs_off + F * 3072 + 1024, u32x4{xf[1][0], xf[1][1], xf[1][2], tag});
    st16c(wrs, xs_off + F * 3072 + 2048, u32x4{xf[0][3], xf[1][3], selkey, tag});
    tv = lane < 8 ? selkey : 1u;
    SST(2);
    tick(nb);
    if (TCH & 2) {
      asm volatile("s_waitcnt vmcnt(0)\n\tbuffer_load_dword %0, %1, %2, 0 offen sc1" : "=&v"(d) : "v"(lane < 24 ? xs_off + F * 3072 + lane * 112 : 0x7fffffffu), "s"(wrs) : "memory");
    }
    prepare();
    asm volatile("s_waitcnt vmcnt(0)" : "+v"(d) :: "memory");
    while (nb < NBAR) tick(nb);
    __builtin_amdgcn_s_setprio(1);
    wait_full(smem, F_FULL, 16);
    tables(1);
  } else {
    u32x4 xs[6];
    auto ask = [&]() {
#define X1K(i, o) "buffer_load_dwordx4 %" #i ", %6, %7, 0 offen offset:" #o " sc1\n\t"
#define X2K(i, o) "buffer_load_dwordx4 %" #i ", %6, %7, %8 offen offset:" #o " sc1\n\t"
      asm volatile(X1K(0, 0) X1K(1, 1024) X1K(2, 2048) X1K(3, 3072) X2K(4, 0) X2K(5, 1024)
                   : "=&v"(xs[0]), "=&v"(xs[1]), "=&v"(xs[2]), "=&v"(xs[3]), "=&v"(xs[4]), "=&v"(xs[5]) : "v"(xs_off), "s"(wrs), "s"(4096) : "memory");
      SCHED0();
    };
    auto good = [&]() {
      unsigned bad = xs[0][3] ^ tag;
#pragma unroll
      for (int j = 1; j < 6; ++j) bad = __builtin_amdgcn_bitop3_b32(bad, xs[j][3], tag, 0xF6);
      return __all(bad == 0);
    };
#define PIN6(a) asm volatile("" : "+v"(a[0]), "+v"(a[1]), "+v"(a[2]), "+v"(a[3]), "+v"(a[4]), "+v"(a[5]))
#define WAITS(snap, n) asm volatile("s_waitcnt vmcnt(%2)\n\ts_cmp_lg_u32 %0, %1\n\ts_cbranch_scc1 1f\n\ts_waitcnt vmcnt(0)\n\t1:" : "+s"(iss) : "s"(snap), "i"(n) : "scc", "memory")
    asm volatile("" : "=v"(cs[0]), "=v"(cs[1]), "=v"(cs[2]), "=v"(cs[3]), "=v"(cs[4]), "=v"(cs[5]), "=v"(cs[6]), "=v"(cs[7]), "=v"(rec[0]), "=v"(rec[1]));
    CH(0, DL - HN, 3);
    tick(nb);
    CH(0, DL - HN, 99);
    tick(nb);
    CH(1, DL - HN, nb);
    if (MG) CH(2, 0, 99);
    tick(nb);
    WAITS(1, MG ? 26 : LC == 3 ? 10 : 18); PIN8(fw[0]); PIN8(fw[1]);
    tr_all(fw[0]);
    CH(1, (DL > 7 ? DL - 7 : 0), nb);
    if (MG) CH(2, 0, 99);
    tick(nb);
    tr_all(fw[1]);
    CH(1, (DL > 7 ? DL - 7 : 0), nb);
    if (MG) CH(2, 0, 99);
    tick(nb);
    CH(1, DL - HN, 99);
    if (MG) CH(2, 0, 99);
    tick(nb);
    if (LC == 0) CH(2, DL - HN, nb);
    tick(nb);
    WAITS(2, 8);
    asm volatile("" : "+v"(rec[0]), "+v"(rec[1])); PIN8(cs); if (LC < 3) PIN8(fw[2]);
    if (!SD) ask();
    prepare();
    if (SD) ask();
    unsigned snap = iss;
    asm volatile("" : "+s"(snap));
    if (LC == 0) CH(2, 0, nb);
    tick(nb);
    WAITS(snap, 8); PIN6(xs);
    SST(1);
    if (!good()) {
      if (LC == 0) CH(2, 0, 99);
#pragma unroll 1
      for (unsigned n = 0; n < SPIN_CAP; ++n) {
        ask(); tick(nb); WAITV(0); PIN6(xs);
        if (good()) break;
        NAP(4);
      }
    }
    SST(2);
    tv = lane < 8 ? xs[2][2] : 1u;
    xs[0][3] = xs[2][0]; xs[1][3] = xs[2][1]; xs[3][3] = xs[5][0]; xs[4][3] = xs[5][1];
    PIN6(xs); asm volatile("" : "+v"(tv));
    if (nb == NBAR) { __builtin_amdgcn_s_setprio(1); wait_full(smem, F_FULL, 16); tables(1); }
    f32x4 acc[8];
    auto mma = [&](int s) {
      SCHED0(); __builtin_amdgcn_s_setprio(3); SCHED0();
#pragma unroll
      for (int i = 0; i < 8; ++i) acc[i] = MFMA16(__builtin_bit_cast(bf16x8, fw[s][i]), __builtin_bit_cast(bf16x8, xs[s + (s >> 1)]), s ? acc[i] : (f32x4{0, 0, 0, 0}));
      SCHED0(); __builtin_amdgcn_s_setprio(0); SCHED0();
      PIN8(acc);
    };
    mma(0);
    if (LC < 3) tr_all(fw[2]);
    if (LC == 0) CH(2, (DL > 9 ? DL - 9 : 0), 99);
    slice();
    mma(1);
    if (LC < 2) mma(2);
    if (LC == 1) { PIN8(fw[3]); tr_all(fw[3]); }
    slice();
    if (LC == 0) { WAITV(0); PIN8(fw[3]); tr_all(fw[3]); }
    if (LC < 2) mma(3);
    if (LC == 2) mma(2);
    auto finish = [&]() {
#pragma unroll
      for (int r = 0; r < 4; ++r) {
        float v[2];
#pragma unroll
        for (int z = 0; z < 2; ++z) {
          auto a = __builtin_amdgcn_permlane32_swap(__float_as_uint(acc[z][r]), __float_as_uint(acc[z + 4][r]), false, false);
          auto b = __builtin_amdgcn_permlane32_swap(__float_as_uint(acc[z + 2][r]), __float_as_uint(acc[z + 6][r]), false, false);
          v[z] = f_of(li < 8 ? a[0] : b[1]) + dpp_f<0x128>(f_of(li < 8 ? b[0] : a[1]));
        }
        st16c(wrs, WS_GU + (unsigned)(((cu * 4 + q) * 2 + F) * 4096 + r * 1024 + lane * 16), u32x4{__float_as_uint(v[0]), tag, __float_as_uint(v[1]), tag});
      }
      SCHED0();
      SST(3);
    };
    if (LC < 2 && EP == 1) finish();
    while (nb < NBAR) tick(nb);
    if (LC < 2 && EP == 0) { slice(); finish(); }
    __builtin_amdgcn_s_setprio(1);
    u32x4 fd[2][8];
    auto down = [&]() {
      const rsrc_t drs = mk_rsrc(wd, DM * FF * 2);
      const unsigned dof = (unsigned)(cg * 64 + 8 * g) * (DM * 2) + q * 512 + F * 256 + li * 16;
#pragma unroll
      for (int s = 0; s < 2; ++s)
#pragma unroll
        for (int j = 0; j < 8; ++j) fd[s][j] = __builtin_amdgcn_raw_buffer_load_b128(drs, dof + (j & 1) * (DM * 2), (s * 32 + (j & 6)) * (DM * 2), 2);
      SCHED0();
    };
    if (!DN) { wait_full(smem, F_FULL, 15); down(); }
    wait_full(smem, F_FULL, 16);
    tables(1);
    SST(4);
    if (LC == 3) { WAITV(24); PIN8(fw[2]); tr_all(fw[2]); mma(2); }
    if (LC >= 2) { WAITV(16); PIN8(fw[3]); tr_all(fw[3]); mma(3); __builtin_amdgcn_s_setprio(1); finish(); }
    if (bid == 0 && F == 0 && lane == 0) __hip_atomic_store((GAS unsigned*)(ws + WS_EP), tag, RLX_AGENT);
    hidden_half(smem, lane, F, cg, ws, tag, fd);
    SST(5);
    if (DN) { wait_bytes<0x80808080u>(smem, F_OL, 1); down(); tr_all(fd[0]); tr_all(fd[1]); }
    wait_bytes<0x8080u>(smem, F_H, 1);
    const bf16x8 h0 = lds_b128(smem + L_HF + (li & 7) * 128 + g * 16), h1 = lds_b128(smem + L_HF + (li & 7) * 128 + 64 + g * 16);
#pragma unroll
    for (int i = 0; i < 8; ++i) {
      acc[i] = MFMA16(h0, __builtin_bit_cast(bf16x8, fd[0][i]), (f32x4{0, 0, 0, 0}));
      acc[i] = MFMA16(h1, __builtin_bit_cast(bf16x8, fd[1][i]), acc[i]);
    }
    const unsigned off = WS_Y + (unsigned)((cg * 8 + (g & 1) * 4) * DM + q * 256 + F * 128 + li * 8 + (g >> 1) * 4) * 4u;
#pragma unroll
    for (int r = 0; r < 4; ++r) {
      const unsigned lo = pack2(acc[0][r], acc[1][r]), hi = pack2(acc[2][r], acc[3][r]), lo2 = pack2(acc[4][r], acc[5][r]), hi2 = pack2(acc[6][r], acc[7][r]);
      st16c(wrs, off + r * (DM * 4), u32x4{g < 2 ? lo : lo2, tag, g < 2 ? hi : hi2, tag});
    }
    SST(6);
  }
  __builtin_amdgcn_s_waitcnt(0x0F70);
}

__global__ __launch_bounds__(512, 2) void fused_block(const u16* __restrict__ x, const u16* __restrict__ wqkv, const u16* __restrict__ wo,
                                                      const u16* __restrict__ wr, const u16* __restrict__ wg, const u16* __restrict__ wu,
                                                      const u16* __restrict__ wd, char* ws, u16* __restrict__ out) {
  __shared__ __attribute__((aligned(128))) char smem[L_SIZE];
  unsigned long long pc;
  unsigned first;
  asm volatile("v_cmp_gt_u32 vcc, 24, %2\n\ts_getpc_b64 %0\n\ts_cbranch_vccz 1f\n\ts_mov_b64 exec, vcc\n\tv_lshlrev_b32 %1, 7, %2\n\tglobal_load_dword %1, %1, %0\n\ts_mov_b64 exec, -1\n\t1:"
               : "=s"(pc), "=&v"(first) : "v"(threadIdx.x) : "vcc", "memory");
  SCHED0();
  const int lane = threadIdx.x & 63;
  const int wid = __builtin_amdgcn_readfirstlane(threadIdx.x >> 6);
  const int bid = blockIdx.x, xcd = bid & 7, off = bid >> 3;
  const int tm = (xcd & 1) * 8 + (xcd & 2) * 2 + (off & 3), tn = (xcd >> 2) * 8 + (off >> 2);
  STAMP(0);
  if (wid == 7) {
    unsigned d;
    asm volatile("s_getpc_b64 %0" : "=s"(pc));
    asm volatile("buffer_load_dword %0, %1, %2, 0 offen\n\ts_waitcnt vmcnt(0)" : "=&v"(d)
                 : "v"((unsigned)(lane * 32 + off) * 128u), "s"(mk_rsrc((const void*)(pc & ~255ull), CODE_BYTES)) : "memory");
  }
  asm volatile("" : "+s"(x), "+s"(wqkv), "+s"(wr), "+s"(ws), "+s"(out), "+s"(wo), "+s"(wd), "+s"(wg), "+s"(wu));
  unsigned rot2 = ((((unsigned)(0x65432106543210ull >> (off & 3) * 8) & 0xfffffffu) + ((tn & 7) + 1) * 0x1111111u) & 0x7777777u) * 2 | (unsigned)tn << 29;
  unsigned tag = 0;
  RowRegs rr;
  EpiRegs e;
  row_request(rr, x, wr, ws, tm * 128 + tn * 8 + wid * 2, lane, wid < 4);
  rr.keep = first;
  SCHED0();
  if ((wid >> 1) == 2) {
    __builtin_amdgcn_s_setprio(2);
    const Stager stage(smem, wid - 4, lane, tm, tn, x, wqkv);
    NAP(4);
    stage_x(stage, 0); stage_w(stage, 0); stage_x(stage, 1); stage_w(stage, 1);
    WAITV(21);
    BAR();
    stage_x(stage, 2); stage_w(stage, 2);
  }
  asm volatile("" ::: "memory");
  if (wid < 4) {
    __builtin_amdgcn_s_setprio(2);
    qkv_setup(smem, wid, lane, tm, tn, e);
    asm volatile("" : "+s"(rot2));
    SCHED0();
    tag = row_stats(rr, ws, tm * 128 + tn * 8 + wid * 2, lane);
    if (wid == 0) *(unsigned*)(smem + L_FLG + lane * 4) = lane * 4 == F_TAG - L_FLG ? tag : 0u;
    STAMP(10);
  }
  asm volatile("" ::: "memory");
  if (wid < 4) {
    if (wid & 1) qkv_attention<1>(smem, wid, lane, tm, tn, ws, tag, x, e);
    else qkv_attention<0>(smem, wid, lane, tm, tn, ws, tag, x, e);
    out_proj(smem, wid, lane, tm, tn, ws, tag, out, e, rot2);
  } else {
    if (wid < 6) loader(smem, wid - 4, lane, tm, tn, unit_of(bid) < 28, rot2, x, wqkv, wo, ws);
    else side_wave(smem, wid - 6, lane, bid, tm, tn, x, wid == 6 ? wg : wu, wd, ws);
    updates(smem, (0x2130 >> ((wid - 4) * 4)) & 3, lane, tm, tn, ws);
    __builtin_amdgcn_s_waitcnt(0x0F70);
  }
}

extern "C" void kernel_launch(void* const* d_in, const int* in_sizes, int n_in, void* d_out, int out_size, void* d_ws,
                              size_t ws_size, hipStream_t stream) {
  fused_block<<<256, 512, 0, stream>>>((const u16*)d_in[0], (const u16*)d_in[1], (const u16*)d_in[2], (const u16*)d_in[4],
                                       (const u16*)d_in[5], (const u16*)d_in[6], (const u16*)d_in[7], (char*)d_ws, (u16*)d_out);
}
```

```cpp
#include <hip/hip_runtime.h>
#include <hip/hip_bf16.h>
#include <stdint.h>

typedef __attribute__((ext_vector_type(8))) short bf16x8;
typedef __attribute__((ext_vector_type(4))) short s16x4;
typedef __attribute__((ext_vector_type(4))) float f32x4;
typedef __attribute__((ext_vector_type(2))) float f32x2;
typedef __attribute__((ext_vector_type(2))) __bf16 bf16x2_t;
typedef __attribute__((ext_vector_type(4))) unsigned u32x4;
typedef unsigned short u16;
#define LAS __attribute__((address_space(3)))
#define GAS __attribute__((address_space(1)))
#define RLX_AGENT __ATOMIC_RELAXED, __HIP_MEMORY_SCOPE_AGENT

#define S_TOK 2048
#define DM 1024
#define NQKV 3072
#define FF 3584

typedef __attribute__((ext_vector_type(2))) unsigned u32x2;

#define WS_EP   0
#define WS_R    4096
#define WS_K    (WS_R + 32768)
#define WS_OF   (WS_K + 8192)
#define WS_CS   (WS_OF + 4096)
#define WS_GU   (WS_CS + 16 * 2 * 4096)
#define WS_Y    (WS_GU + 56 * 4 * 8192)
#define WS_S    (WS_Y + 56 * 8 * DM * 4)
#define WS_O    (8u << 20)

__device__ const unsigned k_tags[32] = {0,  1,  2,  3,  4,  5,  6,  7,  8,  9,  10, 11, 12, 13, 14, 15,
                                        16, 17, 18, 19, 20, 21, 22, 23, 24, 25, 26, 27, 28, 29, 30, 31};

__device__ __forceinline__ unsigned pack2(float a, float b) {
  f32x2 v = {a, b};
  bf16x2_t r = __builtin_convertvector(v, bf16x2_t);
  return __builtin_bit_cast(unsigned, r);
}
__device__ __forceinline__ float rbf(float f) { return __uint_as_float(pack2(f, f) << 16); }
__device__ __forceinline__ float lo2f(unsigned w) { return __uint_as_float(w << 16); }
__device__ __forceinline__ float hi2f(unsigned w) { return __uint_as_float(w & 0xffff0000u); }

__device__ __forceinline__ bf16x8 lds_b128(const char* p) { return *(const bf16x8*)p; }
__device__ __forceinline__ bf16x8 lds_tr2(const char* p) {
  s16x4 lo = __builtin_amdgcn_ds_read_tr16_b64_v4i16((LAS s16x4*)p);
  s16x4 hi = __builtin_amdgcn_ds_read_tr16_b64_v4i16((LAS s16x4*)(p + 512));
  return __builtin_shufflevector(lo, hi, 0, 1, 2, 3, 4, 5, 6, 7);
}
#define MFMA16(a, b, c) __builtin_amdgcn_mfma_f32_16x16x32_bf16(a, b, c, 0, 0, 0)
#define WAITV(n) asm volatile("s_waitcnt vmcnt(%0)" ::"i"(n) : "memory")
#define WAITL0() __builtin_amdgcn_s_waitcnt(0xC07F)
#define BAR() __builtin_amdgcn_s_barrier()
#define SCHED0() __builtin_amdgcn_sched_barrier(0)
#define NAP(n) asm volatile("s_sleep %0" ::"i"(n) : "memory")
__device__ __forceinline__ float xsum16_32(float v) {
  unsigned u = __float_as_uint(v);
  auto a = __builtin_amdgcn_permlane16_swap(u, u, false, false);
  v = __uint_as_float(a[0]) + __uint_as_float(a[1]);
  u = __float_as_uint(v);
  auto b = __builtin_amdgcn_permlane32_swap(u, u, false, false);
  return __uint_as_float(b[0]) + __uint_as_float(b[1]);
}
__device__ __forceinline__ bf16x8 pack8(const f32x4& a, const f32x4& b) {
  u32x4 r = {pack2(a[0], a[1]), pack2(a[2], a[3]), pack2(b[0], b[1]), pack2(b[2], b[3])};
  return __builtin_bit_cast(bf16x8, r);
}
__device__ __forceinline__ unsigned wave_max_u32(unsigned v) {
  v = max(v, (unsigned)__builtin_amdgcn_update_dpp(0, (int)v, 0x111, 0xf, 0xf, false));
  v = max(v, (unsigned)__builtin_amdgcn_update_dpp(0, (int)v, 0x112, 0xf, 0xf, false));
  v = max(v, (unsigned)__builtin_amdgcn_update_dpp(0, (int)v, 0x114, 0xf, 0xf, false));
  v = max(v, (unsigned)__builtin_amdgcn_update_dpp(0, (int)v, 0x118, 0xf, 0xf, false));
  v = max(v, (unsigned)__builtin_amdgcn_update_dpp(0, (int)v, 0x142, 0xa, 0xf, false));
  v = max(v, (unsigned)__builtin_amdgcn_update_dpp(0, (int)v, 0x143, 0xc, 0xf, false));
  return (unsigned)__builtin_amdgcn_readlane((int)v, 63);
}
__device__ __forceinline__ bf16x8 tr_frag(const u32x4 (&w)[8], int i) {
  const unsigned sel = (i & 1) ? 0x07060302u : 0x05040100u;
  u32x4 r = {__builtin_amdgcn_perm(w[1][i >> 1], w[0][i >> 1], sel), __builtin_amdgcn_perm(w[3][i >> 1], w[2][i >> 1], sel),
             __builtin_amdgcn_perm(w[5][i >> 1], w[4][i >> 1], sel), __builtin_amdgcn_perm(w[7][i >> 1], w[6][i >> 1], sel)};
  return __builtin_bit_cast(bf16x8, r);
}

#define XSL_ 18432
#define WSL 24576
#define XB (3 * XSL_)
#define NBAR 14
#define L_UP 0
#define L_WA 6144
#define L_W3 (L_WA + 57344)
#define L_WB (L_W3 + 16384)
#define L_OWN (L_WB + 57344)
#define L_XCH (XB + 4 * WSL)
#define L_HF (L_XCH + 6144)
#define L_CSF (L_HF + 1024)
#define L_QST (L_CSF + 1536)
#define L_FLG (L_QST + 1152)
#define L_SIZE (L_FLG + 256)
#define F_FULL (L_FLG + 0)
#define F_ST (L_FLG + 8)
#define F_WO (L_FLG + 16)
#define F_FREE (L_FLG + 32)
#define F_XCH (L_FLG + 40)
#define F_ODONE (L_FLG + 48)
#define F_SELOK (L_FLG + 52)
#define F_GO (L_FLG + 56)
#define F_OL (L_FLG + 60)
#define F_H (L_FLG + 64)
#define F_TAG (L_FLG + 68)
#define F_UP (L_FLG + 72)
#define F_SEL (L_FLG + 96)
#define F_CND (L_FLG + 128)
#define OD 7
#define SPIN_CAP (1u << 18)
#define NB1 6
#define CODE_BYTES 54272

__device__ __forceinline__ unsigned lds_addr(const char* p) { return (unsigned)(size_t)(LAS const char*)p; }
__device__ __forceinline__ void lds_post(char* smem, int off, unsigned v) {
  asm volatile("s_mov_b64 exec, 1\n\tds_write_b8 %0, %1\n\ts_mov_b64 exec, -1" ::"v"(lds_addr(smem + off)), "v"(v) : "memory");
}
__device__ __forceinline__ unsigned lds_word(const char* smem, int off) { return *(const volatile LAS unsigned*)(LAS const char*)(smem + off); }
__device__ __forceinline__ u32x4 lds_word4(const char* smem, int off) { return *(const volatile LAS u32x4*)(LAS const char*)(smem + off); }
template <unsigned MASK, bool COND = false>
__device__ __forceinline__ unsigned wait_bytes(const char* smem, int off, unsigned need, unsigned skip = 0) {
  unsigned t, f;
  asm volatile(
      ".if %6\n\ts_cmp_lg_u32 %5, 0\n\ts_cbranch_scc1 2f\n\t.endif\n\t"
      "1:\n\t"
      "ds_read_b32 %1, %2\n\t"
      "s_waitcnt lgkmcnt(0)\n\t"
      "v_readfirstlane_b32 %0, %1\n\t"
      "s_or_b32 %0, %0, 0x80808080\n\t"
      "s_sub_u32 %0, %0, %3\n\t"
      "s_and_b32 %0, %0, %4\n\t"
      "s_cmp_eq_u32 %0, %4\n\t"
      "s_cbranch_scc1 2f\n\t"
      "s_sleep 1\n\t"
      "s_branch 1b\n\t"
      "2:"
      : "=&s"(t), "=&v"(f)
      : "v"(lds_addr(smem + off)), "s"(need * 0x01010101u), "s"(MASK), "s"(COND ? skip : MASK), "i"((int)COND)
      : "scc", "memory");
  return f;
}
__device__ __forceinline__ void wait_full(const char* smem, int off, unsigned need, unsigned cur = 0) {
  unsigned a, b;
  asm volatile(
      "s_cmp_ge_u32 %0, %4\n\t"
      "s_cbranch_scc1 3f\n\t"
      "s_branch 2f\n\t"
      "1:\n\t"
      "s_sleep 1\n\t"
      "2:\n\t"
      "ds_read_b32 %1, %3\n\t"
      "ds_read_b32 %2, %3 offset:4\n\t"
      "s_waitcnt lgkmcnt(0)\n\t"
      "v_min_u32 %1, %1, %2\n\t"
      "s_nop 0\n\t"
      "v_readfirstlane_b32 %0, %1\n\t"
      "s_cmp_lt_u32 %0, %4\n\t"
      "s_cbranch_scc1 1b\n\t"
      "3:"
      : "+s"(cur), "=&v"(a), "=&v"(b)
      : "v"(lds_addr(smem + off)), "s"(need)
      : "scc", "memory");
}
__device__ __forceinline__ unsigned uni(unsigned v) { return (unsigned)__builtin_amdgcn_readfirstlane((int)v); }
__device__ __forceinline__ void dma_tag(char* smem, int off, int n) {
  unsigned keep;
  asm volatile("s_nop 2\n\ts_mov_b32 %0, m0\n\ts_mov_b32 m0, %1\n\ts_mov_b64 exec, 1\n\tglobal_load_lds_dword %2, %3\n\ts_mov_b64 exec, -1\n\ts_mov_b32 m0, %0\n\ts_nop 0"
               : "=&s"(keep) : "s"(uni(lds_addr(smem + off))), "v"(n * 4), "s"(k_tags) : "memory");
}
template <int V> struct IC { static constexpr int v = V; };
struct QFrag { bf16x8 xa[5]; bf16x8 w[3][2]; };
__device__ __forceinline__ void q_read(QFrag& f, const char* sa, const char* sb0, const char* sb1, int ks) {
#pragma unroll
  for (int i = 0; i < 5; ++i) f.xa[i] = lds_b128(sa + i * 2048);
#pragma unroll
  for (int s = 0; s < 3; ++s) {
    f.w[s][0] = lds_tr2(sb0 + s * 8192 + ks * 4096);
    f.w[s][1] = lds_tr2(sb1 + s * 8192 + ks * 4096);
  }
}
__device__ constexpr int acc_no(int HALF, int m, int i, int t) {
  int n = 0;
  for (int ii = 0; ii < 5; ++ii)
    for (int tt = 0; tt < 2; ++tt)
      for (int mm = 0; mm < 3; ++mm) {
        const bool on = mm == 0 ? (HALF == 1 || (ii >= 1 && ii <= 3)) : (HALF == 0 || ii >= 1);
        if (mm == m && ii == i && tt == t) return on ? n : -1;
        if (on) ++n;
      }
  return -1;
}
__device__ constexpr int seq_of(int HALF, int z) {
  int n = 0;
  for (int a = 0; a < 30; ++a) {
    const int m = a / 10, t0 = (a / 5) % 2;
    const int i = m < 2 ? (t0 ? a % 5 : 4 - a % 5) : 4 - (a - 20) / 2, t = m < 2 ? t0 : (a ^ i) & 1;
    if (acc_no(HALF, m, i, t) >= 0) { if (n == z) return m * 100 + i * 10 + t; ++n; }
  }
  return -1;
}
template <int HALF, int Z, int E, bool FIRST>
__device__ __forceinline__ void q_one(const QFrag& f, f32x4 (&acc)[26]) {
  if constexpr (Z < E) {
    constexpr int m = seq_of(HALF, Z) / 100, i = seq_of(HALF, Z) / 10 % 10, t = seq_of(HALF, Z) % 10;
    constexpr int n = acc_no(HALF, m, i, t);
    const f32x4 c = FIRST ? f32x4{0, 0, 0, 0} : acc[n];
    acc[n] = m == 2 ? MFMA16(f.xa[i], f.w[2][t], c) : MFMA16(f.w[m][t], f.xa[i], c);
    q_one<HALF, Z + 1, E, FIRST>(f, acc);
  }
}
template <int HALF, int B, int E, int NRD, bool FIRST = false>
__device__ __forceinline__ void q_mma(const QFrag& f, f32x4 (&acc)[26]) {
  q_one<HALF, B, E, FIRST>(f, acc);
#pragma unroll
  for (int i = 0; i < NRD; ++i) {
    __builtin_amdgcn_sched_group_barrier(0x008, 1, 0);
    __builtin_amdgcn_sched_group_barrier(0x100, 1, 0);
  }
  __builtin_amdgcn_sched_group_barrier(0x008, E - B - NRD, 0);
}

__device__ __forceinline__ void tr_all(u32x4 (&w)[8]) {
  u32x4 t[8];
#pragma unroll
  for (int i = 0; i < 8; ++i) t[i] = __builtin_bit_cast(u32x4, tr_frag(w, i));
#pragma unroll
  for (int i = 0; i < 8; ++i) w[i] = t[i];
  asm volatile("" : "+v"(w[0]), "+v"(w[1]), "+v"(w[2]), "+v"(w[3]), "+v"(w[4]), "+v"(w[5]), "+v"(w[6]), "+v"(w[7]));
}
typedef __amdgpu_buffer_rsrc_t rsrc_t;
__device__ __forceinline__ rsrc_t mk_rsrc(const void* p, unsigned bytes) { return __builtin_amdgcn_make_buffer_rsrc((void*)p, 0, bytes, 0x00020000); }
__device__ __forceinline__ u32x4 ld16(rsrc_t rs, unsigned voff, unsigned soff) { return __builtin_amdgcn_raw_buffer_load_b128(rs, voff, soff, 0); }
__device__ __forceinline__ u32x4 ld16c(rsrc_t rs, unsigned voff, unsigned soff) { return __builtin_amdgcn_raw_buffer_load_b128(rs, voff, soff, 16); }
__device__ __forceinline__ void st16c(rsrc_t rs, unsigned voff, u32x4 v) { __builtin_amdgcn_raw_buffer_store_b128(v, rs, voff, 0, 16); }
__device__ __forceinline__ void st8c(rsrc_t rs, unsigned voff, unsigned v, unsigned tag) { __builtin_amdgcn_raw_buffer_store_b64(u32x2{v, tag}, rs, voff, 0, 16); }
__device__ __forceinline__ unsigned chk(unsigned bad, const u32x4& v, unsigned tag) {
  return __builtin_amdgcn_bitop3_b32(__builtin_amdgcn_bitop3_b32(bad, v[1], tag, 0xF6), v[3], tag, 0xF6);
}
__device__ __forceinline__ float f_of(unsigned u) { return __uint_as_float(u); }
#define LANE_VARS                                                                   \
  int lane = lane_in; asm volatile("" : "+v"(lane));                                \
  const int g = lane >> 4, r16 = lane & 15, q = (lane >> 2) & 3, p = lane & 3;      \
  const int hk = (q >> 1) | ((g & 1) << 1), brow = (8 * g + q) * 128 + 8 * p;       \
  (void)r16; (void)hk; (void)brow;

template <bool MAX>
__device__ __forceinline__ float rows_reduce(const float (&v)[4]) {
  auto op = [](unsigned a, unsigned b) {
    return __float_as_uint(MAX ? __builtin_elementwise_maximum(__uint_as_float(a), __uint_as_float(b)) : __uint_as_float(a) + __uint_as_float(b));
  };
  auto p = __builtin_amdgcn_permlane16_swap(__float_as_uint(v[0]), __float_as_uint(v[1]), false, false);
  auto q = __builtin_amdgcn_permlane16_swap(__float_as_uint(v[2]), __float_as_uint(v[3]), false, false);
  auto t = __builtin_amdgcn_permlane32_swap(op(p[0], p[1]), op(q[0], q[1]), false, false);
  return __uint_as_float(op(t[0], t[1]));
}
__device__ __forceinline__ void rows_spread(float t, float (&v)[4]) {
  auto h = __builtin_amdgcn_permlane32_swap(__float_as_uint(t), __float_as_uint(t), false, false);
  auto p = __builtin_amdgcn_permlane16_swap(h[0], h[0], false, false);
  auto q = __builtin_amdgcn_permlane16_swap(h[1], h[1], false, false);
  v[0] = __uint_as_float(p[0]); v[1] = __uint_as_float(p[1]); v[2] = __uint_as_float(q[0]); v[3] = __uint_as_float(q[1]);
}
typedef __attribute__((ext_vector_type(16))) unsigned u32x16;

template <int CTL>
__device__ __forceinline__ float dpp_f(float v) { return __uint_as_float(__builtin_amdgcn_update_dpp(0, (int)__float_as_uint(v), CTL, 0xf, 0xf, false)); }
__device__ __forceinline__ float sum16(float v) {
  v += dpp_f<0xB1>(v);
  v += dpp_f<0x4E>(v);
  v += dpp_f<0x141>(v);
  return v + dpp_f<0x140>(v);
}
__device__ __forceinline__ float dot2(unsigned a, unsigned b, float c) {
  return __builtin_amdgcn_fdot2_f32_bf16(__builtin_bit_cast(bf16x2_t, a), __builtin_bit_cast(bf16x2_t, b), c, false);
}
__device__ __forceinline__ float sum32(float v) {
  v = sum16(v);
  auto a = __builtin_amdgcn_permlane16_swap(__float_as_uint(v), __float_as_uint(v), false, false);
  return __uint_as_float(a[0]) + __uint_as_float(a[1]);
}
struct RowRegs { u32x4 x[4], w[2]; unsigned ep, keep; };
__device__ __forceinline__ void row_request(RowRegs& r, const u16* __restrict__ x, const u16* __restrict__ wr, char* ws, int row0, int lane, bool on) {
  if (!on) return;
  const rsrc_t xrs = mk_rsrc(x, S_TOK * DM * 2), rrs = mk_rsrc(wr, DM * 2);
  const unsigned xo = (unsigned)(row0 + (lane >> 5)) * 2048u + (lane & 31) * 16, wo = lane * 16;
  asm volatile("buffer_load_dword %0, off, %1, 0 sc1" : "=v"(r.ep) : "s"(mk_rsrc(ws + WS_EP, 4)) : "memory");
#pragma unroll
  for (int j = 0; j < 4; ++j) asm volatile("buffer_load_dwordx4 %0, %1, %2, 0 offen offset:%3" : "=v"(r.x[j]) : "v"(xo), "s"(xrs), "i"(j * 512) : "memory");
#pragma unroll
  for (int j = 0; j < 2; ++j) asm volatile("buffer_load_dwordx4 %0, %1, %2, 0 offen offset:%3" : "=v"(r.w[j]) : "v"(wo), "s"(rrs), "i"(j * 1024) : "memory");
}
__device__ __forceinline__ unsigned row_stats(RowRegs& r, char* ws, int row0, int lane) {
  float s0 = 0.f, s1 = 0.f;
  asm volatile("s_waitcnt vmcnt(2)" : "+v"(r.x[0]), "+v"(r.x[1]), "+v"(r.x[2]), "+v"(r.x[3]), "+v"(r.ep), "+v"(r.keep) :: "memory");
  const unsigned tag = uni(r.ep) + 1u;
#pragma unroll
  for (int j = 0; j < 4; ++j) {
    s0 = dot2(r.x[j][0], 0x3f803f80u, s0); s1 = dot2(r.x[j][1], 0x3f803f80u, s1);
    s0 = dot2(r.x[j][2], 0x3f803f80u, s0); s1 = dot2(r.x[j][3], 0x3f803f80u, s1);
  }
  const float mean = sum32(s0 + s1) * (1.0f / DM), mb = rbf(mean);
  s0 = 0.f; s1 = 0.f;
#pragma unroll
  for (int j = 0; j < 4; ++j)
#pragma unroll
    for (int i = 0; i < 4; ++i) {
      const float a = lo2f(r.x[j][i]) - mb, b = hi2f(r.x[j][i]) - mb;
      s0 = fmaf(a, a, s0); s1 = fmaf(b, b, s1);
      r.x[j][i] = pack2(a, b);
    }
  const float dm = mean - mb;
  const float var = rbf(fmaxf(sum32(s0 + s1) * (1.0f / DM) - dm * dm, 0.f));
  const float den = rbf(__builtin_amdgcn_sqrtf(rbf(var + 1e-5f))), rd = __builtin_amdgcn_rcpf(den);
  s0 = 0.f; s1 = 0.f;
  asm volatile("s_waitcnt vmcnt(0)" : "+v"(r.w[0]), "+v"(r.w[1]) :: "memory");
#pragma unroll
  for (int j = 0; j < 4; ++j)
#pragma unroll
    for (int i = 0; i < 4; ++i) {
      const unsigned n = pack2(lo2f(r.x[j][i]) * rd, hi2f(r.x[j][i]) * rd);
      const unsigned w = __builtin_amdgcn_permlane32_swap(r.w[j >> 1][i], r.w[j >> 1][i], false, false)[j & 1];
      if (i & 1) s1 = dot2(n, w, s1); else s0 = dot2(n, w, s0);
    }
  const float dot = sum32(s0 + s1);
  unsigned k = pack2(dot, dot) & 0xffffu;
  k = (k & 0x8000u) ? (~k & 0xffffu) : (k | 0x8000u);
  const rsrc_t wrs = mk_rsrc(ws, WS_O);
  st16c(wrs, (lane & 31) == 0 ? WS_R + (unsigned)(row0 + (lane >> 5)) * 16u : 0x7fffffffu,
        u32x4{(k << 16) | (__float_as_uint(mb) >> 16), tag, __float_as_uint(den), tag});
  const unsigned k01 = k | ((unsigned)__builtin_amdgcn_readlane((int)k, 32) << 16);
  st8c(wrs, lane == 0 ? WS_K + (unsigned)(((row0 >> 2) & 7) * 1024 + (row0 >> 5) * 16 + (row0 & 2) * 4) : 0x7fffffffu, k01, tag);
  return tag;
}
__device__ __forceinline__ void k_order(int tm, int tn, int& pa, int& pb) {
  pb = tn & 3;
  pa = (tm * 0x1111) ^ ((tn & 4) * 0x51 & 0x144) ^ ((~tn & 2) * 0x2200) ^ ((~tn & 1) << 9) ^ (pb == 3 ? 1 : tm & 1);
}
__device__ __forceinline__ int k_tile(int t, int pa, int pb) { return ((((t + pb) & 12) ? t + pb : t & 3) ^ (pa >> ((t + pb) & 12))) & 15; }
struct EpiRegs { int a0, b0, tx; unsigned xr, go; u32x4 xv[2][2]; };
__device__ __forceinline__ float opq(float v) { asm("" : "+v"(v)); return v; }
typedef __attribute__((ext_vector_type(10))) unsigned u32x10;
#define GRP(N, M, K, C) _Pragma("unroll") for (int z_ = 0; z_ < (N); ++z_) { __builtin_amdgcn_sched_group_barrier(0x008, M, 0); __builtin_amdgcn_sched_group_barrier(K, C, 0); }
__device__ constexpr int tail_of(int HALF, int z) {
  int n = 0;
  for (int a = 0; a < 30; ++a) {
    const int i = HALF ? a / 6 : 4 - a / 6, m = 2 - a % 6 / 2, t = a & 1;
    if (acc_no(HALF, m, i, t) >= 0 && (m < 2 || (HALF == 0 && i == 4))) { if (n == z) return m * 100 + i * 10 + t; ++n; }
  }
  return -1;
}
__device__ __forceinline__ void qkv_setup(char* smem, int wid, int lane_in, int tm, int tn, EpiRegs& e) {
  LANE_VARS
  e.a0 = (wid & 1) * 8192 + r16 * 128 + ((g ^ (r16 >> 1)) << 4);
  e.b0 = XB + brow + (((wid & 2) ^ hk) << 5);
  int pa, pb;
  k_order(tm, tn, pa, pb);
  e.tx = __builtin_ctz((unsigned)__ballot(k_tile(r16, pa, pb) == tn));
  e.xr = lds_addr(smem) + (16 + wid * 32 + r16) * 128 + ((((g & 1) * 2 + (g >> 1)) ^ (r16 >> 1)) << 4);
  asm volatile("" : "+v"(e.a0), "+v"(e.b0), "+v"(e.xr));
  asm volatile("" : "+s"(e.tx));
}
template <int HALF>
__device__ __forceinline__ void qkv_attention(char* smem, int wid, int lane_in, int tm, int tn, char* ws, unsigned tag,
                                              const u16* __restrict__ x, EpiRegs& e) {
  const int h = wid >> 1;
  f32x4 acc[26];
  float ra[5], rb[5], sa[4], sb[4];
  QFrag f0, f1;
  u32x4 fl;
  unsigned aq, ac, aa, ab, xa, xv, xf, ooff, sel[4];
  {
    LANE_VARS
    const int a0 = e.a0, b0 = e.b0, tx = uni(e.tx);
    const unsigned xr = e.xr;
    asm volatile("" : "=v"(e.xv[0][0]), "=v"(e.xv[0][1]), "=v"(e.xv[1][0]), "=v"(e.xv[1][1]));
    auto keep = [&](int t, int slot, int tx) {
      unsigned p0, p1;
      asm volatile("s_cmp_lg_u32 %7, %8\n\ts_cbranch_scc1 1f\n\tv_add_u32 %4, %9, %6\n\tv_xor_b32 %5, 64, %4\n\tds_read_b128 %0, %4\n\tds_read_b128 %1, %5\n\t"
                   "ds_read_b128 %2, %4 offset:2048\n\tds_read_b128 %3, %5 offset:2048\n\t1:"
                   : "+v"(e.xv[0][0]), "+v"(e.xv[0][1]), "+v"(e.xv[1][0]), "+v"(e.xv[1][1]), "=&v"(p0), "=&v"(p1) : "v"(xr), "s"(t), "s"(tx), "s"(slot) : "scc", "memory");
    };
    SCHED0();
    WAITL0();
    BAR();
    q_read(f0, smem + a0, smem + b0, smem + (b0 ^ 32), 0);
    SCHED0();
    q_read(f1, smem + (a0 ^ 64), smem + b0, smem + (b0 ^ 32), 1);
    int qa = XSL_ + a0, qc = XSL_ + (a0 ^ 64), qb = WSL + b0, qd = WSL + (b0 ^ 32), so = XSL_;
    q_mma<HALF, 0, 26, 17, true>(f0, acc);
    asm volatile("" : "+v"(qa), "+v"(qb), "+v"(qc), "+v"(qd));
    SCHED0();
    keep(0, 0, tx);
#pragma unroll 1
    for (int t = 1; t < NBAR - 1; ) {
      SCHED0();
      WAITL0();
      BAR();
      SCHED0();
      q_read(f0, smem + qa, smem + qb, smem + qd, 0);
      q_mma<HALF, 0, 26, 17>(f1, acc);
      SCHED0();
      q_read(f1, smem + qc, smem + qb, smem + qd, 1);
      q_mma<HALF, 0, 19, 17>(f0, acc);
      SCHED0();
      int d = so == 2 * XSL_ ? -2 * XSL_ : XSL_;
      asm volatile("" : "+s"(d));
      SCHED0();
      q_mma<HALF, 19, 20, 0>(f0, acc);
      SCHED0();
      keep(t, so, tx);
      SCHED0();
      q_mma<HALF, 20, 21, 0>(f0, acc);
      SCHED0(); qa += d; qc += d; asm volatile("" : "+v"(qa), "+v"(qc)); SCHED0();
      q_mma<HALF, 21, 22, 0>(f0, acc);
      SCHED0(); so += d; ++t; asm volatile("" : "+s"(so), "+s"(t)); SCHED0();
      q_mma<HALF, 22, 23, 0>(f0, acc);
      SCHED0(); int e = (t & 3) ? WSL : -3 * WSL; asm volatile("" : "+s"(e)); SCHED0();
      q_mma<HALF, 23, 24, 0>(f0, acc);
      SCHED0(); qb += e; qd += e; asm volatile("" : "+v"(qb), "+v"(qd)); SCHED0();
      q_mma<HALF, 24, 26, 0>(f0, acc);
      SCHED0();
    }
    WAITL0();
    BAR();
#pragma unroll 1
    for (int t = NBAR + 1; t < 17; ) {
      SCHED0();
      q_read(f0, smem + qa, smem + qb, smem + qd, 0);
      q_mma<HALF, 0, 26, 17>(f1, acc);
      SCHED0();
      q_read(f1, smem + qc, smem + qb, smem + qd, 1);
      const u32x4 fl = lds_word4(smem, F_FULL);
      q_mma<HALF, 0, 21, 18>(f0, acc);
      SCHED0();
      const unsigned cur = uni(min(fl[0], fl[1])), need = t;
      SCHED0();
      q_mma<HALF, 21, 22, 0>(f0, acc);
      SCHED0();
      keep(t, so, tx + 2);
      SCHED0();
      qa += XSL_; qb += WSL; qc += XSL_; qd += WSL; so += XSL_; ++t;
      q_one<HALF, 22, 25, false>(f0, acc);
      GRP(2, 1, 0x002, 2);
      asm volatile("" : "+v"(qa), "+v"(qb), "+v"(qc), "+v"(qd), "+s"(so), "+s"(t));
      SCHED0();
      q_mma<HALF, 25, 26, 0>(f0, acc);
      SCHED0();
      wait_full(smem, F_FULL, need, cur);
    }
    SCHED0();
    q_read(f0, smem + a0, smem + qb, smem + qd, 0);
    q_one<HALF, 0, 26, false>(f1, acc);
    {
      const int pa = 4 * g + (r16 & 1);
      const bool odd = r16 & 1, cur_a = pa <= r16, cur_b = pa + 2 <= r16;
      const unsigned qs = lds_addr(smem + L_QST + HALF * 512);
      aq = qs + r16 * 8; ac = lds_addr(smem + L_CSF + h * 256) + r16 * 8;
      aa = qs + pa * 8 + (cur_a ? 128 : 0); ab = qs + pa * 8 + (cur_b ? 128 : 0);
      xa = lds_addr(smem + L_XCH + h * 3072) + lane * 16; xv = xa + 2048 - lane * 8; xf = lds_addr(smem + F_XCH + h * 4);
      const unsigned la = odd ? 0x01000c0cu : 0x0c0c0100u, lb = odd ? 0x03020c0cu : 0x0c0c0302u;
      sel[0] = cur_a ? 0x0c0c0c0cu : la; sel[1] = cur_b ? 0x0c0c0c0cu : lb; sel[2] = cur_a ? la : 0x0c0c0c0cu; sel[3] = cur_b ? lb : 0x0c0c0c0cu;
      ooff = (unsigned)(tm * 8 + HALF * 4) * 32768u + (tn * 2 + h) * 1024 + lane * 16;
#pragma unroll
      for (int z = 0; z < 17; ++z) { GRP(1, 1, 0x100, 1); __builtin_amdgcn_sched_group_barrier(0x002, 1, 0); }
      GRP(9, 1, 0x002, 3);
      asm volatile("" : "+v"(aq), "+v"(ac), "+v"(aa), "+v"(ab), "+v"(xa), "+v"(xv), "+v"(xf), "+v"(ooff));
      asm volatile("" : "+v"(sel[0]), "+v"(sel[1]), "+v"(sel[2]), "+v"(sel[3]));
    }
    SCHED0();
#pragma unroll
    for (int i = 0; i < 5; ++i) f1.xa[i] = lds_b128(smem + (a0 ^ 64) + i * 2048);
#pragma unroll
    for (int s = 0; s < 3; ++s) {
      if (s == 2) fl = lds_word4(smem, F_FULL);
      f1.w[s][0] = lds_tr2(smem + qb + s * 8192 + 4096);
      f1.w[s][1] = lds_tr2(smem + qd + s * 8192 + 4096);
    }
    q_mma<HALF, 0, 26 - 0, 18>(f0, acc);
    SCHED0();
    keep(15, 0, tx);
    SCHED0();
  }
  LANE_VARS
  auto mm = [&](const QFrag& f, int m, int i, int t) {
    const int n = max(acc_no(HALF, m, i, t), 0);
    acc[n] = m == 2 ? MFMA16(f.xa[i], f.w[2][t], acc[n]) : MFMA16(f.w[m][t], f.xa[i], acc[n]);
  };
  auto last = [&](int z0, int z1) {
#pragma unroll
    for (int z = z0; z < z1; ++z) {
      const int c = tail_of(HALF, HALF ? z : z < 6 ? z + 2 : z < 8 ? z - 6 : z);
      mm(f1, c / 100, c / 10 % 10, c % 10);
    }
  };
  last(0, 3);
  SCHED0();
  const unsigned cur = uni(min(fl[2], fl[3]));
  SCHED0();
  last(3, 3 + 1);
  SCHED0();
  wait_full(smem, F_ST, 1, cur);
  SCHED0();
  u32x2 sq[5], cs[3][2];
#pragma unroll
  for (int i = 0; i < 5; ++i) sq[i] = *(const LAS u32x2*)(aq + i * 128);
#pragma unroll
  for (int s = 0; s < 3; ++s)
#pragma unroll
    for (int t = 0; t < 2; ++t) cs[s][t] = *(const LAS u32x2*)(ac + s * 512 + t * 128);
#pragma unroll
  for (int i = 1; i < 5; ++i) { ra[i] = *(const LAS float*)(aa + (i - 1) * 128 + 4); rb[i] = *(const LAS float*)(ab + (i - 1) * 128 + 20); }
  last(3 + 1, 3 + 5);
  __builtin_amdgcn_sched_group_barrier(0x100, 3, 0); GRP(3, 1, 0x100, 3);
  SCHED0();
  lds_post(smem, F_FREE + wid, 16);
  SCHED0();
  last(3 + 5, 18);
  const unsigned gm = g == 0 ? ~0u : 0u;
  u32x2 lx[5];
#pragma unroll
  for (int i = 0; i < 5; ++i) lx[i] = u32x2{sq[i][0] & gm, sq[i][0] & gm & 0xffffu};
#pragma unroll
  for (int i = 1; i < 5; ++i) {
    const float c = f_of(sq[i][1]) * (0.17677669529663687f * 1.4426950408889634f);
    sa[i - 1] = opq(c * ra[i]); sb[i - 1] = opq(c * rb[i]);
  }
  __builtin_amdgcn_sched_group_barrier(0x008, 5 - 3, 0); GRP(8, 1, 0x002, 3);
  asm volatile("" : "+v"(lx[0]), "+v"(lx[1]), "+v"(lx[2]), "+v"(lx[3]), "+v"(lx[4]));
  asm volatile("" : "+v"(sa[0]), "+v"(sa[1]), "+v"(sa[2]), "+v"(sa[3]), "+v"(sb[0]), "+v"(sb[1]), "+v"(sb[2]), "+v"(sb[3]));
  SCHED0();
  auto ln = [&](int m, int i, int t) {
    const int n = max(acc_no(HALF, m, i, t), 0);
    const s16x4 lxi = __builtin_bit_cast(s16x4, lx[i]), lw = __builtin_bit_cast(s16x4, cs[m][t]);
    acc[n] = m == 2 ? __builtin_amdgcn_mfma_f32_16x16x16bf16_1k(lxi, lw, acc[n], 0, 0, 0) : __builtin_amdgcn_mfma_f32_16x16x16bf16_1k(lw, lxi, acc[n], 0, 0, 0);
  };
#pragma unroll
  for (int z = 0; z < 18; ++z) ln(tail_of(HALF, z) / 100, tail_of(HALF, z) / 10 % 10, tail_of(HALF, z) % 10);
  bf16x8 qf[5], kf[5];
  u32x10 vf[2];
  auto pack_qk = [&](int i) {
    if (acc_no(HALF, 0, i, 0) >= 0) qf[i] = pack8(acc[max(acc_no(HALF, 0, i, 0), 0)], acc[max(acc_no(HALF, 0, i, 1), 0)]);
    if (acc_no(HALF, 1, i, 0) >= 0) kf[i] = pack8(acc[max(acc_no(HALF, 1, i, 0), 0)], acc[max(acc_no(HALF, 1, i, 1), 0)]);
  };
  auto pack_v = [&](int i, int t) {
    const f32x4& v = acc[max(acc_no(HALF, 2, i, t), 0)];
    return u32x2{pack2(v[0], v[1]), pack2(v[2], v[3])};
  };
  u32x2 vx[2];
  pack_qk(HALF == 0 ? 4 : 0);
  if (HALF == 0) {
    vx[0] = pack_v(4, 0); vx[1] = pack_v(4, 1);
    asm volatile("ds_write_b128 %0, %2 offset:1024\n\tds_write2st64_b64 %1, %3, %4 offset1:1" :: "v"(xa), "v"(xv), "v"(kf[4]), "v"(vx[0]), "v"(vx[1]) : "memory");
  } else {
    asm volatile("ds_write_b128 %0, %1" :: "v"(xa), "v"(qf[0]) : "memory");
  }
  asm volatile("s_mov_b64 exec, 1\n\tds_write_b8 %0, %0 offset:%1\n\ts_mov_b64 exec, -1" ::"v"(xf), "i"(HALF) : "memory");
  static_assert((F_XCH & 0xf) != 0 && ((F_XCH + 4) & 0xf) != 0, "the low byte of the address is the flag");
#pragma unroll
  for (int i = 0; i < 4; ++i) pack_qk(HALF == 0 ? 3 - i : i + 1);
  const f32x4 z4 = {0.f, 0.f, 0.f, 0.f};
  f32x4 sp[4], sc[4];
#pragma unroll
  for (int j = 0; j < 3; ++j) {
    const int i = HALF == 0 ? 3 - j : j + 2;
    sc[i - 1] = MFMA16(kf[i], qf[i], z4); sp[i - 1] = MFMA16(kf[i - 1], qf[i], z4);
  }
  if (HALF) sc[0] = MFMA16(kf[1], qf[1], z4);
  GRP(1, 6, 0x002, 4); GRP(12, 1, 0x002, 2); GRP(HALF ? 7 : 6, 1, 0x002, 3);
  SCHED0();
  unsigned flag;
  bf16x8 xq;
  if (HALF == 0) asm volatile("ds_read_b32 %0, %2\n\tds_read_b128 %1, %3" : "=&v"(flag), "=&v"(xq) : "v"(xf), "v"(xa) : "memory");
  else asm volatile("ds_read_b32 %0, %4\n\tds_read_b128 %1, %5 offset:1024\n\tds_read_b64 %2, %6\n\tds_read_b64 %3, %6 offset:512"
                    : "=&v"(flag), "=&v"(xq), "=&v"(vx[0]), "=&v"(vx[1]) : "v"(xf), "v"(xa), "v"(xv) : "memory");
  SCHED0();
  const bool odd = r16 & 1, cur_a = sel[0] == 0x0c0c0c0cu, cur_b = sel[1] == 0x0c0c0c0cu;
  float a[4], b[4], m[4];
  auto logits = [&](int j) {
    const f32x4 p4 = sp[j], c4 = sc[j];
    const float p0 = p4[0], p1 = p4[1], p2 = p4[2], p3 = p4[3], c0 = c4[0], c1 = c4[1], c2 = c4[2], c3 = c4[3];
    const float ca = odd ? c1 : c0, cb = odd ? c3 : c2, pa_ = odd ? p1 : p0, pb = odd ? p3 : p2;
    a[j] = opq((cur_a ? ca : pa_) * sa[j]);
    b[j] = opq((cur_b ? cb : pb) * sb[j]);
    if (HALF == 0 && j == 0) {
      a[j] = (!cur_a && tm == 0) ? -INFINITY : a[j];
      b[j] = (!cur_b && tm == 0) ? -INFINITY : b[j];
    }
    m[j] = __builtin_elementwise_maximum(a[j], b[j]);
  };
  auto late = [&](int z0, int z1) {
#pragma unroll
    for (int y = z0; y < z1; ++y) {
      const int c = seq_of(HALF, 18 + ((y + 8 - 0) & 7));
      if (y < 8 + 0) mm(y < 0 ? f0 : f1, 2, c / 10 % 10, c % 10);
    }
  };
#pragma unroll
  for (int j = 0; j < 2; ++j) logits(HALF == 0 ? 2 - j : j + 1);
  late(0, 6);
  GRP(6, 1, 0x002, 3);
  asm volatile("" : "+v"(m[1]), "+v"(m[2]));
  SCHED0();
  {
    unsigned t;
#define XWAIT(RD) "s_waitcnt lgkmcnt(0)\n\tv_readfirstlane_b32 %0, %1\n\ts_and_b32 %0, %0, %[mask]\n\ts_cbranch_scc1 2f\n\t1:\n\t" RD \
                  "\n\ts_waitcnt lgkmcnt(0)\n\tv_readfirstlane_b32 %0, %1\n\ts_and_b32 %0, %0, %[mask]\n\ts_cbranch_scc0 1b\n\t2:"
    if (HALF == 0) asm volatile(XWAIT("ds_read_b32 %1, %3\n\tds_read_b128 %2, %4") : "=&s"(t), "+v"(flag), "+v"(xq) : "v"(xf), "v"(xa), [mask] "s"(0xff00u) : "scc", "memory");
    else asm volatile(XWAIT("ds_read_b32 %1, %5\n\tds_read_b128 %2, %6 offset:1024\n\tds_read_b64 %3, %7\n\tds_read_b64 %4, %7 offset:512")
                      : "=&s"(t), "+v"(flag), "+v"(xq), "+v"(vx[0]), "+v"(vx[1]) : "v"(xf), "v"(xa), "v"(xv), [mask] "s"(0xffu) : "scc", "memory");
  }
  SCHED0();
  if (HALF == 0) { sc[3] = MFMA16(kf[4], xq, z4); sp[3] = MFMA16(kf[3], xq, z4); }
  else sp[0] = MFMA16(xq, qf[1], z4);
  logits(HALF == 0 ? 0 : 3);
  late(6, 8);
  GRP(1, HALF ? 1 : 2, 0x002, 3); GRP(2, 1, 0x002, 3);
  asm volatile("" : "+v"(m[HALF ? 3 : 0]));
  SCHED0();
  logits(HALF == 0 ? 3 : 0);
  rows_spread(rows_reduce<true>(m), m);
#pragma unroll
  for (int j = 0; j < 4; ++j) {
    a[j] = __builtin_amdgcn_exp2f(opq(a[j] - m[j]));
    b[j] = __builtin_amdgcn_exp2f(opq(b[j] - m[j]));
    m[j] = opq(a[j] + b[j]);
    a[j] = opq(a[j] * ra[j + 1]); b[j] = opq(b[j] * rb[j + 1]);
  }
  late(8, 16);
#pragma unroll
  for (int z = 0; z < 8; ++z) ln(2, seq_of(HALF, 18 + z) / 10 % 10, seq_of(HALF, 18 + z) % 10);
  GRP(0, 1, 0x402, 3); GRP(8, 1, 0x402, 2);
  asm volatile("" : "+v"(m[0]), "+v"(m[1]), "+v"(m[2]), "+v"(m[3]));
  SCHED0();
#pragma unroll
  for (int t = 0; t < 2; ++t) {
#pragma unroll
    for (int i = 0; i < 5; ++i) {
      const u32x2 v = i == (HALF ? 0 : 4) ? vx[t] : pack_v(i, t);
      vf[t][2 * i] = v[0]; vf[t][2 * i + 1] = v[1];
    }
    asm("" : "+v"(vf[t]));
  }
  rows_spread(__builtin_amdgcn_rcpf(rows_reduce<false>(m)), m);
  const rsrc_t ors = mk_rsrc(ws + WS_O, S_TOK * DM * 2);
#pragma unroll
  for (int j = 0; j < 4; ++j) {
    const unsigned pk = pack2(a[j] * m[j], b[j] * m[j]);
    const u32x4 pw = {__builtin_amdgcn_perm(0u, pk, sel[0]), __builtin_amdgcn_perm(0u, pk, sel[1]), __builtin_amdgcn_perm(0u, pk, sel[2]), __builtin_amdgcn_perm(0u, pk, sel[3])};
    const u32x4 v0 = {vf[0][2 * j], vf[0][2 * j + 1], vf[0][2 * j + 2], vf[0][2 * j + 3]}, v1 = {vf[1][2 * j], vf[1][2 * j + 1], vf[1][2 * j + 2], vf[1][2 * j + 3]};
    sp[j] = MFMA16(__builtin_bit_cast(bf16x8, v0), __builtin_bit_cast(bf16x8, pw), z4);
    sc[j] = MFMA16(__builtin_bit_cast(bf16x8, v1), __builtin_bit_cast(bf16x8, pw), z4);
  }
#pragma unroll
  for (int j = 0; j < 4; ++j) { qf[j] = pack8(sp[j], sc[j]); st16c(ors, ooff + j * 32768, __builtin_bit_cast(u32x4, qf[j])); }
  SCHED0();
  asm volatile("ds_read_b32 %0, %1 offset:%2" : "=v"(e.go) : "v"(xf - h * 4), "i"(F_GO - F_XCH) : "memory");
  wait_bytes<0x80808080u>(smem, F_FREE, 16);
  asm volatile("" : "+v"(e.go));
  asm volatile("ds_write_b128 %0, %1\n\tds_write_b128 %0, %2 offset:1024\n\tds_write_b128 %0, %3 offset:4096\n\tds_write_b128 %0, %4 offset:5120"
               :: "v"(lds_addr(smem + L_OWN) + HALF * 8192 + h * 2048 + lane * 16), "v"(qf[0]), "v"(qf[1]), "v"(qf[2]), "v"(qf[3]) : "memory");
  lds_post(smem, F_OL + wid, 1);
}

__device__ __forceinline__ unsigned ln2s(unsigned w, float mb, float r) {
  const unsigned d = pack2(opq(lo2f(w) - mb), opq(hi2f(w) - mb));
  return pack2(opq(lo2f(d) * r), opq(hi2f(d) * r));
}

__device__ __forceinline__ void await_attention(char* smem, int lane, int tm, int tn, char* ws, unsigned tag) {
  const rsrc_t wrs = mk_rsrc(ws, WS_O);
  const unsigned off = WS_OF + (tm * 64 + ((lane >> 3) & 1) * 32 + (lane & 7)) * 4;
  const int own = tn >> 3;
  bool first = true;
  unsigned seen = 0, since = 0;
#pragma unroll 1
  for (unsigned n = 0; n < SPIN_CAP; ++n) {
    unsigned want = ~seen & 0xffffu;
    if (since) ++since; else since = uni(lds_word(smem, F_ODONE)) >> 2;
    if (since <= (first ? 2 : 0)) want &= own ? 0x00ffu : 0xff00u;
    const unsigned f = __builtin_amdgcn_raw_buffer_load_b32(wrs, ((want >> (lane & 15)) & 1) ? off : 0x7fffffffu, 0, 16);
    seen |= (unsigned)__ballot(f == tag) & want;
    if (seen == 0xffffu) break;
    if (first && ((seen & 0xffu) == 0xffu || (seen >> 8) == 0xffu)) { lds_post(smem, F_GO, (seen & 0xffu) == 0xffu ? 1 : 2); first = false; NAP(8); }
    NAP(2);
  }
  lds_post(smem, F_GO + 1, 1);
  if (first) lds_post(smem, F_GO, 1);
}

__device__ __forceinline__ void out_proj(char* smem, int wid, int lane_in, int tm, int tn, char* ws, unsigned tag,
                                         u16* __restrict__ out, EpiRegs& e, unsigned rot2) {
  LANE_VARS
  const rsrc_t ors = mk_rsrc(ws + WS_O, S_TOK * DM * 2), yrs = mk_rsrc(out, S_TOK * DM * 2);
  const int tok = tm * 128 + wid * 32 + r16, co = (g & 1) * 16 + (g >> 1) * 8;
  unsigned eoff = (unsigned)(tok * DM + tn * 64 + co) * 2u;
  const unsigned vo = (unsigned)(tm * 8 + wid * 2) * 32768u + lane * 16;
  bf16x8 wb[3][4], oa[OD][2];
  int bc[4];
#pragma unroll
  for (int c = 0; c < 4; ++c) bc[c] = L_WA + brow + ((c ^ hk) << 5);
  f32x4 acc[2][4];
  SCHED0();
  unsigned go, oth = (tn & 8) * 2 ^ 16;
  asm volatile("" : "+s"(oth));
  auto o_request = [&](int s) {
    asm volatile("buffer_load_dwordx4 %0, %2, %4, %5 offen sc1\n\tbuffer_load_dwordx4 %1, %3, %4, %5 offen sc1" : "+v"(oa[s % OD][0]), "+v"(oa[s % OD][1])
                 : "v"(vo), "v"(vo + 32768), "s"(ors), "s"(((s >> 1 == 7 ? oth : (s & 16) ^ ((go & 2) * 8)) + ((rot2 >> (s & 14) * 2) & 14) + (s & 1)) * 1024) : "memory");
  };
  auto start = [&](unsigned word) {
    go = uni(word) & 3;
    if (go) {
#pragma unroll
      for (int s = 0; s < OD - 1; ++s) o_request(s);
    }
    SCHED0();
  };
#pragma unroll
  for (int s = 0; s < OD; ++s) asm volatile("" : "=v"(oa[s][0]), "=v"(oa[s][1]));
  {
    const unsigned w0 = uni(e.go);
    if (w0 & 3) wait_bytes<0x80808080u>(smem, F_OL, 1);
    start(w0);
  }
  const unsigned early = go;
  auto publish = [&](auto nn) {
    WAITV(decltype(nn)::v);
    unsigned old = 0;
    if (lane == 0) old = __hip_atomic_fetch_add((LAS unsigned*)(LAS char*)(smem + F_ODONE), 1u, __ATOMIC_RELAXED, __HIP_MEMORY_SCOPE_WORKGROUP);
    if (uni(old) == 3u) {
      if (lane == 0) __hip_atomic_store((GAS unsigned*)(ws + WS_OF) + tm * 64 + (tn >> 3) * 32 + (tn & 7), tag, RLX_AGENT);
    }
    SCHED0();
  };
  if (!early) publish(IC<0>{});
  const unsigned w1 = lds_word(smem, F_GO);
  const u32x2 fl = *(const volatile LAS u32x2*)(LAS const char*)(smem + F_FULL);
  const unsigned srow = 2047u - (*(const unsigned*)(smem + F_SEL + (lane & 7) * 4) & 0xffffu);
  f32x4 res[2][4];
  const uint2 st[2] = {*(const uint2*)(smem + L_QST + (16 + wid * 32 + r16) * 8), *(const uint2*)(smem + L_QST + (32 + wid * 32 + r16) * 8)};
  auto norm = [&](int u) {
    const float rd = f_of(st[u].y), k = 1.0f + rd, b = lo2f(st[u].x) * rd;
#pragma unroll
    for (int pr = 0; pr < 2; ++pr) {
      auto sa = __builtin_amdgcn_permlane16_swap(e.xv[u][pr][0], e.xv[u][pr][2], false, false);
      auto sb = __builtin_amdgcn_permlane16_swap(e.xv[u][pr][1], e.xv[u][pr][3], false, false);
#pragma unroll
      for (int z = 0; z < 2; ++z) res[u][2 * pr + z] = f32x4{lo2f(sa[z]), hi2f(sa[z]), lo2f(sb[z]), hi2f(sb[z])} * k + b;
    }
    asm volatile("" : "+v"(res[u][0]), "+v"(res[u][1]), "+v"(res[u][2]), "+v"(res[u][3]));
  };
  f32x4 ac2[2][4], (&ac3)[2][4] = res;
  auto w_frag = [&](int slot, const int (&b)[4], int z) {
#pragma unroll
    for (int c = 0; c < 4; ++c) wb[slot][c] = lds_tr2(smem + b[c] + z * 4096);
  };
  auto step = [&](f32x4 (&d)[2][4], int slot, const bf16x8 (&o)[2], bool fresh) {
#pragma unroll
    for (int c = 0; c < 4; ++c)
#pragma unroll
      for (int uu = 0; uu < 2; ++uu) {
        const int u = uu ^ (c & 1);
        d[u][c] = MFMA16(wb[slot][c], o[u], fresh ? (f32x4{0, 0, 0, 0}) : d[u][c]);
      }
  };
  int b3[4];
#pragma unroll
  for (int c = 0; c < 4; ++c) b3[c] = bc[c] + (L_W3 - L_WA);
  auto own_heads = [&](unsigned fu) {
    wait_full(smem, F_FULL, 17, fu);
    w_frag(0, b3, 2); w_frag(1, b3, 3);
    const unsigned w2 = lds_word(smem, F_GO);
    SCHED0();
    if (wid < 2) wait_bytes<0x00800080u>(smem, F_OL, 1); else wait_bytes<0x80008000u>(smem, F_OL, 1);
    if (!go) start(w2);
    bf16x8 own[2][2];
#pragma unroll
    for (int z = 0; z < 4; ++z) own[z >> 1][z & 1] = lds_b128(smem + L_OWN + wid * 4096 + lane * 16 + z * 1024);
    step(ac3, 0, own[0], false); step(ac3, 1, own[1], false);
    asm volatile("" : "+v"(ac3[0][0]), "+v"(ac3[0][1]), "+v"(ac3[0][2]), "+v"(ac3[0][3]), "+v"(ac3[1][0]), "+v"(ac3[1][1]), "+v"(ac3[1][2]), "+v"(ac3[1][3]));
    SCHED0();
  };
  unsigned mine = (unsigned)__ballot((srow >> 5) == (unsigned)(tm * 4 + wid)) & 0xffu;
  asm volatile("" : "+s"(mine));
  asm volatile("" : "+v"(eoff));
  if (!go) start(w1);
  norm(0);
  norm(1);
  SCHED0();
  if (early) publish(IC<2 * OD - 2>{});
  own_heads(uni(min(fl[0], fl[1])));
  if (!go) start(wait_bytes<0x80u>(smem, F_GO, 1));
  go = uni(go);
  int b2[4];
  const int mine1st = ((go >> 1) ^ (tn >> 3) ^ 1) & 1;
#pragma unroll
  for (int c = 0; c < 4; ++c) { b2[c] = bc[c] + (go & 1) * (L_WB - L_WA); bc[c] += (go >> 1) * (L_WB - L_WA); }
  unsigned so[15];
#pragma unroll
  for (int z = (OD - 1) / 2; z < 15; ++z) { so[z] = ((z == 7 ? oth : (2 * z & 16) ^ ((go & 2) * 8)) + ((rot2 >> (z & 7) * 4) & 14)) * 1024; asm volatile("" : "+s"(so[z])); }
  s16x4 wh[3][8];
  auto w_half = [&](int s, int z0, int z1) {
#pragma unroll
    for (int z = z0; z < z1; ++z)
      if (s < 30) wh[s % 3][z] = __builtin_amdgcn_ds_read_tr16_b64_v4i16((LAS s16x4*)(smem + (s < 14 ? bc[z >> 1] + s * 4096 : s < 16 ? b3[z >> 1] + (s - 14) * 4096 : b2[z >> 1] + (s - 16) * 4096) + (z & 1) * 512));
  };
  wait_full(smem, F_WO, 1);
  wait_bytes<0x80u, true>(smem, F_WO + 8, 1, go & 1);
  w_half(0, 0, 8); w_half(1, 0, 8);
  SCHED0();
  asm volatile("s_waitcnt vmcnt(%2)" : "+v"(oa[0][0]), "+v"(oa[0][1]) : "i"(2 * OD - 4) : "memory");
  auto one = [&](int s) __attribute__((always_inline)) {
    f32x4 (&d)[2][4] = s < 14 ? ac2 : s < 16 ? ac3 : acc;
    auto part = [&](int z0, int z1) {
      SCHED0();
      w_half(s + 2, z0, z1);
#pragma unroll
      for (int z = z0; z < z1; ++z) {
        const int c = z >> 1, u = (z ^ c) & 1;
        d[u][c] = MFMA16(__builtin_shufflevector(wh[s % 3][2 * c], wh[s % 3][2 * c + 1], 0, 1, 2, 3, 4, 5, 6, 7), oa[s % OD][u], s == 0 || s == 16 ? (f32x4{0, 0, 0, 0}) : d[u][c]);
        __builtin_amdgcn_sched_group_barrier(0x008, 1, 0);
        __builtin_amdgcn_sched_group_barrier(0x100, 1, 0);
      }
      SCHED0();
    };
    const int n = s + OD - 1;
    auto ask = [&](int u) {
      if (n < 30) asm volatile("buffer_load_dwordx4 %0, %1, %2, %3 offen offset:%4 sc1" : "+v"(oa[n % OD][u]) : "v"(vo + u * 32768), "s"(ors), "s"(so[n >> 1]), "i"((n & 1) * 1024) : "memory");
    };
    part(0, 1);
    if (n == 14 || n == 16) wait_bytes<0x8000u, true>(smem, F_GO, 1, n == 14 ? mine1st ^ 1 : mine1st);
    ask(0);
    part(1, 3);
    ask(1);
    if (s == 13) wait_bytes<0x80u>(smem, F_WO + 8, 1);
    part(3, 5);
    if (s < 29) asm volatile("s_waitcnt vmcnt(%2)" : "+v"(oa[(s + 1) % OD][0]), "+v"(oa[(s + 1) % OD][1]) : "i"(2 * ((n < 29 ? n : 29) - s - 1)) : "memory");
    part(5, 8);
    asm volatile("" : "+v"(d[0][0]), "+v"(d[0][1]), "+v"(d[0][2]), "+v"(d[0][3]), "+v"(d[1][0]), "+v"(d[1][1]), "+v"(d[1][2]), "+v"(d[1][3]));
  };
#pragma unroll
  for (int s = 0; s < 10; ++s) one(s);
#pragma unroll
  for (int s = 10; s < 20; ++s) one(s);
#pragma unroll
  for (int s = 20; s < 28; ++s) one(s);
  asm volatile("s_waitcnt vmcnt(0)" : "+v"(oa[29 % OD][0]), "+v"(oa[29 % OD][1]) :: "memory");
  if (mine != 0) {
    wait_bytes<0x80808080u>(smem, F_UP, 1);
#pragma unroll 1
    for (; mine != 0; mine &= mine - 1) {
      const int c = __builtin_ctz(mine), row = __builtin_amdgcn_readlane((int)srow, c) & 31;
#pragma unroll
      for (int u = 0; u < 2; ++u)
        if (row == u * 16 + r16) {
#pragma unroll
          for (int z = 0; z < 4; ++z) res[u][z] += *(const f32x4*)(smem + L_UP + c * 256 + (z * 16 + g * 4) * 4);
        }
    }
  }
  SCHED0();
#pragma unroll
  for (int u = 0; u < 2; ++u)
#pragma unroll
    for (int z = 0; z < 8; ++z)
      acc[u][z & 3] = MFMA16(__builtin_shufflevector(wh[(28 + (z >> 2)) % 3][2 * (z & 3)], wh[(28 + (z >> 2)) % 3][2 * (z & 3) + 1], 0, 1, 2, 3, 4, 5, 6, 7), oa[(28 + (z >> 2)) % OD][u], acc[u][z & 3]);
#pragma unroll
  for (int u = 0; u < 2; ++u)
#pragma unroll
    for (int pr = 0; pr < 2; ++pr) {
      const f32x4 c0 = (acc[u][2 * pr] + ac2[u][2 * pr]) + ac3[u][2 * pr], c1 = (acc[u][2 * pr + 1] + ac2[u][2 * pr + 1]) + ac3[u][2 * pr + 1];
      auto sa = __builtin_amdgcn_permlane16_swap(pack2(c0[0], c0[1]), pack2(c1[0], c1[1]), false, false);
      auto sb = __builtin_amdgcn_permlane16_swap(pack2(c0[2], c0[3]), pack2(c1[2], c1[3]), false, false);
      __builtin_amdgcn_raw_buffer_store_b128(u32x4{sa[0], sb[0], sa[1], sb[1]}, yrs, eoff + pr * 64 + u * 16 * DM * 2, 0, 16);
    }
  __builtin_amdgcn_sched_group_barrier(0x008, 9, 0);
  GRP(7, 1, 0x002, 4);
  SCHED0();
}

__device__ __forceinline__ unsigned med3(unsigned a, unsigned b, unsigned c) { return max(min(a, b), min(max(a, b), c)); }
#define PIN8(a) asm volatile("" : "+v"(a[0]), "+v"(a[1]), "+v"(a[2]), "+v"(a[3]), "+v"(a[4]), "+v"(a[5]), "+v"(a[6]), "+v"(a[7]))
__device__ __forceinline__ void tick(int& nb) {
  SCHED0();
  if (nb < NBAR) { BAR(); ++nb; NAP(2); }
  asm volatile("" ::: "memory");
  SCHED0();
}
__device__ __forceinline__ void hidden_half(char* smem, int lane, int l, int cg, char* ws, unsigned tag, u32x4 (&fd)[2][8]) {
  const rsrc_t wrs = mk_rsrc(ws, WS_O);
  const unsigned off = WS_GU + (unsigned)(cg * 32768 + l * 2048 + lane * 16);
  u32x4 ps[2][8];
  auto request = [&]() {
#pragma unroll
    for (int p = 0; p < 16; ++p) ps[p & 1][p >> 1] = ld16c(wrs, off + (p & 1) * 1024, (p >> 1) * 4096);
    SCHED0();
  };
  auto good = [&]() {
    unsigned bad = 0;
#pragma unroll
    for (int p = 0; p < 16; ++p) bad = chk(bad, ps[p & 1][p >> 1], tag);
    return __all(bad == 0);
  };
#pragma unroll 1
  for (unsigned n = 0; n < SPIN_CAP; ++n) {
    const unsigned t = __builtin_amdgcn_raw_buffer_load_b32(wrs, WS_GU + (unsigned)(cg * 32768 + (lane >> 3) * 4096 + l * 2048 + (lane & 7) * 256 + 132), 0, 16);
    if (__all(t == tag)) break;
    NAP(8);
  }
  request();
  tr_all(fd[0]); tr_all(fd[1]);
  SCHED0();
#pragma unroll 1
  for (unsigned n = 0; n < SPIN_CAP && !good(); ++n) { NAP(4); request(); }
  unsigned hp[2];
#pragma unroll
  for (int j = 0; j < 2; ++j) {
    float hv[2];
#pragma unroll
    for (int z = 0; z < 2; ++z) {
      const float gb = rbf((f_of(ps[j][0][2 * z]) + f_of(ps[j][2][2 * z])) + (f_of(ps[j][4][2 * z]) + f_of(ps[j][6][2 * z])));
      const float ub = rbf((f_of(ps[j][1][2 * z]) + f_of(ps[j][3][2 * z])) + (f_of(ps[j][5][2 * z]) + f_of(ps[j][7][2 * z])));
      hv[z] = rbf(gb / (1.0f + __expf(-gb))) * ub;
    }
    hp[j] = pack2(hv[0], hv[1]);
  }
#pragma unroll
  for (int j = 0; j < 2; ++j)
    *(unsigned*)(smem + L_HF + (lane & 7) * 128 + (((lane >> 4) & 1) * 4 + 2 * l + j) * 16 + ((lane >> 3) & 1) * 4 + (lane >> 5) * 8) = hp[j];
  lds_post(smem, F_H + l, 1);
}

struct Stager {
  rsrc_t xrs, qrs;
  LAS char* lds;
  unsigned va, vb;
  int arow, pa, pb;
  __device__ __forceinline__ Stager(char* smem, int l, int lane, int tm, int tn, const u16* x, const u16* w) {
    asm volatile("" : "+v"(lane));
    const int kr = lane >> 3;
    xrs = mk_rsrc(x, S_TOK * DM * 2); qrs = mk_rsrc(w, DM * NQKV * 2);
    lds = (LAS char*)smem + l * 1024;
    va = (unsigned)(kr + 8 * l) * 2048u + (((lane & 7) ^ (kr >> 1) ^ (l << 2)) << 4);
    vb = (unsigned)(kr + 8 * l) * 6144u + tn * 128 + (((lane & 7) ^ (((kr >> 1) & 1) << 1) ^ (l << 2)) << 4);
    arow = tm * 128 - 16;
    k_order(tm, tn, pa, pb);
  }
};
#define DM1(l, o) "s_add_u32 m0, %2, " #l "\n\ts_add_u32 %0, %5, " #o "\n\tbuffer_load_dwordx4 %3, %4, %0 offen lds\n\t"
#define DMA_(str, v_, rs_, so_, s0_) asm volatile("s_mov_b32 %1, m0\n\t" str "s_mov_b32 m0, %1" : "=&s"(tmp), "=&s"(keep) : "s"(base), "v"(v_), "s"(rs_), "s"(so_), "s"(s0_) : "scc", "memory")
__device__ __forceinline__ void stage_x(const Stager& g, int t) {
  const int kt = k_tile(t, g.pa, g.pb);
  const unsigned base = (unsigned)(size_t)g.lds + (t % 3) * XSL_;
  unsigned keep, tmp;
  DMA_("s_mov_b32 m0, %2\n\ts_nop 0\n\tbuffer_load_dwordx4 %3, %4, %6 offen lds\n\t" DM1(2048, 32768) DM1(4096, 65536) DM1(6144, 98304) DM1(8192, 131072)
       DM1(10240, 163840) DM1(12288, 196608) DM1(14336, 229376) DM1(16384, 262144), g.va, g.xrs, g.arow * 2048 + kt * 128, max(g.arow, 0) * 2048 + kt * 128);
}
__device__ __forceinline__ void stage_w(const Stager& g, int t) {
  const int kt = k_tile(t, g.pa, g.pb);
  const unsigned base = (unsigned)(size_t)g.lds + XB + (t & 3) * WSL;
  unsigned keep, tmp;
  DMA_(DM1(0, 0) DM1(2048, 98304) DM1(4096, 196608) DM1(6144, 294912) DM1(8192, 2048) DM1(10240, 100352) DM1(12288, 198656) DM1(14336, 296960)
       DM1(16384, 4096) DM1(18432, 102400) DM1(20480, 200704) DM1(22528, 299008), g.vb, g.qrs, kt * 64 * 6144, 0);
}
__device__ __forceinline__ int unit_of(int bid) {
  const int off = bid >> 3;
  return (off >> 2) == 3 ? off + 16 : (off >> 2) == 7 ? off - 16 : off;
}
__device__ constexpr int wo_at(int n) { return (n < 28 ? L_WA + n * 2048 : n < 56 ? L_WB + (n - 28) * 2048 : L_W3 + (n - 56) * 2048); }
__device__ __forceinline__ void loader(char* smem, int l, int lane, int tm, int tn, unsigned rot2, const u16* __restrict__ x,
                                       const u16* __restrict__ w, const u16* __restrict__ wo, char* ws) {
  const Stager stage(smem, l, lane, tm, tn, x, w);
  dma_tag(smem, F_FULL + l * 4, 0);
  const rsrc_t prs = mk_rsrc(wo, DM * DM * 2);
  const int kr = lane >> 3;
  const unsigned vo = (unsigned)((kr < 4 ? kr : 12 + kr) + 4 * l) * 2048u + tn * 128 + (((lane & 7) ^ (((kr >> 1) & 1) << 1) ^ (l << 2)) << 4);
  int oth = (tn & 8) * 2 ^ 16;
  asm volatile("" : "+s"(oth));
  auto source = [&](int n) {
    const int k = n >> 1, half = k < 14 ? 0 : k < 28 ? 16 : k >= 30 ? (tn & 8) * 2 : oth;
    return (unsigned)((half + ((rot2 >> ((k < 14 ? k : k < 28 ? k - 14 : 14) & 14) * 2) & 14) + (k & 1)) * 32 + 8 * (n & 1)) * 2048;
  };
  auto fetch = [&](int n) {
    unsigned keep;
    asm volatile("s_mov_b32 %0, m0\n\ts_add_u32 m0, %4, %5\n\ts_nop 2\n\tbuffer_load_dwordx4 %1, %2, %3 offen lds\n\ts_mov_b32 m0, %0"
                 : "=&s"(keep) : "v"(vo), "s"(prs), "s"(source(n)), "s"((unsigned)(size_t)stage.lds), "i"(wo_at(n)) : "scc", "memory");
  };
  static_assert(L_WA + 24 * 2048 == XB && L_WB == XB + WSL && L_OWN + 16384 <= L_XCH, "layout");
#pragma unroll
  for (int t = -1; t < NBAR - 2; ++t) {
    if (t == 0) stage_w(stage, 3);
    if (t >= 0) stage_x(stage, t + 3);
    if (t == NBAR - 3) dma_tag(smem, F_FULL + l * 4, 15);
    if (t >= 0) stage_w(stage, t + 4);
    SCHED0();
    WAITV(t < 0 ? 22 : 33 + (t == 0 || t == NBAR - 3));
    BAR();
  }
  stage_x(stage, 15);
  dma_tag(smem, F_FULL + l * 4, 16);
  SCHED0();
#pragma unroll
  for (int y = 0; y < 12; ++y) fetch(y < 8 ? 63 - y : 16 + y);
  dma_tag(smem, F_FULL + l * 4, 17);
  const unsigned tag = uni(lds_word(smem, F_TAG));
  wait_bytes<0x80808080u>(smem, F_FREE, 16);
#pragma unroll
  for (int n = 0; n < 24; ++n) fetch(n);
  dma_tag(smem, F_WO + l * 4, 1);
#pragma unroll
  for (int n = 28; n < 28 + NB1; ++n) fetch(n);
  if (l == 0) {
    const unsigned v1 = (vo + 4 * 2048) ^ 64;
    wait_bytes<0x80808080u>(smem, F_OL, 1);
#pragma unroll
    for (int n = 28 + NB1; n < 56; ++n) {
      unsigned keep;
      asm volatile("s_mov_b32 %0, m0\n\ts_add_u32 m0, %4, %5\n\ts_nop 0\n\tbuffer_load_dwordx4 %1, %2, %3 offen lds\n\ts_add_u32 m0, m0, 1024\n\ts_nop 0\n\tbuffer_load_dwordx4 %6, %2, %3 offen lds\n\ts_mov_b32 m0, %0"
                   : "=&s"(keep) : "v"(vo), "s"(prs), "s"(source(n)), "s"((unsigned)(size_t)stage.lds), "i"(wo_at(n)), "v"(v1) : "scc", "memory");
    }
    wait_bytes<0x80u>(smem, F_WO + 12, 1);
    dma_tag(smem, F_WO + 8, 1);
  } else dma_tag(smem, F_WO + 12, 1);
  __builtin_amdgcn_s_setprio(1);
  if (l) await_attention(smem, lane, tm, tn, ws, tag);
}

__device__ __forceinline__ void updates(char* smem, int w, int lane, int tm, int tn, char* ws) {
  const rsrc_t wrs = mk_rsrc(ws, WS_O);
  const unsigned tag = uni(lds_word(smem, F_TAG));
  const int g = lane >> 4, li = lane & 15;
  wait_bytes<0x80u>(smem, F_SELOK, 1);
  const unsigned selkey = *(const unsigned*)(smem + F_SEL + (lane & 7) * 4);
  unsigned mine = (unsigned)__ballot(((2047u - (selkey & 0xffffu)) >> 7) == (unsigned)tm) & 0xffu;
#pragma unroll
  for (int i = 0; i < 3; ++i) mine &= i < w ? mine - 1 : ~0u;
  if (mine != 0 && w < 3) {
    wait_bytes<0x80u>(smem, F_GO, 1);
    NAP(24);
  }
  for (; mine != 0; mine &= mine - 1, mine &= mine - 1, mine &= mine - 1, mine &= mine - 1) {
    const int c = __builtin_ctz(mine);
    const unsigned yoff = WS_Y + (unsigned)((g * 14 * 8 + c) * DM + tn * 64 + li * 4) * 4u;
    u32x4 yv[14];
    const unsigned key = (unsigned)__builtin_amdgcn_readlane((int)selkey, c), kb = key >> 16;
#pragma unroll 1
    for (unsigned n = 0; n < SPIN_CAP; ++n) {
      unsigned bad = 0;
#pragma unroll
      for (int j = 0; j < 14; ++j) yv[j] = ld16c(wrs, yoff, j * (8 * DM * 4));
#pragma unroll
      for (int j = 0; j < 14; ++j) bad = chk(bad, yv[j], tag);
      if (__all(bad == 0)) break;
      NAP(8);
    }
    float y[4] = {0.f, 0.f, 0.f, 0.f};
#pragma unroll
    for (int j = 0; j < 14; ++j) { y[0] += lo2f(yv[j][0]); y[1] += hi2f(yv[j][0]); y[2] += lo2f(yv[j][2]); y[3] += hi2f(yv[j][2]); }
    const float r = __uint_as_float(((kb & 0x8000u) ? (kb & 0x7fffu) : (~kb & 0xffffu)) << 16);
    const float rs = rbf(1.0f / (1.0f + __expf(-r)));
#pragma unroll
    for (int e = 0; e < 4; ++e) y[e] = rbf(rbf(xsum16_32(y[e])) * rs);
    wait_bytes<0x80808080u>(smem, F_FREE, 16);
    *(f32x4*)(smem + L_UP + c * 256 + li * 16) = f32x4{y[0], y[1], y[2], y[3]};
  }
  lds_post(smem, F_UP + w, 1);
}

__device__ __forceinline__ void side_wave(char* smem, int F, int lane, int bid, int tm, int tn, const u16* __restrict__ x,
                                          const u16* __restrict__ wgu, const u16* __restrict__ wd, char* ws) {
  const rsrc_t wrs = mk_rsrc(ws, WS_O), xrs = mk_rsrc(x, S_TOK * DM * 2);
  const int u = unit_of(bid), cg = (bid & 7) * 7 + ((u >> 2) % 7), q = u & 3;
  const int g = lane >> 4, li = lane & 15;
  u32x4 fw[4][8];
  const rsrc_t grs = mk_rsrc(wgu, DM * FF * 2);
  const int cu = (bid & 7) * 7 + (((u >> 2) + F) % 7);
  const unsigned fo = (unsigned)(q * 256 + (li >> 3) * 128 + 8 * g) * (FF * 2) + cu * 128 + (li & 7) * 16;
#pragma unroll
  for (int s = 0; s < 4; ++s) asm volatile("" : "=v"(fw[s][0]), "=v"(fw[s][1]), "=v"(fw[s][2]), "=v"(fw[s][3]), "=v"(fw[s][4]), "=v"(fw[s][5]), "=v"(fw[s][6]), "=v"(fw[s][7]));
  const unsigned csb = WS_CS + (tn * 2 + F) * 4096;
  u32x4 cs[4], rec[2];
  unsigned ca = csb + lane * 16, ra = WS_R + (unsigned)max(tm * 128 - 16 + F * 72 + lane, 0) * 16, rb = WS_R + (unsigned)max(tm * 128 - 8 + F * 72 + lane, 0) * 16;
  asm volatile("" : "+v"(ca), "+v"(ra), "+v"(rb));
  unsigned due = u < 28 ? (0x3012 >> ((tn & 3) * 4)) & 15 : 9, iss = 0;
#define FW(n) fw[(n) >> 3][(n) & 7]
#define LD(i) "s_mov_b32 %[t], (((%[n0]+" #i ")>>3)*32+((%[n0]+" #i ")&7))*7168\n\tbuffer_load_dwordx4 %" #i ", %[fo], %[rs], %[t] offen nt\n\t"
#define LD8 LD(0) LD(1) LD(2) LD(3) LD(4) LD(5) LD(6) LD(7)
#define R8(n) "+v"(FW(n)), "+v"(FW(n + 1)), "+v"(FW(n + 2)), "+v"(FW(n + 3)), "+v"(FW(n + 4)), "+v"(FW(n + 5)), "+v"(FW(n + 6)), "+v"(FW(n + 7))
#define SITE(lds_, ...) asm volatile("s_cmp_lg_u32 %[iss], %[k]\n\ts_cbranch_scc1 1f\n\ts_cmp_le_u32 %[nb], %[due]\n\ts_cbranch_scc1 1f\n\ts_sleep %[dly]\n\t" lds_ \
    "s_add_u32 %[iss], %[iss], 1\n\ts_add_u32 %[due], %[due], 4\n\t1:" : __VA_ARGS__, [iss] "+s"(iss), [due] "+s"(due), [t] "=&s"(t) \
    : [fo] "v"(fo), [rs] "s"(grs), [nb] "s"(nb), [k] "i"(K), [n0] "i"(n0), [dly] "i"(D), [ca] "v"(ca), [ra] "v"(ra), [rb] "v"(rb), [ws] "s"(wrs) : "scc", "memory")
#define C1K(i, a, o) "buffer_load_dwordx4 %" #i ", %[" #a "], %[ws], 0 offen offset:" #o " sc1\n\t"
#define G2(a, b, c, d, e, f) C1K(a, ca, 0) C1K(b, ca, 1024) C1K(c, ca, 2048) C1K(d, ca, 3072) C1K(e, ra, 0) C1K(f, rb, 0)
#define PING2() asm volatile("" : "+v"(cs[0]), "+v"(cs[1]), "+v"(cs[2]), "+v"(cs[3]), "+v"(rec[0]), "+v"(rec[1]))
  auto site = [&](auto kk, auto dd, unsigned nb) {
    constexpr int K = decltype(kk)::v, D = decltype(dd)::v, n0 = K == 0 ? 0 : 8 + K * 8;
    unsigned t;
    SCHED0();
    if constexpr (K == 0) SITE(LD8 LD(8) LD(9) LD(10) LD(11) LD(12) LD(13) LD(14) LD(15), R8(n0), R8(n0 + 8));
    else if constexpr (K == 1) SITE(G2(8, 9, 10, 11, 12, 13) LD8, R8(n0), "+v"(cs[0]), "+v"(cs[1]), "+v"(cs[2]), "+v"(cs[3]), "+v"(rec[0]), "+v"(rec[1]));
    else SITE(LD8, R8(n0));
    SCHED0();
  };
#define CH(k, d, n) site(IC<k>{}, IC<d>{}, n)
  unsigned tag;
  {
    const int qq = (lane >> 2) & 3, hk = (qq >> 1) | ((g & 1) << 1);
    const char* pb = smem + XB + (8 * g + qq) * 128 + 8 * (lane & 3) + (((2 * F + (tm & 1)) ^ hk) << 5);
    const bf16x8 ones = {0x3f80, 0x3f80, 0x3f80, 0x3f80, 0x3f80, 0x3f80, 0x3f80, 0x3f80};
    f32x4 a[3];
#pragma unroll
    for (int t = 0; t < 2; ++t) {
      SCHED0(); BAR(); NAP(2); SCHED0();
      __builtin_amdgcn_s_setprio(3);
#pragma unroll
      for (int c = 0; c < 3; ++c) {
        a[c] = MFMA16(ones, lds_tr2(pb + t * WSL + c * 8192), t ? a[c] : (f32x4{0, 0, 0, 0}));
        a[c] = MFMA16(ones, lds_tr2(pb + t * WSL + c * 8192 + 4096), a[c]);
      }
      asm volatile("" : "+v"(a[0]), "+v"(a[1]), "+v"(a[2]));
      if (t == 0) { tag = uni(lds_word(smem, F_TAG)); CH(0, 4, 1); }
    }
    st16c(wrs, lane < 16 ? csb + ((tm & 12) + (tm & 1) * 2 + ((tm >> 1) & 1)) * 256 + lane * 16 : 0x7fffffffu,
          u32x4{__float_as_uint(a[0][0]), __float_as_uint(a[1][0]), __float_as_uint(a[2][0]), tag});
    __builtin_amdgcn_s_setprio(0);
    CH(0, 4, 2);
  }
  int nb = 2;
  tick(nb);
  const unsigned xs_off = WS_S + (unsigned)(((bid & 7) * 4 + q) * 6144 + lane * 16);
  uint2 tc[2], tq[2];
  unsigned ta[3], tv, todo = 1;
  auto gather2 = [&]() {
    asm volatile(G2(0, 1, 2, 3, 4, 5) : "=&v"(cs[0]), "=&v"(cs[1]), "=&v"(cs[2]), "=&v"(cs[3]), "=&v"(rec[0]), "=&v"(rec[1])
                 : [ca] "v"(ca), [ra] "v"(ra), [rb] "v"(rb), [ws] "s"(wrs) : "memory");
    SCHED0();
  };
  auto prepare = [&]() {
#pragma unroll 1
    for (unsigned n = 0; n < SPIN_CAP; ++n) {
      unsigned bad = chk(chk(0u, rec[0], tag), rec[1], tag);
#pragma unroll
      for (int j = 0; j < 4; ++j) bad = __builtin_amdgcn_bitop3_b32(bad, cs[j][3], tag, 0xF6);
      if (__all(bad == 0)) break;
      NAP(8);
      gather2();
      tick(nb); tick(nb); WAITV(0); PING2();
    }
    float cz[3];
#pragma unroll
    for (int z = 0; z < 3; ++z) {
      const unsigned v = __float_as_uint(opq(opq(f_of(cs[0][z]) + f_of(cs[1][z])) + opq(f_of(cs[2][z]) + f_of(cs[3][z]))));
      auto p = __builtin_amdgcn_permlane16_swap(v, v, false, false);
      cz[z] = opq(f_of(p[0]) + f_of(p[1]));
    }
    const float s0 = (g & 1) ? cz[1] : cz[0], s1 = (g & 1) ? cz[2] : cz[1];
    const float h0 = rbf(s0), d0 = opq(s0 - h0), c0 = rbf(d0), h1 = rbf(s1), d1 = opq(s1 - h1), c1 = rbf(d1);
    tc[0] = make_uint2(pack2(h0, c0), pack2(opq(d0 - c0), 0.f)); tc[1] = make_uint2(pack2(h1, c1), pack2(opq(d1 - c1), 0.f));
    asm volatile("" : "+v"(tc[0].x), "+v"(tc[0].y), "+v"(tc[1].x), "+v"(tc[1].y));
#pragma unroll
    for (int j = 0; j < 2; ++j) {
      const unsigned nm = (rec[j][0] & 0xffffu) ^ 0x8000u;
      tq[j] = make_uint2(nm | (nm << 16), __float_as_uint(__builtin_amdgcn_rcpf(f_of(rec[j][2]))));
      asm volatile("" : "+v"(tq[j].x), "+v"(tq[j].y));
    }
    ta[0] = lds_addr(smem + L_CSF + ((g & 1) * 64 + F * 32 + (g >> 1) * 16 + li) * 8);
    ta[1] = lds_addr(smem + L_QST + (F * 72 + lane) * 8);
    ta[2] = lds_addr(smem + (lane < 8 ? F_SEL + lane * 4 : lane == 8 ? F_SELOK : F_ST + F * 4));
    asm volatile("" : "+v"(ta[0]), "+v"(ta[1]), "+v"(ta[2]));
    SCHED0();
  };
  auto tables = [&](unsigned now) {
    asm volatile("s_and_b32 %0, %0, %8\n\ts_cbranch_scc0 1f\n\tds_write2_b64 %1, %2, %9 offset1:64\n\tds_write2_b64 %3, %4, %5 offset1:8\n\ts_mov_b64 exec, 0x3ff\n\tds_write_b32 %6, %7\n\ts_mov_b64 exec, -1\n\t1:"
                 : "+s"(now) : "v"(ta[0]), "v"(tc[0]), "v"(ta[1]), "v"(tq[0]), "v"(tq[1]), "v"(ta[2]), "v"(tv), "s"(todo), "v"(tc[1]) : "scc", "memory");
    todo &= ~now;
  };
  auto slice = [&]() {
    if (nb < NBAR) tick(nb);
    else { const u32x2 fl = *(const volatile LAS u32x2*)(LAS const char*)(smem + F_FULL); tables(uni(min(fl[0], fl[1])) >> 4); SCHED0(); }
  };
  if (u >= 28) {
    u32x4 kd[4];
#pragma unroll 1
    for (unsigned n = 0; n < SPIN_CAP; ++n) {
#define L1K(i, o) "buffer_load_dwordx4 %" #i ", %4, %5, 0 offen offset:" #o " sc1\n\t"
      asm volatile("s_nop 4\n\t" L1K(0, 0) L1K(1, 1024) L1K(2, 2048) L1K(3, 3072)
                   : "=&v"(kd[0]), "=&v"(kd[1]), "=&v"(kd[2]), "=&v"(kd[3]) : "v"(WS_K + F * 4096 + lane * 16), "s"(wrs) : "memory");
#undef L1K
      tick(nb);
      asm volatile("s_waitcnt vmcnt(0)" : "+v"(kd[0]), "+v"(kd[1]), "+v"(kd[2]), "+v"(kd[3]) :: "memory");
      if (__all(chk(chk(chk(chk(0u, kd[0], tag), kd[1], tag), kd[2], tag), kd[3], tag) == 0)) break;
      NAP(4);
    }
    unsigned m1 = 0, m2 = 0, m3 = 0, m4 = 0;
#pragma unroll
    for (int j = 0; j < 4; ++j) {
      kd[j] = u32x4{(kd[j][0] << 16) | (15 - 4 * j), (kd[j][0] & 0xffff0000u) | (14 - 4 * j), (kd[j][2] << 16) | (13 - 4 * j), (kd[j][2] & 0xffff0000u) | (12 - 4 * j)};
#pragma unroll
      for (int i = 0; i < 4; ++i) {
        const unsigned v = kd[j][i];
        m4 = med3(m3, m4, v); m3 = med3(m2, m3, v); m2 = med3(m1, m2, v); m1 = max(m1, v);
      }
    }
    const unsigned lb = ((63 - lane) << 5) | (F ? 0 : 16);
    m1 |= lb; m2 |= lb; m3 |= lb; m4 |= lb;
    asm volatile("" : "+v"(m1), "+v"(m2), "+v"(m3), "+v"(m4));
    tick(nb);
    unsigned best = 0;
#pragma unroll
    for (int r = 0; r < 8; ++r) {
      const unsigned wm = wave_max_u32(m1);
      best = (lane & 7) == r ? wm : best;
      const bool won = m1 == wm;
      m1 = won ? m2 : m1; m2 = won ? m3 : m2; m3 = won ? m4 : m3; m4 = won ? 0u : m4;
      if (r == 3) { asm volatile("" : "+v"(m1), "+v"(m2), "+v"(m3), "+v"(m4), "+v"(best)); tick(nb); }
    }
    if (__any(m1 == 0)) {
      unsigned wm = 0;
#pragma unroll 1
      for (int r = 0; r < 8; ++r) {
        if (r & 1) tick(nb);
        unsigned m = 0;
#pragma unroll
        for (int j = 0; j < 4; ++j)
#pragma unroll
          for (int i = 0; i < 4; ++i) m = max(m, (kd[j][i] | lb) - wm);
        wm += wave_max_u32(m);
        best = (lane & 7) == r ? wm : best;
      }
    }
    *(volatile LAS unsigned*)(LAS char*)(smem + F_CND + (F ? 60 - (lane & 7) * 4 : (lane & 7) * 4)) = best + 1;
    unsigned d;
    const unsigned ct = 2047u - (best & 0xffffu);
    asm volatile("buffer_load_dword %0, %1, %2, 0 offen\n\tbuffer_load_dword %0, %3, %4, 0 offen sc1" : "=&v"(d)
                 : "v"(lane < 32 ? ct * 2048u + q * 512 + (lane >> 3) * 128 : 0x7fffffffu), "s"(xrs), "v"(lane < 8 ? WS_R + ct * 16 : 0x7fffffffu), "s"(wrs) : "memory");
    gather2();
    unsigned ka, kb;
#pragma unroll 1
    for (unsigned n = 0; n < SPIN_CAP; ++n) {
      tick(nb);
      ka = *(const volatile LAS unsigned*)(LAS const char*)(smem + F_CND + (lane & 7) * 4);
      kb = *(const volatile LAS unsigned*)(LAS const char*)(smem + F_CND + 32 + (lane & 7) * 4);
      if (__all(min(ka, kb) != 0)) break;
      NAP(1);
    }
    const unsigned selkey = max(ka, kb) - 1, tok = 2047u - (selkey & 0xffffu);
    u32x4 xf[2], trec;
#pragma unroll 1
    for (unsigned n = 0; n < SPIN_CAP; ++n) {
      asm volatile("buffer_load_dwordx4 %0, %3, %4, 0 offen\n\tbuffer_load_dwordx4 %1, %3, %4, 0 offen offset:64\n\tbuffer_load_dwordx4 %2, %5, %6, 0 offen sc1"
                   : "=&v"(xf[0]), "=&v"(xf[1]), "=&v"(trec) : "v"(tok * 2048u + q * 512 + (li >> 3) * 256 + g * 16 + F * 128), "s"(xrs), "v"(WS_R + tok * 16), "s"(wrs) : "memory");
      if (n) tick(nb);
      asm volatile("s_waitcnt vmcnt(0)" : "+v"(xf[0]), "+v"(xf[1]), "+v"(trec), "+v"(d) :: "memory"); PING2();
      if (__all(chk(0u, trec, tag) == 0)) break;
      NAP(1);
    }
    const float mb = lo2f(trec[0]), rd = 1.0f / f_of(trec[2]);
#pragma unroll
    for (int s = 0; s < 2; ++s)
#pragma unroll
      for (int j = 0; j < 4; ++j) xf[s][j] = ln2s(xf[s][j], mb, rd);
    st16c(wrs, xs_off + F * 3072, u32x4{xf[0][0], xf[0][1], xf[0][2], tag});
    st16c(wrs, xs_off + F * 3072 + 1024, u32x4{xf[1][0], xf[1][1], xf[1][2], tag});
    st16c(wrs, xs_off + F * 3072 + 2048, u32x4{xf[0][3], xf[1][3], selkey, tag});
    tv = lane < 8 ? selkey : 1u;
    tick(nb);
    asm volatile("s_waitcnt vmcnt(0)\n\tbuffer_load_dword %0, %1, %2, 0 offen sc1" : "=&v"(d) : "v"(lane < 24 ? xs_off + F * 3072 + lane * 112 : 0x7fffffffu), "s"(wrs) : "memory");
    prepare();
    asm volatile("s_waitcnt vmcnt(0)" : "+v"(d) :: "memory");
    while (nb < NBAR) tick(nb);
    __builtin_amdgcn_s_setprio(1);
    wait_full(smem, F_FULL, 16);
    tables(1);
  } else {
    u32x4 xs[6];
    auto ask = [&]() {
#define X1K(i, o) "buffer_load_dwordx4 %" #i ", %6, %7, 0 offen offset:" #o " sc1\n\t"
#define X2K(i, o) "buffer_load_dwordx4 %" #i ", %6, %7, %8 offen offset:" #o " sc1\n\t"
      asm volatile(X1K(0, 0) X1K(1, 1024) X1K(2, 2048) X1K(3, 3072) X2K(4, 0) X2K(5, 1024)
                   : "=&v"(xs[0]), "=&v"(xs[1]), "=&v"(xs[2]), "=&v"(xs[3]), "=&v"(xs[4]), "=&v"(xs[5]) : "v"(xs_off), "s"(wrs), "s"(4096) : "memory");
      SCHED0();
    };
    auto good = [&]() {
      unsigned bad = xs[0][3] ^ tag;
#pragma unroll
      for (int j = 1; j < 6; ++j) bad = __builtin_amdgcn_bitop3_b32(bad, xs[j][3], tag, 0xF6);
      return __all(bad == 0);
    };
#define PIN6(a) asm volatile("" : "+v"(a[0]), "+v"(a[1]), "+v"(a[2]), "+v"(a[3]), "+v"(a[4]), "+v"(a[5]))
#define WAITS(snap, n) asm volatile("s_waitcnt vmcnt(%2)\n\ts_cmp_lg_u32 %0, %1\n\ts_cbranch_scc1 1f\n\ts_waitcnt vmcnt(0)\n\t1:" : "+s"(iss) : "s"(snap), "i"(n) : "scc", "memory")
    asm volatile("" : "=v"(cs[0]), "=v"(cs[1]), "=v"(cs[2]), "=v"(cs[3]), "=v"(rec[0]), "=v"(rec[1]));
    CH(0, 12 - 2, 3);
    tick(nb);
    CH(0, 12 - 2, 99);
    tick(nb);
    CH(1, 12 - 2, nb);
    tick(nb);
    WAITS(1, 14); PIN8(fw[0]); PIN8(fw[1]);
    tr_all(fw[0]);
    CH(1, 12 - 7, nb);
    tick(nb);
    tr_all(fw[1]);
    CH(1, 12 - 7, nb);
    tick(nb);
    CH(1, 12 - 2, 99);
    tick(nb);
    CH(2, 12 - 2, nb);
    tick(nb);
    WAITS(2, 8); PING2(); PIN8(fw[2]);
    ask();
    prepare();
    unsigned snap = iss;
    asm volatile("" : "+s"(snap));
    CH(2, 3, nb);
    tick(nb);
    WAITS(snap, 8); PIN6(xs);
    if (!good()) {
      CH(2, 0, 99);
#pragma unroll 1
      for (unsigned n = 0; n < SPIN_CAP; ++n) {
        ask(); tick(nb); WAITV(0); PIN6(xs);
        if (good()) break;
        NAP(4);
      }
    }
    tv = lane < 8 ? xs[2][2] : 1u;
    xs[0][3] = xs[2][0]; xs[1][3] = xs[2][1]; xs[3][3] = xs[5][0]; xs[4][3] = xs[5][1];
    PIN6(xs); asm volatile("" : "+v"(tv));
    if (nb == NBAR) { __builtin_amdgcn_s_setprio(1); wait_full(smem, F_FULL, 16); tables(1); }
    f32x4 acc[8];
    auto mma = [&](int s) {
      SCHED0(); __builtin_amdgcn_s_setprio(3); SCHED0();
#pragma unroll
      for (int i = 0; i < 8; ++i) acc[i] = MFMA16(__builtin_bit_cast(bf16x8, fw[s][i]), __builtin_bit_cast(bf16x8, xs[s + (s >> 1)]), s ? acc[i] : (f32x4{0, 0, 0, 0}));
      SCHED0(); __builtin_amdgcn_s_setprio(0); SCHED0();
      PIN8(acc);
    };
    mma(0);
    tr_all(fw[2]);
    CH(2, 12 - 9, 99);
    slice();
    mma(1);
    mma(2);
    slice();
    WAITV(0); PIN8(fw[3]);
    tr_all(fw[3]);
    mma(3);
    {
#pragma unroll
      for (int r = 0; r < 4; ++r) {
        float v[2];
#pragma unroll
        for (int z = 0; z < 2; ++z) {
          auto a = __builtin_amdgcn_permlane32_swap(__float_as_uint(acc[z][r]), __float_as_uint(acc[z + 4][r]), false, false);
          auto b = __builtin_amdgcn_permlane32_swap(__float_as_uint(acc[z + 2][r]), __float_as_uint(acc[z + 6][r]), false, false);
          v[z] = f_of(li < 8 ? a[0] : b[1]) + dpp_f<0x128>(f_of(li < 8 ? b[0] : a[1]));
        }
        st16c(wrs, WS_GU + (unsigned)(((cu * 4 + q) * 2 + F) * 4096 + r * 1024 + lane * 16), u32x4{__float_as_uint(v[0]), tag, __float_as_uint(v[1]), tag});
      }
      SCHED0();
    }
    while (nb < NBAR) tick(nb);
    __builtin_amdgcn_s_setprio(1);
    u32x4 fd[2][8];
    {
      const rsrc_t drs = mk_rsrc(wd, DM * FF * 2);
      const unsigned dof = (unsigned)(cg * 64 + 8 * g) * (DM * 2) + q * 512 + F * 256 + li * 16;
      NAP(4);
#pragma unroll
      for (int s = 0; s < 2; ++s)
#pragma unroll
        for (int j = 0; j < 8; ++j) fd[s][j] = __builtin_amdgcn_raw_buffer_load_b128(drs, dof + (j & 1) * (DM * 2), (s * 32 + (j & 6)) * (DM * 2), 2);
      SCHED0();
    }
    wait_full(smem, F_FULL, 16);
    tables(1);
    if (bid == 0 && F == 0 && lane == 0) __hip_atomic_store((GAS unsigned*)(ws + WS_EP), tag, RLX_AGENT);
    hidden_half(smem, lane, F, cg, ws, tag, fd);
    wait_bytes<0x8080u>(smem, F_H, 1);
    const bf16x8 h0 = lds_b128(smem + L_HF + (li & 7) * 128 + g * 16), h1 = lds_b128(smem + L_HF + (li & 7) * 128 + 64 + g * 16);
#pragma unroll
    for (int i = 0; i < 8; ++i) {
      acc[i] = MFMA16(h0, __builtin_bit_cast(bf16x8, fd[0][i]), (f32x4{0, 0, 0, 0}));
      acc[i] = MFMA16(h1, __builtin_bit_cast(bf16x8, fd[1][i]), acc[i]);
    }
    const unsigned off = WS_Y + (unsigned)((cg * 8 + (g & 1) * 4) * DM + q * 256 + F * 128 + li * 8 + (g >> 1) * 4) * 4u;
#pragma unroll
    for (int r = 0; r < 4; ++r) {
      const unsigned lo = pack2(acc[0][r], acc[1][r]), hi = pack2(acc[2][r], acc[3][r]), lo2 = pack2(acc[4][r], acc[5][r]), hi2 = pack2(acc[6][r], acc[7][r]);
      st16c(wrs, off + r * (DM * 4), u32x4{g < 2 ? lo : lo2, tag, g < 2 ? hi : hi2, tag});
    }
  }
  __builtin_amdgcn_s_waitcnt(0x0F70);
}

__global__ __launch_bounds__(512, 2) void fused_block(const u16* __restrict__ x, const u16* __restrict__ wqkv, const u16* __restrict__ wo,
                                                      const u16* __restrict__ wr, const u16* __restrict__ wg, const u16* __restrict__ wu,
                                                      const u16* __restrict__ wd, char* ws, u16* __restrict__ out) {
  __shared__ __attribute__((aligned(128))) char smem[L_SIZE];
  unsigned long long pc;
  unsigned first;
  asm volatile("v_cmp_gt_u32 vcc, 24, %2\n\ts_getpc_b64 %0\n\ts_cbranch_vccz 1f\n\ts_mov_b64 exec, vcc\n\tv_lshlrev_b32 %1, 7, %2\n\tglobal_load_dword %1, %1, %0\n\ts_mov_b64 exec, -1\n\t1:"
               : "=s"(pc), "=&v"(first) : "v"(threadIdx.x) : "vcc", "memory");
  SCHED0();
  const int lane = threadIdx.x & 63;
  const int wid = __builtin_amdgcn_readfirstlane(threadIdx.x >> 6);
  const int bid = blockIdx.x, xcd = bid & 7, off = bid >> 3;
  const int tm = (xcd & 1) * 8 + (xcd & 2) * 2 + (off & 3), tn = (xcd >> 2) * 8 + (off >> 2);
  if (wid == 7) {
    unsigned d;
    asm volatile("s_getpc_b64 %0" : "=s"(pc));
    asm volatile("buffer_load_dword %0, %1, %2, 0 offen\n\ts_waitcnt vmcnt(0)" : "=&v"(d)
                 : "v"((unsigned)(lane * 32 + off) * 128u), "s"(mk_rsrc((const void*)(pc & ~255ull), CODE_BYTES)) : "memory");
  }
  asm volatile("" : "+s"(x), "+s"(wqkv), "+s"(wr), "+s"(ws), "+s"(out), "+s"(wo), "+s"(wd), "+s"(wg), "+s"(wu));
  unsigned rot2 = ((((unsigned)(0x65432106543210ull >> (off & 3) * 8) & 0xfffffffu) + ((tn & 7) + 1) * 0x1111111u) & 0x7777777u) * 2 | (unsigned)tn << 29;
  unsigned tag = 0;
  RowRegs rr;
  EpiRegs e;
  row_request(rr, x, wr, ws, tm * 128 + tn * 8 + wid * 2, lane, wid < 4);
  rr.keep = first;
  SCHED0();
  if ((wid >> 1) == 2) {
    __builtin_amdgcn_s_setprio(2);
    const Stager stage(smem, wid - 4, lane, tm, tn, x, wqkv);
    NAP(4);
    stage_x(stage, 0); stage_w(stage, 0); stage_x(stage, 1); stage_w(stage, 1);
    WAITV(21);
    BAR();
    stage_x(stage, 2); stage_w(stage, 2);
  }
  asm volatile("" ::: "memory");
  if (wid < 4) {
    __builtin_amdgcn_s_setprio(2);
    qkv_setup(smem, wid, lane, tm, tn, e);
    asm volatile("" : "+s"(rot2));
    SCHED0();
    tag = row_stats(rr, ws, tm * 128 + tn * 8 + wid * 2, lane);
    if (wid == 0) *(unsigned*)(smem + L_FLG + lane * 4) = lane * 4 == F_TAG - L_FLG ? tag : 0u;
  }
  asm volatile("" ::: "memory");
  if (wid < 4) {
    if (wid & 1) qkv_attention<1>(smem, wid, lane, tm, tn, ws, tag, x, e);
    else qkv_attention<0>(smem, wid, lane, tm, tn, ws, tag, x, e);
    out_proj(smem, wid, lane, tm, tn, ws, tag, out, e, rot2);
  } else {
    if (wid < 6) loader(smem, wid - 4, lane, tm, tn, rot2, x, wqkv, wo, ws);
    else side_wave(smem, wid - 6, lane, bid, tm, tn, x, wid == 6 ? wg : wu, wd, ws);
    updates(smem, (0x2130 >> ((wid - 4) * 4)) & 3, lane, tm, tn, ws);
    __builtin_amdgcn_s_waitcnt(0x0F70);
  }
}

extern "C" void kernel_launch(void* const* d_in, const int* in_sizes, int n_in, void* d_out, int out_size, void* d_ws,
                              size_t ws_size, hipStream_t stream) {
  fused_block<<<256, 512, 0, stream>>>((const u16*)d_in[0], (const u16*)d_in[1], (const u16*)d_in[2], (const u16*)d_in[4],
                                       (const u16*)d_in[5], (const u16*)d_in[6], (const u16*)d_in[7], (char*)d_ws, (u16*)d_out);
}
```

```cpp
#include <hip/hip_runtime.h>
#include <hip/hip_bf16.h>
#include <stdint.h>

typedef __attribute__((ext_vector_type(8))) short bf16x8;
typedef __attribute__((ext_vector_type(4))) short s16x4;
typedef __attribute__((ext_vector_type(4))) float f32x4;
typedef __attribute__((ext_vector_type(2))) float f32x2;
typedef __attribute__((ext_vector_type(2))) __bf16 bf16x2_t;
typedef __attribute__((ext_vector_type(4))) unsigned u32x4;
typedef unsigned short u16;
#define LAS __attribute__((address_space(3)))
#define GAS __attribute__((address_space(1)))
#define RLX_AGENT __ATOMIC_RELAXED, __HIP_MEMORY_SCOPE_AGENT

#define S_TOK 2048
#define DM 1024
#define NQKV 3072
#define FF 3584

typedef __attribute__((ext_vector_type(2))) unsigned u32x2;

#define WS_EP   0
#define WS_R    4096
#define WS_K    (WS_R + 32768)
#define WS_OF   (WS_K + 8192)
#define WS_CS   (WS_OF + 4096)
#define WS_GU   (WS_CS + 16 * 2 * 4096)
#define WS_Y    (WS_GU + 56 * 4 * 8192)
#define WS_S    (WS_Y + 56 * 8 * DM * 4)
#define WS_O    (8u << 20)

__device__ const unsigned k_tags[32] = {0,  1,  2,  3,  4,  5,  6,  7,  8,  9,  10, 11, 12, 13, 14, 15,
                                        16, 17, 18, 19, 20, 21, 22, 23, 24, 25, 26, 27, 28, 29, 30, 31};

__device__ __forceinline__ unsigned pack2(float a, float b) {
  f32x2 v = {a, b};
  bf16x2_t r = __builtin_convertvector(v, bf16x2_t);
  return __builtin_bit_cast(unsigned, r);
}
__device__ __forceinline__ float rbf(float f) { return __uint_as_float(pack2(f, f) << 16); }
__device__ __forceinline__ float lo2f(unsigned w) { return __uint_as_float(w << 16); }
__device__ __forceinline__ float hi2f(unsigned w) { return __uint_as_float(w & 0xffff0000u); }

__device__ __forceinline__ bf16x8 lds_b128(const char* p) { return *(const bf16x8*)p; }
__device__ __forceinline__ bf16x8 lds_tr2(const char* p) {
  s16x4 lo = __builtin_amdgcn_ds_read_tr16_b64_v4i16((LAS s16x4*)p);
  s16x4 hi = __builtin_amdgcn_ds_read_tr16_b64_v4i16((LAS s16x4*)(p + 512));
  return __builtin_shufflevector(lo, hi, 0, 1, 2, 3, 4, 5, 6, 7);
}
#define MFMA16(a, b, c) __builtin_amdgcn_mfma_f32_16x16x32_bf16(a, b, c, 0, 0, 0)
#define WAITV(n) asm volatile("s_waitcnt vmcnt(%0)" ::"i"(n) : "memory")
#define WAITL0() __builtin_amdgcn_s_waitcnt(0xC07F)
#define BAR() __builtin_amdgcn_s_barrier()
#define SCHED0() __builtin_amdgcn_sched_barrier(0)
#define NAP(n) asm volatile("s_sleep %0" ::"i"(n) : "memory")
__device__ __forceinline__ float xsum16_32(float v) {
  unsigned u = __float_as_uint(v);
  auto a = __builtin_amdgcn_permlane16_swap(u, u, false, false);
  v = __uint_as_float(a[0]) + __uint_as_float(a[1]);
  u = __float_as_uint(v);
  auto b = __builtin_amdgcn_permlane32_swap(u, u, false, false);
  return __uint_as_float(b[0]) + __uint_as_float(b[1]);
}
__device__ __forceinline__ bf16x8 pack8(const f32x4& a, const f32x4& b) {
  u32x4 r = {pack2(a[0], a[1]), pack2(a[2], a[3]), pack2(b[0], b[1]), pack2(b[2], b[3])};
  return __builtin_bit_cast(bf16x8, r);
}
__device__ __forceinline__ unsigned wave_max_u32(unsigned v) {
  v = max(v, (unsigned)__builtin_amdgcn_update_dpp(0, (int)v, 0x111, 0xf, 0xf, false));
  v = max(v, (unsigned)__builtin_amdgcn_update_dpp(0, (int)v, 0x112, 0xf, 0xf, false));
  v = max(v, (unsigned)__builtin_amdgcn_update_dpp(0, (int)v, 0x114, 0xf, 0xf, false));
  v = max(v, (unsigned)__builtin_amdgcn_update_dpp(0, (int)v, 0x118, 0xf, 0xf, false));
  v = max(v, (unsigned)__builtin_amdgcn_update_dpp(0, (int)v, 0x142, 0xa, 0xf, false));
  v = max(v, (unsigned)__builtin_amdgcn_update_dpp(0, (int)v, 0x143, 0xc, 0xf, false));
  return (unsigned)__builtin_amdgcn_readlane((int)v, 63);
}
__device__ __forceinline__ bf16x8 tr_frag(const u32x4 (&w)[8], int i) {
  const unsigned sel = (i & 1) ? 0x07060302u : 0x05040100u;
  u32x4 r = {__builtin_amdgcn_perm(w[1][i >> 1], w[0][i >> 1], sel), __builtin_amdgcn_perm(w[3][i >> 1], w[2][i >> 1], sel),
             __builtin_amdgcn_perm(w[5][i >> 1], w[4][i >> 1], sel), __builtin_amdgcn_perm(w[7][i >> 1], w[6][i >> 1], sel)};
  return __builtin_bit_cast(bf16x8, r);
}

#define XSL_ 18432
#define WSL 24576
#define XB (3 * XSL_)
#define NBAR 14
#define L_UP 0
#define L_WA 6144
#define L_W3 (L_WA + 57344)
#define L_WB (L_W3 + 16384)
#define L_OWN (L_WB + 57344)
#define L_XCH (XB + 4 * WSL)
#define L_HF (L_XCH + 6144)
#define L_CSF (L_HF + 1024)
#define L_QST (L_CSF + 1536)
#define L_FLG (L_QST + 1152)
#define L_SIZE (L_FLG + 256)
#define F_FULL (L_FLG + 0)
#define F_ST (L_FLG + 8)
#define F_WO (L_FLG + 16)
#define F_FREE (L_FLG + 32)
#define F_XCH (L_FLG + 40)
#define F_ODONE (L_FLG + 48)
#define F_SELOK (L_FLG + 52)
#define F_GO (L_FLG + 56)
#define F_OL (L_FLG + 60)
#define F_H (L_FLG + 64)
#define F_TAG (L_FLG + 68)
#define F_UP (L_FLG + 72)
#define F_SEL (L_FLG + 96)
#define F_CND (L_FLG + 128)
#define OD 7
#define SPIN_CAP (1u << 18)
#define FDN 4
#define D10 3
#define TW 1
#define TK 2
#define DL 12
#define NB1 6
#define DUE 0x3012
#define CD 4
#define EV 4
#define PN 2
#define YD 24
#define OK 2
#define ON 8
#define HN 2
#define PR 8
#define TS 0
#define SX 0
#define CODE_BYTES 54656

#define RSC(i)
#define STAMP(i)
#define RST(i)

__device__ __forceinline__ unsigned lds_addr(const char* p) { return (unsigned)(size_t)(LAS const char*)p; }
__device__ __forceinline__ void lds_post(char* smem, int off, unsigned v) {
  asm volatile("s_mov_b64 exec, 1\n\tds_write_b8 %0, %1\n\ts_mov_b64 exec, -1" ::"v"(lds_addr(smem + off)), "v"(v) : "memory");
}
__device__ __forceinline__ unsigned lds_word(const char* smem, int off) { return *(const volatile LAS unsigned*)(LAS const char*)(smem + off); }
__device__ __forceinline__ u32x4 lds_word4(const char* smem, int off) { return *(const volatile LAS u32x4*)(LAS const char*)(smem + off); }
template <unsigned MASK, bool COND = false>
__device__ __forceinline__ unsigned wait_bytes(const char* smem, int off, unsigned need, unsigned skip = 0) {
  unsigned t, f;
  asm volatile(
      ".if %6\n\ts_cmp_lg_u32 %5, 0\n\ts_cbranch_scc1 2f\n\t.endif\n\t"
      "1:\n\t"
      "ds_read_b32 %1, %2\n\t"
      "s_waitcnt lgkmcnt(0)\n\t"
      "v_readfirstlane_b32 %0, %1\n\t"
      "s_or_b32 %0, %0, 0x80808080\n\t"
      "s_sub_u32 %0, %0, %3\n\t"
      "s_and_b32 %0, %0, %4\n\t"
      "s_cmp_eq_u32 %0, %4\n\t"
      "s_cbranch_scc1 2f\n\t"
      "s_sleep 1\n\t"
      "s_branch 1b\n\t"
      "2:"
      : "=&s"(t), "=&v"(f)
      : "v"(lds_addr(smem + off)), "s"(need * 0x01010101u), "s"(MASK), "s"(COND ? skip : MASK), "i"((int)COND)
      : "scc", "memory");
  return f;
}
__device__ __forceinline__ void wait_full(const char* smem, int off, unsigned need, unsigned cur = 0) {
  unsigned a, b;
  asm volatile(
      "s_cmp_ge_u32 %0, %4\n\t"
      "s_cbranch_scc1 3f\n\t"
      "s_branch 2f\n\t"
      "1:\n\t"
      "s_sleep 1\n\t"
      "2:\n\t"
      "ds_read_b32 %1, %3\n\t"
      "ds_read_b32 %2, %3 offset:4\n\t"
      "s_waitcnt lgkmcnt(0)\n\t"
      "v_min_u32 %1, %1, %2\n\t"
      "s_nop 0\n\t"
      "v_readfirstlane_b32 %0, %1\n\t"
      "s_cmp_lt_u32 %0, %4\n\t"
      "s_cbranch_scc1 1b\n\t"
      "3:"
      : "+s"(cur), "=&v"(a), "=&v"(b)
      : "v"(lds_addr(smem + off)), "s"(need)
      : "scc", "memory");
}
__device__ __forceinline__ unsigned uni(unsigned v) { return (unsigned)__builtin_amdgcn_readfirstlane((int)v); }
__device__ __forceinline__ void dma_tag(char* smem, int off, int n) {
  unsigned keep;
  asm volatile("s_nop 2\n\ts_mov_b32 %0, m0\n\ts_mov_b32 m0, %1\n\ts_mov_b64 exec, 1\n\tglobal_load_lds_dword %2, %3\n\ts_mov_b64 exec, -1\n\ts_mov_b32 m0, %0\n\ts_nop 0"
               : "=&s"(keep) : "s"(uni(lds_addr(smem + off))), "v"(n * 4), "s"(k_tags) : "memory");
}
template <int V> struct IC { static constexpr int v = V; };
struct QFrag { bf16x8 xa[5]; bf16x8 w[3][2]; };
__device__ __forceinline__ void q_read(QFrag& f, const char* sa, const char* sb0, const char* sb1, int ks) {
#pragma unroll
  for (int i = 0; i < 5; ++i) f.xa[i] = lds_b128(sa + i * 2048);
#pragma unroll
  for (int s = 0; s < 3; ++s) {
    f.w[s][0] = lds_tr2(sb0 + s * 8192 + ks * 4096);
    f.w[s][1] = lds_tr2(sb1 + s * 8192 + ks * 4096);
  }
}
__device__ constexpr int acc_no(int HALF, int m, int i, int t) {
  int n = 0;
  for (int ii = 0; ii < 5; ++ii)
    for (int tt = 0; tt < 2; ++tt)
      for (int mm = 0; mm < 3; ++mm) {
        const bool on = mm == 0 ? (HALF == 1 || (ii >= 1 && ii <= 3)) : (HALF == 0 || ii >= 1);
        if (mm == m && ii == i && tt == t) return on ? n : -1;
        if (on) ++n;
      }
  return -1;
}
__device__ constexpr int seq_of(int HALF, int z) {
  int n = 0;
  for (int a = 0; a < 30; ++a) {
    const int m = a / 10, t0 = (a / 5) % 2;
    const int i = m < 2 ? (t0 ? a % 5 : 4 - a % 5) : 4 - (a - 20) / 2, t = m < 2 ? t0 : (a ^ i) & 1;
    if (acc_no(HALF, m, i, t) >= 0) { if (n == z) return m * 100 + i * 10 + t; ++n; }
  }
  return -1;
}
template <int HALF, int Z, int E, bool FIRST>
__device__ __forceinline__ void q_one(const QFrag& f, f32x4 (&acc)[26]) {
  if constexpr (Z < E) {
    constexpr int m = seq_of(HALF, Z) / 100, i = seq_of(HALF, Z) / 10 % 10, t = seq_of(HALF, Z) % 10;
    constexpr int n = acc_no(HALF, m, i, t);
    const f32x4 c = FIRST ? f32x4{0, 0, 0, 0} : acc[n];
    acc[n] = m == 2 ? MFMA16(f.xa[i], f.w[2][t], c) : MFMA16(f.w[m][t], f.xa[i], c);
    q_one<HALF, Z + 1, E, FIRST>(f, acc);
  }
}
template <int HALF, int B, int E, int NRD, bool FIRST = false>
__device__ __forceinline__ void q_mma(const QFrag& f, f32x4 (&acc)[26]) {
  q_one<HALF, B, E, FIRST>(f, acc);
#pragma unroll
  for (int i = 0; i < NRD; ++i) {
    __builtin_amdgcn_sched_group_barrier(0x008, 1, 0);
    __builtin_amdgcn_sched_group_barrier(0x100, 1, 0);
  }
  __builtin_amdgcn_sched_group_barrier(0x008, E - B - NRD, 0);
}

__device__ __forceinline__ void tr_all(u32x4 (&w)[8]) {
  u32x4 t[8];
#pragma unroll
  for (int i = 0; i < 8; ++i) t[i] = __builtin_bit_cast(u32x4, tr_frag(w, i));
#pragma unroll
  for (int i = 0; i < 8; ++i) w[i] = t[i];
  asm volatile("" : "+v"(w[0]), "+v"(w[1]), "+v"(w[2]), "+v"(w[3]), "+v"(w[4]), "+v"(w[5]), "+v"(w[6]), "+v"(w[7]));
}
typedef __amdgpu_buffer_rsrc_t rsrc_t;
__device__ __forceinline__ rsrc_t mk_rsrc(const void* p, unsigned bytes) { return __builtin_amdgcn_make_buffer_rsrc((void*)p, 0, bytes, 0x00020000); }
__device__ __forceinline__ u32x4 ld16(rsrc_t rs, unsigned voff, unsigned soff) { return __builtin_amdgcn_raw_buffer_load_b128(rs, voff, soff, 0); }
__device__ __forceinline__ u32x4 ld16c(rsrc_t rs, unsigned voff, unsigned soff) { return __builtin_amdgcn_raw_buffer_load_b128(rs, voff, soff, 16); }
__device__ __forceinline__ void st16c(rsrc_t rs, unsigned voff, u32x4 v) { __builtin_amdgcn_raw_buffer_store_b128(v, rs, voff, 0, 16); }
__device__ __forceinline__ void st8c(rsrc_t rs, unsigned voff, unsigned v, unsigned tag) { __builtin_amdgcn_raw_buffer_store_b64(u32x2{v, tag}, rs, voff, 0, 16); }
__device__ __forceinline__ unsigned chk(unsigned bad, const u32x4& v, unsigned tag) {
  return __builtin_amdgcn_bitop3_b32(__builtin_amdgcn_bitop3_b32(bad, v[1], tag, 0xF6), v[3], tag, 0xF6);
}
__device__ __forceinline__ float f_of(unsigned u) { return __uint_as_float(u); }
#define LANE_VARS                                                                   \
  int lane = lane_in; asm volatile("" : "+v"(lane));                                \
  const int g = lane >> 4, r16 = lane & 15, q = (lane >> 2) & 3, p = lane & 3;      \
  const int hk = (q >> 1) | ((g & 1) << 1), brow = (8 * g + q) * 128 + 8 * p;       \
  (void)r16; (void)hk; (void)brow;

template <bool MAX>
__device__ __forceinline__ float rows_reduce(const float (&v)[4]) {
  auto op = [](unsigned a, unsigned b) {
    return __float_as_uint(MAX ? __builtin_elementwise_maximum(__uint_as_float(a), __uint_as_float(b)) : __uint_as_float(a) + __uint_as_float(b));
  };
  auto p = __builtin_amdgcn_permlane16_swap(__float_as_uint(v[0]), __float_as_uint(v[1]), false, false);
  auto q = __builtin_amdgcn_permlane16_swap(__float_as_uint(v[2]), __float_as_uint(v[3]), false, false);
  auto t = __builtin_amdgcn_permlane32_swap(op(p[0], p[1]), op(q[0], q[1]), false, false);
  return __uint_as_float(op(t[0], t[1]));
}
__device__ __forceinline__ void rows_spread(float t, float (&v)[4]) {
  auto h = __builtin_amdgcn_permlane32_swap(__float_as_uint(t), __float_as_uint(t), false, false);
  auto p = __builtin_amdgcn_permlane16_swap(h[0], h[0], false, false);
  auto q = __builtin_amdgcn_permlane16_swap(h[1], h[1], false, false);
  v[0] = __uint_as_float(p[0]); v[1] = __uint_as_float(p[1]); v[2] = __uint_as_float(q[0]); v[3] = __uint_as_float(q[1]);
}
typedef __attribute__((ext_vector_type(16))) unsigned u32x16;

template <int CTL>
__device__ __forceinline__ float dpp_f(float v) { return __uint_as_float(__builtin_amdgcn_update_dpp(0, (int)__float_as_uint(v), CTL, 0xf, 0xf, false)); }
__device__ __forceinline__ float sum16(float v) {
  v += dpp_f<0xB1>(v);
  v += dpp_f<0x4E>(v);
  v += dpp_f<0x141>(v);
  return v + dpp_f<0x140>(v);
}
__device__ __forceinline__ float dot2(unsigned a, unsigned b, float c) {
  return __builtin_amdgcn_fdot2_f32_bf16(__builtin_bit_cast(bf16x2_t, a), __builtin_bit_cast(bf16x2_t, b), c, false);
}
__device__ __forceinline__ float sum32(float v) {
  v = sum16(v);
  auto a = __builtin_amdgcn_permlane16_swap(__float_as_uint(v), __float_as_uint(v), false, false);
  return __uint_as_float(a[0]) + __uint_as_float(a[1]);
}
struct RowRegs { u32x4 x[4], w[2]; unsigned ep, keep; };
__device__ __forceinline__ void row_request(RowRegs& r, const u16* __restrict__ x, const u16* __restrict__ wr, char* ws, int row0, int lane, bool on) {
  if (!on) return;
  const rsrc_t xrs = mk_rsrc(x, S_TOK * DM * 2), rrs = mk_rsrc(wr, DM * 2);
  const unsigned xo = (unsigned)(row0 + (lane >> 5)) * 2048u + (lane & 31) * 16, wo = lane * 16;
  asm volatile("buffer_load_dword %0, off, %1, 0 sc1" : "=v"(r.ep) : "s"(mk_rsrc(ws + WS_EP, 4)) : "memory");
#pragma unroll
  for (int j = 0; j < 4; ++j) asm volatile("buffer_load_dwordx4 %0, %1, %2, 0 offen offset:%3" : "=v"(r.x[j]) : "v"(xo), "s"(xrs), "i"(j * 512) : "memory");
#pragma unroll
  for (int j = 0; j < 2; ++j) asm volatile("buffer_load_dwordx4 %0, %1, %2, 0 offen offset:%3" : "=v"(r.w[j]) : "v"(wo), "s"(rrs), "i"(j * 1024) : "memory");
}
__device__ __forceinline__ unsigned row_stats(RowRegs& r, char* ws, int row0, int lane) {
  float s0 = 0.f, s1 = 0.f;
  asm volatile("s_waitcnt vmcnt(2)" : "+v"(r.x[0]), "+v"(r.x[1]), "+v"(r.x[2]), "+v"(r.x[3]), "+v"(r.ep), "+v"(r.keep) :: "memory");
  const unsigned tag = uni(r.ep) + 1u;
#pragma unroll
  for (int j = 0; j < 4; ++j) {
    s0 = dot2(r.x[j][0], 0x3f803f80u, s0); s1 = dot2(r.x[j][1], 0x3f803f80u, s1);
    s0 = dot2(r.x[j][2], 0x3f803f80u, s0); s1 = dot2(r.x[j][3], 0x3f803f80u, s1);
  }
  const float mean = sum32(s0 + s1) * (1.0f / DM), mb = rbf(mean);
  s0 = 0.f; s1 = 0.f;
#pragma unroll
  for (int j = 0; j < 4; ++j)
#pragma unroll
    for (int i = 0; i < 4; ++i) {
      const float a = lo2f(r.x[j][i]) - mb, b = hi2f(r.x[j][i]) - mb;
      s0 = fmaf(a, a, s0); s1 = fmaf(b, b, s1);
      r.x[j][i] = pack2(a, b);
    }
  const float dm = mean - mb;
  const float var = rbf(fmaxf(sum32(s0 + s1) * (1.0f / DM) - dm * dm, 0.f));
  const float den = rbf(__builtin_amdgcn_sqrtf(rbf(var + 1e-5f))), rd = __builtin_amdgcn_rcpf(den);
  s0 = 0.f; s1 = 0.f;
  asm volatile("s_waitcnt vmcnt(0)" : "+v"(r.w[0]), "+v"(r.w[1]) :: "memory");
#pragma unroll
  for (int j = 0; j < 4; ++j)
#pragma unroll
    for (int i = 0; i < 4; ++i) {
      const unsigned n = pack2(lo2f(r.x[j][i]) * rd, hi2f(r.x[j][i]) * rd);
      const unsigned w = __builtin_amdgcn_permlane32_swap(r.w[j >> 1][i], r.w[j >> 1][i], false, false)[j & 1];
      if (i & 1) s1 = dot2(n, w, s1); else s0 = dot2(n, w, s0);
    }
  const float dot = sum32(s0 + s1);
  unsigned k = pack2(dot, dot) & 0xffffu;
  k = (k & 0x8000u) ? (~k & 0xffffu) : (k | 0x8000u);
  const rsrc_t wrs = mk_rsrc(ws, WS_O);
  st16c(wrs, (lane & 31) == 0 ? WS_R + (unsigned)(row0 + (lane >> 5)) * 16u : 0x7fffffffu,
        u32x4{(k << 16) | (__float_as_uint(mb) >> 16), tag, __float_as_uint(den), tag});
  const unsigned k01 = k | ((unsigned)__builtin_amdgcn_readlane((int)k, 32) << 16);
  st8c(wrs, lane == 0 ? WS_K + (unsigned)(((row0 >> 2) & 7) * 1024 + (row0 >> 5) * 16 + (row0 & 2) * 4) : 0x7fffffffu, k01, tag);
  return tag;
}
__device__ __forceinline__ void k_order(int tm, int tn, int& pa, int& pb) {
  pb = tn & 3;
  pa = (tm * 0x1111) ^ ((tn & 4) * 0x51 & 0x144) ^ ((~tn & 2) * 0x2200) ^ ((~tn & 1) << 9) ^ (pb == 3 ? 1 : tm & 1);
}
__device__ __forceinline__ int k_tile(int t, int pa, int pb) { return ((((t + pb) & 12) ? t + pb : t & 3) ^ (pa >> ((t + pb) & 12))) & 15; }
struct EpiRegs { int a0, b0, tx; unsigned xr, go; u32x4 xv[2][2]; unsigned t[16]; };
__device__ __forceinline__ float opq(float v) { asm("" : "+v"(v)); return v; }
typedef __attribute__((ext_vector_type(10))) unsigned u32x10;
#define DV 0
#define PRE 3
#define GRP(N, M, K, C) _Pragma("unroll") for (int z_ = 0; z_ < (N); ++z_) { __builtin_amdgcn_sched_group_barrier(0x008, M, 0); __builtin_amdgcn_sched_group_barrier(K, C, 0); }
__device__ constexpr int tail_of(int HALF, int z) {
  int n = 0;
  for (int a = 0; a < 30; ++a) {
    const int i = HALF ? a / 6 : 4 - a / 6, m = 2 - a % 6 / 2, t = a & 1;
    if (acc_no(HALF, m, i, t) >= 0 && (m < 2 || (HALF == 0 && i == 4))) { if (n == z) return m * 100 + i * 10 + t; ++n; }
  }
  return -1;
}
__device__ __forceinline__ void qkv_setup(char* smem, int wid, int lane_in, int tm, int tn, EpiRegs& e) {
  LANE_VARS
  e.a0 = (wid & 1) * 8192 + r16 * 128 + ((g ^ (r16 >> 1)) << 4);
  e.b0 = XB + brow + (((wid & 2) ^ hk) << 5);
  int pa, pb;
  k_order(tm, tn, pa, pb);
  e.tx = __builtin_ctz((unsigned)__ballot(k_tile(r16, pa, pb) == tn));
  e.xr = lds_addr(smem) + (16 + wid * 32 + r16) * 128 + ((((g & 1) * 2 + (g >> 1)) ^ (r16 >> 1)) << 4);
  asm volatile("" : "+v"(e.a0), "+v"(e.b0), "+v"(e.xr));
  asm volatile("" : "+s"(e.tx));
}
template <int HALF>
__device__ __forceinline__ void qkv_attention(char* smem, int wid, int lane_in, int tm, int tn, char* ws, unsigned tag,
                                              const u16* __restrict__ x, EpiRegs& e) {
  const int h = wid >> 1;
  f32x4 acc[26];
  float ra[5], rb[5], sa[4], sb[4];
  QFrag f0, f1;
  u32x4 fl;
  unsigned aq, ac, aa, ab, xa, xv, xf, ooff, sel[4];
  {
    LANE_VARS
    const int a0 = e.a0, b0 = e.b0, tx = uni(e.tx);
    const unsigned xr = e.xr;
    asm volatile("" : "=v"(e.xv[0][0]), "=v"(e.xv[0][1]), "=v"(e.xv[1][0]), "=v"(e.xv[1][1]));
    auto keep = [&](int t, int slot, int tx) {
      unsigned p0, p1;
      asm volatile("s_cmp_lg_u32 %7, %8\n\ts_cbranch_scc1 1f\n\tv_add_u32 %4, %9, %6\n\tv_xor_b32 %5, 64, %4\n\tds_read_b128 %0, %4\n\tds_read_b128 %1, %5\n\t"
                   "ds_read_b128 %2, %4 offset:2048\n\tds_read_b128 %3, %5 offset:2048\n\t1:"
                   : "+v"(e.xv[0][0]), "+v"(e.xv[0][1]), "+v"(e.xv[1][0]), "+v"(e.xv[1][1]), "=&v"(p0), "=&v"(p1) : "v"(xr), "s"(t), "s"(tx), "s"(slot) : "scc", "memory");
    };
    SCHED0();
    WAITL0();
    BAR();
    STAMP(1);
    q_read(f0, smem + a0, smem + b0, smem + (b0 ^ 32), 0);
    SCHED0();
    q_read(f1, smem + (a0 ^ 64), smem + b0, smem + (b0 ^ 32), 1);
    int qa = XSL_ + a0, qc = XSL_ + (a0 ^ 64), qb = WSL + b0, qd = WSL + (b0 ^ 32), so = XSL_;
    q_mma<HALF, 0, 26, 17, true>(f0, acc);
    asm volatile("" : "+v"(qa), "+v"(qb), "+v"(qc), "+v"(qd));
    SCHED0();
    keep(0, 0, tx);
#pragma unroll 1
    for (int t = 1; t < NBAR - 1; ) {
      SCHED0();
      WAITL0();
      BAR();
      SCHED0();
      q_read(f0, smem + qa, smem + qb, smem + qd, 0);
      q_mma<HALF, 0, 26, 17>(f1, acc);
      SCHED0();
      q_read(f1, smem + qc, smem + qb, smem + qd, 1);
      q_mma<HALF, 0, 19, 17>(f0, acc);
      SCHED0();
      int d = so == 2 * XSL_ ? -2 * XSL_ : XSL_;
      asm volatile("" : "+s"(d));
      SCHED0();
      q_mma<HALF, 19, 20, 0>(f0, acc);
      SCHED0();
      keep(t, so, tx);
      SCHED0();
      q_mma<HALF, 20, 21, 0>(f0, acc);
      SCHED0(); qa += d; qc += d; asm volatile("" : "+v"(qa), "+v"(qc)); SCHED0();
      q_mma<HALF, 21, 22, 0>(f0, acc);
      SCHED0(); so += d; ++t; asm volatile("" : "+s"(so), "+s"(t)); SCHED0();
      q_mma<HALF, 22, 23, 0>(f0, acc);
      SCHED0(); int e = (t & 3) ? WSL : -3 * WSL; asm volatile("" : "+s"(e)); SCHED0();
      q_mma<HALF, 23, 24, 0>(f0, acc);
      SCHED0(); qb += e; qd += e; asm volatile("" : "+v"(qb), "+v"(qd)); SCHED0();
      q_mma<HALF, 24, 26, 0>(f0, acc);
      SCHED0();
    }
    WAITL0();
    BAR();
    STAMP(2);
#pragma unroll 1
    for (int t = NBAR + 1; t < 17; ) {
      SCHED0();
      q_read(f0, smem + qa, smem + qb, smem + qd, 0);
      q_mma<HALF, 0, 26, 17>(f1, acc);
      SCHED0();
      q_read(f1, smem + qc, smem + qb, smem + qd, 1);
      const u32x4 fl = lds_word4(smem, F_FULL);
      q_mma<HALF, 0, 21, 18>(f0, acc);
      SCHED0();
      const unsigned cur = uni(min(fl[0], fl[1])), need = t;
      SCHED0();
      q_mma<HALF, 21, 22, 0>(f0, acc);
      SCHED0();
      keep(t, so, tx + 2);
      SCHED0();
      qa += XSL_; qb += WSL; qc += XSL_; qd += WSL; so += XSL_; ++t;
      q_one<HALF, 22, 25, false>(f0, acc);
      GRP(2, 1, 0x002, 2);
      asm volatile("" : "+v"(qa), "+v"(qb), "+v"(qc), "+v"(qd), "+s"(so), "+s"(t));
      SCHED0();
      q_mma<HALF, 25, 26, 0>(f0, acc);
      SCHED0();
      wait_full(smem, F_FULL, need, cur);
    }
    SCHED0();
    q_read(f0, smem + a0, smem + qb, smem + qd, 0);
    q_one<HALF, 0, 26, false>(f1, acc);
    {
      const int pa = 4 * g + (r16 & 1);
      const bool odd = r16 & 1, cur_a = pa <= r16, cur_b = pa + 2 <= r16;
      const unsigned qs = lds_addr(smem + L_QST + HALF * 512);
      aq = qs + r16 * 8; ac = lds_addr(smem + L_CSF + h * 256) + r16 * 8;
      aa = qs + pa * 8 + (cur_a ? 128 : 0); ab = qs + pa * 8 + (cur_b ? 128 : 0);
      xa = lds_addr(smem + L_XCH + h * 3072) + lane * 16; xv = xa + 2048 - lane * 8; xf = lds_addr(smem + F_XCH + h * 4);
      const unsigned la = odd ? 0x01000c0cu : 0x0c0c0100u, lb = odd ? 0x03020c0cu : 0x0c0c0302u;
      sel[0] = cur_a ? 0x0c0c0c0cu : la; sel[1] = cur_b ? 0x0c0c0c0cu : lb; sel[2] = cur_a ? la : 0x0c0c0c0cu; sel[3] = cur_b ? lb : 0x0c0c0c0cu;
      ooff = (unsigned)(tm * 8 + HALF * 4) * 32768u + (tn * 2 + h) * 1024 + lane * 16;
#pragma unroll
      for (int z = 0; z < 17; ++z) { GRP(1, 1, 0x100, 1); __builtin_amdgcn_sched_group_barrier(0x002, 1, 0); }
      GRP(9, 1, 0x002, 3);
      asm volatile("" : "+v"(aq), "+v"(ac), "+v"(aa), "+v"(ab), "+v"(xa), "+v"(xv), "+v"(xf), "+v"(ooff));
      asm volatile("" : "+v"(sel[0]), "+v"(sel[1]), "+v"(sel[2]), "+v"(sel[3]));
    }
    SCHED0();
#pragma unroll
    for (int i = 0; i < 5; ++i) f1.xa[i] = lds_b128(smem + (a0 ^ 64) + i * 2048);
#pragma unroll
    for (int s = 0; s < 3; ++s) {
      if (s == 2) fl = lds_word4(smem, F_FULL);
      f1.w[s][0] = lds_tr2(smem + qb + s * 8192 + 4096);
      f1.w[s][1] = lds_tr2(smem + qd + s * 8192 + 4096);
    }
    q_mma<HALF, 0, 26 - DV, 18>(f0, acc);
    SCHED0();
    keep(15, 0, tx);
    SCHED0();
  }
  LANE_VARS
  auto mm = [&](const QFrag& f, int m, int i, int t) {
    const int n = max(acc_no(HALF, m, i, t), 0);
    acc[n] = m == 2 ? MFMA16(f.xa[i], f.w[2][t], acc[n]) : MFMA16(f.w[m][t], f.xa[i], acc[n]);
  };
  auto last = [&](int z0, int z1) {
#pragma unroll
    for (int z = z0; z < z1; ++z) {
      const int c = tail_of(HALF, HALF ? z : z < 6 ? z + 2 : z < 8 ? z - 6 : z);
      mm(f1, c / 100, c / 10 % 10, c % 10);
    }
  };
  last(0, PRE);
  SCHED0();
  const unsigned cur = uni(min(fl[2], fl[3]));
  SCHED0();
  last(PRE, PRE + 1);
  SCHED0();
  wait_full(smem, F_ST, 1, cur);
  SCHED0();
  u32x2 sq[5], cs[3][2];
#pragma unroll
  for (int i = 0; i < 5; ++i) sq[i] = *(const LAS u32x2*)(aq + i * 128);
#pragma unroll
  for (int s = 0; s < 3; ++s)
#pragma unroll
    for (int t = 0; t < 2; ++t) cs[s][t] = *(const LAS u32x2*)(ac + s * 512 + t * 128);
#pragma unroll
  for (int i = 1; i < 5; ++i) { ra[i] = *(const LAS float*)(aa + (i - 1) * 128 + 4); rb[i] = *(const LAS float*)(ab + (i - 1) * 128 + 20); }
  last(PRE + 1, PRE + 5);
  __builtin_amdgcn_sched_group_barrier(0x100, 3, 0); GRP(3, 1, 0x100, 3);
  SCHED0();
  lds_post(smem, F_FREE + wid, 16);
  SCHED0();
  last(PRE + 5, 18);
  const unsigned gm = g == 0 ? ~0u : 0u;
  u32x2 lx[5];
#pragma unroll
  for (int i = 0; i < 5; ++i) lx[i] = u32x2{sq[i][0] & gm, sq[i][0] & gm & 0xffffu};
#pragma unroll
  for (int i = 1; i < 5; ++i) {
    const float c = f_of(sq[i][1]) * (0.17677669529663687f * 1.4426950408889634f);
    sa[i - 1] = opq(c * ra[i]); sb[i - 1] = opq(c * rb[i]);
  }
  __builtin_amdgcn_sched_group_barrier(0x008, 5 - PRE, 0); GRP(8, 1, 0x002, 3);
  asm volatile("" : "+v"(lx[0]), "+v"(lx[1]), "+v"(lx[2]), "+v"(lx[3]), "+v"(lx[4]));
  asm volatile("" : "+v"(sa[0]), "+v"(sa[1]), "+v"(sa[2]), "+v"(sa[3]), "+v"(sb[0]), "+v"(sb[1]), "+v"(sb[2]), "+v"(sb[3]));
  SCHED0();
  auto ln = [&](int m, int i, int t) {
    const int n = max(acc_no(HALF, m, i, t), 0);
    const s16x4 lxi = __builtin_bit_cast(s16x4, lx[i]), lw = __builtin_bit_cast(s16x4, cs[m][t]);
    acc[n] = m == 2 ? __builtin_amdgcn_mfma_f32_16x16x16bf16_1k(lxi, lw, acc[n], 0, 0, 0) : __builtin_amdgcn_mfma_f32_16x16x16bf16_1k(lw, lxi, acc[n], 0, 0, 0);
  };
#pragma unroll
  for (int z = 0; z < 18; ++z) ln(tail_of(HALF, z) / 100, tail_of(HALF, z) / 10 % 10, tail_of(HALF, z) % 10);
  STAMP(3);
  bf16x8 qf[5], kf[5];
  u32x10 vf[2];
  auto pack_qk = [&](int i) {
    if (acc_no(HALF, 0, i, 0) >= 0) qf[i] = pack8(acc[max(acc_no(HALF, 0, i, 0), 0)], acc[max(acc_no(HALF, 0, i, 1), 0)]);
    if (acc_no(HALF, 1, i, 0) >= 0) kf[i] = pack8(acc[max(acc_no(HALF, 1, i, 0), 0)], acc[max(acc_no(HALF, 1, i, 1), 0)]);
  };
  auto pack_v = [&](int i, int t) {
    const f32x4& v = acc[max(acc_no(HALF, 2, i, t), 0)];
    return u32x2{pack2(v[0], v[1]), pack2(v[2], v[3])};
  };
  u32x2 vx[2];
  pack_qk(HALF == 0 ? 4 : 0);
  if (HALF == 0) {
    vx[0] = pack_v(4, 0); vx[1] = pack_v(4, 1);
    asm volatile("ds_write_b128 %0, %2 offset:1024\n\tds_write2st64_b64 %1, %3, %4 offset1:1" :: "v"(xa), "v"(xv), "v"(kf[4]), "v"(vx[0]), "v"(vx[1]) : "memory");
  } else {
    asm volatile("ds_write_b128 %0, %1" :: "v"(xa), "v"(qf[0]) : "memory");
  }
  asm volatile("s_mov_b64 exec, 1\n\tds_write_b8 %0, %0 offset:%1\n\ts_mov_b64 exec, -1" ::"v"(xf), "i"(HALF) : "memory");
  static_assert((F_XCH & 0xf) != 0 && ((F_XCH + 4) & 0xf) != 0, "the low byte of the address is the flag");
#pragma unroll
  for (int i = 0; i < 4; ++i) pack_qk(HALF == 0 ? 3 - i : i + 1);
  const f32x4 z4 = {0.f, 0.f, 0.f, 0.f};
  f32x4 sp[4], sc[4];
#pragma unroll
  for (int j = 0; j < 3; ++j) {
    const int i = HALF == 0 ? 3 - j : j + 2;
    sc[i - 1] = MFMA16(kf[i], qf[i], z4); sp[i - 1] = MFMA16(kf[i - 1], qf[i], z4);
  }
  if (HALF) sc[0] = MFMA16(kf[1], qf[1], z4);
  GRP(1, 6, 0x002, 4); GRP(12, 1, 0x002, 2); GRP(HALF ? 7 : 6, 1, 0x002, 3);
  SCHED0();
  unsigned flag;
  bf16x8 xq;
  if (HALF == 0) asm volatile("ds_read_b32 %0, %2\n\tds_read_b128 %1, %3" : "=&v"(flag), "=&v"(xq) : "v"(xf), "v"(xa) : "memory");
  else asm volatile("ds_read_b32 %0, %4\n\tds_read_b128 %1, %5 offset:1024\n\tds_read_b64 %2, %6\n\tds_read_b64 %3, %6 offset:512"
                    : "=&v"(flag), "=&v"(xq), "=&v"(vx[0]), "=&v"(vx[1]) : "v"(xf), "v"(xa), "v"(xv) : "memory");
  SCHED0();
  const bool odd = r16 & 1, cur_a = sel[0] == 0x0c0c0c0cu, cur_b = sel[1] == 0x0c0c0c0cu;
  float a[4], b[4], m[4];
  auto logits = [&](int j) {
    const f32x4 p4 = sp[j], c4 = sc[j];
    const float p0 = p4[0], p1 = p4[1], p2 = p4[2], p3 = p4[3], c0 = c4[0], c1 = c4[1], c2 = c4[2], c3 = c4[3];
    const float ca = odd ? c1 : c0, cb = odd ? c3 : c2, pa_ = odd ? p1 : p0, pb = odd ? p3 : p2;
    a[j] = opq((cur_a ? ca : pa_) * sa[j]);
    b[j] = opq((cur_b ? cb : pb) * sb[j]);
    if (HALF == 0 && j == 0) {
      a[j] = (!cur_a && tm == 0) ? -INFINITY : a[j];
      b[j] = (!cur_b && tm == 0) ? -INFINITY : b[j];
    }
    m[j] = __builtin_elementwise_maximum(a[j], b[j]);
  };
  auto late = [&](int z0, int z1) {
#pragma unroll
    for (int y = z0; y < z1; ++y) {
      const int c = seq_of(HALF, 18 + ((y + 8 - DV) & 7));
      if (y < 8 + DV) mm(y < DV ? f0 : f1, 2, c / 10 % 10, c % 10);
    }
  };
#pragma unroll
  for (int j = 0; j < 2; ++j) logits(HALF == 0 ? 2 - j : j + 1);
  late(0, 6);
  GRP(6, 1, 0x002, 3);
  asm volatile("" : "+v"(m[1]), "+v"(m[2]));
  SCHED0();
  {
    unsigned t;
#define XWAIT(RD) "s_waitcnt lgkmcnt(0)\n\tv_readfirstlane_b32 %0, %1\n\ts_and_b32 %0, %0, %[mask]\n\ts_cbranch_scc1 2f\n\t1:\n\t" RD \
                  "\n\ts_waitcnt lgkmcnt(0)\n\tv_readfirstlane_b32 %0, %1\n\ts_and_b32 %0, %0, %[mask]\n\ts_cbranch_scc0 1b\n\t2:"
    if (HALF == 0) asm volatile(XWAIT("ds_read_b32 %1, %3\n\tds_read_b128 %2, %4") : "=&s"(t), "+v"(flag), "+v"(xq) : "v"(xf), "v"(xa), [mask] "s"(0xff00u) : "scc", "memory");
    else asm volatile(XWAIT("ds_read_b32 %1, %5\n\tds_read_b128 %2, %6 offset:1024\n\tds_read_b64 %3, %7\n\tds_read_b64 %4, %7 offset:512")
                      : "=&s"(t), "+v"(flag), "+v"(xq), "+v"(vx[0]), "+v"(vx[1]) : "v"(xf), "v"(xa), "v"(xv), [mask] "s"(0xffu) : "scc", "memory");
  }
  SCHED0();
  if (HALF == 0) { sc[3] = MFMA16(kf[4], xq, z4); sp[3] = MFMA16(kf[3], xq, z4); }
  else sp[0] = MFMA16(xq, qf[1], z4);
  logits(HALF == 0 ? 0 : 3);
  late(6, 8);
  GRP(1, HALF ? 1 : 2, 0x002, 3); GRP(2, 1, 0x002, 3);
  asm volatile("" : "+v"(m[HALF ? 3 : 0]));
  SCHED0();
  logits(HALF == 0 ? 3 : 0);
  rows_spread(rows_reduce<true>(m), m);
#pragma unroll
  for (int j = 0; j < 4; ++j) {
    a[j] = __builtin_amdgcn_exp2f(opq(a[j] - m[j]));
    b[j] = __builtin_amdgcn_exp2f(opq(b[j] - m[j]));
    m[j] = opq(a[j] + b[j]);
    a[j] = opq(a[j] * ra[j + 1]); b[j] = opq(b[j] * rb[j + 1]);
  }
  late(8, 16);
#pragma unroll
  for (int z = 0; z < 8; ++z) ln(2, seq_of(HALF, 18 + z) / 10 % 10, seq_of(HALF, 18 + z) % 10);
  GRP(DV, 1, 0x402, 3); GRP(8, 1, 0x402, 2);
  asm volatile("" : "+v"(m[0]), "+v"(m[1]), "+v"(m[2]), "+v"(m[3]));
  SCHED0();
#pragma unroll
  for (int t = 0; t < 2; ++t) {
#pragma unroll
    for (int i = 0; i < 5; ++i) {
      const u32x2 v = i == (HALF ? 0 : 4) ? vx[t] : pack_v(i, t);
      vf[t][2 * i] = v[0]; vf[t][2 * i + 1] = v[1];
    }
    asm("" : "+v"(vf[t]));
  }
  rows_spread(__builtin_amdgcn_rcpf(rows_reduce<false>(m)), m);
  const rsrc_t ors = mk_rsrc(ws + WS_O, S_TOK * DM * 2);
#pragma unroll
  for (int j = 0; j < 4; ++j) {
    const unsigned pk = pack2(a[j] * m[j], b[j] * m[j]);
    const u32x4 pw = {__builtin_amdgcn_perm(0u, pk, sel[0]), __builtin_amdgcn_perm(0u, pk, sel[1]), __builtin_amdgcn_perm(0u, pk, sel[2]), __builtin_amdgcn_perm(0u, pk, sel[3])};
    const u32x4 v0 = {vf[0][2 * j], vf[0][2 * j + 1], vf[0][2 * j + 2], vf[0][2 * j + 3]}, v1 = {vf[1][2 * j], vf[1][2 * j + 1], vf[1][2 * j + 2], vf[1][2 * j + 3]};
    sp[j] = MFMA16(__builtin_bit_cast(bf16x8, v0), __builtin_bit_cast(bf16x8, pw), z4);
    sc[j] = MFMA16(__builtin_bit_cast(bf16x8, v1), __builtin_bit_cast(bf16x8, pw), z4);
  }
#pragma unroll
  for (int j = 0; j < 4; ++j) { qf[j] = pack8(sp[j], sc[j]); st16c(ors, ooff + j * 32768, __builtin_bit_cast(u32x4, qf[j])); }
  RST(4);
  SCHED0();
  asm volatile("ds_read_b32 %0, %1 offset:%2" : "=v"(e.go) : "v"(xf - h * 4), "i"(F_GO - F_XCH) : "memory");
  wait_bytes<0x80808080u>(smem, F_FREE, 16);
  asm volatile("" : "+v"(e.go));
  asm volatile("ds_write_b128 %0, %1\n\tds_write_b128 %0, %2 offset:1024\n\tds_write_b128 %0, %3 offset:4096\n\tds_write_b128 %0, %4 offset:5120"
               :: "v"(lds_addr(smem + L_OWN) + HALF * 8192 + h * 2048 + lane * 16), "v"(qf[0]), "v"(qf[1]), "v"(qf[2]), "v"(qf[3]) : "memory");
  lds_post(smem, F_OL + wid, 1);
}

__device__ __forceinline__ unsigned ln2s(unsigned w, float mb, float r) {
  const unsigned d = pack2(opq(lo2f(w) - mb), opq(hi2f(w) - mb));
  return pack2(opq(lo2f(d) * r), opq(hi2f(d) * r));
}

__device__ __forceinline__ void await_attention(char* smem, int lane, int tm, int tn, char* ws, unsigned tag, unsigned& tp) {
  const rsrc_t wrs = mk_rsrc(ws, WS_O);
  const unsigned off = WS_OF + (tm * 64 + ((lane >> 3) & 1) * 32 + (lane & 7)) * 4;
  const int own = tn >> 3;
  bool first = true;
  unsigned seen = 0, since = 0;
#pragma unroll 1
  for (unsigned n = 0; n < SPIN_CAP; ++n) {
    unsigned want = ~seen & 0xffffu;
    if (since) ++since; else since = uni(lds_word(smem, F_ODONE)) >> 2;
    if (since <= (first ? OK : 0)) want &= own ? 0x00ffu : 0xff00u;
    const unsigned f = __builtin_amdgcn_raw_buffer_load_b32(wrs, ((want >> (lane & 15)) & 1) ? off : 0x7fffffffu, 0, 16);
    seen |= (unsigned)__ballot(f == tag) & want;
    if (seen == 0xffffu) break;
    if (first && ((seen & 0xffu) == 0xffu || (seen >> 8) == 0xffu)) { lds_post(smem, F_GO, (seen & 0xffu) == 0xffu ? 1 : 2); first = false; NAP(ON); }
    NAP(PN);
  }
  lds_post(smem, F_GO + 1, 1);
  if (first) lds_post(smem, F_GO, 1);
}

__device__ __forceinline__ void out_proj(char* smem, int wid, int lane_in, int tm, int tn, char* ws, unsigned tag,
                                         u16* __restrict__ out, EpiRegs& e, unsigned rot2) {
  LANE_VARS
  const rsrc_t ors = mk_rsrc(ws + WS_O, S_TOK * DM * 2), yrs = mk_rsrc(out, S_TOK * DM * 2);
  const int tok = tm * 128 + wid * 32 + r16, co = (g & 1) * 16 + (g >> 1) * 8;
  unsigned eoff = (unsigned)(tok * DM + tn * 64 + co) * 2u;
  const unsigned vo = (unsigned)(tm * 8 + wid * 2) * 32768u + lane * 16;
  bf16x8 wb[3][4], oa[OD][2];
  int bc[4];
#pragma unroll
  for (int c = 0; c < 4; ++c) bc[c] = L_WA + brow + ((c ^ hk) << 5);
  f32x4 acc[2][4];
  SCHED0();
  unsigned go, oth = (tn & 8) * 2 ^ 16;
  asm volatile("" : "+s"(oth));
  auto o_request = [&](int s) {
    asm volatile("buffer_load_dwordx4 %0, %2, %4, %5 offen sc1\n\tbuffer_load_dwordx4 %1, %3, %4, %5 offen sc1" : "+v"(oa[s % OD][0]), "+v"(oa[s % OD][1])
                 : "v"(vo), "v"(vo + 32768), "s"(ors), "s"(((s >> 1 == 7 ? oth : (s & 16) ^ ((go & 2) * 8)) + ((rot2 >> (s & 14) * 2) & 14) + (s & 1)) * 1024) : "memory");
  };
  auto start = [&](unsigned word) {
    go = uni(word) & 3;
    if (go) {
#pragma unroll
      for (int s = 0; s < OD - 1; ++s) o_request(s);
    }
    SCHED0();
  };
#pragma unroll
  for (int s = 0; s < OD; ++s) asm volatile("" : "=v"(oa[s][0]), "=v"(oa[s][1]));
  {
    const unsigned w0 = uni(e.go);
    if (w0 & 3) wait_bytes<0x80808080u>(smem, F_OL, 1);
    start(w0);
  }
  const unsigned early = go;
  RST(11);
  auto publish = [&](auto nn) {
    WAITV(decltype(nn)::v);
    unsigned old = 0;
    if (lane == 0) old = __hip_atomic_fetch_add((LAS unsigned*)(LAS char*)(smem + F_ODONE), 1u, __ATOMIC_RELAXED, __HIP_MEMORY_SCOPE_WORKGROUP);
    if (uni(old) == 3u) {
      if (lane == 0) __hip_atomic_store((GAS unsigned*)(ws + WS_OF) + tm * 64 + (tn >> 3) * 32 + (tn & 7), tag, RLX_AGENT);
    }
    RST(5);
    SCHED0();
  };
  if (!early) publish(IC<0>{});
  const unsigned w1 = lds_word(smem, F_GO);
  const u32x2 fl = *(const volatile LAS u32x2*)(LAS const char*)(smem + F_FULL);
  const unsigned srow = 2047u - (*(const unsigned*)(smem + F_SEL + (lane & 7) * 4) & 0xffffu);
  f32x4 res[2][4];
  const uint2 st[2] = {*(const uint2*)(smem + L_QST + (16 + wid * 32 + r16) * 8), *(const uint2*)(smem + L_QST + (32 + wid * 32 + r16) * 8)};
  auto norm = [&](int u) {
    const float rd = f_of(st[u].y), k = 1.0f + rd, b = lo2f(st[u].x) * rd;
#pragma unroll
    for (int pr = 0; pr < 2; ++pr) {
      auto sa = __builtin_amdgcn_permlane16_swap(e.xv[u][pr][0], e.xv[u][pr][2], false, false);
      auto sb = __builtin_amdgcn_permlane16_swap(e.xv[u][pr][1], e.xv[u][pr][3], false, false);
#pragma unroll
      for (int z = 0; z < 2; ++z) res[u][2 * pr + z] = f32x4{lo2f(sa[z]), hi2f(sa[z]), lo2f(sb[z]), hi2f(sb[z])} * k + b;
    }
    asm volatile("" : "+v"(res[u][0]), "+v"(res[u][1]), "+v"(res[u][2]), "+v"(res[u][3]));
  };
  f32x4 ac2[2][4], (&ac3)[2][4] = res;
  auto w_frag = [&](int slot, const int (&b)[4], int z) {
#pragma unroll
    for (int c = 0; c < 4; ++c) wb[slot][c] = lds_tr2(smem + b[c] + z * 4096);
  };
  auto step = [&](f32x4 (&d)[2][4], int slot, const bf16x8 (&o)[2], bool fresh) {
#pragma unroll
    for (int c = 0; c < 4; ++c)
#pragma unroll
      for (int uu = 0; uu < 2; ++uu) {
        const int u = uu ^ (c & 1);
        d[u][c] = MFMA16(wb[slot][c], o[u], fresh ? (f32x4{0, 0, 0, 0}) : d[u][c]);
      }
  };
  int b3[4];
#pragma unroll
  for (int c = 0; c < 4; ++c) b3[c] = bc[c] + (L_W3 - L_WA);
  auto own_heads = [&](unsigned fu) {
    wait_full(smem, F_FULL, 17, fu);
    w_frag(0, b3, 2); w_frag(1, b3, 3);
    const unsigned w2 = lds_word(smem, F_GO);
    SCHED0();
    if (wid < 2) wait_bytes<0x00800080u>(smem, F_OL, 1); else wait_bytes<0x80008000u>(smem, F_OL, 1);
    if (!go) start(w2);
    bf16x8 own[2][2];
#pragma unroll
    for (int z = 0; z < 4; ++z) own[z >> 1][z & 1] = lds_b128(smem + L_OWN + wid * 4096 + lane * 16 + z * 1024);
    step(ac3, 0, own[0], false); step(ac3, 1, own[1], false);
    asm volatile("" : "+v"(ac3[0][0]), "+v"(ac3[0][1]), "+v"(ac3[0][2]), "+v"(ac3[0][3]), "+v"(ac3[1][0]), "+v"(ac3[1][1]), "+v"(ac3[1][2]), "+v"(ac3[1][3]));
    SCHED0();
  };
  unsigned mine = (unsigned)__ballot((srow >> 5) == (unsigned)(tm * 4 + wid)) & 0xffu;
  asm volatile("" : "+s"(mine));
  asm volatile("" : "+v"(eoff));
  if (!go) start(w1);
  norm(0);
  norm(1);
  SCHED0();
  RST(12);
  if (early) publish(IC<2 * OD - 2>{});
  own_heads(uni(min(fl[0], fl[1])));
  RST(10);
  if (!go) start(wait_bytes<0x80u>(smem, F_GO, 1));
  go = uni(go);
  RST(6);
  int b2[4];
  const int mine1st = ((go >> 1) ^ (tn >> 3) ^ 1) & 1;
#pragma unroll
  for (int c = 0; c < 4; ++c) { b2[c] = bc[c] + (go & 1) * (L_WB - L_WA); bc[c] += (go >> 1) * (L_WB - L_WA); }
  unsigned so[15];
#pragma unroll
  for (int z = (OD - 1) / 2; z < 15; ++z) { so[z] = ((z == 7 ? oth : (2 * z & 16) ^ ((go & 2) * 8)) + ((rot2 >> (z & 7) * 4) & 14)) * 1024; asm volatile("" : "+s"(so[z])); }
  s16x4 wh[3][8];
  auto w_half = [&](int s, int z0, int z1) {
#pragma unroll
    for (int z = z0; z < z1; ++z)
      if (s < 30) wh[s % 3][z] = __builtin_amdgcn_ds_read_tr16_b64_v4i16((LAS s16x4*)(smem + (s < 14 ? bc[z >> 1] + s * 4096 : s < 16 ? b3[z >> 1] + (s - 14) * 4096 : b2[z >> 1] + (s - 16) * 4096) + (z & 1) * 512));
  };
  wait_full(smem, F_WO, 1);
  wait_bytes<0x80u, true>(smem, F_WO + 8, 1, go & 1);
  RST(7);
  RSC(0);
  w_half(0, 0, 8); w_half(1, 0, 8);
  SCHED0();
  asm volatile("s_waitcnt vmcnt(%2)" : "+v"(oa[0][0]), "+v"(oa[0][1]) : "i"(2 * OD - 4) : "memory");
  auto one = [&](int s) __attribute__((always_inline)) {
    f32x4 (&d)[2][4] = s < 14 ? ac2 : s < 16 ? ac3 : acc;
    auto part = [&](int z0, int z1) {
      SCHED0();
      w_half(s + 2, z0, z1);
#pragma unroll
      for (int z = z0; z < z1; ++z) {
        const int c = z >> 1, u = (z ^ c) & 1;
        d[u][c] = MFMA16(__builtin_shufflevector(wh[s % 3][2 * c], wh[s % 3][2 * c + 1], 0, 1, 2, 3, 4, 5, 6, 7), oa[s % OD][u], s == 0 || s == 16 ? (f32x4{0, 0, 0, 0}) : d[u][c]);
        __builtin_amdgcn_sched_group_barrier(0x008, 1, 0);
        __builtin_amdgcn_sched_group_barrier(0x100, 1, 0);
      }
      SCHED0();
    };
    const int n = s + OD - 1;
    auto ask = [&](int u) {
      if (n < 30) asm volatile("buffer_load_dwordx4 %0, %1, %2, %3 offen offset:%4 sc1" : "+v"(oa[n % OD][u]) : "v"(vo + u * 32768), "s"(ors), "s"(so[n >> 1]), "i"((n & 1) * 1024) : "memory");
    };
    part(0, 1);
    if (n == 14 || n == 16) wait_bytes<0x8000u, true>(smem, F_GO, 1, n == 14 ? mine1st ^ 1 : mine1st);
    ask(0);
    part(1, 3);
    ask(1);
    if (s == 13) wait_bytes<0x80u>(smem, F_WO + 8, 1);
    part(3, 5);
    if (s < 29) asm volatile("s_waitcnt vmcnt(%2)" : "+v"(oa[(s + 1) % OD][0]), "+v"(oa[(s + 1) % OD][1]) : "i"(2 * ((n < 29 ? n : 29) - s - 1)) : "memory");
    part(5, 8);
    asm volatile("" : "+v"(d[0][0]), "+v"(d[0][1]), "+v"(d[0][2]), "+v"(d[0][3]), "+v"(d[1][0]), "+v"(d[1][1]), "+v"(d[1][2]), "+v"(d[1][3]));
    if (s == 0) { RST(14); RSC(1); }
    if (s == 14) { RST(15); RSC(2); }
  };
#pragma unroll
  for (int s = 0; s < 10; ++s) one(s);
#pragma unroll
  for (int s = 10; s < 20; ++s) one(s);
#pragma unroll
  for (int s = 20; s < 28; ++s) one(s);
  asm volatile("s_waitcnt vmcnt(0)" : "+v"(oa[29 % OD][0]), "+v"(oa[29 % OD][1]) :: "memory");
  if (mine != 0) {
    wait_bytes<0x80808080u>(smem, F_UP, 1);
#pragma unroll 1
    for (; mine != 0; mine &= mine - 1) {
      const int c = __builtin_ctz(mine), row = __builtin_amdgcn_readlane((int)srow, c) & 31;
#pragma unroll
      for (int u = 0; u < 2; ++u)
        if (row == u * 16 + r16) {
#pragma unroll
          for (int z = 0; z < 4; ++z) res[u][z] += *(const f32x4*)(smem + L_UP + c * 256 + (z * 16 + g * 4) * 4);
        }
    }
  }
  SCHED0();
#pragma unroll
  for (int u = 0; u < 2; ++u)
#pragma unroll
    for (int z = 0; z < 8; ++z)
      acc[u][z & 3] = MFMA16(__builtin_shufflevector(wh[(28 + (z >> 2)) % 3][2 * (z & 3)], wh[(28 + (z >> 2)) % 3][2 * (z & 3) + 1], 0, 1, 2, 3, 4, 5, 6, 7), oa[(28 + (z >> 2)) % OD][u], acc[u][z & 3]);
#pragma unroll
  for (int u = 0; u < 2; ++u)
#pragma unroll
    for (int pr = 0; pr < 2; ++pr) {
      const f32x4 c0 = (acc[u][2 * pr] + ac2[u][2 * pr]) + ac3[u][2 * pr], c1 = (acc[u][2 * pr + 1] + ac2[u][2 * pr + 1]) + ac3[u][2 * pr + 1];
      auto sa = __builtin_amdgcn_permlane16_swap(pack2(c0[0], c0[1]), pack2(c1[0], c1[1]), false, false);
      auto sb = __builtin_amdgcn_permlane16_swap(pack2(c0[2], c0[3]), pack2(c1[2], c1[3]), false, false);
      __builtin_amdgcn_raw_buffer_store_b128(u32x4{sa[0], sb[0], sa[1], sb[1]}, yrs, eoff + pr * 64 + u * 16 * DM * 2, 0, 16);
    }
  __builtin_amdgcn_sched_group_barrier(0x008, 9, 0);
  GRP(7, 1, 0x002, EV);
  SCHED0();
  RST(8);
  STAMP(9);
}

__device__ __forceinline__ unsigned med3(unsigned a, unsigned b, unsigned c) { return max(min(a, b), min(max(a, b), c)); }
#define PIN8(a) asm volatile("" : "+v"(a[0]), "+v"(a[1]), "+v"(a[2]), "+v"(a[3]), "+v"(a[4]), "+v"(a[5]), "+v"(a[6]), "+v"(a[7]))
__device__ __forceinline__ void tick(int& nb) {
  SCHED0();
  if (nb < NBAR) { BAR(); ++nb; if (HN) NAP(HN); }
  asm volatile("" ::: "memory");
  SCHED0();
}
__device__ __forceinline__ void hidden_half(char* smem, int lane, int l, int cg, char* ws, unsigned tag, u32x4 (&fd)[2][8]) {
  const rsrc_t wrs = mk_rsrc(ws, WS_O);
  const unsigned off = WS_GU + (unsigned)(cg * 32768 + l * 2048 + lane * 16);
  u32x4 ps[2][8];
  auto request = [&]() {
#pragma unroll
    for (int p = 0; p < 16; ++p) ps[p & 1][p >> 1] = ld16c(wrs, off + (p & 1) * 1024, (p >> 1) * 4096);
    SCHED0();
  };
  auto good = [&]() {
    unsigned bad = 0;
#pragma unroll
    for (int p = 0; p < 16; ++p) bad = chk(bad, ps[p & 1][p >> 1], tag);
    return __all(bad == 0);
  };
#pragma unroll 1
  for (unsigned n = 0; n < SPIN_CAP; ++n) {
    const unsigned t = __builtin_amdgcn_raw_buffer_load_b32(wrs, WS_GU + (unsigned)(cg * 32768 + (lane >> 3) * 4096 + l * 2048 + (lane & 7) * 256 + 132), 0, 16);
    if (__all(t == tag)) break;
    NAP(PR);
  }
  request();
  tr_all(fd[0]); tr_all(fd[1]);
  SCHED0();
#pragma unroll 1
  for (unsigned n = 0; n < SPIN_CAP && !good(); ++n) { NAP(4); request(); }
  unsigned hp[2];
#pragma unroll
  for (int j = 0; j < 2; ++j) {
    float hv[2];
#pragma unroll
    for (int z = 0; z < 2; ++z) {
      const float gb = rbf((f_of(ps[j][0][2 * z]) + f_of(ps[j][2][2 * z])) + (f_of(ps[j][4][2 * z]) + f_of(ps[j][6][2 * z])));
      const float ub = rbf((f_of(ps[j][1][2 * z]) + f_of(ps[j][3][2 * z])) + (f_of(ps[j][5][2 * z]) + f_of(ps[j][7][2 * z])));
      hv[z] = rbf(gb / (1.0f + __expf(-gb))) * ub;
    }
    hp[j] = pack2(hv[0], hv[1]);
  }
#pragma unroll
  for (int j = 0; j < 2; ++j)
    *(unsigned*)(smem + L_HF + (lane & 7) * 128 + (((lane >> 4) & 1) * 4 + 2 * l + j) * 16 + ((lane >> 3) & 1) * 4 + (lane >> 5) * 8) = hp[j];
  lds_post(smem, F_H + l, 1);
}

struct Stager {
  rsrc_t xrs, qrs;
  LAS char* lds;
  unsigned va, vb;
  int arow, pa, pb;
  __device__ __forceinline__ Stager(char* smem, int l, int lane, int tm, int tn, const u16* x, const u16* w) {
    asm volatile("" : "+v"(lane));
    const int kr = lane >> 3;
    xrs = mk_rsrc(x, S_TOK * DM * 2); qrs = mk_rsrc(w, DM * NQKV * 2);
    lds = (LAS char*)smem + l * 1024;
    va = (unsigned)(kr + 8 * l) * 2048u + (((lane & 7) ^ (kr >> 1) ^ (l << 2)) << 4);
    vb = (unsigned)(kr + 8 * l) * 6144u + tn * 128 + (((lane & 7) ^ (((kr >> 1) & 1) << 1) ^ (l << 2)) << 4);
    arow = tm * 128 - 16;
    k_order(tm, tn, pa, pb);
  }
};
#define DM1(l, o) "s_add_u32 m0, %2, " #l "\n\ts_add_u32 %0, %5, " #o "\n\tbuffer_load_dwordx4 %3, %4, %0 offen lds\n\t"
#define DMA_(str, v_, rs_, so_, s0_) asm volatile("s_mov_b32 %1, m0\n\t" str "s_mov_b32 m0, %1" : "=&s"(tmp), "=&s"(keep) : "s"(base), "v"(v_), "s"(rs_), "s"(so_), "s"(s0_) : "scc", "memory")
__device__ __forceinline__ void stage_x(const Stager& g, int t) {
  const int kt = k_tile(t, g.pa, g.pb);
  const unsigned base = (unsigned)(size_t)g.lds + (t % 3) * XSL_;
  unsigned keep, tmp;
  DMA_("s_mov_b32 m0, %2\n\ts_nop 0\n\tbuffer_load_dwordx4 %3, %4, %6 offen lds\n\t" DM1(2048, 32768) DM1(4096, 65536) DM1(6144, 98304) DM1(8192, 131072)
       DM1(10240, 163840) DM1(12288, 196608) DM1(14336, 229376) DM1(16384, 262144), g.va, g.xrs, g.arow * 2048 + kt * 128, max(g.arow, 0) * 2048 + kt * 128);
}
__device__ __forceinline__ void stage_w(const Stager& g, int t) {
  const int kt = k_tile(t, g.pa, g.pb);
  const unsigned base = (unsigned)(size_t)g.lds + XB + (t & 3) * WSL;
  unsigned keep, tmp;
  DMA_(DM1(0, 0) DM1(2048, 98304) DM1(4096, 196608) DM1(6144, 294912) DM1(8192, 2048) DM1(10240, 100352) DM1(12288, 198656) DM1(14336, 296960)
       DM1(16384, 4096) DM1(18432, 102400) DM1(20480, 200704) DM1(22528, 299008), g.vb, g.qrs, kt * 64 * 6144, 0);
}
__device__ __forceinline__ int unit_of(int bid) {
  const int off = bid >> 3;
  return (off >> 2) == 3 ? off + 16 : (off >> 2) == 7 ? off - 16 : off;
}
__device__ constexpr int wo_at(int n) { return (n < 28 ? L_WA + n * 2048 : n < 56 ? L_WB + (n - 28) * 2048 : L_W3 + (n - 56) * 2048); }
__device__ __forceinline__ void loader(char* smem, int l, int lane, int tm, int tn, unsigned rot2, const u16* __restrict__ x,
                                       const u16* __restrict__ w, const u16* __restrict__ wo, char* ws) {
  const Stager stage(smem, l, lane, tm, tn, x, w);
  dma_tag(smem, F_FULL + l * 4, 0);
  const rsrc_t prs = mk_rsrc(wo, DM * DM * 2);
  const int kr = lane >> 3;
  const unsigned vo = (unsigned)((kr < 4 ? kr : 12 + kr) + 4 * l) * 2048u + tn * 128 + (((lane & 7) ^ (((kr >> 1) & 1) << 1) ^ (l << 2)) << 4);
  int oth = (tn & 8) * 2 ^ 16;
  asm volatile("" : "+s"(oth));
  auto source = [&](int n) {
    const int k = n >> 1, half = k < 14 ? 0 : k < 28 ? 16 : k >= 30 ? (tn & 8) * 2 : oth;
    return (unsigned)((half + ((rot2 >> ((k < 14 ? k : k < 28 ? k - 14 : 14) & 14) * 2) & 14) + (k & 1)) * 32 + 8 * (n & 1)) * 2048;
  };
  auto fetch = [&](int n) {
    unsigned keep;
    asm volatile("s_mov_b32 %0, m0\n\ts_add_u32 m0, %4, %5\n\ts_nop 2\n\tbuffer_load_dwordx4 %1, %2, %3 offen lds\n\ts_mov_b32 m0, %0"
                 : "=&s"(keep) : "v"(vo), "s"(prs), "s"(source(n)), "s"((unsigned)(size_t)stage.lds), "i"(wo_at(n)) : "scc", "memory");
  };
  static_assert(L_WA + 24 * 2048 == XB && L_WB == XB + WSL && L_OWN + 16384 <= L_XCH, "layout");
#pragma unroll
  for (int t = -1; t < NBAR - 2; ++t) {
    if (t == 0) stage_w(stage, 3);
    if (t >= 0) stage_x(stage, t + 3);
    if (t == NBAR - 3) dma_tag(smem, F_FULL + l * 4, 15);
    if (t >= 0) stage_w(stage, t + 4);
    SCHED0();
    WAITV(t < 0 ? 22 : 33 + (t == 0 || t == NBAR - 3));
    BAR();
  }
  unsigned tp = 0;
  stage_x(stage, 15);
  dma_tag(smem, F_FULL + l * 4, 16);
  SCHED0();
#pragma unroll
  for (int y = 0; y < 12; ++y) fetch(y < 8 ? 63 - y : 16 + y);
  dma_tag(smem, F_FULL + l * 4, 17);
  const unsigned tag = uni(lds_word(smem, F_TAG));
  wait_bytes<0x80808080u>(smem, F_FREE, 16);
#pragma unroll
  for (int n = 0; n < 24; ++n) fetch(n);
  dma_tag(smem, F_WO + l * 4, 1);
#pragma unroll
  for (int n = 28; n < 28 + NB1; ++n) fetch(n);
  if (l == 0) {
    const unsigned v1 = (vo + 4 * 2048) ^ 64;
    wait_bytes<0x80808080u>(smem, F_OL, 1);
#pragma unroll
    for (int n = 28 + NB1; n < 56; ++n) {
      unsigned keep;
      asm volatile("s_mov_b32 %0, m0\n\ts_add_u32 m0, %4, %5\n\ts_nop 0\n\tbuffer_load_dwordx4 %1, %2, %3 offen lds\n\ts_add_u32 m0, m0, 1024\n\ts_nop 0\n\tbuffer_load_dwordx4 %6, %2, %3 offen lds\n\ts_mov_b32 m0, %0"
                   : "=&s"(keep) : "v"(vo), "s"(prs), "s"(source(n)), "s"((unsigned)(size_t)stage.lds), "i"(wo_at(n)), "v"(v1) : "scc", "memory");
    }
    wait_bytes<0x80u>(smem, F_WO + 12, 1);
    dma_tag(smem, F_WO + 8, 1);
  } else dma_tag(smem, F_WO + 12, 1);
  __builtin_amdgcn_s_setprio(1);
  if (l) await_attention(smem, lane, tm, tn, ws, tag, tp);
}

__device__ __forceinline__ void updates(char* smem, int w, int lane, int tm, int tn, char* ws) {
  const rsrc_t wrs = mk_rsrc(ws, WS_O);
  const unsigned tag = uni(lds_word(smem, F_TAG));
  const int g = lane >> 4, li = lane & 15;
  wait_bytes<0x80u>(smem, F_SELOK, 1);
  const unsigned selkey = *(const unsigned*)(smem + F_SEL + (lane & 7) * 4);
  unsigned mine = (unsigned)__ballot(((2047u - (selkey & 0xffffu)) >> 7) == (unsigned)tm) & 0xffu;
#pragma unroll
  for (int i = 0; i < 3; ++i) mine &= i < w ? mine - 1 : ~0u;
  if (mine != 0 && w < 3) {
    wait_bytes<0x80u>(smem, F_GO, 1);
    NAP(YD);
  }
  for (; mine != 0; mine &= mine - 1, mine &= mine - 1, mine &= mine - 1, mine &= mine - 1) {
    const int c = __builtin_ctz(mine);
    const unsigned yoff = WS_Y + (unsigned)((g * 14 * 8 + c) * DM + tn * 64 + li * 4) * 4u;
    u32x4 yv[14];
    const unsigned key = (unsigned)__builtin_amdgcn_readlane((int)selkey, c), kb = key >> 16;
#pragma unroll 1
    for (unsigned n = 0; n < SPIN_CAP; ++n) {
      unsigned bad = 0;
#pragma unroll
      for (int j = 0; j < 14; ++j) yv[j] = ld16c(wrs, yoff, j * (8 * DM * 4));
#pragma unroll
      for (int j = 0; j < 14; ++j) bad = chk(bad, yv[j], tag);
      if (__all(bad == 0)) break;
      NAP(8);
    }
    float y[4] = {0.f, 0.f, 0.f, 0.f};
#pragma unroll
    for (int j = 0; j < 14; ++j) { y[0] += lo2f(yv[j][0]); y[1] += hi2f(yv[j][0]); y[2] += lo2f(yv[j][2]); y[3] += hi2f(yv[j][2]); }
    const float r = __uint_as_float(((kb & 0x8000u) ? (kb & 0x7fffu) : (~kb & 0xffffu)) << 16);
    const float rs = rbf(1.0f / (1.0f + __expf(-r)));
#pragma unroll
    for (int e = 0; e < 4; ++e) y[e] = rbf(rbf(xsum16_32(y[e])) * rs);
    wait_bytes<0x80808080u>(smem, F_FREE, 16);
    *(f32x4*)(smem + L_UP + c * 256 + li * 16) = f32x4{y[0], y[1], y[2], y[3]};
  }
  lds_post(smem, F_UP + w, 1);
  STAMP(7 + (w == 0 || w == 3 ? -5 : 0));
}

__device__ __forceinline__ void side_wave(char* smem, int F, int lane, int bid, int tm, int tn, const u16* __restrict__ x,
                                          const u16* __restrict__ wgu, const u16* __restrict__ wd, const u16* __restrict__ wo, char* ws) {
  const rsrc_t wrs = mk_rsrc(ws, WS_O), xrs = mk_rsrc(x, S_TOK * DM * 2);
  const int u = unit_of(bid), cg = (bid & 7) * 7 + ((u >> 2) % 7), q = u & 3;
  const int g = lane >> 4, li = lane & 15;
  u32x4 fw[4][8];
  const rsrc_t grs = mk_rsrc(wgu, DM * FF * 2);
  const int cu = (bid & 7) * 7 + (((u >> 2) + F) % 7);
  const unsigned fo = (unsigned)(q * 256 + (li >> 3) * 128 + 8 * g) * (FF * 2) + cu * 128 + (li & 7) * 16;
#pragma unroll
  for (int s = 0; s < 4; ++s) asm volatile("" : "=v"(fw[s][0]), "=v"(fw[s][1]), "=v"(fw[s][2]), "=v"(fw[s][3]), "=v"(fw[s][4]), "=v"(fw[s][5]), "=v"(fw[s][6]), "=v"(fw[s][7]));
  const unsigned csb = WS_CS + (tn * 2 + F) * 4096;
  u32x4 cs[4], rec[2];
  unsigned ca = csb + lane * 16, ra = WS_R + (unsigned)max(tm * 128 - 16 + F * 72 + lane, 0) * 16, rb = WS_R + (unsigned)max(tm * 128 - 8 + F * 72 + lane, 0) * 16;
  asm volatile("" : "+v"(ca), "+v"(ra), "+v"(rb));
  const rsrc_t prs = mk_rsrc(wo, DM * DM * 2);
  const unsigned wa = TW == 4 ? (unsigned)((tm & 3) * 256 + F * 128 + lane) * 2048u + tn * 128 : (lane < 32 ? (unsigned)(tm * 64 + F * 32 + lane) * 2048u + tn * 128 : 0x7fffffffu);
  unsigned td[2] = {0, 0};
  constexpr int TN = TW == 4 ? 2 : 1, C1N = 14 + (TK == 1 ? TN : 0), C2N = 8 + (TK == 2 ? TN : 0);
  unsigned due = u < 28 ? (DUE >> ((tn & 3) * 4)) & 15 : 9, iss = 0;
#define FW(n) fw[(n) >> 3][(n) & 7]
#define LD(i) "s_mov_b32 %[t], (((%[n0]+" #i ")>>3)*32+((%[n0]+" #i ")&7))*7168\n\tbuffer_load_dwordx4 %" #i ", %[fo], %[rs], %[t] offen nt\n\t"
#define LD8 LD(0) LD(1) LD(2) LD(3) LD(4) LD(5) LD(6) LD(7)
#define R8(n) "+v"(FW(n)), "+v"(FW(n + 1)), "+v"(FW(n + 2)), "+v"(FW(n + 3)), "+v"(FW(n + 4)), "+v"(FW(n + 5)), "+v"(FW(n + 6)), "+v"(FW(n + 7))
#define SITE(lds_, ...) asm volatile("s_cmp_lg_u32 %[iss], %[k]\n\ts_cbranch_scc1 1f\n\ts_cmp_le_u32 %[nb], %[due]\n\ts_cbranch_scc1 1f\n\ts_sleep %[dly]\n\t" lds_ \
    "s_add_u32 %[iss], %[iss], 1\n\ts_add_u32 %[due], %[due], 4\n\t1:" : __VA_ARGS__, [iss] "+s"(iss), [due] "+s"(due), [t] "=&s"(t) \
    : [fo] "v"(fo), [rs] "s"(grs), [nb] "s"(nb), [k] "i"(K), [n0] "i"(n0), [dly] "i"(D), [ca] "v"(ca), [ra] "v"(ra), [rb] "v"(rb), [ws] "s"(wrs), [wa] "v"(wa), [wr] "s"(prs), [w2] "s"(64 * 2048), [tk] "i"(TK), [tw] "i"(TW) : "scc", "memory")
#define C1K(i, a, o) "buffer_load_dwordx4 %" #i ", %[" #a "], %[ws], 0 offen offset:" #o " sc1\n\t"
#define G2(a, b, c, d, e, f) C1K(a, ca, 0) C1K(b, ca, 1024) C1K(c, ca, 2048) C1K(d, ca, 3072) C1K(e, ra, 0) C1K(f, rb, 0)
#define PING2() asm volatile("" : "+v"(cs[0]), "+v"(cs[1]), "+v"(cs[2]), "+v"(cs[3]), "+v"(rec[0]), "+v"(rec[1]))
  auto site = [&](auto kk, auto dd, unsigned nb) {
    constexpr int K = decltype(kk)::v, D = decltype(dd)::v, n0 = K == 0 ? 0 : 8 + K * 8;
    unsigned t;
    SCHED0();
    if constexpr (K == 0) SITE(LD8 LD(8) LD(9) LD(10) LD(11) LD(12) LD(13) LD(14) LD(15), R8(n0), R8(n0 + 8));
    else if constexpr (K == 1) SITE(G2(8, 9, 10, 11, 12, 13) LD8 ".if %[tk] == 1\n\tbuffer_load_dword %14, %[wa], %[wr], 0 offen\n\t.if %[tw] == 4\n\tbuffer_load_dword %15, %[wa], %[wr], %[w2] offen\n\t.endif\n\t.endif\n\t",
                                    R8(n0), "+v"(cs[0]), "+v"(cs[1]), "+v"(cs[2]), "+v"(cs[3]), "+v"(rec[0]), "+v"(rec[1]), "+v"(td[0]), "+v"(td[1]));
    else SITE(LD8 ".if %[tk] == 2\n\tbuffer_load_dword %8, %[wa], %[wr], 0 offen\n\t.if %[tw] == 4\n\tbuffer_load_dword %9, %[wa], %[wr], %[w2] offen\n\t.endif\n\t.endif\n\t", R8(n0), "+v"(td[0]), "+v"(td[1]));
    SCHED0();
  };
#define CH(k, d, n) site(IC<k>{}, IC<d>{}, n)
  unsigned tag;
  {
    const int qq = (lane >> 2) & 3, hk = (qq >> 1) | ((g & 1) << 1);
    const char* pb = smem + XB + (8 * g + qq) * 128 + 8 * (lane & 3) + (((2 * F + (tm & 1)) ^ hk) << 5);
    const bf16x8 ones = {0x3f80, 0x3f80, 0x3f80, 0x3f80, 0x3f80, 0x3f80, 0x3f80, 0x3f80};
    f32x4 a[3];
#pragma unroll
    for (int t = 0; t < 2; ++t) {
      SCHED0(); BAR(); if (HN) NAP(HN); SCHED0();
      __builtin_amdgcn_s_setprio(3);
#pragma unroll
      for (int c = 0; c < 3; ++c) {
        a[c] = MFMA16(ones, lds_tr2(pb + t * WSL + c * 8192), t ? a[c] : (f32x4{0, 0, 0, 0}));
        a[c] = MFMA16(ones, lds_tr2(pb + t * WSL + c * 8192 + 4096), a[c]);
      }
      asm volatile("" : "+v"(a[0]), "+v"(a[1]), "+v"(a[2]));
      if (t == 0) { tag = uni(lds_word(smem, F_TAG)); CH(0, CD, 1); }
    }
    st16c(wrs, lane < 16 ? csb + ((tm & 12) + (tm & 1) * 2 + ((tm >> 1) & 1)) * 256 + lane * 16 : 0x7fffffffu,
          u32x4{__float_as_uint(a[0][0]), __float_as_uint(a[1][0]), __float_as_uint(a[2][0]), tag});
    __builtin_amdgcn_s_setprio(0);
    CH(0, CD, 2);
  }
#define SST(i)
  int nb = 2;
  tick(nb);
  const unsigned xs_off = WS_S + (unsigned)(((bid & 7) * 4 + q) * 6144 + lane * 16);
  uint2 tc[2], tq[2];
  unsigned ta[3], tv, todo = 1;
  auto gather2 = [&]() {
    asm volatile(G2(0, 1, 2, 3, 4, 5) : "=&v"(cs[0]), "=&v"(cs[1]), "=&v"(cs[2]), "=&v"(cs[3]), "=&v"(rec[0]), "=&v"(rec[1])
                 : [ca] "v"(ca), [ra] "v"(ra), [rb] "v"(rb), [ws] "s"(wrs) : "memory");
    SCHED0();
  };
  auto prepare = [&]() {
#pragma unroll 1
    for (unsigned n = 0; n < SPIN_CAP; ++n) {
      unsigned bad = chk(chk(0u, rec[0], tag), rec[1], tag);
#pragma unroll
      for (int j = 0; j < 4; ++j) bad = __builtin_amdgcn_bitop3_b32(bad, cs[j][3], tag, 0xF6);
      if (__all(bad == 0)) break;
      NAP(8);
      gather2();
      tick(nb); tick(nb); WAITV(0); PING2();
    }
    float cz[3];
#pragma unroll
    for (int z = 0; z < 3; ++z) {
      const unsigned v = __float_as_uint(opq(opq(f_of(cs[0][z]) + f_of(cs[1][z])) + opq(f_of(cs[2][z]) + f_of(cs[3][z]))));
      auto p = __builtin_amdgcn_permlane16_swap(v, v, false, false);
      cz[z] = opq(f_of(p[0]) + f_of(p[1]));
    }
    const float s0 = (g & 1) ? cz[1] : cz[0], s1 = (g & 1) ? cz[2] : cz[1];
    const float h0 = rbf(s0), d0 = opq(s0 - h0), c0 = rbf(d0), h1 = rbf(s1), d1 = opq(s1 - h1), c1 = rbf(d1);
    tc[0] = make_uint2(pack2(h0, c0), pack2(opq(d0 - c0), 0.f)); tc[1] = make_uint2(pack2(h1, c1), pack2(opq(d1 - c1), 0.f));
    asm volatile("" : "+v"(tc[0].x), "+v"(tc[0].y), "+v"(tc[1].x), "+v"(tc[1].y));
#pragma unroll
    for (int j = 0; j < 2; ++j) {
      const unsigned nm = (rec[j][0] & 0xffffu) ^ 0x8000u;
      tq[j] = make_uint2(nm | (nm << 16), __float_as_uint(__builtin_amdgcn_rcpf(f_of(rec[j][2]))));
      asm volatile("" : "+v"(tq[j].x), "+v"(tq[j].y));
    }
    ta[0] = lds_addr(smem + L_CSF + ((g & 1) * 64 + F * 32 + (g >> 1) * 16 + li) * 8);
    ta[1] = lds_addr(smem + L_QST + (F * 72 + lane) * 8);
    ta[2] = lds_addr(smem + (lane < 8 ? F_SEL + lane * 4 : lane == 8 ? F_SELOK : F_ST + F * 4));
    asm volatile("" : "+v"(ta[0]), "+v"(ta[1]), "+v"(ta[2]));
    SCHED0();
  };
  auto tables = [&](unsigned now) {
    asm volatile("s_and_b32 %0, %0, %8\n\ts_cbranch_scc0 1f\n\tds_write2_b64 %1, %2, %9 offset1:64\n\tds_write2_b64 %3, %4, %5 offset1:8\n\ts_mov_b64 exec, 0x3ff\n\tds_write_b32 %6, %7\n\ts_mov_b64 exec, -1\n\t1:"
                 : "+s"(now) : "v"(ta[0]), "v"(tc[0]), "v"(ta[1]), "v"(tq[0]), "v"(tq[1]), "v"(ta[2]), "v"(tv), "s"(todo), "v"(tc[1]) : "scc", "memory");
    todo &= ~now;
  };
  auto slice = [&]() {
    if (nb < NBAR) tick(nb);
    else { const u32x2 fl = *(const volatile LAS u32x2*)(LAS const char*)(smem + F_FULL); tables(uni(min(fl[0], fl[1])) >> 4); SCHED0(); }
  };
  if (u >= 28) {
    u32x4 kd[4];
#pragma unroll 1
    for (unsigned n = 0; n < SPIN_CAP; ++n) {
#define L1K(i, o) "buffer_load_dwordx4 %" #i ", %4, %5, 0 offen offset:" #o " sc1\n\t"
      asm volatile("s_nop 4\n\t" L1K(0, 0) L1K(1, 1024) L1K(2, 2048) L1K(3, 3072)
                   : "=&v"(kd[0]), "=&v"(kd[1]), "=&v"(kd[2]), "=&v"(kd[3]) : "v"(WS_K + F * 4096 + lane * 16), "s"(wrs) : "memory");
#undef L1K
      tick(nb);
      asm volatile("s_waitcnt vmcnt(0)" : "+v"(kd[0]), "+v"(kd[1]), "+v"(kd[2]), "+v"(kd[3]) :: "memory");
      if (__all(chk(chk(chk(chk(0u, kd[0], tag), kd[1], tag), kd[2], tag), kd[3], tag) == 0)) break;
      NAP(4);
    }
    SST(8);
    unsigned m1 = 0, m2 = 0, m3 = 0, m4 = 0;
#pragma unroll
    for (int j = 0; j < 4; ++j) {
      kd[j] = u32x4{(kd[j][0] << 16) | (15 - 4 * j), (kd[j][0] & 0xffff0000u) | (14 - 4 * j), (kd[j][2] << 16) | (13 - 4 * j), (kd[j][2] & 0xffff0000u) | (12 - 4 * j)};
#pragma unroll
      for (int i = 0; i < 4; ++i) {
        const unsigned v = kd[j][i];
        m4 = med3(m3, m4, v); m3 = med3(m2, m3, v); m2 = med3(m1, m2, v); m1 = max(m1, v);
      }
    }
    const unsigned lb = ((63 - lane) << 5) | (F ? 0 : 16);
    m1 |= lb; m2 |= lb; m3 |= lb; m4 |= lb;
    asm volatile("" : "+v"(m1), "+v"(m2), "+v"(m3), "+v"(m4));
    tick(nb);
    unsigned best = 0;
#pragma unroll
    for (int r = 0; r < 8; ++r) {
      const unsigned wm = wave_max_u32(m1);
      best = (lane & 7) == r ? wm : best;
      const bool won = m1 == wm;
      m1 = won ? m2 : m1; m2 = won ? m3 : m2; m3 = won ? m4 : m3; m4 = won ? 0u : m4;
      if (r == 3) { asm volatile("" : "+v"(m1), "+v"(m2), "+v"(m3), "+v"(m4), "+v"(best)); tick(nb); }
    }
    if (__any(m1 == 0)) {
      unsigned wm = 0;
#pragma unroll 1
      for (int r = 0; r < 8; ++r) {
        if (r & 1) tick(nb);
        unsigned m = 0;
#pragma unroll
        for (int j = 0; j < 4; ++j)
#pragma unroll
          for (int i = 0; i < 4; ++i) m = max(m, (kd[j][i] | lb) - wm);
        wm += wave_max_u32(m);
        best = (lane & 7) == r ? wm : best;
      }
    }
    *(volatile LAS unsigned*)(LAS char*)(smem + F_CND + (F ? 60 - (lane & 7) * 4 : (lane & 7) * 4)) = best + 1;
    unsigned d;
    const unsigned ct = 2047u - (best & 0xffffu);
    asm volatile("buffer_load_dword %0, %1, %2, 0 offen\n\tbuffer_load_dword %0, %3, %4, 0 offen sc1" : "=&v"(d)
                 : "v"(lane < 32 ? ct * 2048u + q * 512 + (lane >> 3) * 128 : 0x7fffffffu), "s"(xrs), "v"(lane < 8 ? WS_R + ct * 16 : 0x7fffffffu), "s"(wrs) : "memory");
    SST(9);
    gather2();
    unsigned ka, kb;
#pragma unroll 1
    for (unsigned n = 0; n < SPIN_CAP; ++n) {
      tick(nb);
      ka = *(const volatile LAS unsigned*)(LAS const char*)(smem + F_CND + (lane & 7) * 4);
      kb = *(const volatile LAS unsigned*)(LAS const char*)(smem + F_CND + 32 + (lane & 7) * 4);
      if (__all(min(ka, kb) != 0)) break;
      NAP(1);
    }
    const unsigned selkey = max(ka, kb) - 1, tok = 2047u - (selkey & 0xffffu);
    u32x4 xf[2], trec;
#pragma unroll 1
    for (unsigned n = 0; n < SPIN_CAP; ++n) {
      asm volatile("buffer_load_dwordx4 %0, %3, %4, 0 offen\n\tbuffer_load_dwordx4 %1, %3, %4, 0 offen offset:64\n\tbuffer_load_dwordx4 %2, %5, %6, 0 offen sc1"
                   : "=&v"(xf[0]), "=&v"(xf[1]), "=&v"(trec) : "v"(tok * 2048u + q * 512 + (li >> 3) * 256 + g * 16 + F * 128), "s"(xrs), "v"(WS_R + tok * 16), "s"(wrs) : "memory");
      if (n) tick(nb);
      asm volatile("s_waitcnt vmcnt(0)" : "+v"(xf[0]), "+v"(xf[1]), "+v"(trec), "+v"(d) :: "memory"); PING2();
      if (__all(chk(0u, trec, tag) == 0)) break;
      NAP(1);
    }
    const float mb = lo2f(trec[0]), rd = 1.0f / f_of(trec[2]);
#pragma unroll
    for (int s = 0; s < 2; ++s)
#pragma unroll
      for (int j = 0; j < 4; ++j) xf[s][j] = ln2s(xf[s][j], mb, rd);
    st16c(wrs, xs_off + F * 3072, u32x4{xf[0][0], xf[0][1], xf[0][2], tag});
    st16c(wrs, xs_off + F * 3072 + 1024, u32x4{xf[1][0], xf[1][1], xf[1][2], tag});
    st16c(wrs, xs_off + F * 3072 + 2048, u32x4{xf[0][3], xf[1][3], selkey, tag});
    tv = lane < 8 ? selkey : 1u;
    SST(2);
    tick(nb);
    asm volatile("s_waitcnt vmcnt(0)\n\tbuffer_load_dword %0, %1, %2, 0 offen sc1" : "=&v"(d) : "v"(lane < 24 ? xs_off + F * 3072 + lane * 112 : 0x7fffffffu), "s"(wrs) : "memory");
    prepare();
    tick(nb); NAP(8);
    asm volatile("buffer_load_dword %0, %2, %3, 0 offen\n\t.if %5 == 4\n\tbuffer_load_dword %1, %2, %3, %4 offen\n\t.endif" : "+v"(td[0]), "+v"(td[1]) : "v"(wa), "s"(prs), "s"(64 * 2048), "i"(TW) : "memory");
    tick(nb); tick(nb);
    asm volatile("s_waitcnt vmcnt(0)" : "+v"(d), "+v"(td[0]), "+v"(td[1]) :: "memory");
    while (nb < NBAR) tick(nb);
    __builtin_amdgcn_s_setprio(1);
    wait_full(smem, F_FULL, 16);
    tables(1);
  } else {
    u32x4 xs[6];
    auto ask = [&]() {
#define X1K(i, o) "buffer_load_dwordx4 %" #i ", %6, %7, 0 offen offset:" #o " sc1\n\t"
#define X2K(i, o) "buffer_load_dwordx4 %" #i ", %6, %7, %8 offen offset:" #o " sc1\n\t"
      asm volatile(X1K(0, 0) X1K(1, 1024) X1K(2, 2048) X1K(3, 3072) X2K(4, 0) X2K(5, 1024)
                   : "=&v"(xs[0]), "=&v"(xs[1]), "=&v"(xs[2]), "=&v"(xs[3]), "=&v"(xs[4]), "=&v"(xs[5]) : "v"(xs_off), "s"(wrs), "s"(4096) : "memory");
      SCHED0();
    };
    auto good = [&]() {
      unsigned bad = xs[0][3] ^ tag;
#pragma unroll
      for (int j = 1; j < 6; ++j) bad = __builtin_amdgcn_bitop3_b32(bad, xs[j][3], tag, 0xF6);
      return __all(bad == 0);
    };
#define PIN6(a) asm volatile("" : "+v"(a[0]), "+v"(a[1]), "+v"(a[2]), "+v"(a[3]), "+v"(a[4]), "+v"(a[5]))
#define WAITS(snap, n) asm volatile("s_waitcnt vmcnt(%2)\n\ts_cmp_lg_u32 %0, %1\n\ts_cbranch_scc1 1f\n\ts_waitcnt vmcnt(0)\n\t1:" : "+s"(iss) : "s"(snap), "i"(n) : "scc", "memory")
    asm volatile("" : "=v"(cs[0]), "=v"(cs[1]), "=v"(cs[2]), "=v"(cs[3]), "=v"(rec[0]), "=v"(rec[1]));
    CH(0, DL - HN, 3);
    tick(nb);
    CH(0, DL - HN, 99);
    tick(nb);
    CH(1, DL - HN, nb);
    tick(nb);
    WAITS(1, C1N); PIN8(fw[0]); PIN8(fw[1]);
    tr_all(fw[0]);
    CH(1, DL - 7, nb);
    tick(nb);
    tr_all(fw[1]);
    CH(1, DL - 7, nb);
    tick(nb);
    CH(1, DL - HN, 99);
    tick(nb);
    CH(2, DL - HN, nb);
    tick(nb);
    WAITS(2, C2N); PING2(); PIN8(fw[2]);
    ask();
    prepare();
    unsigned snap = iss;
    asm volatile("" : "+s"(snap));
    CH(2, D10, nb);
    tick(nb);
    WAITS(snap, C2N); PIN6(xs);
    SST(1);
    if (!good()) {
      CH(2, 0, 99);
#pragma unroll 1
      for (unsigned n = 0; n < SPIN_CAP; ++n) {
        ask(); tick(nb); WAITV(0); PIN6(xs);
        if (good()) break;
        NAP(4);
      }
    }
    SST(2);
    tv = lane < 8 ? xs[2][2] : 1u;
    xs[0][3] = xs[2][0]; xs[1][3] = xs[2][1]; xs[3][3] = xs[5][0]; xs[4][3] = xs[5][1];
    PIN6(xs); asm volatile("" : "+v"(tv));
    if (nb == NBAR) { __builtin_amdgcn_s_setprio(1); wait_full(smem, F_FULL, 16); tables(1); }
    f32x4 acc[8];
    auto mma = [&](int s) {
      SCHED0(); __builtin_amdgcn_s_setprio(3); SCHED0();
#pragma unroll
      for (int i = 0; i < 8; ++i) acc[i] = MFMA16(__builtin_bit_cast(bf16x8, fw[s][i]), __builtin_bit_cast(bf16x8, xs[s + (s >> 1)]), s ? acc[i] : (f32x4{0, 0, 0, 0}));
      SCHED0(); __builtin_amdgcn_s_setprio(0); SCHED0();
      PIN8(acc);
    };
    mma(0);
    tr_all(fw[2]);
    CH(2, DL - 9, 99);
    slice();
    mma(1);
    mma(2);
    slice();
    WAITV(0); PIN8(fw[3]); asm volatile("" : "+v"(td[0]), "+v"(td[1]));
    tr_all(fw[3]);
    mma(3);
    {
#pragma unroll
      for (int r = 0; r < 4; ++r) {
        float v[2];
#pragma unroll
        for (int z = 0; z < 2; ++z) {
          auto a = __builtin_amdgcn_permlane32_swap(__float_as_uint(acc[z][r]), __float_as_uint(acc[z + 4][r]), false, false);
          auto b = __builtin_amdgcn_permlane32_swap(__float_as_uint(acc[z + 2][r]), __float_as_uint(acc[z + 6][r]), false, false);
          v[z] = f_of(li < 8 ? a[0] : b[1]) + dpp_f<0x128>(f_of(li < 8 ? b[0] : a[1]));
        }
        st16c(wrs, WS_GU + (unsigned)(((cu * 4 + q) * 2 + F) * 4096 + r * 1024 + lane * 16), u32x4{__float_as_uint(v[0]), tag, __float_as_uint(v[1]), tag});
      }
      SCHED0();
      SST(3);
    }
    while (nb < NBAR) tick(nb);
    __builtin_amdgcn_s_setprio(1);
    u32x4 fd[2][8];
    {
      const rsrc_t drs = mk_rsrc(wd, DM * FF * 2);
      const unsigned dof = (unsigned)(cg * 64 + 8 * g) * (DM * 2) + q * 512 + F * 256 + li * 16;
      if (FDN) NAP(FDN);
#pragma unroll
      for (int s = 0; s < 2; ++s)
#pragma unroll
        for (int j = 0; j < 8; ++j) fd[s][j] = __builtin_amdgcn_raw_buffer_load_b128(drs, dof + (j & 1) * (DM * 2), (s * 32 + (j & 6)) * (DM * 2), 2);
      SCHED0();
    }
    wait_full(smem, F_FULL, 16);
    tables(1);
    SST(4);
    if (bid == 0 && F == 0 && lane == 0) __hip_atomic_store((GAS unsigned*)(ws + WS_EP), tag, RLX_AGENT);
    hidden_half(smem, lane, F, cg, ws, tag, fd);
    SST(5);
    wait_bytes<0x8080u>(smem, F_H, 1);
    const bf16x8 h0 = lds_b128(smem + L_HF + (li & 7) * 128 + g * 16), h1 = lds_b128(smem + L_HF + (li & 7) * 128 + 64 + g * 16);
#pragma unroll
    for (int i = 0; i < 8; ++i) {
      acc[i] = MFMA16(h0, __builtin_bit_cast(bf16x8, fd[0][i]), (f32x4{0, 0, 0, 0}));
      acc[i] = MFMA16(h1, __builtin_bit_cast(bf16x8, fd[1][i]), acc[i]);
    }
    const unsigned off = WS_Y + (unsigned)((cg * 8 + (g & 1) * 4) * DM + q * 256 + F * 128 + li * 8 + (g >> 1) * 4) * 4u;
#pragma unroll
    for (int r = 0; r < 4; ++r) {
      const unsigned lo = pack2(acc[0][r], acc[1][r]), hi = pack2(acc[2][r], acc[3][r]), lo2 = pack2(acc[4][r], acc[5][r]), hi2 = pack2(acc[6][r], acc[7][r]);
      st16c(wrs, off + r * (DM * 4), u32x4{g < 2 ? lo : lo2, tag, g < 2 ? hi : hi2, tag});
    }
    SST(6);
  }
  __builtin_amdgcn_s_waitcnt(0x0F70);
}

__global__ __launch_bounds__(512, 2) void fused_block(const u16* __restrict__ x, const u16* __restrict__ wqkv, const u16* __restrict__ wo,
                                                      const u16* __restrict__ wr, const u16* __restrict__ wg, const u16* __restrict__ wu,
                                                      const u16* __restrict__ wd, char* ws, u16* __restrict__ out) {
  __shared__ __attribute__((aligned(128))) char smem[L_SIZE];
  unsigned long long pc;
  unsigned first;
  asm volatile("v_cmp_gt_u32 vcc, 24, %2\n\ts_getpc_b64 %0\n\ts_cbranch_vccz 1f\n\ts_mov_b64 exec, vcc\n\tv_lshlrev_b32 %1, 7, %2\n\tglobal_load_dword %1, %1, %0\n\ts_mov_b64 exec, -1\n\t1:"
               : "=s"(pc), "=&v"(first) : "v"(threadIdx.x) : "vcc", "memory");
  SCHED0();
  const int lane = threadIdx.x & 63;
  const int wid = __builtin_amdgcn_readfirstlane(threadIdx.x >> 6);
  const int bid = blockIdx.x, xcd = bid & 7, off = bid >> 3;
  const int tm = (xcd & 1) * 8 + (xcd & 2) * 2 + (off & 3), tn = (xcd >> 2) * 8 + (off >> 2);
  STAMP(0);
  if (wid == 7) {
    unsigned d;
    asm volatile("s_getpc_b64 %0" : "=s"(pc));
    asm volatile("buffer_load_dword %0, %1, %2, 0 offen\n\ts_waitcnt vmcnt(0)" : "=&v"(d)
                 : "v"((unsigned)(lane * 32 + off) * 128u), "s"(mk_rsrc((const void*)(pc & ~255ull), CODE_BYTES)) : "memory");
  }
  asm volatile("" : "+s"(x), "+s"(wqkv), "+s"(wr), "+s"(ws), "+s"(out), "+s"(wo), "+s"(wd), "+s"(wg), "+s"(wu));
  unsigned rot2 = ((((unsigned)(0x65432106543210ull >> (off & 3) * 8) & 0xfffffffu) + ((tn & 7) + 1) * 0x1111111u) & 0x7777777u) * 2 | (unsigned)tn << 29;
  unsigned tag = 0;
  RowRegs rr;
  EpiRegs e;
  row_request(rr, x, wr, ws, tm * 128 + tn * 8 + wid * 2, lane, wid < 4);
  rr.keep = first;
  SCHED0();
  if ((wid >> 1) == 2) {
    __builtin_amdgcn_s_setprio(2);
    const Stager stage(smem, wid - 4, lane, tm, tn, x, wqkv);
    NAP(4);
    stage_x(stage, 0); stage_w(stage, 0); stage_x(stage, 1); stage_w(stage, 1);
    WAITV(21);
    BAR();
    stage_x(stage, 2); stage_w(stage, 2);
  }
  asm volatile("" ::: "memory");
  if (wid < 4) {
    __builtin_amdgcn_s_setprio(2);
    qkv_setup(smem, wid, lane, tm, tn, e);
    asm volatile("" : "+s"(rot2));
    SCHED0();
    tag = row_stats(rr, ws, tm * 128 + tn * 8 + wid * 2, lane);
    if (wid == 0) *(unsigned*)(smem + L_FLG + lane * 4) = lane * 4 == F_TAG - L_FLG ? tag : 0u;
    STAMP(10);
  }
  asm volatile("" ::: "memory");
  if (wid < 4) {
    if (wid & 1) qkv_attention<1>(smem, wid, lane, tm, tn, ws, tag, x, e);
    else qkv_attention<0>(smem, wid, lane, tm, tn, ws, tag, x, e);
    out_proj(smem, wid, lane, tm, tn, ws, tag, out, e, rot2);
  } else {
    if (wid < 6) loader(smem, wid - 4, lane, tm, tn, rot2, x, wqkv, wo, ws);
    else side_wave(smem, wid - 6, lane, bid, tm, tn, x, wid == 6 ? wg : wu, wd, wo, ws);
    updates(smem, (0x2130 >> ((wid - 4) * 4)) & 3, lane, tm, tn, ws);
    __builtin_amdgcn_s_waitcnt(0x0F70);
  }
}

extern "C" void kernel_launch(void* const* d_in, const int* in_sizes, int n_in, void* d_out, int out_size, void* d_ws,
                              size_t ws_size, hipStream_t stream) {
  fused_block<<<256, 512, 0, stream>>>((const u16*)d_in[0], (const u16*)d_in[1], (const u16*)d_in[2], (const u16*)d_in[4],
                                       (const u16*)d_in[5], (const u16*)d_in[6], (const u16*)d_in[7], (char*)d_ws, (u16*)d_out);
}
```

```cpp
#define GS 28
#define NB1 6
#define L1 0
#define DL 12
#define EPOS 2
#include <hip/hip_runtime.h>
#include <hip/hip_bf16.h>
#include <stdint.h>

typedef __attribute__((ext_vector_type(8))) short bf16x8;
typedef __attribute__((ext_vector_type(4))) short s16x4;
typedef __attribute__((ext_vector_type(4))) float f32x4;
typedef __attribute__((ext_vector_type(2))) float f32x2;
typedef __attribute__((ext_vector_type(2))) __bf16 bf16x2_t;
typedef __attribute__((ext_vector_type(4))) unsigned u32x4;
typedef unsigned short u16;
#define LAS __attribute__((address_space(3)))
#define GAS __attribute__((address_space(1)))
#define RLX_AGENT __ATOMIC_RELAXED, __HIP_MEMORY_SCOPE_AGENT

#define S_TOK 2048
#define DM 1024
#define NQKV 3072
#define FF 3584

typedef __attribute__((ext_vector_type(2))) unsigned u32x2;

#define WS_EP   0
#define WS_R    4096
#define WS_K    (WS_R + 32768)
#define WS_OF   (WS_K + 8192)
#define WS_CS   (WS_OF + 4096)
#define WS_GU   (WS_CS + 16 * 2 * 16 * 1024)
#define WS_Y    (WS_GU + 56 * 4 * 8192)
#define WS_S    (WS_Y + 56 * 8 * DM * 4)
#define WS_O    (8u << 20)

__device__ const unsigned k_tags[32] = {0,  1,  2,  3,  4,  5,  6,  7,  8,  9,  10, 11, 12, 13, 14, 15,
                                        16, 17, 18, 19, 20, 21, 22, 23, 24, 25, 26, 27, 28, 29, 30, 31};

__device__ __forceinline__ unsigned pack2(float a, float b) {
  f32x2 v = {a, b};
  bf16x2_t r = __builtin_convertvector(v, bf16x2_t);
  return __builtin_bit_cast(unsigned, r);
}
__device__ __forceinline__ float rbf(float f) { return __uint_as_float(pack2(f, f) << 16); }
__device__ __forceinline__ float lo2f(unsigned w) { return __uint_as_float(w << 16); }
__device__ __forceinline__ float hi2f(unsigned w) { return __uint_as_float(w & 0xffff0000u); }

__device__ __forceinline__ bf16x8 lds_b128(const char* p) { return *(const bf16x8*)p; }
__device__ __forceinline__ bf16x8 lds_tr2(const char* p) {
  s16x4 lo = __builtin_amdgcn_ds_read_tr16_b64_v4i16((LAS s16x4*)p);
  s16x4 hi = __builtin_amdgcn_ds_read_tr16_b64_v4i16((LAS s16x4*)(p + 512));
  return __builtin_shufflevector(lo, hi, 0, 1, 2, 3, 4, 5, 6, 7);
}
#define MFMA16(a, b, c) __builtin_amdgcn_mfma_f32_16x16x32_bf16(a, b, c, 0, 0, 0)
#define WAITV(n) asm volatile("s_waitcnt vmcnt(%0)" ::"i"(n) : "memory")
#define WAITL0() __builtin_amdgcn_s_waitcnt(0xC07F)
#define BAR() __builtin_amdgcn_s_barrier()
#define SCHED0() __builtin_amdgcn_sched_barrier(0)
#define NAP(n) asm volatile("s_sleep %0" ::"i"(n) : "memory")
__device__ __forceinline__ float xsum16_32(float v) {
  unsigned u = __float_as_uint(v);
  auto a = __builtin_amdgcn_permlane16_swap(u, u, false, false);
  v = __uint_as_float(a[0]) + __uint_as_float(a[1]);
  u = __float_as_uint(v);
  auto b = __builtin_amdgcn_permlane32_swap(u, u, false, false);
  return __uint_as_float(b[0]) + __uint_as_float(b[1]);
}
__device__ __forceinline__ bf16x8 pack8(const f32x4& a, const f32x4& b) {
  u32x4 r = {pack2(a[0], a[1]), pack2(a[2], a[3]), pack2(b[0], b[1]), pack2(b[2], b[3])};
  return __builtin_bit_cast(bf16x8, r);
}
__device__ __forceinline__ unsigned wave_max_u32(unsigned v) {
  v = max(v, (unsigned)__builtin_amdgcn_update_dpp(0, (int)v, 0x111, 0xf, 0xf, false));
  v = max(v, (unsigned)__builtin_amdgcn_update_dpp(0, (int)v, 0x112, 0xf, 0xf, false));
  v = max(v, (unsigned)__builtin_amdgcn_update_dpp(0, (int)v, 0x114, 0xf, 0xf, false));
  v = max(v, (unsigned)__builtin_amdgcn_update_dpp(0, (int)v, 0x118, 0xf, 0xf, false));
  v = max(v, (unsigned)__builtin_amdgcn_update_dpp(0, (int)v, 0x142, 0xa, 0xf, false));
  v = max(v, (unsigned)__builtin_amdgcn_update_dpp(0, (int)v, 0x143, 0xc, 0xf, false));
  return (unsigned)__builtin_amdgcn_readlane((int)v, 63);
}
__device__ __forceinline__ bf16x8 tr_frag(const u32x4 (&w)[8], int i) {
  const unsigned sel = (i & 1) ? 0x07060302u : 0x05040100u;
  u32x4 r = {__builtin_amdgcn_perm(w[1][i >> 1], w[0][i >> 1], sel), __builtin_amdgcn_perm(w[3][i >> 1], w[2][i >> 1], sel),
             __builtin_amdgcn_perm(w[5][i >> 1], w[4][i >> 1], sel), __builtin_amdgcn_perm(w[7][i >> 1], w[6][i >> 1], sel)};
  return __builtin_bit_cast(bf16x8, r);
}

#define XSL_ 18432
#define WSL 24576
#define XB (3 * XSL_)
#define NBAR 14
#define L_UP 0
#define L_WA 6144
#define L_W3 (L_WA + 57344)
#define L_WB (L_W3 + 16384)
#define L_OWN (L_WB + 57344)
#define L_XCH (XB + 4 * WSL)
#define L_HF (L_XCH + 6144)
#define L_CSF (L_HF + 1024)
#define L_QST (L_CSF + 1536)
#define L_FLG (L_QST + 1152)
#define L_SIZE (L_FLG + 256)
#define F_FULL (L_FLG + 0)
#define F_ST (L_FLG + 8)
#define F_WO (L_FLG + 16)
#define F_FREE (L_FLG + 32)
#define F_XCH (L_FLG + 40)
#define F_ODONE (L_FLG + 48)
#define F_SELOK (L_FLG + 52)
#define F_GO (L_FLG + 56)
#define F_OL (L_FLG + 60)
#define F_H (L_FLG + 64)
#define F_TAG (L_FLG + 68)
#define F_UP (L_FLG + 72)
#define F_SEL (L_FLG + 96)
#define F_CND (L_FLG + 128)
#define OD 7
#define SPIN_CAP (1u << 18)
#define LA 0
#define LB 0
#define L0 3
#define GW 0
#define WD 4
#define LF 0
#define DUE 0x3012
#define NW 3
#define TW0 3
#define CD 4
#define EV 4
#define PN 2
#define YD 24
#define OK 2
#define ON 8
#define HN 2
#define SD 0
#define PW 10
#define W2 1
#define LC 0
#define EP 1
#define DN 0
#define PR 8
#define MG (LC == 1)
#define XSL 10
#define KQ 1
#define TCH 3
#define TS 0
#define SX 0
#define CODE_BYTES 55424

#define RSC(i)
#define STAMP(i)
#define RST(i)

__device__ __forceinline__ unsigned lds_addr(const char* p) { return (unsigned)(size_t)(LAS const char*)p; }
__device__ __forceinline__ void lds_post(char* smem, int off, unsigned v) {
  asm volatile("s_mov_b64 exec, 1\n\tds_write_b8 %0, %1\n\ts_mov_b64 exec, -1" ::"v"(lds_addr(smem + off)), "v"(v) : "memory");
}
__device__ __forceinline__ unsigned lds_word(const char* smem, int off) { return *(const volatile LAS unsigned*)(LAS const char*)(smem + off); }
__device__ __forceinline__ u32x4 lds_word4(const char* smem, int off) { return *(const volatile LAS u32x4*)(LAS const char*)(smem + off); }
template <unsigned MASK, bool COND = false>
__device__ __forceinline__ unsigned wait_bytes(const char* smem, int off, unsigned need, unsigned skip = 0) {
  unsigned t, f;
  asm volatile(
      ".if %6\n\ts_cmp_lg_u32 %5, 0\n\ts_cbranch_scc1 2f\n\t.endif\n\t"
      "1:\n\t"
      "ds_read_b32 %1, %2\n\t"
      "s_waitcnt lgkmcnt(0)\n\t"
      "v_readfirstlane_b32 %0, %1\n\t"
      "s_or_b32 %0, %0, 0x80808080\n\t"
      "s_sub_u32 %0, %0, %3\n\t"
      "s_and_b32 %0, %0, %4\n\t"
      "s_cmp_eq_u32 %0, %4\n\t"
      "s_cbranch_scc1 2f\n\t"
      "s_sleep 1\n\t"
      "s_branch 1b\n\t"
      "2:"
      : "=&s"(t), "=&v"(f)
      : "v"(lds_addr(smem + off)), "s"(need * 0x01010101u), "s"(MASK), "s"(COND ? skip : MASK), "i"((int)COND)
      : "scc", "memory");
  return f;
}
__device__ __forceinline__ void wait_full(const char* smem, int off, unsigned need, unsigned cur = 0) {
  unsigned a, b;
  asm volatile(
      "s_cmp_ge_u32 %0, %4\n\t"
      "s_cbranch_scc1 3f\n\t"
      "s_branch 2f\n\t"
      "1:\n\t"
      "s_sleep 1\n\t"
      "2:\n\t"
      "ds_read_b32 %1, %3\n\t"
      "ds_read_b32 %2, %3 offset:4\n\t"
      "s_waitcnt lgkmcnt(0)\n\t"
      "v_min_u32 %1, %1, %2\n\t"
      "s_nop 0\n\t"
      "v_readfirstlane_b32 %0, %1\n\t"
      "s_cmp_lt_u32 %0, %4\n\t"
      "s_cbranch_scc1 1b\n\t"
      "3:"
      : "+s"(cur), "=&v"(a), "=&v"(b)
      : "v"(lds_addr(smem + off)), "s"(need)
      : "scc", "memory");
}
__device__ __forceinline__ unsigned uni(unsigned v) { return (unsigned)__builtin_amdgcn_readfirstlane((int)v); }
__device__ __forceinline__ void dma_tag(char* smem, int off, int n) {
  unsigned keep;
  asm volatile("s_nop 2\n\ts_mov_b32 %0, m0\n\ts_mov_b32 m0, %1\n\ts_mov_b64 exec, 1\n\tglobal_load_lds_dword %2, %3\n\ts_mov_b64 exec, -1\n\ts_mov_b32 m0, %0\n\ts_nop 0"
               : "=&s"(keep) : "s"(uni(lds_addr(smem + off))), "v"(n * 4), "s"(k_tags) : "memory");
}
template <int V> struct IC { static constexpr int v = V; };
struct QFrag { bf16x8 xa[5]; bf16x8 w[3][2]; };
__device__ __forceinline__ void q_read(QFrag& f, const char* sa, const char* sb0, const char* sb1, int ks) {
#pragma unroll
  for (int i = 0; i < 5; ++i) f.xa[i] = lds_b128(sa + i * 2048);
#pragma unroll
  for (int s = 0; s < 3; ++s) {
    f.w[s][0] = lds_tr2(sb0 + s * 8192 + ks * 4096);
    f.w[s][1] = lds_tr2(sb1 + s * 8192 + ks * 4096);
  }
}
__device__ constexpr int acc_no(int HALF, int m, int i, int t) {
  int n = 0;
  for (int ii = 0; ii < 5; ++ii)
    for (int tt = 0; tt < 2; ++tt)
      for (int mm = 0; mm < 3; ++mm) {
        const bool on = mm == 0 ? (HALF == 1 || (ii >= 1 && ii <= 3)) : (HALF == 0 || ii >= 1);
        if (mm == m && ii == i && tt == t) return on ? n : -1;
        if (on) ++n;
      }
  return -1;
}
__device__ constexpr int seq_of(int HALF, int z) {
  int n = 0;
  for (int a = 0; a < 30; ++a) {
    const int m = a / 10, t0 = (a / 5) % 2;
    const int i = m < 2 ? (t0 ? a % 5 : 4 - a % 5) : 4 - (a - 20) / 2, t = m < 2 ? t0 : (a ^ i) & 1;
    if (acc_no(HALF, m, i, t) >= 0) { if (n == z) return m * 100 + i * 10 + t; ++n; }
  }
  return -1;
}
template <int HALF, int Z, int E, bool FIRST>
__device__ __forceinline__ void q_one(const QFrag& f, f32x4 (&acc)[26]) {
  if constexpr (Z < E) {
    constexpr int m = seq_of(HALF, Z) / 100, i = seq_of(HALF, Z) / 10 % 10, t = seq_of(HALF, Z) % 10;
    constexpr int n = acc_no(HALF, m, i, t);
    const f32x4 c = FIRST ? f32x4{0, 0, 0, 0} : acc[n];
    acc[n] = m == 2 ? MFMA16(f.xa[i], f.w[2][t], c) : MFMA16(f.w[m][t], f.xa[i], c);
    q_one<HALF, Z + 1, E, FIRST>(f, acc);
  }
}
template <int HALF, int B, int E, int NRD, bool FIRST = false>
__device__ __forceinline__ void q_mma(const QFrag& f, f32x4 (&acc)[26]) {
  q_one<HALF, B, E, FIRST>(f, acc);
#pragma unroll
  for (int i = 0; i < NRD; ++i) {
    __builtin_amdgcn_sched_group_barrier(0x008, 1, 0);
    __builtin_amdgcn_sched_group_barrier(0x100, 1, 0);
  }
  __builtin_amdgcn_sched_group_barrier(0x008, E - B - NRD, 0);
}

__device__ __forceinline__ void tr_all(u32x4 (&w)[8]) {
  u32x4 t[8];
#pragma unroll
  for (int i = 0; i < 8; ++i) t[i] = __builtin_bit_cast(u32x4, tr_frag(w, i));
#pragma unroll
  for (int i = 0; i < 8; ++i) w[i] = t[i];
  asm volatile("" : "+v"(w[0]), "+v"(w[1]), "+v"(w[2]), "+v"(w[3]), "+v"(w[4]), "+v"(w[5]), "+v"(w[6]), "+v"(w[7]));
}
typedef __amdgpu_buffer_rsrc_t rsrc_t;
__device__ __forceinline__ rsrc_t mk_rsrc(const void* p, unsigned bytes) { return __builtin_amdgcn_make_buffer_rsrc((void*)p, 0, bytes, 0x00020000); }
__device__ __forceinline__ u32x4 ld16(rsrc_t rs, unsigned voff, unsigned soff) { return __builtin_amdgcn_raw_buffer_load_b128(rs, voff, soff, 0); }
__device__ __forceinline__ u32x4 ld16c(rsrc_t rs, unsigned voff, unsigned soff) { return __builtin_amdgcn_raw_buffer_load_b128(rs, voff, soff, 16); }
__device__ __forceinline__ void st16c(rsrc_t rs, unsigned voff, u32x4 v) { __builtin_amdgcn_raw_buffer_store_b128(v, rs, voff, 0, 16); }
__device__ __forceinline__ void st8c(rsrc_t rs, unsigned voff, unsigned v, unsigned tag) { __builtin_amdgcn_raw_buffer_store_b64(u32x2{v, tag}, rs, voff, 0, 16); }
__device__ __forceinline__ unsigned chk(unsigned bad, const u32x4& v, unsigned tag) {
  return __builtin_amdgcn_bitop3_b32(__builtin_amdgcn_bitop3_b32(bad, v[1], tag, 0xF6), v[3], tag, 0xF6);
}
__device__ __forceinline__ float f_of(unsigned u) { return __uint_as_float(u); }
#define LANE_VARS                                                                   \
  int lane = lane_in; asm volatile("" : "+v"(lane));                                \
  const int g = lane >> 4, r16 = lane & 15, q = (lane >> 2) & 3, p = lane & 3;      \
  const int hk = (q >> 1) | ((g & 1) << 1), brow = (8 * g + q) * 128 + 8 * p;       \
  (void)r16; (void)hk; (void)brow;

template <bool MAX>
__device__ __forceinline__ float rows_reduce(const float (&v)[4]) {
  auto op = [](unsigned a, unsigned b) {
    return __float_as_uint(MAX ? __builtin_elementwise_maximum(__uint_as_float(a), __uint_as_float(b)) : __uint_as_float(a) + __uint_as_float(b));
  };
  auto p = __builtin_amdgcn_permlane16_swap(__float_as_uint(v[0]), __float_as_uint(v[1]), false, false);
  auto q = __builtin_amdgcn_permlane16_swap(__float_as_uint(v[2]), __float_as_uint(v[3]), false, false);
  auto t = __builtin_amdgcn_permlane32_swap(op(p[0], p[1]), op(q[0], q[1]), false, false);
  return __uint_as_float(op(t[0], t[1]));
}
__device__ __forceinline__ void rows_spread(float t, float (&v)[4]) {
  auto h = __builtin_amdgcn_permlane32_swap(__float_as_uint(t), __float_as_uint(t), false, false);
  auto p = __builtin_amdgcn_permlane16_swap(h[0], h[0], false, false);
  auto q = __builtin_amdgcn_permlane16_swap(h[1], h[1], false, false);
  v[0] = __uint_as_float(p[0]); v[1] = __uint_as_float(p[1]); v[2] = __uint_as_float(q[0]); v[3] = __uint_as_float(q[1]);
}
typedef __attribute__((ext_vector_type(16))) unsigned u32x16;

template <int CTL>
__device__ __forceinline__ float dpp_f(float v) { return __uint_as_float(__builtin_amdgcn_update_dpp(0, (int)__float_as_uint(v), CTL, 0xf, 0xf, false)); }
__device__ __forceinline__ float sum16(float v) {
  v += dpp_f<0xB1>(v);
  v += dpp_f<0x4E>(v);
  v += dpp_f<0x141>(v);
  return v + dpp_f<0x140>(v);
}
__device__ __forceinline__ float dot2(unsigned a, unsigned b, float c) {
  return __builtin_amdgcn_fdot2_f32_bf16(__builtin_bit_cast(bf16x2_t, a), __builtin_bit_cast(bf16x2_t, b), c, false);
}
__device__ __forceinline__ float sum32(float v) {
  v = sum16(v);
  auto a = __builtin_amdgcn_permlane16_swap(__float_as_uint(v), __float_as_uint(v), false, false);
  return __uint_as_float(a[0]) + __uint_as_float(a[1]);
}
struct RowRegs { u32x4 x[4], w[2]; unsigned ep, keep; };
__device__ __forceinline__ void row_request(RowRegs& r, const u16* __restrict__ x, const u16* __restrict__ wr, char* ws, int row0, int lane, bool on) {
  if (!on) return;
  const rsrc_t xrs = mk_rsrc(x, S_TOK * DM * 2), rrs = mk_rsrc(wr, DM * 2);
  const unsigned xo = (unsigned)(row0 + (lane >> 5)) * 2048u + (lane & 31) * 16, wo = lane * 16;
  asm volatile("buffer_load_dword %0, off, %1, 0 sc1" : "=v"(r.ep) : "s"(mk_rsrc(ws + WS_EP, 4)) : "memory");
#pragma unroll
  for (int j = 0; j < 4; ++j) asm volatile("buffer_load_dwordx4 %0, %1, %2, 0 offen offset:%3" : "=v"(r.x[j]) : "v"(xo), "s"(xrs), "i"(j * 512) : "memory");
#pragma unroll
  for (int j = 0; j < 2; ++j) asm volatile("buffer_load_dwordx4 %0, %1, %2, 0 offen offset:%3" : "=v"(r.w[j]) : "v"(wo), "s"(rrs), "i"(j * 1024) : "memory");
}
__device__ __forceinline__ unsigned row_stats(RowRegs& r, char* ws, int row0, int lane) {
  float s0 = 0.f, s1 = 0.f;
  asm volatile("s_waitcnt vmcnt(2)" : "+v"(r.x[0]), "+v"(r.x[1]), "+v"(r.x[2]), "+v"(r.x[3]), "+v"(r.ep), "+v"(r.keep) :: "memory");
  const unsigned tag = uni(r.ep) + 1u;
#pragma unroll
  for (int j = 0; j < 4; ++j) {
    s0 = dot2(r.x[j][0], 0x3f803f80u, s0); s1 = dot2(r.x[j][1], 0x3f803f80u, s1);
    s0 = dot2(r.x[j][2], 0x3f803f80u, s0); s1 = dot2(r.x[j][3], 0x3f803f80u, s1);
  }
  const float mean = sum32(s0 + s1) * (1.0f / DM), mb = rbf(mean);
  s0 = 0.f; s1 = 0.f;
#pragma unroll
  for (int j = 0; j < 4; ++j)
#pragma unroll
    for (int i = 0; i < 4; ++i) {
      const float a = lo2f(r.x[j][i]) - mb, b = hi2f(r.x[j][i]) - mb;
      s0 = fmaf(a, a, s0); s1 = fmaf(b, b, s1);
      r.x[j][i] = pack2(a, b);
    }
  const float dm = mean - mb;
  const float var = rbf(fmaxf(sum32(s0 + s1) * (1.0f / DM) - dm * dm, 0.f));
  const float den = rbf(__builtin_amdgcn_sqrtf(rbf(var + 1e-5f))), rd = __builtin_amdgcn_rcpf(den);
  s0 = 0.f; s1 = 0.f;
  asm volatile("s_waitcnt vmcnt(0)" : "+v"(r.w[0]), "+v"(r.w[1]) :: "memory");
#pragma unroll
  for (int j = 0; j < 4; ++j)
#pragma unroll
    for (int i = 0; i < 4; ++i) {
      const unsigned n = pack2(lo2f(r.x[j][i]) * rd, hi2f(r.x[j][i]) * rd);
      const unsigned w = __builtin_amdgcn_permlane32_swap(r.w[j >> 1][i], r.w[j >> 1][i], false, false)[j & 1];
      if (i & 1) s1 = dot2(n, w, s1); else s0 = dot2(n, w, s0);
    }
  const float dot = sum32(s0 + s1);
  unsigned k = pack2(dot, dot) & 0xffffu;
  k = (k & 0x8000u) ? (~k & 0xffffu) : (k | 0x8000u);
  const rsrc_t wrs = mk_rsrc(ws, WS_O);
  st16c(wrs, (lane & 31) == 0 ? WS_R + (unsigned)(row0 + (lane >> 5)) * 16u : 0x7fffffffu,
        u32x4{(k << 16) | (__float_as_uint(mb) >> 16), tag, __float_as_uint(den), tag});
  const unsigned k01 = k | ((unsigned)__builtin_amdgcn_readlane((int)k, 32) << 16);
  st8c(wrs, lane == 0 ? WS_K + (unsigned)(((row0 >> 2) & 7) * 1024 + (row0 >> 5) * 16 + (row0 & 2) * 4) : 0x7fffffffu, k01, tag);
  return tag;
}
__device__ __forceinline__ void k_order(int tm, int tn, int& pa, int& pb) {
  pb = tn & 3;
  pa = (tm * 0x1111) ^ ((tn & 4) * 0x51 & 0x144) ^ ((~tn & 2) * 0x2200) ^ ((~tn & 1) << 9) ^ (pb == 3 ? 1 : tm & 1);
}
__device__ __forceinline__ int k_tile(int t, int pa, int pb) { return ((((t + pb) & 12) ? t + pb : t & 3) ^ (pa >> ((t + pb) & 12))) & 15; }
struct EpiRegs { int a0, b0, tx; unsigned xr, go; u32x4 xv[2][2]; unsigned t[16]; };
__device__ __forceinline__ float opq(float v) { asm("" : "+v"(v)); return v; }
typedef __attribute__((ext_vector_type(10))) unsigned u32x10;
#define DV 0
#define PRE 3
#define GRP(N, M, K, C) _Pragma("unroll") for (int z_ = 0; z_ < (N); ++z_) { __builtin_amdgcn_sched_group_barrier(0x008, M, 0); __builtin_amdgcn_sched_group_barrier(K, C, 0); }
__device__ constexpr int tail_of(int HALF, int z) {
  int n = 0;
  for (int a = 0; a < 30; ++a) {
    const int i = HALF ? a / 6 : 4 - a / 6, m = 2 - a % 6 / 2, t = a & 1;
    if (acc_no(HALF, m, i, t) >= 0 && (m < 2 || (HALF == 0 && i == 4))) { if (n == z) return m * 100 + i * 10 + t; ++n; }
  }
  return -1;
}
__device__ __forceinline__ void qkv_setup(char* smem, int wid, int lane_in, int tm, int tn, EpiRegs& e) {
  LANE_VARS
  e.a0 = (wid & 1) * 8192 + r16 * 128 + ((g ^ (r16 >> 1)) << 4);
  e.b0 = XB + brow + (((wid & 2) ^ hk) << 5);
  int pa, pb;
  k_order(tm, tn, pa, pb);
  e.tx = __builtin_ctz((unsigned)__ballot(k_tile(r16, pa, pb) == tn));
  e.xr = lds_addr(smem) + (16 + wid * 32 + r16) * 128 + ((((g & 1) * 2 + (g >> 1)) ^ (r16 >> 1)) << 4);
  asm volatile("" : "+v"(e.a0), "+v"(e.b0), "+v"(e.xr));
  asm volatile("" : "+s"(e.tx));
}
template <int HALF>
__device__ __forceinline__ void qkv_attention(char* smem, int wid, int lane_in, int tm, int tn, char* ws, unsigned tag,
                                              const u16* __restrict__ x, EpiRegs& e) {
  const int h = wid >> 1;
  f32x4 acc[26];
  float ra[5], rb[5], sa[4], sb[4];
  QFrag f0, f1;
  u32x4 fl;
  unsigned aq, ac, aa, ab, xa, xv, xf, ooff, sel[4];
  {
    LANE_VARS
    const int a0 = e.a0, b0 = e.b0, tx = uni(e.tx);
    const unsigned xr = e.xr;
    asm volatile("" : "=v"(e.xv[0][0]), "=v"(e.xv[0][1]), "=v"(e.xv[1][0]), "=v"(e.xv[1][1]));
    auto keep = [&](int t, int slot, int tx) {
      unsigned p0, p1;
      asm volatile("s_cmp_lg_u32 %7, %8\n\ts_cbranch_scc1 1f\n\tv_add_u32 %4, %9, %6\n\tv_xor_b32 %5, 64, %4\n\tds_read_b128 %0, %4\n\tds_read_b128 %1, %5\n\t"
                   "ds_read_b128 %2, %4 offset:2048\n\tds_read_b128 %3, %5 offset:2048\n\t1:"
                   : "+v"(e.xv[0][0]), "+v"(e.xv[0][1]), "+v"(e.xv[1][0]), "+v"(e.xv[1][1]), "=&v"(p0), "=&v"(p1) : "v"(xr), "s"(t), "s"(tx), "s"(slot) : "scc", "memory");
    };
    SCHED0();
    WAITL0();
    BAR();
    STAMP(1);
    q_read(f0, smem + a0, smem + b0, smem + (b0 ^ 32), 0);
    SCHED0();
    q_read(f1, smem + (a0 ^ 64), smem + b0, smem + (b0 ^ 32), 1);
    int qa = XSL_ + a0, qc = XSL_ + (a0 ^ 64), qb = WSL + b0, qd = WSL + (b0 ^ 32), so = XSL_;
    q_mma<HALF, 0, 26, 17, true>(f0, acc);
    asm volatile("" : "+v"(qa), "+v"(qb), "+v"(qc), "+v"(qd));
    SCHED0();
    keep(0, 0, tx);
#pragma unroll 1
    for (int t = 1; t < NBAR - 1; ) {
      SCHED0();
      WAITL0();
      BAR();
      SCHED0();
      q_read(f0, smem + qa, smem + qb, smem + qd, 0);
      q_mma<HALF, 0, 26, 17>(f1, acc);
      SCHED0();
      q_read(f1, smem + qc, smem + qb, smem + qd, 1);
      q_mma<HALF, 0, 19, 17>(f0, acc);
      SCHED0();
      int d = so == 2 * XSL_ ? -2 * XSL_ : XSL_;
      asm volatile("" : "+s"(d));
      SCHED0();
      q_mma<HALF, 19, 20, 0>(f0, acc);
      SCHED0();
      keep(t, so, tx);
      SCHED0();
      q_mma<HALF, 20, 21, 0>(f0, acc);
      SCHED0(); qa += d; qc += d; asm volatile("" : "+v"(qa), "+v"(qc)); SCHED0();
      q_mma<HALF, 21, 22, 0>(f0, acc);
      SCHED0(); so += d; ++t; asm volatile("" : "+s"(so), "+s"(t)); SCHED0();
      q_mma<HALF, 22, 23, 0>(f0, acc);
      SCHED0(); int e = (t & 3) ? WSL : -3 * WSL; asm volatile("" : "+s"(e)); SCHED0();
      q_mma<HALF, 23, 24, 0>(f0, acc);
      SCHED0(); qb += e; qd += e; asm volatile("" : "+v"(qb), "+v"(qd)); SCHED0();
      q_mma<HALF, 24, 26, 0>(f0, acc);
      SCHED0();
    }
    WAITL0();
    BAR();
    STAMP(2);
#pragma unroll 1
    for (int t = NBAR + 1; t < 17; ) {
      SCHED0();
      q_read(f0, smem + qa, smem + qb, smem + qd, 0);
      q_mma<HALF, 0, 26, 17>(f1, acc);
      SCHED0();
      q_read(f1, smem + qc, smem + qb, smem + qd, 1);
      const u32x4 fl = lds_word4(smem, F_FULL);
      q_mma<HALF, 0, 21, 18>(f0, acc);
      SCHED0();
      const unsigned cur = uni(min(fl[0], fl[1])), need = t;
      SCHED0();
      q_mma<HALF, 21, 22, 0>(f0, acc);
      SCHED0();
      keep(t, so, tx + 2);
      SCHED0();
      qa += XSL_; qb += WSL; qc += XSL_; qd += WSL; so += XSL_; ++t;
      q_one<HALF, 22, 25, false>(f0, acc);
      GRP(2, 1, 0x002, 2);
      asm volatile("" : "+v"(qa), "+v"(qb), "+v"(qc), "+v"(qd), "+s"(so), "+s"(t));
      SCHED0();
      q_mma<HALF, 25, 26, 0>(f0, acc);
      SCHED0();
      wait_full(smem, F_FULL, need, cur);
    }
    SCHED0();
    q_read(f0, smem + a0, smem + qb, smem + qd, 0);
    q_one<HALF, 0, 26, false>(f1, acc);
    {
      const int pa = 4 * g + (r16 & 1);
      const bool odd = r16 & 1, cur_a = pa <= r16, cur_b = pa + 2 <= r16;
      const unsigned qs = lds_addr(smem + L_QST + HALF * 512);
      aq = qs + r16 * 8; ac = lds_addr(smem + L_CSF + h * 256) + r16 * 8;
      aa = qs + pa * 8 + (cur_a ? 128 : 0); ab = qs + pa * 8 + (cur_b ? 128 : 0);
      xa = lds_addr(smem + L_XCH + h * 3072) + lane * 16; xv = xa + 2048 - lane * 8; xf = lds_addr(smem + F_XCH + h * 4);
      const unsigned la = odd ? 0x01000c0cu : 0x0c0c0100u, lb = odd ? 0x03020c0cu : 0x0c0c0302u;
      sel[0] = cur_a ? 0x0c0c0c0cu : la; sel[1] = cur_b ? 0x0c0c0c0cu : lb; sel[2] = cur_a ? la : 0x0c0c0c0cu; sel[3] = cur_b ? lb : 0x0c0c0c0cu;
      ooff = (unsigned)(tm * 8 + HALF * 4) * 32768u + (tn * 2 + h) * 1024 + lane * 16;
#pragma unroll
      for (int z = 0; z < 17; ++z) { GRP(1, 1, 0x100, 1); __builtin_amdgcn_sched_group_barrier(0x002, 1, 0); }
      GRP(9, 1, 0x002, 3);
      asm volatile("" : "+v"(aq), "+v"(ac), "+v"(aa), "+v"(ab), "+v"(xa), "+v"(xv), "+v"(xf), "+v"(ooff));
      asm volatile("" : "+v"(sel[0]), "+v"(sel[1]), "+v"(sel[2]), "+v"(sel[3]));
    }
    SCHED0();
#pragma unroll
    for (int i = 0; i < 5; ++i) f1.xa[i] = lds_b128(smem + (a0 ^ 64) + i * 2048);
#pragma unroll
    for (int s = 0; s < 3; ++s) {
      if (s == 2) fl = lds_word4(smem, F_FULL);
      f1.w[s][0] = lds_tr2(smem + qb + s * 8192 + 4096);
      f1.w[s][1] = lds_tr2(smem + qd + s * 8192 + 4096);
    }
    q_mma<HALF, 0, 26 - DV, 18>(f0, acc);
    SCHED0();
    keep(15, 0, tx);
    SCHED0();
  }
  LANE_VARS
  auto mm = [&](const QFrag& f, int m, int i, int t) {
    const int n = max(acc_no(HALF, m, i, t), 0);
    acc[n] = m == 2 ? MFMA16(f.xa[i], f.w[2][t], acc[n]) : MFMA16(f.w[m][t], f.xa[i], acc[n]);
  };
  auto last = [&](int z0, int z1) {
#pragma unroll
    for (int z = z0; z < z1; ++z) {
      const int c = tail_of(HALF, HALF ? z : z < 6 ? z + 2 : z < 8 ? z - 6 : z);
      mm(f1, c / 100, c / 10 % 10, c % 10);
    }
  };
  last(0, PRE);
  SCHED0();
  const unsigned cur = uni(min(fl[2], fl[3]));
  SCHED0();
  last(PRE, PRE + 1);
  SCHED0();
  wait_full(smem, F_ST, 1, cur);
  SCHED0();
  u32x2 sq[5], cs[3][2];
#pragma unroll
  for (int i = 0; i < 5; ++i) sq[i] = *(const LAS u32x2*)(aq + i * 128);
#pragma unroll
  for (int s = 0; s < 3; ++s)
#pragma unroll
    for (int t = 0; t < 2; ++t) cs[s][t] = *(const LAS u32x2*)(ac + s * 512 + t * 128);
#pragma unroll
  for (int i = 1; i < 5; ++i) { ra[i] = *(const LAS float*)(aa + (i - 1) * 128 + 4); rb[i] = *(const LAS float*)(ab + (i - 1) * 128 + 20); }
  last(PRE + 1, PRE + 5);
  __builtin_amdgcn_sched_group_barrier(0x100, 3, 0); GRP(3, 1, 0x100, 3);
  SCHED0();
  lds_post(smem, F_FREE + wid, 16);
  SCHED0();
  last(PRE + 5, 18);
  const unsigned gm = g == 0 ? ~0u : 0u;
  u32x2 lx[5];
#pragma unroll
  for (int i = 0; i < 5; ++i) lx[i] = u32x2{sq[i][0] & gm, sq[i][0] & gm & 0xffffu};
#pragma unroll
  for (int i = 1; i < 5; ++i) {
    const float c = f_of(sq[i][1]) * (0.17677669529663687f * 1.4426950408889634f);
    sa[i - 1] = opq(c * ra[i]); sb[i - 1] = opq(c * rb[i]);
  }
  __builtin_amdgcn_sched_group_barrier(0x008, 5 - PRE, 0); GRP(8, 1, 0x002, 3);
  asm volatile("" : "+v"(lx[0]), "+v"(lx[1]), "+v"(lx[2]), "+v"(lx[3]), "+v"(lx[4]));
  asm volatile("" : "+v"(sa[0]), "+v"(sa[1]), "+v"(sa[2]), "+v"(sa[3]), "+v"(sb[0]), "+v"(sb[1]), "+v"(sb[2]), "+v"(sb[3]));
  SCHED0();
  auto ln = [&](int m, int i, int t) {
    const int n = max(acc_no(HALF, m, i, t), 0);
    const s16x4 lxi = __builtin_bit_cast(s16x4, lx[i]), lw = __builtin_bit_cast(s16x4, cs[m][t]);
    acc[n] = m == 2 ? __builtin_amdgcn_mfma_f32_16x16x16bf16_1k(lxi, lw, acc[n], 0, 0, 0) : __builtin_amdgcn_mfma_f32_16x16x16bf16_1k(lw, lxi, acc[n], 0, 0, 0);
  };
#pragma unroll
  for (int z = 0; z < 18; ++z) ln(tail_of(HALF, z) / 100, tail_of(HALF, z) / 10 % 10, tail_of(HALF, z) % 10);
  STAMP(3);
  bf16x8 qf[5], kf[5];
  u32x10 vf[2];
  auto pack_qk = [&](int i) {
    if (acc_no(HALF, 0, i, 0) >= 0) qf[i] = pack8(acc[max(acc_no(HALF, 0, i, 0), 0)], acc[max(acc_no(HALF, 0, i, 1), 0)]);
    if (acc_no(HALF, 1, i, 0) >= 0) kf[i] = pack8(acc[max(acc_no(HALF, 1, i, 0), 0)], acc[max(acc_no(HALF, 1, i, 1), 0)]);
  };
  auto pack_v = [&](int i, int t) {
    const f32x4& v = acc[max(acc_no(HALF, 2, i, t), 0)];
    return u32x2{pack2(v[0], v[1]), pack2(v[2], v[3])};
  };
  u32x2 vx[2];
  pack_qk(HALF == 0 ? 4 : 0);
  if (HALF == 0) {
    vx[0] = pack_v(4, 0); vx[1] = pack_v(4, 1);
    asm volatile("ds_write_b128 %0, %2 offset:1024\n\tds_write2st64_b64 %1, %3, %4 offset1:1" :: "v"(xa), "v"(xv), "v"(kf[4]), "v"(vx[0]), "v"(vx[1]) : "memory");
  } else {
    asm volatile("ds_write_b128 %0, %1" :: "v"(xa), "v"(qf[0]) : "memory");
  }
  asm volatile("s_mov_b64 exec, 1\n\tds_write_b8 %0, %0 offset:%1\n\ts_mov_b64 exec, -1" ::"v"(xf), "i"(HALF) : "memory");
  static_assert((F_XCH & 0xf) != 0 && ((F_XCH + 4) & 0xf) != 0, "the low byte of the address is the flag");
#pragma unroll
  for (int i = 0; i < 4; ++i) pack_qk(HALF == 0 ? 3 - i : i + 1);
  const f32x4 z4 = {0.f, 0.f, 0.f, 0.f};
  f32x4 sp[4], sc[4];
#pragma unroll
  for (int j = 0; j < 3; ++j) {
    const int i = HALF == 0 ? 3 - j : j + 2;
    sc[i - 1] = MFMA16(kf[i], qf[i], z4); sp[i - 1] = MFMA16(kf[i - 1], qf[i], z4);
  }
  if (HALF) sc[0] = MFMA16(kf[1], qf[1], z4);
  GRP(1, 6, 0x002, 4); GRP(12, 1, 0x002, 2); GRP(HALF ? 7 : 6, 1, 0x002, 3);
  SCHED0();
  unsigned flag;
  bf16x8 xq;
  if (HALF == 0) asm volatile("ds_read_b32 %0, %2\n\tds_read_b128 %1, %3" : "=&v"(flag), "=&v"(xq) : "v"(xf), "v"(xa) : "memory");
  else asm volatile("ds_read_b32 %0, %4\n\tds_read_b128 %1, %5 offset:1024\n\tds_read_b64 %2, %6\n\tds_read_b64 %3, %6 offset:512"
                    : "=&v"(flag), "=&v"(xq), "=&v"(vx[0]), "=&v"(vx[1]) : "v"(xf), "v"(xa), "v"(xv) : "memory");
  SCHED0();
  const bool odd = r16 & 1, cur_a = sel[0] == 0x0c0c0c0cu, cur_b = sel[1] == 0x0c0c0c0cu;
  float a[4], b[4], m[4];
  auto logits = [&](int j) {
    const f32x4 p4 = sp[j], c4 = sc[j];
    const float p0 = p4[0], p1 = p4[1], p2 = p4[2], p3 = p4[3], c0 = c4[0], c1 = c4[1], c2 = c4[2], c3 = c4[3];
    const float ca = odd ? c1 : c0, cb = odd ? c3 : c2, pa_ = odd ? p1 : p0, pb = odd ? p3 : p2;
    a[j] = opq((cur_a ? ca : pa_) * sa[j]);
    b[j] = opq((cur_b ? cb : pb) * sb[j]);
    if (HALF == 0 && j == 0) {
      a[j] = (!cur_a && tm == 0) ? -INFINITY : a[j];
      b[j] = (!cur_b && tm == 0) ? -INFINITY : b[j];
    }
    m[j] = __builtin_elementwise_maximum(a[j], b[j]);
  };
  auto late = [&](int z0, int z1) {
#pragma unroll
    for (int y = z0; y < z1; ++y) {
      const int c = seq_of(HALF, 18 + ((y + 8 - DV) & 7));
      if (y < 8 + DV) mm(y < DV ? f0 : f1, 2, c / 10 % 10, c % 10);
    }
  };
#pragma unroll
  for (int j = 0; j < 2; ++j) logits(HALF == 0 ? 2 - j : j + 1);
  late(0, 6);
  GRP(6, 1, 0x002, 3);
  asm volatile("" : "+v"(m[1]), "+v"(m[2]));
  SCHED0();
  {
    unsigned t;
#define XWAIT(RD) "s_waitcnt lgkmcnt(0)\n\tv_readfirstlane_b32 %0, %1\n\ts_and_b32 %0, %0, %[mask]\n\ts_cbranch_scc1 2f\n\t1:\n\t" RD \
                  "\n\ts_waitcnt lgkmcnt(0)\n\tv_readfirstlane_b32 %0, %1\n\ts_and_b32 %0, %0, %[mask]\n\ts_cbranch_scc0 1b\n\t2:"
    if (HALF == 0) asm volatile(XWAIT("ds_read_b32 %1, %3\n\tds_read_b128 %2, %4") : "=&s"(t), "+v"(flag), "+v"(xq) : "v"(xf), "v"(xa), [mask] "s"(0xff00u) : "scc", "memory");
    else asm volatile(XWAIT("ds_read_b32 %1, %5\n\tds_read_b128 %2, %6 offset:1024\n\tds_read_b64 %3, %7\n\tds_read_b64 %4, %7 offset:512")
                      : "=&s"(t), "+v"(flag), "+v"(xq), "+v"(vx[0]), "+v"(vx[1]) : "v"(xf), "v"(xa), "v"(xv), [mask] "s"(0xffu) : "scc", "memory");
  }
  SCHED0();
  if (HALF == 0) { sc[3] = MFMA16(kf[4], xq, z4); sp[3] = MFMA16(kf[3], xq, z4); }
  else sp[0] = MFMA16(xq, qf[1], z4);
  logits(HALF == 0 ? 0 : 3);
  late(6, 8);
  GRP(1, HALF ? 1 : 2, 0x002, 3); GRP(2, 1, 0x002, 3);
  asm volatile("" : "+v"(m[HALF ? 3 : 0]));
  SCHED0();
  logits(HALF == 0 ? 3 : 0);
  rows_spread(rows_reduce<true>(m), m);
#pragma unroll
  for (int j = 0; j < 4; ++j) {
    a[j] = __builtin_amdgcn_exp2f(opq(a[j] - m[j]));
    b[j] = __builtin_amdgcn_exp2f(opq(b[j] - m[j]));
    m[j] = opq(a[j] + b[j]);
    a[j] = opq(a[j] * ra[j + 1]); b[j] = opq(b[j] * rb[j + 1]);
  }
  late(8, 16);
#pragma unroll
  for (int z = 0; z < 8; ++z) ln(2, seq_of(HALF, 18 + z) / 10 % 10, seq_of(HALF, 18 + z) % 10);
  GRP(DV, 1, 0x402, 3); GRP(8, 1, 0x402, 2);
  asm volatile("" : "+v"(m[0]), "+v"(m[1]), "+v"(m[2]), "+v"(m[3]));
  SCHED0();
#pragma unroll
  for (int t = 0; t < 2; ++t) {
#pragma unroll
    for (int i = 0; i < 5; ++i) {
      const u32x2 v = i == (HALF ? 0 : 4) ? vx[t] : pack_v(i, t);
      vf[t][2 * i] = v[0]; vf[t][2 * i + 1] = v[1];
    }
    asm("" : "+v"(vf[t]));
  }
  rows_spread(__builtin_amdgcn_rcpf(rows_reduce<false>(m)), m);
  const rsrc_t ors = mk_rsrc(ws + WS_O, S_TOK * DM * 2);
#pragma unroll
  for (int j = 0; j < 4; ++j) {
    const unsigned pk = pack2(a[j] * m[j], b[j] * m[j]);
    const u32x4 pw = {__builtin_amdgcn_perm(0u, pk, sel[0]), __builtin_amdgcn_perm(0u, pk, sel[1]), __builtin_amdgcn_perm(0u, pk, sel[2]), __builtin_amdgcn_perm(0u, pk, sel[3])};
    const u32x4 v0 = {vf[0][2 * j], vf[0][2 * j + 1], vf[0][2 * j + 2], vf[0][2 * j + 3]}, v1 = {vf[1][2 * j], vf[1][2 * j + 1], vf[1][2 * j + 2], vf[1][2 * j + 3]};
    sp[j] = MFMA16(__builtin_bit_cast(bf16x8, v0), __builtin_bit_cast(bf16x8, pw), z4);
    sc[j] = MFMA16(__builtin_bit_cast(bf16x8, v1), __builtin_bit_cast(bf16x8, pw), z4);
  }
#pragma unroll
  for (int j = 0; j < 4; ++j) { qf[j] = pack8(sp[j], sc[j]); st16c(ors, ooff + j * 32768, __builtin_bit_cast(u32x4, qf[j])); }
  RST(4);
  SCHED0();
  asm volatile("ds_read_b32 %0, %1 offset:%2" : "=v"(e.go) : "v"(xf - h * 4), "i"(F_GO - F_XCH) : "memory");
  wait_bytes<0x80808080u>(smem, F_FREE, 16);
  asm volatile("" : "+v"(e.go));
  asm volatile("ds_write_b128 %0, %1\n\tds_write_b128 %0, %2 offset:1024\n\tds_write_b128 %0, %3 offset:4096\n\tds_write_b128 %0, %4 offset:5120"
               :: "v"(lds_addr(smem + L_OWN) + HALF * 8192 + h * 2048 + lane * 16), "v"(qf[0]), "v"(qf[1]), "v"(qf[2]), "v"(qf[3]) : "memory");
  lds_post(smem, F_OL + wid, 1);
}

__device__ __forceinline__ unsigned ln2s(unsigned w, float mb, float r) {
  const unsigned d = pack2(opq(lo2f(w) - mb), opq(hi2f(w) - mb));
  return pack2(opq(lo2f(d) * r), opq(hi2f(d) * r));
}

__device__ __forceinline__ void await_attention(char* smem, int lane, int tm, int tn, char* ws, unsigned tag, unsigned seen, unsigned& tp) {
  const rsrc_t wrs = mk_rsrc(ws, WS_O);
  const unsigned off = WS_OF + (tm * 64 + ((lane >> 3) & 1) * 32 + (lane & 7)) * 4;
  const int own = tn >> 3;
  bool first = seen == 0;
  unsigned since = 0;
#pragma unroll 1
  for (unsigned n = 0; n < SPIN_CAP; ++n) {
    unsigned want = ~seen & 0xffffu;
    if (since) ++since; else since = uni(lds_word(smem, F_ODONE)) >> 2;
    if (since <= (first ? OK : 0)) want &= own ? 0x00ffu : 0xff00u;
    const unsigned f = __builtin_amdgcn_raw_buffer_load_b32(wrs, ((want >> (lane & 15)) & 1) ? off : 0x7fffffffu, 0, 16);
    seen |= (unsigned)__ballot(f == tag) & want;
    if (seen == 0xffffu) break;
    if (first && ((seen & 0xffu) == 0xffu || (seen >> 8) == 0xffu)) { lds_post(smem, F_GO, (seen & 0xffu) == 0xffu ? 1 : 2); first = false; NAP(ON); }
    NAP(PN);
  }
  lds_post(smem, F_GO + 1, 1);
  if (first) lds_post(smem, F_GO, 1);
}

__device__ __forceinline__ void out_proj(char* smem, int wid, int lane_in, int tm, int tn, char* ws, unsigned tag,
                                         u16* __restrict__ out, EpiRegs& e, unsigned rot2) {
  LANE_VARS
  const rsrc_t ors = mk_rsrc(ws + WS_O, S_TOK * DM * 2), yrs = mk_rsrc(out, S_TOK * DM * 2);
  const int tok = tm * 128 + wid * 32 + r16, co = (g & 1) * 16 + (g >> 1) * 8;
  unsigned eoff = (unsigned)(tok * DM + tn * 64 + co) * 2u;
  const unsigned vo = (unsigned)(tm * 8 + wid * 2) * 32768u + lane * 16;
  bf16x8 wb[3][4], oa[OD][2];
  int bc[4];
#pragma unroll
  for (int c = 0; c < 4; ++c) bc[c] = L_WA + brow + ((c ^ hk) << 5);
  f32x4 acc[2][4];
  SCHED0();
  unsigned go, oth = (tn & 8) * 2 ^ 16;
  asm volatile("" : "+s"(oth));
  auto o_request = [&](int s) {
    asm volatile("buffer_load_dwordx4 %0, %2, %4, %5 offen sc1\n\tbuffer_load_dwordx4 %1, %3, %4, %5 offen sc1" : "+v"(oa[s % OD][0]), "+v"(oa[s % OD][1])
                 : "v"(vo), "v"(vo + 32768), "s"(ors), "s"(((s >> 1 == 7 ? oth : (s & 16) ^ ((go & 2) * 8)) + ((rot2 >> (s & 14) * 2) & 14) + (s & 1)) * 1024) : "memory");
  };
  auto start = [&](unsigned word) {
    go = uni(word) & 3;
    if (go) {
#pragma unroll
      for (int s = 0; s < OD - 1; ++s) o_request(s);
    }
    SCHED0();
  };
#pragma unroll
  for (int s = 0; s < OD; ++s) asm volatile("" : "=v"(oa[s][0]), "=v"(oa[s][1]));
  {
    const unsigned w0 = uni(e.go);
    if (w0 & 3) wait_bytes<0x80808080u>(smem, F_OL, 1);
    start(w0);
  }
  const unsigned early = go;
  RST(11);
  auto publish = [&](auto nn) {
    WAITV(decltype(nn)::v);
    unsigned old = 0;
    if (lane == 0) old = __hip_atomic_fetch_add((LAS unsigned*)(LAS char*)(smem + F_ODONE), 1u, __ATOMIC_RELAXED, __HIP_MEMORY_SCOPE_WORKGROUP);
    if (uni(old) == 3u) {
      if (lane == 0) __hip_atomic_store((GAS unsigned*)(ws + WS_OF) + tm * 64 + (tn >> 3) * 32 + (tn & 7), tag, RLX_AGENT);
    }
    RST(5);
    SCHED0();
  };
  if (!early) publish(IC<0>{});
  else if (EPOS == 0) publish(IC<2 * OD - 2>{});
  const unsigned w1 = lds_word(smem, F_GO);
  const u32x2 fl = *(const volatile LAS u32x2*)(LAS const char*)(smem + F_FULL);
  const unsigned srow = 2047u - (*(const unsigned*)(smem + F_SEL + (lane & 7) * 4) & 0xffffu);
  f32x4 res[2][4];
  const uint2 st[2] = {*(const uint2*)(smem + L_QST + (16 + wid * 32 + r16) * 8), *(const uint2*)(smem + L_QST + (32 + wid * 32 + r16) * 8)};
  auto norm = [&](int u) {
    const float rd = f_of(st[u].y), k = 1.0f + rd, b = lo2f(st[u].x) * rd;
#pragma unroll
    for (int pr = 0; pr < 2; ++pr) {
      auto sa = __builtin_amdgcn_permlane16_swap(e.xv[u][pr][0], e.xv[u][pr][2], false, false);
      auto sb = __builtin_amdgcn_permlane16_swap(e.xv[u][pr][1], e.xv[u][pr][3], false, false);
#pragma unroll
      for (int z = 0; z < 2; ++z) res[u][2 * pr + z] = f32x4{lo2f(sa[z]), hi2f(sa[z]), lo2f(sb[z]), hi2f(sb[z])} * k + b;
    }
    asm volatile("" : "+v"(res[u][0]), "+v"(res[u][1]), "+v"(res[u][2]), "+v"(res[u][3]));
  };
  f32x4 ac2[2][4], (&ac3)[2][4] = res;
  auto w_frag = [&](int slot, const int (&b)[4], int z) {
#pragma unroll
    for (int c = 0; c < 4; ++c) wb[slot][c] = lds_tr2(smem + b[c] + z * 4096);
  };
  auto step = [&](f32x4 (&d)[2][4], int slot, const bf16x8 (&o)[2], bool fresh) {
#pragma unroll
    for (int c = 0; c < 4; ++c)
#pragma unroll
      for (int uu = 0; uu < 2; ++uu) {
        const int u = uu ^ (c & 1);
        d[u][c] = MFMA16(wb[slot][c], o[u], fresh ? (f32x4{0, 0, 0, 0}) : d[u][c]);
      }
  };
  int b3[4];
#pragma unroll
  for (int c = 0; c < 4; ++c) b3[c] = bc[c] + (L_W3 - L_WA);
  auto own_heads = [&](unsigned fu) {
    wait_full(smem, F_FULL, 17, fu);
    w_frag(0, b3, 2); w_frag(1, b3, 3);
    const unsigned w2 = lds_word(smem, F_GO);
    SCHED0();
    if (wid < 2) wait_bytes<0x00800080u>(smem, F_OL, 1); else wait_bytes<0x80008000u>(smem, F_OL, 1);
    if (!go) start(w2);
    bf16x8 own[2][2];
#pragma unroll
    for (int z = 0; z < 4; ++z) own[z >> 1][z & 1] = lds_b128(smem + L_OWN + wid * 4096 + lane * 16 + z * 1024);
    step(ac3, 0, own[0], false); step(ac3, 1, own[1], false);
    asm volatile("" : "+v"(ac3[0][0]), "+v"(ac3[0][1]), "+v"(ac3[0][2]), "+v"(ac3[0][3]), "+v"(ac3[1][0]), "+v"(ac3[1][1]), "+v"(ac3[1][2]), "+v"(ac3[1][3]));
    SCHED0();
  };
  unsigned mine = (unsigned)__ballot((srow >> 5) == (unsigned)(tm * 4 + wid)) & 0xffu;
  asm volatile("" : "+s"(mine));
  asm volatile("" : "+v"(eoff));
  if (!go) start(w1);
  norm(0);
  norm(1);
  SCHED0();
  RST(12);
  if (EPOS == 1 && early) publish(IC<2 * OD - 2>{});
  own_heads(uni(min(fl[0], fl[1])));
  RST(10);
  if (EPOS == 2 && early) publish(IC<2 * OD - 2>{});
  if (!go) start(wait_bytes<0x80u>(smem, F_GO, 1));
  go = uni(go);
  RST(6);
  int b2[4];
  const int mine1st = ((go >> 1) ^ (tn >> 3) ^ 1) & 1;
#pragma unroll
  for (int c = 0; c < 4; ++c) { b2[c] = bc[c] + (go & 1) * (L_WB - L_WA); bc[c] += (go >> 1) * (L_WB - L_WA); }
  unsigned so[15];
#pragma unroll
  for (int z = (OD - 1) / 2; z < 15; ++z) { so[z] = ((z == 7 ? oth : (2 * z & 16) ^ ((go & 2) * 8)) + ((rot2 >> (z & 7) * 4) & 14)) * 1024; asm volatile("" : "+s"(so[z])); }
  s16x4 wh[3][8];
  auto w_half = [&](int s, int z0, int z1) {
#pragma unroll
    for (int z = z0; z < z1; ++z)
      if (s < 30) wh[s % 3][z] = __builtin_amdgcn_ds_read_tr16_b64_v4i16((LAS s16x4*)(smem + (s < 14 ? bc[z >> 1] + s * 4096 : s < 16 ? b3[z >> 1] + (s - 14) * 4096 : b2[z >> 1] + (s - 16) * 4096) + (z & 1) * 512));
  };
  wait_full(smem, F_WO, 1);
  wait_bytes<0x80u, true>(smem, F_WO + 8, 1, go & 1);
  RST(7);
  RSC(0);
  w_half(0, 0, 8); w_half(1, 0, 8);
  SCHED0();
  if (EPOS == 3 && early) publish(IC<2 * OD - 2>{});
  asm volatile("s_waitcnt vmcnt(%2)" : "+v"(oa[0][0]), "+v"(oa[0][1]) : "i"(2 * OD - 4) : "memory");
  auto one = [&](int s) __attribute__((always_inline)) {
    f32x4 (&d)[2][4] = s < 14 ? ac2 : s < 16 ? ac3 : acc;
    auto part = [&](int z0, int z1) {
      SCHED0();
      w_half(s + 2, z0, z1);
#pragma unroll
      for (int z = z0; z < z1; ++z) {
        const int c = z >> 1, u = (z ^ c) & 1;
        d[u][c] = MFMA16(__builtin_shufflevector(wh[s % 3][2 * c], wh[s % 3][2 * c + 1], 0, 1, 2, 3, 4, 5, 6, 7), oa[s % OD][u], s == 0 || s == 16 ? (f32x4{0, 0, 0, 0}) : d[u][c]);
        __builtin_amdgcn_sched_group_barrier(0x008, 1, 0);
        __builtin_amdgcn_sched_group_barrier(0x100, 1, 0);
      }
      SCHED0();
    };
    const int n = s + OD - 1;
    auto ask = [&](int u) {
      if (n < 30) asm volatile("buffer_load_dwordx4 %0, %1, %2, %3 offen offset:%4 sc1" : "+v"(oa[n % OD][u]) : "v"(vo + u * 32768), "s"(ors), "s"(so[n >> 1]), "i"((n & 1) * 1024) : "memory");
    };
    part(0, 1);
    if (n == 14 || n == 16) wait_bytes<0x8000u, true>(smem, F_GO, 1, n == 14 ? mine1st ^ 1 : mine1st);
    ask(0);
    part(1, 3);
    ask(1);
    if (s == 13) wait_bytes<0x80u>(smem, F_WO + 8, 1);
    part(3, 5);
    if (s < 29) asm volatile("s_waitcnt vmcnt(%2)" : "+v"(oa[(s + 1) % OD][0]), "+v"(oa[(s + 1) % OD][1]) : "i"(2 * ((n < 29 ? n : 29) - s - 1)) : "memory");
    part(5, 8);
    asm volatile("" : "+v"(d[0][0]), "+v"(d[0][1]), "+v"(d[0][2]), "+v"(d[0][3]), "+v"(d[1][0]), "+v"(d[1][1]), "+v"(d[1][2]), "+v"(d[1][3]));
    if (s == 0) { RST(14); RSC(1); }
    if (s == 14) { RST(15); RSC(2); }
  };
#pragma unroll
  for (int s = 0; s < 10; ++s) one(s);
#pragma unroll
  for (int s = 10; s < 20; ++s) one(s);
#pragma unroll
  for (int s = 20; s < 28; ++s) one(s);
  asm volatile("s_waitcnt vmcnt(0)" : "+v"(oa[29 % OD][0]), "+v"(oa[29 % OD][1]) :: "memory");
  if (mine != 0) {
    wait_bytes<0x80808080u>(smem, F_UP, 1);
#pragma unroll 1
    for (; mine != 0; mine &= mine - 1) {
      const int c = __builtin_ctz(mine), row = __builtin_amdgcn_readlane((int)srow, c) & 31;
#pragma unroll
      for (int u = 0; u < 2; ++u)
        if (row == u * 16 + r16) {
#pragma unroll
          for (int z = 0; z < 4; ++z) res[u][z] += *(const f32x4*)(smem + L_UP + c * 256 + (z * 16 + g * 4) * 4);
        }
    }
  }
  SCHED0();
#pragma unroll
  for (int u = 0; u < 2; ++u)
#pragma unroll
    for (int z = 0; z < 8; ++z)
      acc[u][z & 3] = MFMA16(__builtin_shufflevector(wh[(28 + (z >> 2)) % 3][2 * (z & 3)], wh[(28 + (z >> 2)) % 3][2 * (z & 3) + 1], 0, 1, 2, 3, 4, 5, 6, 7), oa[(28 + (z >> 2)) % OD][u], acc[u][z & 3]);
#pragma unroll
  for (int u = 0; u < 2; ++u)
#pragma unroll
    for (int pr = 0; pr < 2; ++pr) {
      const f32x4 c0 = (acc[u][2 * pr] + ac2[u][2 * pr]) + ac3[u][2 * pr], c1 = (acc[u][2 * pr + 1] + ac2[u][2 * pr + 1]) + ac3[u][2 * pr + 1];
      auto sa = __builtin_amdgcn_permlane16_swap(pack2(c0[0], c0[1]), pack2(c1[0], c1[1]), false, false);
      auto sb = __builtin_amdgcn_permlane16_swap(pack2(c0[2], c0[3]), pack2(c1[2], c1[3]), false, false);
      __builtin_amdgcn_raw_buffer_store_b128(u32x4{sa[0], sb[0], sa[1], sb[1]}, yrs, eoff + pr * 64 + u * 16 * DM * 2, 0, 16);
    }
  __builtin_amdgcn_sched_group_barrier(0x008, 9, 0);
  GRP(7, 1, 0x002, EV);
  SCHED0();
  RST(8);
  STAMP(9);
}

__device__ __forceinline__ unsigned med3(unsigned a, unsigned b, unsigned c) { return max(min(a, b), min(max(a, b), c)); }
#define PIN8(a) asm volatile("" : "+v"(a[0]), "+v"(a[1]), "+v"(a[2]), "+v"(a[3]), "+v"(a[4]), "+v"(a[5]), "+v"(a[6]), "+v"(a[7]))
__device__ __forceinline__ void tick(int& nb) {
  SCHED0();
  if (nb < NBAR) { BAR(); ++nb; if (HN) NAP(HN); }
  asm volatile("" ::: "memory");
  SCHED0();
}
__device__ __forceinline__ void hidden_half(char* smem, int lane, int l, int cg, char* ws, unsigned tag, u32x4 (&fd)[2][8]) {
  const rsrc_t wrs = mk_rsrc(ws, WS_O);
  const unsigned off = WS_GU + (unsigned)(cg * 32768 + l * 2048 + lane * 16);
  u32x4 ps[2][8];
  auto request = [&]() {
#pragma unroll
    for (int p = 0; p < 16; ++p) ps[p & 1][p >> 1] = ld16c(wrs, off + (p & 1) * 1024, (p >> 1) * 4096);
    SCHED0();
  };
  auto good = [&]() {
    unsigned bad = 0;
#pragma unroll
    for (int p = 0; p < 16; ++p) bad = chk(bad, ps[p & 1][p >> 1], tag);
    return __all(bad == 0);
  };
  if (PR) {
#pragma unroll 1
    for (unsigned n = 0; n < SPIN_CAP; ++n) {
      const unsigned t = __builtin_amdgcn_raw_buffer_load_b32(wrs, WS_GU + (unsigned)(cg * 32768 + (lane >> 3) * 4096 + l * 2048 + (lane & 7) * 256 + 132), 0, 16);
      if (__all(t == tag)) break;
      NAP(PR);
    }
  }
  request();
  if (!DN) { tr_all(fd[0]); tr_all(fd[1]); }
  SCHED0();
#pragma unroll 1
  for (unsigned n = 0; n < SPIN_CAP && !good(); ++n) { NAP(4); request(); }
  unsigned hp[2];
#pragma unroll
  for (int j = 0; j < 2; ++j) {
    float hv[2];
#pragma unroll
    for (int z = 0; z < 2; ++z) {
      const float gb = rbf((f_of(ps[j][0][2 * z]) + f_of(ps[j][2][2 * z])) + (f_of(ps[j][4][2 * z]) + f_of(ps[j][6][2 * z])));
      const float ub = rbf((f_of(ps[j][1][2 * z]) + f_of(ps[j][3][2 * z])) + (f_of(ps[j][5][2 * z]) + f_of(ps[j][7][2 * z])));
      hv[z] = rbf(gb / (1.0f + __expf(-gb))) * ub;
    }
    hp[j] = pack2(hv[0], hv[1]);
  }
#pragma unroll
  for (int j = 0; j < 2; ++j)
    *(unsigned*)(smem + L_HF + (lane & 7) * 128 + (((lane >> 4) & 1) * 4 + 2 * l + j) * 16 + ((lane >> 3) & 1) * 4 + (lane >> 5) * 8) = hp[j];
  lds_post(smem, F_H + l, 1);
}

struct Stager {
  rsrc_t xrs, qrs;
  LAS char* lds;
  unsigned va, vb;
  int arow, pa, pb;
  __device__ __forceinline__ Stager(char* smem, int l, int lane, int tm, int tn, const u16* x, const u16* w) {
    asm volatile("" : "+v"(lane));
    const int kr = lane >> 3;
    xrs = mk_rsrc(x, S_TOK * DM * 2); qrs = mk_rsrc(w, DM * NQKV * 2);
    lds = (LAS char*)smem + l * 1024;
    va = (unsigned)(kr + 8 * l) * 2048u + (((lane & 7) ^ (kr >> 1) ^ (l << 2)) << 4);
    vb = (unsigned)(kr + 8 * l) * 6144u + tn * 128 + (((lane & 7) ^ (((kr >> 1) & 1) << 1) ^ (l << 2)) << 4);
    arow = tm * 128 - 16;
    k_order(tm, tn, pa, pb);
  }
};
#define DM1(l, o) "s_add_u32 m0, %2, " #l "\n\ts_add_u32 %0, %5, " #o "\n\tbuffer_load_dwordx4 %3, %4, %0 offen lds\n\t"
#define DMA_(str, v_, rs_, so_, s0_) asm volatile("s_mov_b32 %1, m0\n\t" str "s_mov_b32 m0, %1" : "=&s"(tmp), "=&s"(keep) : "s"(base), "v"(v_), "s"(rs_), "s"(so_), "s"(s0_) : "scc", "memory")
__device__ __forceinline__ void stage_x(const Stager& g, int t) {
  const int kt = k_tile(t, g.pa, g.pb);
  const unsigned base = (unsigned)(size_t)g.lds + (t % 3) * XSL_;
  unsigned keep, tmp;
  DMA_("s_mov_b32 m0, %2\n\ts_nop 0\n\tbuffer_load_dwordx4 %3, %4, %6 offen lds\n\t" DM1(2048, 32768) DM1(4096, 65536) DM1(6144, 98304) DM1(8192, 131072)
       DM1(10240, 163840) DM1(12288, 196608) DM1(14336, 229376) DM1(16384, 262144), g.va, g.xrs, g.arow * 2048 + kt * 128, max(g.arow, 0) * 2048 + kt * 128);
}
__device__ __forceinline__ void stage_w(const Stager& g, int t) {
  const int kt = k_tile(t, g.pa, g.pb);
  const unsigned base = (unsigned)(size_t)g.lds + XB + (t & 3) * WSL;
  unsigned keep, tmp;
  DMA_(DM1(0, 0) DM1(2048, 98304) DM1(4096, 196608) DM1(6144, 294912) DM1(8192, 2048) DM1(10240, 100352) DM1(12288, 198656) DM1(14336, 296960)
       DM1(16384, 4096) DM1(18432, 102400) DM1(20480, 200704) DM1(22528, 299008), g.vb, g.qrs, kt * 64 * 6144, 0);
}
__device__ __forceinline__ void stage_w3(const Stager& g, int t, int z) {
  const int kt = k_tile(t, g.pa, g.pb);
  const unsigned base = (unsigned)(size_t)g.lds + XB + (t & 3) * WSL + z * 8192;
  unsigned keep, tmp;
  DMA_(DM1(0, 0) DM1(2048, 98304) DM1(4096, 196608) DM1(6144, 294912), g.vb, g.qrs, kt * 64 * 6144 + z * 2048, 0);
}
__device__ __forceinline__ int unit_of(int bid) {
  const int off = bid >> 3;
  return (off >> 2) == 3 ? off + 16 : (off >> 2) == 7 ? off - 16 : off;
}
__device__ constexpr int wo_early(int t) { return t < TW0 ? 0 : ((t - TW0) * NW < LF ? (t - TW0) * NW : LF); }
__device__ constexpr int wo_at(int n) { return (n < 28 ? L_WA + n * 2048 : n < 56 ? L_WB + (n - 28) * 2048 : L_W3 + (n - 56) * 2048); }
__device__ __forceinline__ void loader(char* smem, int l, int lane, int tm, int tn, bool unit, unsigned rot2, const u16* __restrict__ x,
                                       const u16* __restrict__ w, const u16* __restrict__ wo, char* ws) {
  const Stager stage(smem, l, lane, tm, tn, x, w);
  dma_tag(smem, F_FULL + l * 4, 0);
  const rsrc_t prs = mk_rsrc(wo, DM * DM * 2);
  const int kr = lane >> 3;
  const unsigned vo = (unsigned)((kr < 4 ? kr : 12 + kr) + 4 * l) * 2048u + tn * 128 + (((lane & 7) ^ (((kr >> 1) & 1) << 1) ^ (l << 2)) << 4);
  u32x4 pc[LF + 1];
  int oth = (tn & 8) * 2 ^ 16;
  asm volatile("" : "+s"(oth));
  auto source = [&](int n) {
    const int k = n >> 1, half = k < 14 ? 0 : k < 28 ? 16 : k >= 30 ? (tn & 8) * 2 : oth;
    return (unsigned)((half + ((rot2 >> ((k < 14 ? k : k < 28 ? k - 14 : 14) & 14) * 2) & 14) + (k & 1)) * 32 + 8 * (n & 1)) * 2048;
  };
  auto request = [&](int i) {
    asm volatile("s_nop 4\n\tbuffer_load_dwordx4 %0, %1, %2, %3 offen" : "=v"(pc[i]) : "v"(vo), "s"(prs), "s"(source(i)) : "memory");
  };
  auto fetch = [&](int n) {
    unsigned keep;
    asm volatile("s_mov_b32 %0, m0\n\ts_add_u32 m0, %4, %5\n\ts_nop 2\n\tbuffer_load_dwordx4 %1, %2, %3 offen lds\n\ts_mov_b32 m0, %0"
                 : "=&s"(keep) : "v"(vo), "s"(prs), "s"(source(n)), "s"((unsigned)(size_t)stage.lds), "i"(wo_at(n)) : "scc", "memory");
  };
  static_assert(L_WA + 24 * 2048 == XB && L_WB == XB + WSL && L_OWN + 16384 <= L_XCH && LF <= 24, "layout");
  unsigned plan = L0 | L1 << 2;
  if (unit) {
    const unsigned d0 = (DUE >> ((tn & 3) * 4)) & 15;
    plan |= ((LA | LB << 2) * 0x101u) << (2 * d0 + 2);
    if (LC == 0) plan |= (LA | LB << 2) << (2 * min(d0, 2u) + 18);
  }
#pragma unroll
  for (int t = -1; t < NBAR - 2; ++t) {
    const unsigned lag = (plan >> (2 * t + 2)) & 3;
    if (t >= 0) stage_x(stage, t + 3);
    if (t == NBAR - 3) dma_tag(smem, F_FULL + l * 4, 15);
    SCHED0();
#pragma unroll
    for (int i = wo_early(t); i < wo_early(t + 1); ++i) request(i);
#pragma unroll
    for (int z = 3; z >= 0; --z) {
      if (z < 3) stage_w3(stage, t + 4, 2 - z);
      SCHED0();
      if (lag == (unsigned)z) {
        WAITV(33 - 4 * z + (t <= 0) + (t == NBAR - 3) + wo_early(t + 1) - wo_early(t - 1));
        BAR();
      }
      SCHED0();
    }
  }
  unsigned tp = 0, seen = 0;
  stage_x(stage, 15);
  dma_tag(smem, F_FULL + l * 4, 16);
  SCHED0();
#pragma unroll
  for (int y = 0; y < 12; ++y) fetch(y < 8 ? 63 - y : 16 + y);
  dma_tag(smem, F_FULL + l * 4, 17);
#pragma unroll
  for (int i = wo_early(NBAR - 2); i < LF; ++i) request(i);
  const unsigned tag = uni(lds_word(smem, F_TAG));
  wait_bytes<0x80808080u>(smem, F_FREE, 16);
#pragma unroll
  for (int n = LF; n < 24; ++n) fetch(n);
  if (LF) {
    const unsigned d0 = lds_addr(smem + L_WA + l * 1024 + lane * 16);
    WAITV(24 - LF);
#pragma unroll
    for (int i = 0; i < LF; ++i) asm volatile("ds_write_b128 %1, %0 offset:%2" : "+v"(pc[i]) : "v"(d0), "i"(i * 2048) : "memory");
    WAITL0();
  }
  dma_tag(smem, F_WO + l * 4, 1);
#pragma unroll
  for (int n = 28; n < 28 + NB1; ++n) fetch(n);
  if (l == 0) {
    const unsigned v1 = (vo + 4 * 2048) ^ 64;
    wait_bytes<0x80808080u>(smem, F_OL, 1);
#pragma unroll
    for (int n = 28 + NB1; n < 56; ++n) {
      unsigned keep;
      asm volatile("s_mov_b32 %0, m0\n\ts_add_u32 m0, %4, %5\n\ts_nop 0\n\tbuffer_load_dwordx4 %1, %2, %3 offen lds\n\ts_add_u32 m0, m0, 1024\n\ts_nop 0\n\tbuffer_load_dwordx4 %6, %2, %3 offen lds\n\ts_mov_b32 m0, %0"
                   : "=&s"(keep) : "v"(vo), "s"(prs), "s"(source(n)), "s"((unsigned)(size_t)stage.lds), "i"(wo_at(n)), "v"(v1) : "scc", "memory");
      if ((n - 27 - NB1) % GS == 0 && n < 55) WAITV(GW);
    }
    if (NB1) wait_bytes<0x80u>(smem, F_WO + 12, 1);
    dma_tag(smem, F_WO + 8, 1);
  } else if (NB1) dma_tag(smem, F_WO + 12, 1);
  __builtin_amdgcn_s_setprio(1);
  if (l) await_attention(smem, lane, tm, tn, ws, tag, seen, tp);
}

__device__ __forceinline__ void updates(char* smem, int w, int lane, int tm, int tn, char* ws) {
  const rsrc_t wrs = mk_rsrc(ws, WS_O);
  const unsigned tag = uni(lds_word(smem, F_TAG));
  const int g = lane >> 4, li = lane & 15;
  wait_bytes<0x80u>(smem, F_SELOK, 1);
  const unsigned selkey = *(const unsigned*)(smem + F_SEL + (lane & 7) * 4);
  unsigned mine = (unsigned)__ballot(((2047u - (selkey & 0xffffu)) >> 7) == (unsigned)tm) & 0xffu;
#pragma unroll
  for (int i = 0; i < 3; ++i) mine &= i < w ? mine - 1 : ~0u;
  if (mine != 0 && w < 3) {
    wait_bytes<0x80u>(smem, F_GO, 1);
    NAP(YD);
  }
  for (; mine != 0; mine &= mine - 1, mine &= mine - 1, mine &= mine - 1, mine &= mine - 1) {
    const int c = __builtin_ctz(mine);
    const unsigned yoff = WS_Y + (unsigned)((g * 14 * 8 + c) * DM + tn * 64 + li * 4) * 4u;
    u32x4 yv[14];
    const unsigned key = (unsigned)__builtin_amdgcn_readlane((int)selkey, c), kb = key >> 16;
#pragma unroll 1
    for (unsigned n = 0; n < SPIN_CAP; ++n) {
      unsigned bad = 0;
#pragma unroll
      for (int j = 0; j < 14; ++j) yv[j] = ld16c(wrs, yoff, j * (8 * DM * 4));
#pragma unroll
      for (int j = 0; j < 14; ++j) bad = chk(bad, yv[j], tag);
      if (__all(bad == 0)) break;
      NAP(8);
    }
    float y[4] = {0.f, 0.f, 0.f, 0.f};
#pragma unroll
    for (int j = 0; j < 14; ++j) { y[0] += lo2f(yv[j][0]); y[1] += hi2f(yv[j][0]); y[2] += lo2f(yv[j][2]); y[3] += hi2f(yv[j][2]); }
    const float r = __uint_as_float(((kb & 0x8000u) ? (kb & 0x7fffu) : (~kb & 0xffffu)) << 16);
    const float rs = rbf(1.0f / (1.0f + __expf(-r)));
#pragma unroll
    for (int e = 0; e < 4; ++e) y[e] = rbf(rbf(xsum16_32(y[e])) * rs);
    wait_bytes<0x80808080u>(smem, F_FREE, 16);
    *(f32x4*)(smem + L_UP + c * 256 + li * 16) = f32x4{y[0], y[1], y[2], y[3]};
  }
  lds_post(smem, F_UP + w, 1);
  STAMP(7 + (w == 0 || w == 3 ? -5 : 0));
}

__device__ __forceinline__ void ld8k(u32x4 (&d)[8], rsrc_t rs, unsigned off) {
#define L1K(i, so, o) "buffer_load_dwordx4 %" #i ", %8, %9, " so " offen offset:" #o " sc1\n\t"
  asm volatile("s_nop 4\n\t" L1K(0, "0", 0) L1K(1, "0", 1024) L1K(2, "0", 2048) L1K(3, "0", 3072) L1K(4, "%10", 0) L1K(5, "%10", 1024) L1K(6, "%10", 2048) L1K(7, "%10", 3072)
               : "=&v"(d[0]), "=&v"(d[1]), "=&v"(d[2]), "=&v"(d[3]), "=&v"(d[4]), "=&v"(d[5]), "=&v"(d[6]), "=&v"(d[7]) : "v"(off), "s"(rs), "s"(4096) : "memory");
  SCHED0();
}
__device__ __forceinline__ void side_wave(char* smem, int F, int lane, int bid, int tm, int tn, const u16* __restrict__ x,
                                          const u16* __restrict__ wgu, const u16* __restrict__ wd, char* ws) {
  const rsrc_t wrs = mk_rsrc(ws, WS_O), xrs = mk_rsrc(x, S_TOK * DM * 2);
  const int u = unit_of(bid), cg = (bid & 7) * 7 + ((u >> 2) % 7), q = u & 3;
  const int g = lane >> 4, li = lane & 15;
  u32x4 fw[4][8];
  const rsrc_t grs = mk_rsrc(wgu, u < 28 ? DM * FF * 2 : 0);
  const int cu = (bid & 7) * 7 + (((u >> 2) + F) % 7);
  const unsigned fo = (unsigned)(q * 256 + (li >> 3) * 128 + 8 * g) * (FF * 2) + cu * 128 + (li & 7) * 16;
#pragma unroll
  for (int s = 0; s < 4; ++s) asm volatile("" : "=v"(fw[s][0]), "=v"(fw[s][1]), "=v"(fw[s][2]), "=v"(fw[s][3]), "=v"(fw[s][4]), "=v"(fw[s][5]), "=v"(fw[s][6]), "=v"(fw[s][7]));
  const int csb = WS_CS + ((tn * 2 + F) * 16 + (tm & 1) * 8) * 1024;
  u32x4 cs[8], rec[2];
  unsigned ca = csb + min(lane, 47) * 16, cb = ca + 4096, ra = WS_R + (unsigned)max(tm * 128 - 16 + F * 72 + lane, 0) * 16, rb = WS_R + (unsigned)max(tm * 128 - 8 + F * 72 + lane, 0) * 16;
  asm volatile("" : "+v"(ca), "+v"(cb), "+v"(ra), "+v"(rb));
  unsigned due = (DUE >> ((tn & 3) * 4)) & 15, iss = 0;
#define FW(n) fw[(n) >> 3][(n) & 7]
#define LD(i) "s_mov_b32 %[t], (((%[n0]+" #i ")>>3)*32+((%[n0]+" #i ")&7))*7168\n\tbuffer_load_dwordx4 %" #i ", %[fo], %[rs], %[t] offen nt\n\t"
#define LD8 LD(0) LD(1) LD(2) LD(3) LD(4) LD(5) LD(6) LD(7)
#define R8(n) "+v"(FW(n)), "+v"(FW(n + 1)), "+v"(FW(n + 2)), "+v"(FW(n + 3)), "+v"(FW(n + 4)), "+v"(FW(n + 5)), "+v"(FW(n + 6)), "+v"(FW(n + 7))
#define SITE(lds_, ...) asm volatile("s_cmp_lg_u32 %[iss], %[k]\n\ts_cbranch_scc1 1f\n\ts_cmp_le_u32 %[nb], %[due]\n\ts_cbranch_scc1 1f\n\ts_sleep %[dly]\n\t" lds_ \
    "s_add_u32 %[iss], %[iss], 1\n\ts_add_u32 %[due], %[due], 4\n\t1:" : __VA_ARGS__, [iss] "+s"(iss), [due] "+s"(due), [t] "=&s"(t) \
    : [fo] "v"(fo), [rs] "s"(grs), [nb] "s"(nb), [k] "i"(K), [n0] "i"(n0), [dly] "i"(D), [ca] "v"(ca), [cb] "v"(cb), [ra] "v"(ra), [rb] "v"(rb), [ws] "s"(wrs) : "scc", "memory")
#define C1K(i, a, o) "buffer_load_dwordx4 %" #i ", %[" #a "], %[ws], 0 offen offset:" #o " sc1\n\t"
#define G2N C1K(0, ca, 0) C1K(1, ca, 1024) C1K(2, ca, 2048) C1K(3, ca, 3072) C1K(4, cb, 0) C1K(5, cb, 1024) C1K(6, cb, 2048) C1K(7, cb, 3072) C1K(8, ra, 0) C1K(9, rb, 0)
#define G2 C1K(8, ca, 0) C1K(9, ca, 1024) C1K(10, ca, 2048) C1K(11, ca, 3072) C1K(12, cb, 0) C1K(13, cb, 1024) C1K(14, cb, 2048) C1K(15, cb, 3072) C1K(16, ra, 0) C1K(17, rb, 0)
  auto site = [&](auto kk, auto dd, unsigned nb) {
    constexpr int K = decltype(kk)::v, D = decltype(dd)::v, n0 = K == 0 ? 0 : K == 3 ? 16 : 8 + K * 8;
    unsigned t;
    SCHED0();
    if constexpr (K == 1 && LC == 3) SITE(G2N, "+v"(cs[0]), "+v"(cs[1]), "+v"(cs[2]), "+v"(cs[3]), "+v"(cs[4]), "+v"(cs[5]), "+v"(cs[6]), "+v"(cs[7]), "+v"(rec[0]), "+v"(rec[1]));
    else if constexpr (K == 3) SITE(LD8, R8(16));
    else if constexpr (K == 1) SITE(G2 LD8, R8(n0), "+v"(cs[0]), "+v"(cs[1]), "+v"(cs[2]), "+v"(cs[3]), "+v"(cs[4]), "+v"(cs[5]), "+v"(cs[6]), "+v"(cs[7]), "+v"(rec[0]), "+v"(rec[1]));
    else if constexpr (K == 2) SITE(LD8, R8(n0));
    else SITE(LD8 LD(8) LD(9) LD(10) LD(11) LD(12) LD(13) LD(14) LD(15), R8(n0), R8(n0 + 8));
    SCHED0();
  };
#define CH(k, d, n) site(IC<k>{}, IC<d>{}, n)
#define WAITD(K, d) asm volatile("s_waitcnt vmcnt(%1)\n\ts_cmp_lg_u32 %0, %3\n\ts_cbranch_scc1 1f\n\ts_waitcnt vmcnt(%2)\n\t1:" : "+s"(iss) : "i"((K) + 8), "i"(K), "i"(d) : "scc", "memory")
  unsigned tag;
  {
    const int qq = (lane >> 2) & 3, hk = (qq >> 1) | ((g & 1) << 1);
    const char* pb = smem + XB + (8 * g + qq) * 128 + 8 * (lane & 3);
    const bf16x8 ones = {0x3f80, 0x3f80, 0x3f80, 0x3f80, 0x3f80, 0x3f80, 0x3f80, 0x3f80};
    f32x4 a[6];
#pragma unroll
    for (int t = 0; t < 2; ++t) {
      SCHED0(); BAR(); SCHED0();
      __builtin_amdgcn_s_setprio(3);
#pragma unroll
      for (int c = 0; c < 6; ++c) {
        const char* p = pb + t * WSL + (c >> 1) * 8192 + (((2 * F + (c & 1)) ^ hk) << 5);
        a[c] = MFMA16(ones, lds_tr2(p), t ? a[c] : (f32x4{0, 0, 0, 0}));
        a[c] = MFMA16(ones, lds_tr2(p + 4096), a[c]);
      }
      asm volatile("" : "+v"(a[0]), "+v"(a[1]), "+v"(a[2]), "+v"(a[3]), "+v"(a[4]), "+v"(a[5]));
      if (t == 0) { tag = uni(lds_word(smem, F_TAG)); CH(0, CD, 1); }
    }
    const unsigned off = csb + (tm >> 1) * 1024 + lane * 8;
    st8c(wrs, off, __float_as_uint(g == 0 ? a[0][0] : g == 1 ? a[1][0] : g == 2 ? a[2][0] : a[3][0]), tag);
    st8c(wrs, lane < 32 ? off + 512 : 0x7fffffffu, __float_as_uint(g == 0 ? a[4][0] : a[5][0]), tag);
    __builtin_amdgcn_s_setprio(0);
    CH(0, CD, 2);
  }
#define SST(i)
  int nb = 2;
  tick(nb);
  const unsigned xs_off = WS_S + (unsigned)(((bid & 7) * 4 + q) * 6144 + lane * 16);
  u32x4 tc;
  uint2 tq[2];
  unsigned ta[3], tv, todo = 1;
  auto gather2 = [&]() {
    asm volatile(G2N : "=&v"(cs[0]), "=&v"(cs[1]), "=&v"(cs[2]), "=&v"(cs[3]), "=&v"(cs[4]), "=&v"(cs[5]), "=&v"(cs[6]), "=&v"(cs[7]), "=&v"(rec[0]), "=&v"(rec[1])
                 : [ca] "v"(ca), [cb] "v"(cb), [ra] "v"(ra), [rb] "v"(rb), [ws] "s"(wrs) : "memory");
    SCHED0();
  };
  auto prepare = [&]() {
#pragma unroll 1
    for (unsigned n = 0; n < SPIN_CAP; ++n) {
      unsigned bad = chk(chk(0u, rec[0], tag), rec[1], tag);
#pragma unroll
      for (int j = 0; j < 8; ++j) bad = chk(bad, cs[j], tag);
      if (__all(bad == 0)) break;
      NAP(8);
      gather2();
      tick(nb); tick(nb); WAITV(0); asm volatile("" : "+v"(rec[0]), "+v"(rec[1])); PIN8(cs);
    }
    float csum[2] = {f_of(cs[0][0]), f_of(cs[0][2])};
#pragma unroll
    for (int r = 1; r < 8; ++r) { csum[0] = opq(csum[0] + f_of(cs[r][0])); csum[1] = opq(csum[1] + f_of(cs[r][2])); }
    const float h0 = rbf(csum[0]), d0 = opq(csum[0] - h0), c0 = rbf(d0), h1 = rbf(csum[1]), d1 = opq(csum[1] - h1), c1 = rbf(d1);
    tc = u32x4{pack2(h0, c0), pack2(opq(d0 - c0), 0.f), pack2(h1, c1), pack2(opq(d1 - c1), 0.f)};
    asm volatile("" : "+v"(tc));
#pragma unroll
    for (int j = 0; j < 2; ++j) {
      const unsigned nm = (rec[j][0] & 0xffffu) ^ 0x8000u;
      tq[j] = make_uint2(nm | (nm << 16), __float_as_uint(__builtin_amdgcn_rcpf(f_of(rec[j][2]))));
      asm volatile("" : "+v"(tq[j].x), "+v"(tq[j].y));
    }
    ta[0] = lds_addr(smem + L_CSF + (min(lane >> 4, 2) * 64 + F * 32 + (lane < 48 ? li : 15) * 2) * 8);
    ta[1] = lds_addr(smem + L_QST + (F * 72 + lane) * 8);
    ta[2] = lds_addr(smem + (lane < 8 ? F_SEL + lane * 4 : lane == 8 ? F_SELOK : F_ST + F * 4));
    asm volatile("" : "+v"(ta[0]), "+v"(ta[1]), "+v"(ta[2]));
    SCHED0();
  };
  auto tables = [&](unsigned now) {
    asm volatile("s_and_b32 %0, %0, %8\n\ts_cbranch_scc0 1f\n\tds_write_b128 %1, %2\n\tds_write2_b64 %3, %4, %5 offset1:8\n\ts_mov_b64 exec, 0x3ff\n\tds_write_b32 %6, %7\n\ts_mov_b64 exec, -1\n\t1:"
                 : "+s"(now) : "v"(ta[0]), "v"(tc), "v"(ta[1]), "v"(tq[0]), "v"(tq[1]), "v"(ta[2]), "v"(tv), "s"(todo) : "scc", "memory");
    todo &= ~now;
  };
  auto slice = [&]() {
    if (nb < NBAR) tick(nb);
    else { const u32x2 fl = *(const volatile LAS u32x2*)(LAS const char*)(smem + F_FULL); tables(uni(min(fl[0], fl[1])) >> 4); SCHED0(); }
  };
  if (u >= 28) {
    u32x4 kd[4];
#pragma unroll 1
    for (unsigned n = 0; n < SPIN_CAP; ++n) {
#define L1K(i, o) "buffer_load_dwordx4 %" #i ", %4, %5, 0 offen offset:" #o " sc1\n\t"
      asm volatile("s_nop 4\n\t" L1K(0, 0) L1K(1, 1024) L1K(2, 2048) L1K(3, 3072)
                   : "=&v"(kd[0]), "=&v"(kd[1]), "=&v"(kd[2]), "=&v"(kd[3]) : "v"(WS_K + F * 4096 + lane * 16), "s"(wrs) : "memory");
#undef L1K
      tick(nb);
      asm volatile("s_waitcnt vmcnt(0)" : "+v"(kd[0]), "+v"(kd[1]), "+v"(kd[2]), "+v"(kd[3]) :: "memory");
      if (__all(chk(chk(chk(chk(0u, kd[0], tag), kd[1], tag), kd[2], tag), kd[3], tag) == 0)) break;
      NAP(4);
    }
    SST(8);
    unsigned m1 = 0, m2 = 0, m3 = 0, m4 = 0;
#pragma unroll
    for (int j = 0; j < 4; ++j) {
      kd[j] = u32x4{(kd[j][0] << 16) | (15 - 4 * j), (kd[j][0] & 0xffff0000u) | (14 - 4 * j), (kd[j][2] << 16) | (13 - 4 * j), (kd[j][2] & 0xffff0000u) | (12 - 4 * j)};
#pragma unroll
      for (int i = 0; i < 4; ++i) {
        const unsigned v = kd[j][i];
        m4 = med3(m3, m4, v); m3 = med3(m2, m3, v); m2 = med3(m1, m2, v); m1 = max(m1, v);
      }
    }
    const unsigned lb = ((63 - lane) << 5) | (F ? 0 : 16);
    m1 |= lb; m2 |= lb; m3 |= lb; m4 |= lb;
    asm volatile("" : "+v"(m1), "+v"(m2), "+v"(m3), "+v"(m4));
    tick(nb);
    unsigned best = 0;
#pragma unroll
    for (int r = 0; r < 8; ++r) {
      const unsigned wm = wave_max_u32(m1);
      best = (lane & 7) == r ? wm : best;
      const bool won = m1 == wm;
      m1 = won ? m2 : m1; m2 = won ? m3 : m2; m3 = won ? m4 : m3; m4 = won ? 0u : m4;
      if (r == 3) { asm volatile("" : "+v"(m1), "+v"(m2), "+v"(m3), "+v"(m4), "+v"(best)); tick(nb); }
    }
    if (__any(m1 == 0)) {
      unsigned wm = 0;
#pragma unroll 1
      for (int r = 0; r < 8; ++r) {
        if (r & 1) tick(nb);
        unsigned m = 0;
#pragma unroll
        for (int j = 0; j < 4; ++j)
#pragma unroll
          for (int i = 0; i < 4; ++i) m = max(m, (kd[j][i] | lb) - wm);
        wm += wave_max_u32(m);
        best = (lane & 7) == r ? wm : best;
      }
    }
    *(volatile LAS unsigned*)(LAS char*)(smem + F_CND + (F ? 60 - (lane & 7) * 4 : (lane & 7) * 4)) = best + 1;
    unsigned d = 0;
    if (TCH & 1) {
      const unsigned ct = 2047u - (best & 0xffffu);
      asm volatile("buffer_load_dword %0, %1, %2, 0 offen\n\tbuffer_load_dword %0, %3, %4, 0 offen sc1" : "=&v"(d) : "v"(lane < 32 ? ct * 2048u + q * 512 + (lane >> 3) * 128 : 0x7fffffffu), "s"(xrs), "v"(lane < 8 ? WS_R + ct * 16 : 0x7fffffffu), "s"(wrs) : "memory");
    }
    SST(9);
    gather2();
    unsigned ka, kb;
#pragma unroll 1
    for (unsigned n = 0; n < SPIN_CAP; ++n) {
      tick(nb);
      ka = *(const volatile LAS unsigned*)(LAS const char*)(smem + F_CND + (lane & 7) * 4);
      kb = *(const volatile LAS unsigned*)(LAS const char*)(smem + F_CND + 32 + (lane & 7) * 4);
      if (__all(min(ka, kb) != 0)) break;
      NAP(1);
    }
    const unsigned selkey = max(ka, kb) - 1, tok = 2047u - (selkey & 0xffffu);
    u32x4 xf[2], trec;
#pragma unroll 1
    for (unsigned n = 0; n < SPIN_CAP; ++n) {
      asm volatile("buffer_load_dwordx4 %0, %3, %4, 0 offen\n\tbuffer_load_dwordx4 %1, %3, %4, 0 offen offset:64\n\tbuffer_load_dwordx4 %2, %5, %6, 0 offen sc1"
                   : "=&v"(xf[0]), "=&v"(xf[1]), "=&v"(trec) : "v"(tok * 2048u + q * 512 + (li >> 3) * 256 + g * 16 + F * 128), "s"(xrs), "v"(WS_R + tok * 16), "s"(wrs) : "memory");
      if (!KQ || n) tick(nb);
      asm volatile("s_waitcnt vmcnt(0)" : "+v"(xf[0]), "+v"(xf[1]), "+v"(trec), "+v"(rec[0]), "+v"(rec[1]), "+v"(d) :: "memory"); PIN8(cs);
      if (__all(chk(0u, trec, tag) == 0)) break;
      NAP(1);
    }
    const float mb = lo2f(trec[0]), rd = 1.0f / f_of(trec[2]);
#pragma unroll
    for (int s = 0; s < 2; ++s)
#pragma unroll
      for (int j = 0; j < 4; ++j) xf[s][j] = ln2s(xf[s][j], mb, rd);
    st16c(wrs, xs_off + F * 3072, u32x4{xf[0][0], xf[0][1], xf[0][2], tag});
    st16c(wrs, xs_off + F * 3072 + 1024, u32x4{xf[1][0], xf[1][1], xf[1][2], tag});
    st16c(wrs, xs_off + F * 3072 + 2048, u32x4{xf[0][3], xf[1][3], selkey, tag});
    tv = lane < 8 ? selkey : 1u;
    SST(2);
    tick(nb);
    if (TCH & 2) {
      asm volatile("s_waitcnt vmcnt(0)\n\tbuffer_load_dword %0, %1, %2, 0 offen sc1" : "=&v"(d) : "v"(lane < 24 ? xs_off + F * 3072 + lane * 112 : 0x7fffffffu), "s"(wrs) : "memory");
    }
    prepare();
    asm volatile("s_waitcnt vmcnt(0)" : "+v"(d) :: "memory");
    while (nb < NBAR) tick(nb);
    __builtin_amdgcn_s_setprio(1);
    wait_full(smem, F_FULL, 16);
    tables(1);
  } else {
    u32x4 xs[6];
    auto ask = [&]() {
#define X1K(i, o) "buffer_load_dwordx4 %" #i ", %6, %7, 0 offen offset:" #o " sc1\n\t"
#define X2K(i, o) "buffer_load_dwordx4 %" #i ", %6, %7, %8 offen offset:" #o " sc1\n\t"
      asm volatile(X1K(0, 0) X1K(1, 1024) X1K(2, 2048) X1K(3, 3072) X2K(4, 0) X2K(5, 1024)
                   : "=&v"(xs[0]), "=&v"(xs[1]), "=&v"(xs[2]), "=&v"(xs[3]), "=&v"(xs[4]), "=&v"(xs[5]) : "v"(xs_off), "s"(wrs), "s"(4096) : "memory");
      SCHED0();
    };
    auto good = [&]() {
      unsigned bad = xs[0][3] ^ tag;
#pragma unroll
      for (int j = 1; j < 6; ++j) bad = __builtin_amdgcn_bitop3_b32(bad, xs[j][3], tag, 0xF6);
      return __all(bad == 0);
    };
#define PIN6(a) asm volatile("" : "+v"(a[0]), "+v"(a[1]), "+v"(a[2]), "+v"(a[3]), "+v"(a[4]), "+v"(a[5]))
#define WAITS(snap, n) asm volatile("s_waitcnt vmcnt(%2)\n\ts_cmp_lg_u32 %0, %1\n\ts_cbranch_scc1 1f\n\ts_waitcnt vmcnt(0)\n\t1:" : "+s"(iss) : "s"(snap), "i"(n) : "scc", "memory")
    asm volatile("" : "=v"(cs[0]), "=v"(cs[1]), "=v"(cs[2]), "=v"(cs[3]), "=v"(cs[4]), "=v"(cs[5]), "=v"(cs[6]), "=v"(cs[7]), "=v"(rec[0]), "=v"(rec[1]));
    CH(0, DL - HN, 3);
    tick(nb);
    CH(0, DL - HN, 99);
    tick(nb);
    CH(1, DL - HN, nb);
    if (MG) CH(2, 0, 99);
    tick(nb);
    WAITS(1, MG ? 26 : LC == 3 ? 10 : 18); PIN8(fw[0]); PIN8(fw[1]);
    tr_all(fw[0]);
    CH(1, (DL > 7 ? DL - 7 : 0), nb);
    if (MG) CH(2, 0, 99);
    tick(nb);
    tr_all(fw[1]);
    CH(1, (DL > 7 ? DL - 7 : 0), nb);
    if (MG) CH(2, 0, 99);
    tick(nb);
    CH(1, DL - HN, 99);
    if (MG) CH(2, 0, 99);
    tick(nb);
    if (LC == 0) CH(2, DL - HN, nb);
    tick(nb);
    WAITS(2, 8);
    asm volatile("" : "+v"(rec[0]), "+v"(rec[1])); PIN8(cs); if (LC < 3) PIN8(fw[2]);
    if (!SD) ask();
    prepare();
    if (SD) ask();
    unsigned snap = iss;
    asm volatile("" : "+s"(snap));
    if (LC == 0) CH(2, 0, nb);
    tick(nb);
    WAITS(snap, 8); PIN6(xs);
    SST(1);
    if (!good()) {
      if (LC == 0) CH(2, 0, 99);
#pragma unroll 1
      for (unsigned n = 0; n < SPIN_CAP; ++n) {
        ask(); tick(nb); WAITV(0); PIN6(xs);
        if (good()) break;
        NAP(4);
      }
    }
    SST(2);
    tv = lane < 8 ? xs[2][2] : 1u;
    xs[0][3] = xs[2][0]; xs[1][3] = xs[2][1]; xs[3][3] = xs[5][0]; xs[4][3] = xs[5][1];
    PIN6(xs); asm volatile("" : "+v"(tv));
    if (nb == NBAR) { __builtin_amdgcn_s_setprio(1); wait_full(smem, F_FULL, 16); tables(1); }
    f32x4 acc[8];
    auto mma = [&](int s) {
      SCHED0(); __builtin_amdgcn_s_setprio(3); SCHED0();
#pragma unroll
      for (int i = 0; i < 8; ++i) acc[i] = MFMA16(__builtin_bit_cast(bf16x8, fw[s][i]), __builtin_bit_cast(bf16x8, xs[s + (s >> 1)]), s ? acc[i] : (f32x4{0, 0, 0, 0}));
      SCHED0(); __builtin_amdgcn_s_setprio(0); SCHED0();
      PIN8(acc);
    };
    mma(0);
    if (LC < 3) tr_all(fw[2]);
    if (LC == 0) CH(2, (DL > 9 ? DL - 9 : 0), 99);
    slice();
    mma(1);
    if (LC < 2) mma(2);
    if (LC == 1) { PIN8(fw[3]); tr_all(fw[3]); }
    slice();
    if (LC == 0) { WAITV(0); PIN8(fw[3]); tr_all(fw[3]); }
    if (LC < 2) mma(3);
    if (LC == 2) mma(2);
    auto finish = [&]() {
#pragma unroll
      for (int r = 0; r < 4; ++r) {
        float v[2];
#pragma unroll
        for (int z = 0; z < 2; ++z) {
          auto a = __builtin_amdgcn_permlane32_swap(__float_as_uint(acc[z][r]), __float_as_uint(acc[z + 4][r]), false, false);
          auto b = __builtin_amdgcn_permlane32_swap(__float_as_uint(acc[z + 2][r]), __float_as_uint(acc[z + 6][r]), false, false);
          v[z] = f_of(li < 8 ? a[0] : b[1]) + dpp_f<0x128>(f_of(li < 8 ? b[0] : a[1]));
        }
        st16c(wrs, WS_GU + (unsigned)(((cu * 4 + q) * 2 + F) * 4096 + r * 1024 + lane * 16), u32x4{__float_as_uint(v[0]), tag, __float_as_uint(v[1]), tag});
      }
      SCHED0();
      SST(3);
    };
    if (LC < 2 && EP == 1) finish();
    while (nb < NBAR) tick(nb);
    if (LC < 2 && EP == 0) { slice(); finish(); }
    __builtin_amdgcn_s_setprio(1);
    u32x4 fd[2][8];
    auto down = [&]() {
      const rsrc_t drs = mk_rsrc(wd, DM * FF * 2);
      const unsigned dof = (unsigned)(cg * 64 + 8 * g) * (DM * 2) + q * 512 + F * 256 + li * 16;
#pragma unroll
      for (int s = 0; s < 2; ++s)
#pragma unroll
        for (int j = 0; j < 8; ++j) fd[s][j] = __builtin_amdgcn_raw_buffer_load_b128(drs, dof + (j & 1) * (DM * 2), (s * 32 + (j & 6)) * (DM * 2), 2);
      SCHED0();
    };
    if (!DN) { wait_full(smem, F_FULL, 15); down(); }
    wait_full(smem, F_FULL, 16);
    tables(1);
    SST(4);
    if (LC == 3) { WAITV(24); PIN8(fw[2]); tr_all(fw[2]); mma(2); }
    if (LC >= 2) { WAITV(16); PIN8(fw[3]); tr_all(fw[3]); mma(3); __builtin_amdgcn_s_setprio(1); finish(); }
    if (bid == 0 && F == 0 && lane == 0) __hip_atomic_store((GAS unsigned*)(ws + WS_EP), tag, RLX_AGENT);
    hidden_half(smem, lane, F, cg, ws, tag, fd);
    SST(5);
    if (DN) { wait_bytes<0x80808080u>(smem, F_OL, 1); down(); tr_all(fd[0]); tr_all(fd[1]); }
    wait_bytes<0x8080u>(smem, F_H, 1);
    const bf16x8 h0 = lds_b128(smem + L_HF + (li & 7) * 128 + g * 16), h1 = lds_b128(smem + L_HF + (li & 7) * 128 + 64 + g * 16);
#pragma unroll
    for (int i = 0; i < 8; ++i) {
      acc[i] = MFMA16(h0, __builtin_bit_cast(bf16x8, fd[0][i]), (f32x4{0, 0, 0, 0}));
      acc[i] = MFMA16(h1, __builtin_bit_cast(bf16x8, fd[1][i]), acc[i]);
    }
    const unsigned off = WS_Y + (unsigned)((cg * 8 + (g & 1) * 4) * DM + q * 256 + F * 128 + li * 8 + (g >> 1) * 4) * 4u;
#pragma unroll
    for (int r = 0; r < 4; ++r) {
      const unsigned lo = pack2(acc[0][r], acc[1][r]), hi = pack2(acc[2][r], acc[3][r]), lo2 = pack2(acc[4][r], acc[5][r]), hi2 = pack2(acc[6][r], acc[7][r]);
      st16c(wrs, off + r * (DM * 4), u32x4{g < 2 ? lo : lo2, tag, g < 2 ? hi : hi2, tag});
    }
    SST(6);
  }
  __builtin_amdgcn_s_waitcnt(0x0F70);
}

__global__ __launch_bounds__(512, 2) void fused_block(const u16* __restrict__ x, const u16* __restrict__ wqkv, const u16* __restrict__ wo,
                                                      const u16* __restrict__ wr, const u16* __restrict__ wg, const u16* __restrict__ wu,
                                                      const u16* __restrict__ wd, char* ws, u16* __restrict__ out) {
  __shared__ __attribute__((aligned(128))) char smem[L_SIZE];
  unsigned long long pc;
  unsigned first;
  asm volatile("v_cmp_gt_u32 vcc, 24, %2\n\ts_getpc_b64 %0\n\ts_cbranch_vccz 1f\n\ts_mov_b64 exec, vcc\n\tv_lshlrev_b32 %1, 7, %2\n\tglobal_load_dword %1, %1, %0\n\ts_mov_b64 exec, -1\n\t1:"
               : "=s"(pc), "=&v"(first) : "v"(threadIdx.x) : "vcc", "memory");
  SCHED0();
  unsigned long long t00; asm volatile("s_memrealtime %0\n\ts_waitcnt lgkmcnt(0)" : "=s"(t00) :: "memory");
  const int lane = threadIdx.x & 63;
  const int wid = __builtin_amdgcn_readfirstlane(threadIdx.x >> 6);
  const int bid = blockIdx.x, xcd = bid & 7, off = bid >> 3;
  const int tm = (xcd & 1) * 8 + (xcd & 2) * 2 + (off & 3), tn = (xcd >> 2) * 8 + (off >> 2);
  STAMP(0);
  if (wid == 7) {
    unsigned d;
    asm volatile("s_getpc_b64 %0" : "=s"(pc));
    asm volatile("buffer_load_dword %0, %1, %2, 0 offen\n\ts_waitcnt vmcnt(0)" : "=&v"(d)
                 : "v"((unsigned)(lane * 32 + off) * 128u), "s"(mk_rsrc((const void*)(pc & ~255ull), CODE_BYTES)) : "memory");
  }
  asm volatile("" : "+s"(x), "+s"(wqkv), "+s"(wr), "+s"(ws), "+s"(out), "+s"(wo), "+s"(wd), "+s"(wg), "+s"(wu));
  unsigned rot2 = ((((unsigned)(0x65432106543210ull >> (off & 3) * 8) & 0xfffffffu) + ((tn & 7) + 1) * 0x1111111u) & 0x7777777u) * 2 | (unsigned)tn << 29;
  unsigned tag = 0;
  RowRegs rr;
  EpiRegs e;
  row_request(rr, x, wr, ws, tm * 128 + tn * 8 + wid * 2, lane, wid < 4);
  rr.keep = first;
  SCHED0();
  if ((wid >> 1) == 2) {
    __builtin_amdgcn_s_setprio(2);
    const Stager stage(smem, wid - 4, lane, tm, tn, x, wqkv);
    NAP(4);
    stage_x(stage, 0); stage_w(stage, 0); stage_x(stage, 1); stage_w(stage, 1);
    WAITV(21);
    BAR();
    stage_x(stage, 2); stage_w(stage, 2);
  }
  asm volatile("" ::: "memory");
  if (wid < 4) {
    __builtin_amdgcn_s_setprio(2);
    qkv_setup(smem, wid, lane, tm, tn, e);
    asm volatile("" : "+s"(rot2));
    SCHED0();
    tag = row_stats(rr, ws, tm * 128 + tn * 8 + wid * 2, lane);
    if (wid == 0) *(unsigned*)(smem + L_FLG + lane * 4) = lane * 4 == F_TAG - L_FLG ? tag : 0u;
    STAMP(10);
  }
  asm volatile("" ::: "memory");
  if (wid < 4) {
    if (wid & 1) qkv_attention<1>(smem, wid, lane, tm, tn, ws, tag, x, e);
    else qkv_attention<0>(smem, wid, lane, tm, tn, ws, tag, x, e);
    out_proj(smem, wid, lane, tm, tn, ws, tag, out, e, rot2);
    {
      unsigned t0 = (unsigned)t00;
      const rsrc_t prs = mk_rsrc(ws + (32u << 20), 4096);
      if (wid == 0) __builtin_amdgcn_raw_buffer_store_b32(t0, prs, lane == 0 ? bid * 4 : 0x7fffffff, 0, 16);
      for (int n = 0; n < 100000 && (unsigned)__builtin_amdgcn_s_memrealtime() - t0 < 3200u; ++n) NAP(2);
      const u32x4 v = ld16c(prs, lane * 16, 0);
      int d = min(min((int)(v[0] - t0), (int)(v[1] - t0)), min((int)(v[2] - t0), (int)(v[3] - t0)));
      int e2 = max(max((int)(v[0] - t0), (int)(v[1] - t0)), max((int)(v[2] - t0), (int)(v[3] - t0)));
      const int mn = 100000 - (int)wave_max_u32((unsigned)(100000 - d));
      const int ea = (int)wave_max_u32((lane & 1) ? 0u : (unsigned)(100000 + e2)) - 100000, eb = (int)wave_max_u32((lane & 1) ? (unsigned)(100000 + e2) : 0u) - 100000;
      t0 += mn;
      const unsigned until = 4000u + 0u;
      for (int n = 0; n < 100000 && (unsigned)__builtin_amdgcn_s_memrealtime() - t0 < until; ++n) NAP(2);
    }
  } else {
    if (wid < 6) loader(smem, wid - 4, lane, tm, tn, unit_of(bid) < 28, rot2, x, wqkv, wo, ws);
    else side_wave(smem, wid - 6, lane, bid, tm, tn, x, wid == 6 ? wg : wu, wd, ws);
    updates(smem, (0x2130 >> ((wid - 4) * 4)) & 3, lane, tm, tn, ws);
    __builtin_amdgcn_s_waitcnt(0x0F70);
  }
}

extern "C" void kernel_launch(void* const* d_in, const int* in_sizes, int n_in, void* d_out, int out_size, void* d_ws,
                              size_t ws_size, hipStream_t stream) {
  fused_block<<<256, 512, 0, stream>>>((const u16*)d_in[0], (const u16*)d_in[1], (const u16*)d_in[2], (const u16*)d_in[4],
                                       (const u16*)d_in[5], (const u16*)d_in[6], (const u16*)d_in[7], (char*)d_ws, (u16*)d_out);
}
```

```cpp
#include <hip/hip_runtime.h>
#include <hip/hip_bf16.h>
#include <stdint.h>

typedef __attribute__((ext_vector_type(8))) short bf16x8;
typedef __attribute__((ext_vector_type(4))) short s16x4;
typedef __attribute__((ext_vector_type(4))) float f32x4;
typedef __attribute__((ext_vector_type(2))) float f32x2;
typedef __attribute__((ext_vector_type(2))) __bf16 bf16x2_t;
typedef __attribute__((ext_vector_type(4))) unsigned u32x4;
typedef unsigned short u16;
#define LAS __attribute__((address_space(3)))
#define GAS __attribute__((address_space(1)))
#define RLX_AGENT __ATOMIC_RELAXED, __HIP_MEMORY_SCOPE_AGENT

#define S_TOK 2048
#define DM 1024
#define NQKV 3072
#define FF 3584

typedef __attribute__((ext_vector_type(2))) unsigned u32x2;

#define WS_EP   0
#define WS_R    4096
#define WS_K    (WS_R + 32768)
#define WS_OF   (WS_K + 8192)
#define WS_CS   (WS_OF + 4096)
#define WS_GU   (WS_CS + 16 * 2 * 4096)
#define WS_Y    (WS_GU + 56 * 4 * 8192)
#define WS_S    (WS_Y + 56 * 8 * DM * 4)
#define WS_O    (8u << 20)

__device__ const unsigned k_tags[32] = {0,  1,  2,  3,  4,  5,  6,  7,  8,  9,  10, 11, 12, 13, 14, 15,
                                        16, 17, 18, 19, 20, 21, 22, 23, 24, 25, 26, 27, 28, 29, 30, 31};

__device__ __forceinline__ unsigned pack2(float a, float b) {
  f32x2 v = {a, b};
  bf16x2_t r = __builtin_convertvector(v, bf16x2_t);
  return __builtin_bit_cast(unsigned, r);
}
__device__ __forceinline__ float rbf(float f) { return __uint_as_float(pack2(f, f) << 16); }
__device__ __forceinline__ float lo2f(unsigned w) { return __uint_as_float(w << 16); }
__device__ __forceinline__ float hi2f(unsigned w) { return __uint_as_float(w & 0xffff0000u); }

__device__ __forceinline__ bf16x8 lds_b128(const char* p) { return *(const bf16x8*)p; }
__device__ __forceinline__ bf16x8 lds_tr2(const char* p) {
  s16x4 lo = __builtin_amdgcn_ds_read_tr16_b64_v4i16((LAS s16x4*)p);
  s16x4 hi = __builtin_amdgcn_ds_read_tr16_b64_v4i16((LAS s16x4*)(p + 512));
  return __builtin_shufflevector(lo, hi, 0, 1, 2, 3, 4, 5, 6, 7);
}
#define MFMA16(a, b, c) __builtin_amdgcn_mfma_f32_16x16x32_bf16(a, b, c, 0, 0, 0)
#define WAITV(n) asm volatile("s_waitcnt vmcnt(%0)" ::"i"(n) : "memory")
#define WAITL0() __builtin_amdgcn_s_waitcnt(0xC07F)
#define BAR() __builtin_amdgcn_s_barrier()
#define SCHED0() __builtin_amdgcn_sched_barrier(0)
#define NAP(n) asm volatile("s_sleep %0" ::"i"(n) : "memory")
__device__ __forceinline__ float xsum16_32(float v) {
  unsigned u = __float_as_uint(v);
  auto a = __builtin_amdgcn_permlane16_swap(u, u, false, false);
  v = __uint_as_float(a[0]) + __uint_as_float(a[1]);
  u = __float_as_uint(v);
  auto b = __builtin_amdgcn_permlane32_swap(u, u, false, false);
  return __uint_as_float(b[0]) + __uint_as_float(b[1]);
}
__device__ __forceinline__ bf16x8 pack8(const f32x4& a, const f32x4& b) {
  u32x4 r = {pack2(a[0], a[1]), pack2(a[2], a[3]), pack2(b[0], b[1]), pack2(b[2], b[3])};
  return __builtin_bit_cast(bf16x8, r);
}
__device__ __forceinline__ unsigned wave_max_u32(unsigned v) {
  v = max(v, (unsigned)__builtin_amdgcn_update_dpp(0, (int)v, 0x111, 0xf, 0xf, false));
  v = max(v, (unsigned)__builtin_amdgcn_update_dpp(0, (int)v, 0x112, 0xf, 0xf, false));
  v = max(v, (unsigned)__builtin_amdgcn_update_dpp(0, (int)v, 0x114, 0xf, 0xf, false));
  v = max(v, (unsigned)__builtin_amdgcn_update_dpp(0, (int)v, 0x118, 0xf, 0xf, false));
  v = max(v, (unsigned)__builtin_amdgcn_update_dpp(0, (int)v, 0x142, 0xa, 0xf, false));
  v = max(v, (unsigned)__builtin_amdgcn_update_dpp(0, (int)v, 0x143, 0xc, 0xf, false));
  return (unsigned)__builtin_amdgcn_readlane((int)v, 63);
}
__device__ __forceinline__ bf16x8 tr_frag(const u32x4 (&w)[8], int i) {
  const unsigned sel = (i & 1) ? 0x07060302u : 0x05040100u;
  u32x4 r = {__builtin_amdgcn_perm(w[1][i >> 1], w[0][i >> 1], sel), __builtin_amdgcn_perm(w[3][i >> 1], w[2][i >> 1], sel),
             __builtin_amdgcn_perm(w[5][i >> 1], w[4][i >> 1], sel), __builtin_amdgcn_perm(w[7][i >> 1], w[6][i >> 1], sel)};
  return __builtin_bit_cast(bf16x8, r);
}

#define XSL_ 18432
#define WSL 24576
#define XB (3 * XSL_)
#define NBAR 14
#define L_UP 0
#define L_WA 6144
#define L_W3 (L_WA + 57344)
#define L_WB (L_W3 + 16384)
#define L_OWN (L_WB + 57344)
#define L_XCH (XB + 4 * WSL)
#define L_HF (L_XCH + 6144)
#define L_CSF (L_HF + 1024)
#define L_QST (L_CSF + 1536)
#define L_FLG (L_QST + 1152)
#define L_SIZE (L_FLG + 256)
#define F_FULL (L_FLG + 0)
#define F_ST (L_FLG + 8)
#define F_WO (L_FLG + 16)
#define F_FREE (L_FLG + 32)
#define F_XCH (L_FLG + 40)
#define F_ODONE (L_FLG + 48)
#define F_SELOK (L_FLG + 52)
#define F_GO (L_FLG + 56)
#define F_OL (L_FLG + 60)
#define F_H (L_FLG + 64)
#define F_TAG (L_FLG + 68)
#define F_UP (L_FLG + 72)
#define F_SEL (L_FLG + 96)
#define F_CND (L_FLG + 128)
#define OD 7
#define SPIN_CAP (1u << 18)
#define NB1 6
#define CODE_BYTES 54784

__device__ __forceinline__ unsigned lds_addr(const char* p) { return (unsigned)(size_t)(LAS const char*)p; }
__device__ __forceinline__ void lds_post(char* smem, int off, unsigned v) {
  asm volatile("s_mov_b64 exec, 1\n\tds_write_b8 %0, %1\n\ts_mov_b64 exec, -1" ::"v"(lds_addr(smem + off)), "v"(v) : "memory");
}
__device__ __forceinline__ unsigned lds_word(const char* smem, int off) { return *(const volatile LAS unsigned*)(LAS const char*)(smem + off); }
__device__ __forceinline__ u32x4 lds_word4(const char* smem, int off) { return *(const volatile LAS u32x4*)(LAS const char*)(smem + off); }
template <unsigned MASK, bool COND = false>
__device__ __forceinline__ unsigned wait_bytes(const char* smem, int off, unsigned need, unsigned skip = 0) {
  unsigned t, f;
  asm volatile(
      ".if %6\n\ts_cmp_lg_u32 %5, 0\n\ts_cbranch_scc1 2f\n\t.endif\n\t"
      "1:\n\t"
      "ds_read_b32 %1, %2\n\t"
      "s_waitcnt lgkmcnt(0)\n\t"
      "v_readfirstlane_b32 %0, %1\n\t"
      "s_or_b32 %0, %0, 0x80808080\n\t"
      "s_sub_u32 %0, %0, %3\n\t"
      "s_and_b32 %0, %0, %4\n\t"
      "s_cmp_eq_u32 %0, %4\n\t"
      "s_cbranch_scc1 2f\n\t"
      "s_sleep 1\n\t"
      "s_branch 1b\n\t"
      "2:"
      : "=&s"(t), "=&v"(f)
      : "v"(lds_addr(smem + off)), "s"(need * 0x01010101u), "s"(MASK), "s"(COND ? skip : MASK), "i"((int)COND)
      : "scc", "memory");
  return f;
}
__device__ __forceinline__ void wait_full(const char* smem, int off, unsigned need, unsigned cur = 0) {
  unsigned a, b;
  asm volatile(
      "s_cmp_ge_u32 %0, %4\n\t"
      "s_cbranch_scc1 3f\n\t"
      "s_branch 2f\n\t"
      "1:\n\t"
      "s_sleep 1\n\t"
      "2:\n\t"
      "ds_read_b32 %1, %3\n\t"
      "ds_read_b32 %2, %3 offset:4\n\t"
      "s_waitcnt lgkmcnt(0)\n\t"
      "v_min_u32 %1, %1, %2\n\t"
      "s_nop 0\n\t"
      "v_readfirstlane_b32 %0, %1\n\t"
      "s_cmp_lt_u32 %0, %4\n\t"
      "s_cbranch_scc1 1b\n\t"
      "3:"
      : "+s"(cur), "=&v"(a), "=&v"(b)
      : "v"(lds_addr(smem + off)), "s"(need)
      : "scc", "memory");
}
__device__ __forceinline__ unsigned uni(unsigned v) { return (unsigned)__builtin_amdgcn_readfirstlane((int)v); }
__device__ __forceinline__ void dma_tag(char* smem, int off, int n) {
  unsigned keep;
  asm volatile("s_nop 2\n\ts_mov_b32 %0, m0\n\ts_mov_b32 m0, %1\n\ts_mov_b64 exec, 1\n\tglobal_load_lds_dword %2, %3\n\ts_mov_b64 exec, -1\n\ts_mov_b32 m0, %0\n\ts_nop 0"
               : "=&s"(keep) : "s"(uni(lds_addr(smem + off))), "v"(n * 4), "s"(k_tags) : "memory");
}
template <int V> struct IC { static constexpr int v = V; };
struct QFrag { bf16x8 xa[5]; bf16x8 w[3][2]; };
__device__ __forceinline__ void q_read(QFrag& f, const char* sa, const char* sb0, const char* sb1, int ks) {
#pragma unroll
  for (int i = 0; i < 5; ++i) f.xa[i] = lds_b128(sa + i * 2048);
#pragma unroll
  for (int s = 0; s < 3; ++s) {
    f.w[s][0] = lds_tr2(sb0 + s * 8192 + ks * 4096);
    f.w[s][1] = lds_tr2(sb1 + s * 8192 + ks * 4096);
  }
}
__device__ constexpr int acc_no(int HALF, int m, int i, int t) {
  int n = 0;
  for (int ii = 0; ii < 5; ++ii)
    for (int tt = 0; tt < 2; ++tt)
      for (int mm = 0; mm < 3; ++mm) {
        const bool on = mm == 0 ? (HALF == 1 || (ii >= 1 && ii <= 3)) : (HALF == 0 || ii >= 1);
        if (mm == m && ii == i && tt == t) return on ? n : -1;
        if (on) ++n;
      }
  return -1;
}
__device__ constexpr int seq_of(int HALF, int z) {
  int n = 0;
  for (int a = 0; a < 30; ++a) {
    const int m = a / 10, t0 = (a / 5) % 2;
    const int i = m < 2 ? (t0 ? a % 5 : 4 - a % 5) : 4 - (a - 20) / 2, t = m < 2 ? t0 : (a ^ i) & 1;
    if (acc_no(HALF, m, i, t) >= 0) { if (n == z) return m * 100 + i * 10 + t; ++n; }
  }
  return -1;
}
template <int HALF, int Z, int E, bool FIRST>
__device__ __forceinline__ void q_one(const QFrag& f, f32x4 (&acc)[26]) {
  if constexpr (Z < E) {
    constexpr int m = seq_of(HALF, Z) / 100, i = seq_of(HALF, Z) / 10 % 10, t = seq_of(HALF, Z) % 10;
    constexpr int n = acc_no(HALF, m, i, t);
    const f32x4 c = FIRST ? f32x4{0, 0, 0, 0} : acc[n];
    acc[n] = m == 2 ? MFMA16(f.xa[i], f.w[2][t], c) : MFMA16(f.w[m][t], f.xa[i], c);
    q_one<HALF, Z + 1, E, FIRST>(f, acc);
  }
}
template <int HALF, int B, int E, int NRD, bool FIRST = false>
__device__ __forceinline__ void q_mma(const QFrag& f, f32x4 (&acc)[26]) {
  q_one<HALF, B, E, FIRST>(f, acc);
#pragma unroll
  for (int i = 0; i < NRD; ++i) {
    __builtin_amdgcn_sched_group_barrier(0x008, 1, 0);
    __builtin_amdgcn_sched_group_barrier(0x100, 1, 0);
  }
  __builtin_amdgcn_sched_group_barrier(0x008, E - B - NRD, 0);
}

__device__ __forceinline__ void tr_all(u32x4 (&w)[8]) {
  u32x4 t[8];
#pragma unroll
  for (int i = 0; i < 8; ++i) t[i] = __builtin_bit_cast(u32x4, tr_frag(w, i));
#pragma unroll
  for (int i = 0; i < 8; ++i) w[i] = t[i];
  asm volatile("" : "+v"(w[0]), "+v"(w[1]), "+v"(w[2]), "+v"(w[3]), "+v"(w[4]), "+v"(w[5]), "+v"(w[6]), "+v"(w[7]));
}
typedef __amdgpu_buffer_rsrc_t rsrc_t;
__device__ __forceinline__ rsrc_t mk_rsrc(const void* p, unsigned bytes) { return __builtin_amdgcn_make_buffer_rsrc((void*)p, 0, bytes, 0x00020000); }
__device__ __forceinline__ u32x4 ld16(rsrc_t rs, unsigned voff, unsigned soff) { return __builtin_amdgcn_raw_buffer_load_b128(rs, voff, soff, 0); }
__device__ __forceinline__ u32x4 ld16c(rsrc_t rs, unsigned voff, unsigned soff) { return __builtin_amdgcn_raw_buffer_load_b128(rs, voff, soff, 16); }
__device__ __forceinline__ void st16c(rsrc_t rs, unsigned voff, u32x4 v) { __builtin_amdgcn_raw_buffer_store_b128(v, rs, voff, 0, 16); }
__device__ __forceinline__ void st8c(rsrc_t rs, unsigned voff, unsigned v, unsigned tag) { __builtin_amdgcn_raw_buffer_store_b64(u32x2{v, tag}, rs, voff, 0, 16); }
__device__ __forceinline__ unsigned chk(unsigned bad, const u32x4& v, unsigned tag) {
  return __builtin_amdgcn_bitop3_b32(__builtin_amdgcn_bitop3_b32(bad, v[1], tag, 0xF6), v[3], tag, 0xF6);
}
__device__ __forceinline__ float f_of(unsigned u) { return __uint_as_float(u); }
#define LANE_VARS                                                                   \
  int lane = lane_in; asm volatile("" : "+v"(lane));                                \
  const int g = lane >> 4, r16 = lane & 15, q = (lane >> 2) & 3, p = lane & 3;      \
  const int hk = (q >> 1) | ((g & 1) << 1), brow = (8 * g + q) * 128 + 8 * p;       \
  (void)r16; (void)hk; (void)brow;

template <bool MAX>
__device__ __forceinline__ float rows_reduce(const float (&v)[4]) {
  auto op = [](unsigned a, unsigned b) {
    return __float_as_uint(MAX ? __builtin_elementwise_maximum(__uint_as_float(a), __uint_as_float(b)) : __uint_as_float(a) + __uint_as_float(b));
  };
  auto p = __builtin_amdgcn_permlane16_swap(__float_as_uint(v[0]), __float_as_uint(v[1]), false, false);
  auto q = __builtin_amdgcn_permlane16_swap(__float_as_uint(v[2]), __float_as_uint(v[3]), false, false);
  auto t = __builtin_amdgcn_permlane32_swap(op(p[0], p[1]), op(q[0], q[1]), false, false);
  return __uint_as_float(op(t[0], t[1]));
}
__device__ __forceinline__ void rows_spread(float t, float (&v)[4]) {
  auto h = __builtin_amdgcn_permlane32_swap(__float_as_uint(t), __float_as_uint(t), false, false);
  auto p = __builtin_amdgcn_permlane16_swap(h[0], h[0], false, false);
  auto q = __builtin_amdgcn_permlane16_swap(h[1], h[1], false, false);
  v[0] = __uint_as_float(p[0]); v[1] = __uint_as_float(p[1]); v[2] = __uint_as_float(q[0]); v[3] = __uint_as_float(q[1]);
}
typedef __attribute__((ext_vector_type(16))) unsigned u32x16;

template <int CTL>
__device__ __forceinline__ float dpp_f(float v) { return __uint_as_float(__builtin_amdgcn_update_dpp(0, (int)__float_as_uint(v), CTL, 0xf, 0xf, false)); }
__device__ __forceinline__ float sum16(float v) {
  v += dpp_f<0xB1>(v);
  v += dpp_f<0x4E>(v);
  v += dpp_f<0x141>(v);
  return v + dpp_f<0x140>(v);
}
__device__ __forceinline__ float dot2(unsigned a, unsigned b, float c) {
  return __builtin_amdgcn_fdot2_f32_bf16(__builtin_bit_cast(bf16x2_t, a), __builtin_bit_cast(bf16x2_t, b), c, false);
}
__device__ __forceinline__ float sum32(float v) {
  v = sum16(v);
  auto a = __builtin_amdgcn_permlane16_swap(__float_as_uint(v), __float_as_uint(v), false, false);
  return __uint_as_float(a[0]) + __uint_as_float(a[1]);
}
struct RowRegs { u32x4 x[4], w[2]; unsigned ep, keep; };
__device__ __forceinline__ void row_request(RowRegs& r, const u16* __restrict__ x, const u16* __restrict__ wr, char* ws, int row0, int lane, bool on) {
  if (!on) return;
  const rsrc_t xrs = mk_rsrc(x, S_TOK * DM * 2), rrs = mk_rsrc(wr, DM * 2);
  const unsigned xo = (unsigned)(row0 + (lane >> 5)) * 2048u + (lane & 31) * 16, wo = lane * 16;
  asm volatile("buffer_load_dword %0, off, %1, 0 sc1" : "=v"(r.ep) : "s"(mk_rsrc(ws + WS_EP, 4)) : "memory");
#pragma unroll
  for (int j = 0; j < 4; ++j) asm volatile("buffer_load_dwordx4 %0, %1, %2, 0 offen offset:%3" : "=v"(r.x[j]) : "v"(xo), "s"(xrs), "i"(j * 512) : "memory");
#pragma unroll
  for (int j = 0; j < 2; ++j) asm volatile("buffer_load_dwordx4 %0, %1, %2, 0 offen offset:%3" : "=v"(r.w[j]) : "v"(wo), "s"(rrs), "i"(j * 1024) : "memory");
}
__device__ __forceinline__ unsigned row_stats(RowRegs& r, char* ws, int row0, int lane) {
  float s0 = 0.f, s1 = 0.f;
  asm volatile("s_waitcnt vmcnt(2)" : "+v"(r.x[0]), "+v"(r.x[1]), "+v"(r.x[2]), "+v"(r.x[3]), "+v"(r.ep), "+v"(r.keep) :: "memory");
  const unsigned tag = uni(r.ep) + 1u;
#pragma unroll
  for (int j = 0; j < 4; ++j) {
    s0 = dot2(r.x[j][0], 0x3f803f80u, s0); s1 = dot2(r.x[j][1], 0x3f803f80u, s1);
    s0 = dot2(r.x[j][2], 0x3f803f80u, s0); s1 = dot2(r.x[j][3], 0x3f803f80u, s1);
  }
  const float mean = sum32(s0 + s1) * (1.0f / DM), mb = rbf(mean);
  s0 = 0.f; s1 = 0.f;
#pragma unroll
  for (int j = 0; j < 4; ++j)
#pragma unroll
    for (int i = 0; i < 4; ++i) {
      const float a = lo2f(r.x[j][i]) - mb, b = hi2f(r.x[j][i]) - mb;
      s0 = fmaf(a, a, s0); s1 = fmaf(b, b, s1);
      r.x[j][i] = pack2(a, b);
    }
  const float dm = mean - mb;
  const float var = rbf(fmaxf(sum32(s0 + s1) * (1.0f / DM) - dm * dm, 0.f));
  const float den = rbf(__builtin_amdgcn_sqrtf(rbf(var + 1e-5f))), rd = __builtin_amdgcn_rcpf(den);
  s0 = 0.f; s1 = 0.f;
  asm volatile("s_waitcnt vmcnt(0)" : "+v"(r.w[0]), "+v"(r.w[1]) :: "memory");
#pragma unroll
  for (int j = 0; j < 4; ++j)
#pragma unroll
    for (int i = 0; i < 4; ++i) {
      const unsigned n = pack2(lo2f(r.x[j][i]) * rd, hi2f(r.x[j][i]) * rd);
      const unsigned w = __builtin_amdgcn_permlane32_swap(r.w[j >> 1][i], r.w[j >> 1][i], false, false)[j & 1];
      if (i & 1) s1 = dot2(n, w, s1); else s0 = dot2(n, w, s0);
    }
  const float dot = sum32(s0 + s1);
  unsigned k = pack2(dot, dot) & 0xffffu;
  k = (k & 0x8000u) ? (~k & 0xffffu) : (k | 0x8000u);
  const rsrc_t wrs = mk_rsrc(ws, WS_O);
  st16c(wrs, (lane & 31) == 0 ? WS_R + (unsigned)(row0 + (lane >> 5)) * 16u : 0x7fffffffu,
        u32x4{(k << 16) | (__float_as_uint(mb) >> 16), tag, __float_as_uint(den), tag});
  const unsigned k01 = k | ((unsigned)__builtin_amdgcn_readlane((int)k, 32) << 16);
  st8c(wrs, lane == 0 ? WS_K + (unsigned)(((row0 >> 2) & 7) * 1024 + (row0 >> 5) * 16 + (row0 & 2) * 4) : 0x7fffffffu, k01, tag);
  return tag;
}
__device__ __forceinline__ void k_order(int tm, int tn, int& pa, int& pb) {
  pb = tn & 3;
  pa = (tm * 0x1111) ^ ((tn & 4) * 0x51 & 0x144) ^ ((~tn & 2) * 0x2200) ^ ((~tn & 1) << 9) ^ (pb == 3 ? 1 : tm & 1);
}
__device__ __forceinline__ int k_tile(int t, int pa, int pb) { return ((((t + pb) & 12) ? t + pb : t & 3) ^ (pa >> ((t + pb) & 12))) & 15; }
struct EpiRegs { int a0, b0, tx; unsigned xr, go; u32x4 xv[2][2]; };
__device__ __forceinline__ float opq(float v) { asm("" : "+v"(v)); return v; }
typedef __attribute__((ext_vector_type(10))) unsigned u32x10;
#define GRP(N, M, K, C) _Pragma("unroll") for (int z_ = 0; z_ < (N); ++z_) { __builtin_amdgcn_sched_group_barrier(0x008, M, 0); __builtin_amdgcn_sched_group_barrier(K, C, 0); }
__device__ constexpr int tail_of(int HALF, int z) {
  int n = 0;
  for (int a = 0; a < 30; ++a) {
    const int i = HALF ? a / 6 : 4 - a / 6, m = 2 - a % 6 / 2, t = a & 1;
    if (acc_no(HALF, m, i, t) >= 0 && (m < 2 || (HALF == 0 && i == 4))) { if (n == z) return m * 100 + i * 10 + t; ++n; }
  }
  return -1;
}
__device__ __forceinline__ void qkv_setup(char* smem, int wid, int lane_in, int tm, int tn, EpiRegs& e) {
  LANE_VARS
  e.a0 = (wid & 1) * 8192 + r16 * 128 + ((g ^ (r16 >> 1)) << 4);
  e.b0 = XB + brow + (((wid & 2) ^ hk) << 5);
  int pa, pb;
  k_order(tm, tn, pa, pb);
  e.tx = __builtin_ctz((unsigned)__ballot(k_tile(r16, pa, pb) == tn));
  e.xr = lds_addr(smem) + (16 + wid * 32 + r16) * 128 + ((((g & 1) * 2 + (g >> 1)) ^ (r16 >> 1)) << 4);
  asm volatile("" : "+v"(e.a0), "+v"(e.b0), "+v"(e.xr));
  asm volatile("" : "+s"(e.tx));
}
template <int HALF>
__device__ __forceinline__ void qkv_attention(char* smem, int wid, int lane_in, int tm, int tn, char* ws, unsigned tag,
                                              const u16* __restrict__ x, EpiRegs& e) {
  const int h = wid >> 1;
  f32x4 acc[26];
  float ra[5], rb[5], sa[4], sb[4];
  QFrag f0, f1;
  u32x4 fl;
  unsigned aq, ac, aa, ab, xa, xv, xf, ooff, sel[4];
  {
    LANE_VARS
    const int a0 = e.a0, b0 = e.b0, tx = uni(e.tx);
    const unsigned xr = e.xr;
    asm volatile("" : "=v"(e.xv[0][0]), "=v"(e.xv[0][1]), "=v"(e.xv[1][0]), "=v"(e.xv[1][1]));
    auto keep = [&](int t, int slot, int tx) {
      unsigned p0, p1;
      asm volatile("s_cmp_lg_u32 %7, %8\n\ts_cbranch_scc1 1f\n\tv_add_u32 %4, %9, %6\n\tv_xor_b32 %5, 64, %4\n\tds_read_b128 %0, %4\n\tds_read_b128 %1, %5\n\t"
                   "ds_read_b128 %2, %4 offset:2048\n\tds_read_b128 %3, %5 offset:2048\n\t1:"
                   : "+v"(e.xv[0][0]), "+v"(e.xv[0][1]), "+v"(e.xv[1][0]), "+v"(e.xv[1][1]), "=&v"(p0), "=&v"(p1) : "v"(xr), "s"(t), "s"(tx), "s"(slot) : "scc", "memory");
    };
    SCHED0();
    WAITL0();
    BAR();
    q_read(f0, smem + a0, smem + b0, smem + (b0 ^ 32), 0);
    SCHED0();
    q_read(f1, smem + (a0 ^ 64), smem + b0, smem + (b0 ^ 32), 1);
    int qa = XSL_ + a0, qc = XSL_ + (a0 ^ 64), qb = WSL + b0, qd = WSL + (b0 ^ 32), so = XSL_;
    q_mma<HALF, 0, 26, 17, true>(f0, acc);
    asm volatile("" : "+v"(qa), "+v"(qb), "+v"(qc), "+v"(qd));
    SCHED0();
    keep(0, 0, tx);
#pragma unroll 1
    for (int t = 1; t < NBAR - 1; ) {
      SCHED0();
      WAITL0();
      BAR();
      SCHED0();
      q_read(f0, smem + qa, smem + qb, smem + qd, 0);
      q_mma<HALF, 0, 26, 17>(f1, acc);
      SCHED0();
      q_read(f1, smem + qc, smem + qb, smem + qd, 1);
      q_mma<HALF, 0, 19, 17>(f0, acc);
      SCHED0();
      int d = so == 2 * XSL_ ? -2 * XSL_ : XSL_;
      asm volatile("" : "+s"(d));
      SCHED0();
      q_mma<HALF, 19, 20, 0>(f0, acc);
      SCHED0();
      keep(t, so, tx);
      SCHED0();
      q_mma<HALF, 20, 21, 0>(f0, acc);
      SCHED0(); qa += d; qc += d; asm volatile("" : "+v"(qa), "+v"(qc)); SCHED0();
      q_mma<HALF, 21, 22, 0>(f0, acc);
      SCHED0(); so += d; ++t; asm volatile("" : "+s"(so), "+s"(t)); SCHED0();
      q_mma<HALF, 22, 23, 0>(f0, acc);
      SCHED0(); int e = (t & 3) ? WSL : -3 * WSL; asm volatile("" : "+s"(e)); SCHED0();
      q_mma<HALF, 23, 24, 0>(f0, acc);
      SCHED0(); qb += e; qd += e; asm volatile("" : "+v"(qb), "+v"(qd)); SCHED0();
      q_mma<HALF, 24, 26, 0>(f0, acc);
      SCHED0();
    }
    WAITL0();
    BAR();
#pragma unroll 1
    for (int t = NBAR + 1; t < 17; ) {
      SCHED0();
      q_read(f0, smem + qa, smem + qb, smem + qd, 0);
      q_mma<HALF, 0, 26, 17>(f1, acc);
      SCHED0();
      q_read(f1, smem + qc, smem + qb, smem + qd, 1);
      const u32x4 fl = lds_word4(smem, F_FULL);
      q_mma<HALF, 0, 21, 18>(f0, acc);
      SCHED0();
      const unsigned cur = uni(min(fl[0], fl[1])), need = t;
      SCHED0();
      q_mma<HALF, 21, 22, 0>(f0, acc);
      SCHED0();
      keep(t, so, tx + 2);
      SCHED0();
      qa += XSL_; qb += WSL; qc += XSL_; qd += WSL; so += XSL_; ++t;
      q_one<HALF, 22, 25, false>(f0, acc);
      GRP(2, 1, 0x002, 2);
      asm volatile("" : "+v"(qa), "+v"(qb), "+v"(qc), "+v"(qd), "+s"(so), "+s"(t));
      SCHED0();
      q_mma<HALF, 25, 26, 0>(f0, acc);
      SCHED0();
      wait_full(smem, F_FULL, need, cur);
    }
    SCHED0();
    q_read(f0, smem + a0, smem + qb, smem + qd, 0);
    q_one<HALF, 0, 26, false>(f1, acc);
    {
      const int pa = 4 * g + (r16 & 1);
      const bool odd = r16 & 1, cur_a = pa <= r16, cur_b = pa + 2 <= r16;
      const unsigned qs = lds_addr(smem + L_QST + HALF * 512);
      aq = qs + r16 * 8; ac = lds_addr(smem + L_CSF + h * 256) + r16 * 8;
      aa = qs + pa * 8 + (cur_a ? 128 : 0); ab = qs + pa * 8 + (cur_b ? 128 : 0);
      xa = lds_addr(smem + L_XCH + h * 3072) + lane * 16; xv = xa + 2048 - lane * 8; xf = lds_addr(smem + F_XCH + h * 4);
      const unsigned la = odd ? 0x01000c0cu : 0x0c0c0100u, lb = odd ? 0x03020c0cu : 0x0c0c0302u;
      sel[0] = cur_a ? 0x0c0c0c0cu : la; sel[1] = cur_b ? 0x0c0c0c0cu : lb; sel[2] = cur_a ? la : 0x0c0c0c0cu; sel[3] = cur_b ? lb : 0x0c0c0c0cu;
      ooff = (unsigned)(tm * 8 + HALF * 4) * 32768u + (tn * 2 + h) * 1024 + lane * 16;
#pragma unroll
      for (int z = 0; z < 17; ++z) { GRP(1, 1, 0x100, 1); __builtin_amdgcn_sched_group_barrier(0x002, 1, 0); }
      GRP(9, 1, 0x002, 3);
      asm volatile("" : "+v"(aq), "+v"(ac), "+v"(aa), "+v"(ab), "+v"(xa), "+v"(xv), "+v"(xf), "+v"(ooff));
      asm volatile("" : "+v"(sel[0]), "+v"(sel[1]), "+v"(sel[2]), "+v"(sel[3]));
    }
    SCHED0();
#pragma unroll
    for (int i = 0; i < 5; ++i) f1.xa[i] = lds_b128(smem + (a0 ^ 64) + i * 2048);
#pragma unroll
    for (int s = 0; s < 3; ++s) {
      if (s == 2) fl = lds_word4(smem, F_FULL);
      f1.w[s][0] = lds_tr2(smem + qb + s * 8192 + 4096);
      f1.w[s][1] = lds_tr2(smem + qd + s * 8192 + 4096);
    }
    q_mma<HALF, 0, 26 - 0, 18>(f0, acc);
    SCHED0();
    keep(15, 0, tx);
    SCHED0();
  }
  LANE_VARS
  auto mm = [&](const QFrag& f, int m, int i, int t) {
    const int n = max(acc_no(HALF, m, i, t), 0);
    acc[n] = m == 2 ? MFMA16(f.xa[i], f.w[2][t], acc[n]) : MFMA16(f.w[m][t], f.xa[i], acc[n]);
  };
  auto last = [&](int z0, int z1) {
#pragma unroll
    for (int z = z0; z < z1; ++z) {
      const int c = tail_of(HALF, HALF ? z : z < 6 ? z + 2 : z < 8 ? z - 6 : z);
      mm(f1, c / 100, c / 10 % 10, c % 10);
    }
  };
  last(0, 3);
  SCHED0();
  const unsigned cur = uni(min(fl[2], fl[3]));
  SCHED0();
  last(3, 3 + 1);
  SCHED0();
  wait_full(smem, F_ST, 1, cur);
  SCHED0();
  u32x2 sq[5], cs[3][2];
#pragma unroll
  for (int i = 0; i < 5; ++i) sq[i] = *(const LAS u32x2*)(aq + i * 128);
#pragma unroll
  for (int s = 0; s < 3; ++s)
#pragma unroll
    for (int t = 0; t < 2; ++t) cs[s][t] = *(const LAS u32x2*)(ac + s * 512 + t * 128);
#pragma unroll
  for (int i = 1; i < 5; ++i) { ra[i] = *(const LAS float*)(aa + (i - 1) * 128 + 4); rb[i] = *(const LAS float*)(ab + (i - 1) * 128 + 20); }
  last(3 + 1, 3 + 5);
  __builtin_amdgcn_sched_group_barrier(0x100, 3, 0); GRP(3, 1, 0x100, 3);
  SCHED0();
  lds_post(smem, F_FREE + wid, 16);
  SCHED0();
  last(3 + 5, 18);
  const unsigned gm = g == 0 ? ~0u : 0u;
  u32x2 lx[5];
#pragma unroll
  for (int i = 0; i < 5; ++i) lx[i] = u32x2{sq[i][0] & gm, sq[i][0] & gm & 0xffffu};
#pragma unroll
  for (int i = 1; i < 5; ++i) {
    const float c = f_of(sq[i][1]) * (0.17677669529663687f * 1.4426950408889634f);
    sa[i - 1] = opq(c * ra[i]); sb[i - 1] = opq(c * rb[i]);
  }
  __builtin_amdgcn_sched_group_barrier(0x008, 5 - 3, 0); GRP(8, 1, 0x002, 3);
  asm volatile("" : "+v"(lx[0]), "+v"(lx[1]), "+v"(lx[2]), "+v"(lx[3]), "+v"(lx[4]));
  asm volatile("" : "+v"(sa[0]), "+v"(sa[1]), "+v"(sa[2]), "+v"(sa[3]), "+v"(sb[0]), "+v"(sb[1]), "+v"(sb[2]), "+v"(sb[3]));
  SCHED0();
  auto ln = [&](int m, int i, int t) {
    const int n = max(acc_no(HALF, m, i, t), 0);
    const s16x4 lxi = __builtin_bit_cast(s16x4, lx[i]), lw = __builtin_bit_cast(s16x4, cs[m][t]);
    acc[n] = m == 2 ? __builtin_amdgcn_mfma_f32_16x16x16bf16_1k(lxi, lw, acc[n], 0, 0, 0) : __builtin_amdgcn_mfma_f32_16x16x16bf16_1k(lw, lxi, acc[n], 0, 0, 0);
  };
#pragma unroll
  for (int z = 0; z < 18; ++z) ln(tail_of(HALF, z) / 100, tail_of(HALF, z) / 10 % 10, tail_of(HALF, z) % 10);
  bf16x8 qf[5], kf[5];
  u32x10 vf[2];
  auto pack_qk = [&](int i) {
    if (acc_no(HALF, 0, i, 0) >= 0) qf[i] = pack8(acc[max(acc_no(HALF, 0, i, 0), 0)], acc[max(acc_no(HALF, 0, i, 1), 0)]);
    if (acc_no(HALF, 1, i, 0) >= 0) kf[i] = pack8(acc[max(acc_no(HALF, 1, i, 0), 0)], acc[max(acc_no(HALF, 1, i, 1), 0)]);
  };
  auto pack_v = [&](int i, int t) {
    const f32x4& v = acc[max(acc_no(HALF, 2, i, t), 0)];
    return u32x2{pack2(v[0], v[1]), pack2(v[2], v[3])};
  };
  u32x2 vx[2];
  pack_qk(HALF == 0 ? 4 : 0);
  if (HALF == 0) {
    vx[0] = pack_v(4, 0); vx[1] = pack_v(4, 1);
    asm volatile("ds_write_b128 %0, %2 offset:1024\n\tds_write2st64_b64 %1, %3, %4 offset1:1" :: "v"(xa), "v"(xv), "v"(kf[4]), "v"(vx[0]), "v"(vx[1]) : "memory");
  } else {
    asm volatile("ds_write_b128 %0, %1" :: "v"(xa), "v"(qf[0]) : "memory");
  }
  asm volatile("s_mov_b64 exec, 1\n\tds_write_b8 %0, %0 offset:%1\n\ts_mov_b64 exec, -1" ::"v"(xf), "i"(HALF) : "memory");
  static_assert((F_XCH & 0xf) != 0 && ((F_XCH + 4) & 0xf) != 0, "the low byte of the address is the flag");
#pragma unroll
  for (int i = 0; i < 4; ++i) pack_qk(HALF == 0 ? 3 - i : i + 1);
  const f32x4 z4 = {0.f, 0.f, 0.f, 0.f};
  f32x4 sp[4], sc[4];
#pragma unroll
  for (int j = 0; j < 3; ++j) {
    const int i = HALF == 0 ? 3 - j : j + 2;
    sc[i - 1] = MFMA16(kf[i], qf[i], z4); sp[i - 1] = MFMA16(kf[i - 1], qf[i], z4);
  }
  if (HALF) sc[0] = MFMA16(kf[1], qf[1], z4);
  GRP(1, 6, 0x002, 4); GRP(12, 1, 0x002, 2); GRP(HALF ? 7 : 6, 1, 0x002, 3);
  SCHED0();
  unsigned flag;
  bf16x8 xq;
  if (HALF == 0) asm volatile("ds_read_b32 %0, %2\n\tds_read_b128 %1, %3" : "=&v"(flag), "=&v"(xq) : "v"(xf), "v"(xa) : "memory");
  else asm volatile("ds_read_b32 %0, %4\n\tds_read_b128 %1, %5 offset:1024\n\tds_read_b64 %2, %6\n\tds_read_b64 %3, %6 offset:512"
                    : "=&v"(flag), "=&v"(xq), "=&v"(vx[0]), "=&v"(vx[1]) : "v"(xf), "v"(xa), "v"(xv) : "memory");
  SCHED0();
  const bool odd = r16 & 1, cur_a = sel[0] == 0x0c0c0c0cu, cur_b = sel[1] == 0x0c0c0c0cu;
  float a[4], b[4], m[4];
  auto logits = [&](int j) {
    const f32x4 p4 = sp[j], c4 = sc[j];
    const float p0 = p4[0], p1 = p4[1], p2 = p4[2], p3 = p4[3], c0 = c4[0], c1 = c4[1], c2 = c4[2], c3 = c4[3];
    const float ca = odd ? c1 : c0, cb = odd ? c3 : c2, pa_ = odd ? p1 : p0, pb = odd ? p3 : p2;
    a[j] = opq((cur_a ? ca : pa_) * sa[j]);
    b[j] = opq((cur_b ? cb : pb) * sb[j]);
    if (HALF == 0 && j == 0) {
      a[j] = (!cur_a && tm == 0) ? -INFINITY : a[j];
      b[j] = (!cur_b && tm == 0) ? -INFINITY : b[j];
    }
    m[j] = __builtin_elementwise_maximum(a[j], b[j]);
  };
  auto late = [&](int z0, int z1) {
#pragma unroll
    for (int y = z0; y < z1; ++y) {
      const int c = seq_of(HALF, 18 + ((y + 8 - 0) & 7));
      if (y < 8 + 0) mm(y < 0 ? f0 : f1, 2, c / 10 % 10, c % 10);
    }
  };
#pragma unroll
  for (int j = 0; j < 2; ++j) logits(HALF == 0 ? 2 - j : j + 1);
  late(0, 6);
  GRP(6, 1, 0x002, 3);
  asm volatile("" : "+v"(m[1]), "+v"(m[2]));
  SCHED0();
  {
    unsigned t;
#define XWAIT(RD) "s_waitcnt lgkmcnt(0)\n\tv_readfirstlane_b32 %0, %1\n\ts_and_b32 %0, %0, %[mask]\n\ts_cbranch_scc1 2f\n\t1:\n\t" RD \
                  "\n\ts_waitcnt lgkmcnt(0)\n\tv_readfirstlane_b32 %0, %1\n\ts_and_b32 %0, %0, %[mask]\n\ts_cbranch_scc0 1b\n\t2:"
    if (HALF == 0) asm volatile(XWAIT("ds_read_b32 %1, %3\n\tds_read_b128 %2, %4") : "=&s"(t), "+v"(flag), "+v"(xq) : "v"(xf), "v"(xa), [mask] "s"(0xff00u) : "scc", "memory");
    else asm volatile(XWAIT("ds_read_b32 %1, %5\n\tds_read_b128 %2, %6 offset:1024\n\tds_read_b64 %3, %7\n\tds_read_b64 %4, %7 offset:512")
                      : "=&s"(t), "+v"(flag), "+v"(xq), "+v"(vx[0]), "+v"(vx[1]) : "v"(xf), "v"(xa), "v"(xv), [mask] "s"(0xffu) : "scc", "memory");
  }
  SCHED0();
  if (HALF == 0) { sc[3] = MFMA16(kf[4], xq, z4); sp[3] = MFMA16(kf[3], xq, z4); }
  else sp[0] = MFMA16(xq, qf[1], z4);
  logits(HALF == 0 ? 0 : 3);
  late(6, 8);
  GRP(1, HALF ? 1 : 2, 0x002, 3); GRP(2, 1, 0x002, 3);
  asm volatile("" : "+v"(m[HALF ? 3 : 0]));
  SCHED0();
  logits(HALF == 0 ? 3 : 0);
  rows_spread(rows_reduce<true>(m), m);
#pragma unroll
  for (int j = 0; j < 4; ++j) {
    a[j] = __builtin_amdgcn_exp2f(opq(a[j] - m[j]));
    b[j] = __builtin_amdgcn_exp2f(opq(b[j] - m[j]));
    m[j] = opq(a[j] + b[j]);
    a[j] = opq(a[j] * ra[j + 1]); b[j] = opq(b[j] * rb[j + 1]);
  }
  late(8, 16);
#pragma unroll
  for (int z = 0; z < 8; ++z) ln(2, seq_of(HALF, 18 + z) / 10 % 10, seq_of(HALF, 18 + z) % 10);
  GRP(0, 1, 0x402, 3); GRP(8, 1, 0x402, 2);
  asm volatile("" : "+v"(m[0]), "+v"(m[1]), "+v"(m[2]), "+v"(m[3]));
  SCHED0();
#pragma unroll
  for (int t = 0; t < 2; ++t) {
#pragma unroll
    for (int i = 0; i < 5; ++i) {
      const u32x2 v = i == (HALF ? 0 : 4) ? vx[t] : pack_v(i, t);
      vf[t][2 * i] = v[0]; vf[t][2 * i + 1] = v[1];
    }
    asm("" : "+v"(vf[t]));
  }
  rows_spread(__builtin_amdgcn_rcpf(rows_reduce<false>(m)), m);
  const rsrc_t ors = mk_rsrc(ws + WS_O, S_TOK * DM * 2);
#pragma unroll
  for (int j = 0; j < 4; ++j) {
    const unsigned pk = pack2(a[j] * m[j], b[j] * m[j]);
    const u32x4 pw = {__builtin_amdgcn_perm(0u, pk, sel[0]), __builtin_amdgcn_perm(0u, pk, sel[1]), __builtin_amdgcn_perm(0u, pk, sel[2]), __builtin_amdgcn_perm(0u, pk, sel[3])};
    const u32x4 v0 = {vf[0][2 * j], vf[0][2 * j + 1], vf[0][2 * j + 2], vf[0][2 * j + 3]}, v1 = {vf[1][2 * j], vf[1][2 * j + 1], vf[1][2 * j + 2], vf[1][2 * j + 3]};
    sp[j] = MFMA16(__builtin_bit_cast(bf16x8, v0), __builtin_bit_cast(bf16x8, pw), z4);
    sc[j] = MFMA16(__builtin_bit_cast(bf16x8, v1), __builtin_bit_cast(bf16x8, pw), z4);
  }
#pragma unroll
  for (int j = 0; j < 4; ++j) { qf[j] = pack8(sp[j], sc[j]); st16c(ors, ooff + j * 32768, __builtin_bit_cast(u32x4, qf[j])); }
  SCHED0();
  asm volatile("ds_read_b32 %0, %1 offset:%2" : "=v"(e.go) : "v"(xf - h * 4), "i"(F_GO - F_XCH) : "memory");
  wait_bytes<0x80808080u>(smem, F_FREE, 16);
  asm volatile("" : "+v"(e.go));
  asm volatile("ds_write_b128 %0, %1\n\tds_write_b128 %0, %2 offset:1024\n\tds_write_b128 %0, %3 offset:4096\n\tds_write_b128 %0, %4 offset:5120"
               :: "v"(lds_addr(smem + L_OWN) + HALF * 8192 + h * 2048 + lane * 16), "v"(qf[0]), "v"(qf[1]), "v"(qf[2]), "v"(qf[3]) : "memory");
  lds_post(smem, F_OL + wid, 1);
}

__device__ __forceinline__ unsigned ln2s(unsigned w, float mb, float r) {
  const unsigned d = pack2(opq(lo2f(w) - mb), opq(hi2f(w) - mb));
  return pack2(opq(lo2f(d) * r), opq(hi2f(d) * r));
}

__device__ __forceinline__ void await_attention(char* smem, int lane, int tm, int tn, char* ws, unsigned tag) {
  const rsrc_t wrs = mk_rsrc(ws, WS_O);
  const unsigned off = WS_OF + (tm * 64 + ((lane >> 3) & 1) * 32 + (lane & 7)) * 4;
  const int own = tn >> 3;
  bool first = true;
  unsigned seen = 0, since = 0;
#pragma unroll 1
  for (unsigned n = 0; n < SPIN_CAP; ++n) {
    unsigned want = ~seen & 0xffffu;
    if (since) ++since; else since = uni(lds_word(smem, F_ODONE)) >> 2;
    if (since <= (first ? 2 : 0)) want &= own ? 0x00ffu : 0xff00u;
    const unsigned f = __builtin_amdgcn_raw_buffer_load_b32(wrs, ((want >> (lane & 15)) & 1) ? off : 0x7fffffffu, 0, 16);
    seen |= (unsigned)__ballot(f == tag) & want;
    if (seen == 0xffffu) break;
    if (first && ((seen & 0xffu) == 0xffu || (seen >> 8) == 0xffu)) { lds_post(smem, F_GO, (seen & 0xffu) == 0xffu ? 1 : 2); first = false; NAP(8); }
    NAP(2);
  }
  lds_post(smem, F_GO + 1, 1);
  if (first) lds_post(smem, F_GO, 1);
}

__device__ __forceinline__ void out_proj(char* smem, int wid, int lane_in, int tm, int tn, char* ws, unsigned tag,
                                         u16* __restrict__ out, EpiRegs& e, unsigned rot2) {
  LANE_VARS
  const rsrc_t ors = mk_rsrc(ws + WS_O, S_TOK * DM * 2), yrs = mk_rsrc(out, S_TOK * DM * 2);
  const int tok = tm * 128 + wid * 32 + r16, co = (g & 1) * 16 + (g >> 1) * 8;
  unsigned eoff = (unsigned)(tok * DM + tn * 64 + co) * 2u;
  const unsigned vo = (unsigned)(tm * 8 + wid * 2) * 32768u + lane * 16;
  bf16x8 wb[3][4], oa[OD][2];
  int bc[4];
#pragma unroll
  for (int c = 0; c < 4; ++c) bc[c] = L_WA + brow + ((c ^ hk) << 5);
  f32x4 acc[2][4];
  SCHED0();
  unsigned go, oth = (tn & 8) * 2 ^ 16;
  asm volatile("" : "+s"(oth));
  auto o_request = [&](int s) {
    asm volatile("buffer_load_dwordx4 %0, %2, %4, %5 offen sc1\n\tbuffer_load_dwordx4 %1, %3, %4, %5 offen sc1" : "+v"(oa[s % OD][0]), "+v"(oa[s % OD][1])
                 : "v"(vo), "v"(vo + 32768), "s"(ors), "s"(((s >> 1 == 7 ? oth : (s & 16) ^ ((go & 2) * 8)) + ((rot2 >> (s & 14) * 2) & 14) + (s & 1)) * 1024) : "memory");
  };
  auto start = [&](unsigned word) {
    go = uni(word) & 3;
    if (go) {
#pragma unroll
      for (int s = 0; s < OD - 1; ++s) o_request(s);
    }
    SCHED0();
  };
#pragma unroll
  for (int s = 0; s < OD; ++s) asm volatile("" : "=v"(oa[s][0]), "=v"(oa[s][1]));
  {
    const unsigned w0 = uni(e.go);
    if (w0 & 3) wait_bytes<0x80808080u>(smem, F_OL, 1);
    start(w0);
  }
  const unsigned early = go;
  auto publish = [&](auto nn) {
    WAITV(decltype(nn)::v);
    unsigned old = 0;
    if (lane == 0) old = __hip_atomic_fetch_add((LAS unsigned*)(LAS char*)(smem + F_ODONE), 1u, __ATOMIC_RELAXED, __HIP_MEMORY_SCOPE_WORKGROUP);
    if (uni(old) == 3u) {
      if (lane == 0) __hip_atomic_store((GAS unsigned*)(ws + WS_OF) + tm * 64 + (tn >> 3) * 32 + (tn & 7), tag, RLX_AGENT);
    }
    SCHED0();
  };
  if (!early) publish(IC<0>{});
  const unsigned w1 = lds_word(smem, F_GO);
  const u32x2 fl = *(const volatile LAS u32x2*)(LAS const char*)(smem + F_FULL);
  const unsigned srow = 2047u - (*(const unsigned*)(smem + F_SEL + (lane & 7) * 4) & 0xffffu);
  f32x4 res[2][4];
  const uint2 st[2] = {*(const uint2*)(smem + L_QST + (16 + wid * 32 + r16) * 8), *(const uint2*)(smem + L_QST + (32 + wid * 32 + r16) * 8)};
  auto norm = [&](int u) {
    const float rd = f_of(st[u].y), k = 1.0f + rd, b = lo2f(st[u].x) * rd;
#pragma unroll
    for (int pr = 0; pr < 2; ++pr) {
      auto sa = __builtin_amdgcn_permlane16_swap(e.xv[u][pr][0], e.xv[u][pr][2], false, false);
      auto sb = __builtin_amdgcn_permlane16_swap(e.xv[u][pr][1], e.xv[u][pr][3], false, false);
#pragma unroll
      for (int z = 0; z < 2; ++z) res[u][2 * pr + z] = f32x4{lo2f(sa[z]), hi2f(sa[z]), lo2f(sb[z]), hi2f(sb[z])} * k + b;
    }
    asm volatile("" : "+v"(res[u][0]), "+v"(res[u][1]), "+v"(res[u][2]), "+v"(res[u][3]));
  };
  f32x4 ac2[2][4], (&ac3)[2][4] = res;
  auto w_frag = [&](int slot, const int (&b)[4], int z) {
#pragma unroll
    for (int c = 0; c < 4; ++c) wb[slot][c] = lds_tr2(smem + b[c] + z * 4096);
  };
  auto step = [&](f32x4 (&d)[2][4], int slot, const bf16x8 (&o)[2], bool fresh) {
#pragma unroll
    for (int c = 0; c < 4; ++c)
#pragma unroll
      for (int uu = 0; uu < 2; ++uu) {
        const int u = uu ^ (c & 1);
        d[u][c] = MFMA16(wb[slot][c], o[u], fresh ? (f32x4{0, 0, 0, 0}) : d[u][c]);
      }
  };
  int b3[4];
#pragma unroll
  for (int c = 0; c < 4; ++c) b3[c] = bc[c] + (L_W3 - L_WA);
  auto own_heads = [&](unsigned fu) {
    wait_full(smem, F_FULL, 17, fu);
    w_frag(0, b3, 2); w_frag(1, b3, 3);
    const unsigned w2 = lds_word(smem, F_GO);
    SCHED0();
    if (wid < 2) wait_bytes<0x00800080u>(smem, F_OL, 1); else wait_bytes<0x80008000u>(smem, F_OL, 1);
    if (!go) start(w2);
    bf16x8 own[2][2];
#pragma unroll
    for (int z = 0; z < 4; ++z) own[z >> 1][z & 1] = lds_b128(smem + L_OWN + wid * 4096 + lane * 16 + z * 1024);
    step(ac3, 0, own[0], false); step(ac3, 1, own[1], false);
    asm volatile("" : "+v"(ac3[0][0]), "+v"(ac3[0][1]), "+v"(ac3[0][2]), "+v"(ac3[0][3]), "+v"(ac3[1][0]), "+v"(ac3[1][1]), "+v"(ac3[1][2]), "+v"(ac3[1][3]));
    SCHED0();
  };
  unsigned mine = (unsigned)__ballot((srow >> 5) == (unsigned)(tm * 4 + wid)) & 0xffu;
  asm volatile("" : "+s"(mine));
  asm volatile("" : "+v"(eoff));
  if (!go) start(w1);
  norm(0);
  norm(1);
  SCHED0();
  if (early) publish(IC<2 * OD - 2>{});
  own_heads(uni(min(fl[0], fl[1])));
  if (!go) start(wait_bytes<0x80u>(smem, F_GO, 1));
  go = uni(go);
  int b2[4];
  const int mine1st = ((go >> 1) ^ (tn >> 3) ^ 1) & 1;
#pragma unroll
  for (int c = 0; c < 4; ++c) { b2[c] = bc[c] + (go & 1) * (L_WB - L_WA); bc[c] += (go >> 1) * (L_WB - L_WA); }
  unsigned so[15];
#pragma unroll
  for (int z = (OD - 1) / 2; z < 15; ++z) { so[z] = ((z == 7 ? oth : (2 * z & 16) ^ ((go & 2) * 8)) + ((rot2 >> (z & 7) * 4) & 14)) * 1024; asm volatile("" : "+s"(so[z])); }
  s16x4 wh[3][8];
  auto w_half = [&](int s, int z0, int z1) {
#pragma unroll
    for (int z = z0; z < z1; ++z)
      if (s < 30) wh[s % 3][z] = __builtin_amdgcn_ds_read_tr16_b64_v4i16((LAS s16x4*)(smem + (s < 14 ? bc[z >> 1] + s * 4096 : s < 16 ? b3[z >> 1] + (s - 14) * 4096 : b2[z >> 1] + (s - 16) * 4096) + (z & 1) * 512));
  };
  wait_full(smem, F_WO, 1);
  wait_bytes<0x80u, true>(smem, F_WO + 8, 1, go & 1);
  w_half(0, 0, 8); w_half(1, 0, 8);
  SCHED0();
  asm volatile("s_waitcnt vmcnt(%2)" : "+v"(oa[0][0]), "+v"(oa[0][1]) : "i"(2 * OD - 4) : "memory");
  auto one = [&](int s) __attribute__((always_inline)) {
    f32x4 (&d)[2][4] = s < 14 ? ac2 : s < 16 ? ac3 : acc;
    auto part = [&](int z0, int z1) {
      SCHED0();
      w_half(s + 2, z0, z1);
#pragma unroll
      for (int z = z0; z < z1; ++z) {
        const int c = z >> 1, u = (z ^ c) & 1;
        d[u][c] = MFMA16(__builtin_shufflevector(wh[s % 3][2 * c], wh[s % 3][2 * c + 1], 0, 1, 2, 3, 4, 5, 6, 7), oa[s % OD][u], s == 0 || s == 16 ? (f32x4{0, 0, 0, 0}) : d[u][c]);
        __builtin_amdgcn_sched_group_barrier(0x008, 1, 0);
        __builtin_amdgcn_sched_group_barrier(0x100, 1, 0);
      }
      SCHED0();
    };
    const int n = s + OD - 1;
    auto ask = [&](int u) {
      if (n < 30) asm volatile("buffer_load_dwordx4 %0, %1, %2, %3 offen offset:%4 sc1" : "+v"(oa[n % OD][u]) : "v"(vo + u * 32768), "s"(ors), "s"(so[n >> 1]), "i"((n & 1) * 1024) : "memory");
    };
    part(0, 1);
    if (n == 14 || n == 16) wait_bytes<0x8000u, true>(smem, F_GO, 1, n == 14 ? mine1st ^ 1 : mine1st);
    ask(0);
    part(1, 3);
    ask(1);
    if (s == 13) wait_bytes<0x80u>(smem, F_WO + 8, 1);
    part(3, 5);
    if (s < 29) asm volatile("s_waitcnt vmcnt(%2)" : "+v"(oa[(s + 1) % OD][0]), "+v"(oa[(s + 1) % OD][1]) : "i"(2 * ((n < 29 ? n : 29) - s - 1)) : "memory");
    part(5, 8);
    asm volatile("" : "+v"(d[0][0]), "+v"(d[0][1]), "+v"(d[0][2]), "+v"(d[0][3]), "+v"(d[1][0]), "+v"(d[1][1]), "+v"(d[1][2]), "+v"(d[1][3]));
  };
#pragma unroll
  for (int s = 0; s < 10; ++s) one(s);
#pragma unroll
  for (int s = 10; s < 20; ++s) one(s);
#pragma unroll
  for (int s = 20; s < 28; ++s) one(s);
  asm volatile("s_waitcnt vmcnt(0)" : "+v"(oa[29 % OD][0]), "+v"(oa[29 % OD][1]) :: "memory");
  if (mine != 0) {
    wait_bytes<0x80808080u>(smem, F_UP, 1);
#pragma unroll 1
    for (; mine != 0; mine &= mine - 1) {
      const int c = __builtin_ctz(mine), row = __builtin_amdgcn_readlane((int)srow, c) & 31;
#pragma unroll
      for (int u = 0; u < 2; ++u)
        if (row == u * 16 + r16) {
#pragma unroll
          for (int z = 0; z < 4; ++z) res[u][z] += *(const f32x4*)(smem + L_UP + c * 256 + (z * 16 + g * 4) * 4);
        }
    }
  }
  SCHED0();
#pragma unroll
  for (int u = 0; u < 2; ++u)
#pragma unroll
    for (int z = 0; z < 8; ++z)
      acc[u][z & 3] = MFMA16(__builtin_shufflevector(wh[(28 + (z >> 2)) % 3][2 * (z & 3)], wh[(28 + (z >> 2)) % 3][2 * (z & 3) + 1], 0, 1, 2, 3, 4, 5, 6, 7), oa[(28 + (z >> 2)) % OD][u], acc[u][z & 3]);
#pragma unroll
  for (int u = 0; u < 2; ++u)
#pragma unroll
    for (int pr = 0; pr < 2; ++pr) {
      const f32x4 c0 = (acc[u][2 * pr] + ac2[u][2 * pr]) + ac3[u][2 * pr], c1 = (acc[u][2 * pr + 1] + ac2[u][2 * pr + 1]) + ac3[u][2 * pr + 1];
      auto sa = __builtin_amdgcn_permlane16_swap(pack2(c0[0], c0[1]), pack2(c1[0], c1[1]), false, false);
      auto sb = __builtin_amdgcn_permlane16_swap(pack2(c0[2], c0[3]), pack2(c1[2], c1[3]), false, false);
      __builtin_amdgcn_raw_buffer_store_b128(u32x4{sa[0], sb[0], sa[1], sb[1]}, yrs, eoff + pr * 64 + u * 16 * DM * 2, 0, 16);
    }
  __builtin_amdgcn_sched_group_barrier(0x008, 9, 0);
  GRP(7, 1, 0x002, 4);
  SCHED0();
}

__device__ __forceinline__ unsigned med3(unsigned a, unsigned b, unsigned c) { return max(min(a, b), min(max(a, b), c)); }
#define PIN8(a) asm volatile("" : "+v"(a[0]), "+v"(a[1]), "+v"(a[2]), "+v"(a[3]), "+v"(a[4]), "+v"(a[5]), "+v"(a[6]), "+v"(a[7]))
__device__ __forceinline__ void tick(int& nb) {
  SCHED0();
  if (nb < NBAR) { BAR(); ++nb; NAP(2); }
  asm volatile("" ::: "memory");
  SCHED0();
}
__device__ __forceinline__ void hidden_half(char* smem, int lane, int l, int cg, char* ws, unsigned tag, u32x4 (&fd)[2][8]) {
  const rsrc_t wrs = mk_rsrc(ws, WS_O);
  const unsigned off = WS_GU + (unsigned)(cg * 32768 + l * 2048 + lane * 16);
  u32x4 ps[2][8];
  auto request = [&]() {
#pragma unroll
    for (int p = 0; p < 16; ++p) ps[p & 1][p >> 1] = ld16c(wrs, off + (p & 1) * 1024, (p >> 1) * 4096);
    SCHED0();
  };
  auto good = [&]() {
    unsigned bad = 0;
#pragma unroll
    for (int p = 0; p < 16; ++p) bad = chk(bad, ps[p & 1][p >> 1], tag);
    return __all(bad == 0);
  };
#pragma unroll 1
  for (unsigned n = 0; n < SPIN_CAP; ++n) {
    const unsigned t = __builtin_amdgcn_raw_buffer_load_b32(wrs, WS_GU + (unsigned)(cg * 32768 + (lane >> 3) * 4096 + l * 2048 + (lane & 7) * 256 + 132), 0, 16);
    if (__all(t == tag)) break;
    NAP(8);
  }
  request();
  tr_all(fd[0]); tr_all(fd[1]);
  SCHED0();
#pragma unroll 1
  for (unsigned n = 0; n < SPIN_CAP && !good(); ++n) { NAP(4); request(); }
  unsigned hp[2];
#pragma unroll
  for (int j = 0; j < 2; ++j) {
    float hv[2];
#pragma unroll
    for (int z = 0; z < 2; ++z) {
      const float gb = rbf((f_of(ps[j][0][2 * z]) + f_of(ps[j][2][2 * z])) + (f_of(ps[j][4][2 * z]) + f_of(ps[j][6][2 * z])));
      const float ub = rbf((f_of(ps[j][1][2 * z]) + f_of(ps[j][3][2 * z])) + (f_of(ps[j][5][2 * z]) + f_of(ps[j][7][2 * z])));
      hv[z] = rbf(gb / (1.0f + __expf(-gb))) * ub;
    }
    hp[j] = pack2(hv[0], hv[1]);
  }
#pragma unroll
  for (int j = 0; j < 2; ++j)
    *(unsigned*)(smem + L_HF + (lane & 7) * 128 + (((lane >> 4) & 1) * 4 + 2 * l + j) * 16 + ((lane >> 3) & 1) * 4 + (lane >> 5) * 8) = hp[j];
  lds_post(smem, F_H + l, 1);
}

struct Stager {
  rsrc_t xrs, qrs;
  LAS char* lds;
  unsigned va, vb;
  int arow, pa, pb;
  __device__ __forceinline__ Stager(char* smem, int l, int lane, int tm, int tn, const u16* x, const u16* w) {
    asm volatile("" : "+v"(lane));
    const int kr = lane >> 3;
    xrs = mk_rsrc(x, S_TOK * DM * 2); qrs = mk_rsrc(w, DM * NQKV * 2);
    lds = (LAS char*)smem + l * 1024;
    va = (unsigned)(kr + 8 * l) * 2048u + (((lane & 7) ^ (kr >> 1) ^ (l << 2)) << 4);
    vb = (unsigned)(kr + 8 * l) * 6144u + tn * 128 + (((lane & 7) ^ (((kr >> 1) & 1) << 1) ^ (l << 2)) << 4);
    arow = tm * 128 - 16;
    k_order(tm, tn, pa, pb);
  }
};
#define DM1(l, o) "s_add_u32 m0, %2, " #l "\n\ts_add_u32 %0, %5, " #o "\n\tbuffer_load_dwordx4 %3, %4, %0 offen lds\n\t"
#define DMA_(str, v_, rs_, so_, s0_) asm volatile("s_mov_b32 %1, m0\n\t" str "s_mov_b32 m0, %1" : "=&s"(tmp), "=&s"(keep) : "s"(base), "v"(v_), "s"(rs_), "s"(so_), "s"(s0_) : "scc", "memory")
__device__ __forceinline__ void stage_x(const Stager& g, int t) {
  const int kt = k_tile(t, g.pa, g.pb);
  const unsigned base = (unsigned)(size_t)g.lds + (t % 3) * XSL_;
  unsigned keep, tmp;
  DMA_("s_mov_b32 m0, %2\n\ts_nop 0\n\tbuffer_load_dwordx4 %3, %4, %6 offen lds\n\t" DM1(2048, 32768) DM1(4096, 65536) DM1(6144, 98304) DM1(8192, 131072)
       DM1(10240, 163840) DM1(12288, 196608) DM1(14336, 229376) DM1(16384, 262144), g.va, g.xrs, g.arow * 2048 + kt * 128, max(g.arow, 0) * 2048 + kt * 128);
}
__device__ __forceinline__ void stage_w(const Stager& g, int t) {
  const int kt = k_tile(t, g.pa, g.pb);
  const unsigned base = (unsigned)(size_t)g.lds + XB + (t & 3) * WSL;
  unsigned keep, tmp;
  DMA_(DM1(0, 0) DM1(2048, 98304) DM1(4096, 196608) DM1(6144, 294912) DM1(8192, 2048) DM1(10240, 100352) DM1(12288, 198656) DM1(14336, 296960)
       DM1(16384, 4096) DM1(18432, 102400) DM1(20480, 200704) DM1(22528, 299008), g.vb, g.qrs, kt * 64 * 6144, 0);
}
__device__ __forceinline__ int unit_of(int bid) {
  const int off = bid >> 3;
  return (off >> 2) == 3 ? off + 16 : (off >> 2) == 7 ? off - 16 : off;
}
__device__ constexpr int wo_at(int n) { return (n < 28 ? L_WA + n * 2048 : n < 56 ? L_WB + (n - 28) * 2048 : L_W3 + (n - 56) * 2048); }
__device__ __forceinline__ void loader(char* smem, int l, int lane, int tm, int tn, unsigned rot2, const u16* __restrict__ x,
                                       const u16* __restrict__ w, const u16* __restrict__ wo, const u16* wg, const u16* wu, const u16* wd, int bid, char* ws) {
  const Stager stage(smem, l, lane, tm, tn, x, w);
  dma_tag(smem, F_FULL + l * 4, 0);
  const rsrc_t prs = mk_rsrc(wo, DM * DM * 2);
  const int kr = lane >> 3;
  const unsigned vo = (unsigned)((kr < 4 ? kr : 12 + kr) + 4 * l) * 2048u + tn * 128 + (((lane & 7) ^ (((kr >> 1) & 1) << 1) ^ (l << 2)) << 4);
  int oth = (tn & 8) * 2 ^ 16;
  asm volatile("" : "+s"(oth));
  auto source = [&](int n) {
    const int k = n >> 1, half = k < 14 ? 0 : k < 28 ? 16 : k >= 30 ? (tn & 8) * 2 : oth;
    return (unsigned)((half + ((rot2 >> ((k < 14 ? k : k < 28 ? k - 14 : 14) & 14) * 2) & 14) + (k & 1)) * 32 + 8 * (n & 1)) * 2048;
  };
  auto fetch = [&](int n) {
    unsigned keep;
    asm volatile("s_mov_b32 %0, m0\n\ts_add_u32 m0, %4, %5\n\ts_nop 2\n\tbuffer_load_dwordx4 %1, %2, %3 offen lds\n\ts_mov_b32 m0, %0"
                 : "=&s"(keep) : "v"(vo), "s"(prs), "s"(source(n)), "s"((unsigned)(size_t)stage.lds), "i"(wo_at(n)) : "scc", "memory");
  };
  unsigned junk = 0;
  static_assert(L_WA + 24 * 2048 == XB && L_WB == XB + WSL && L_OWN + 16384 <= L_XCH, "layout");
#pragma unroll
  for (int t = -1; t < NBAR - 2; ++t) {
    if (t == 0) stage_w(stage, 3);
    if (t >= 0) stage_x(stage, t + 3);
    if (t == NBAR - 3) dma_tag(smem, F_FULL + l * 4, 15);
    if (t >= 0) stage_w(stage, t + 4);
    if (t == 1) {
      const unsigned z = ((bid >> 3) * 2 + l) * 65536u;
#define SL(r, o) "s_buffer_load_dword %0, %" #r ", %" #o "\n\t"
      asm volatile(SL(1, 6) SL(1, 7) SL(2, 6) SL(2, 7) SL(3, 6) SL(3, 7) SL(4, 6) SL(5, 8) : "=&s"(junk)
                   : "s"(stage.qrs), "s"(mk_rsrc(wg, DM * FF * 2)), "s"(mk_rsrc(wu, DM * FF * 2)), "s"(prs), "s"(mk_rsrc(wd, DM * FF * 2)), "s"(z), "s"(z + 64 * 65536u),
                     "s"((bid & 7) * 7 * 131072 + (z & 0xf0000u)) : "memory");
    }
    SCHED0();
    WAITV(t < 0 ? 22 : 33 + (t == 0 || t == NBAR - 3));
    BAR();
  }
  stage_x(stage, 15);
  dma_tag(smem, F_FULL + l * 4, 16);
  SCHED0();
#pragma unroll
  for (int y = 0; y < 12; ++y) fetch(y < 8 ? 63 - y : 16 + y);
  dma_tag(smem, F_FULL + l * 4, 17);
  asm volatile("s_waitcnt lgkmcnt(0)" : "+s"(junk) :: "memory");
  const unsigned tag = uni(lds_word(smem, F_TAG));
  wait_bytes<0x80808080u>(smem, F_FREE, 16);
#pragma unroll
  for (int n = 0; n < 24; ++n) fetch(n);
  dma_tag(smem, F_WO + l * 4, 1);
#pragma unroll
  for (int n = 28; n < 28 + NB1; ++n) fetch(n);
  if (l == 0) {
    const unsigned v1 = (vo + 4 * 2048) ^ 64;
    wait_bytes<0x80808080u>(smem, F_OL, 1);
#pragma unroll
    for (int n = 28 + NB1; n < 56; ++n) {
      unsigned keep;
      asm volatile("s_mov_b32 %0, m0\n\ts_add_u32 m0, %4, %5\n\ts_nop 0\n\tbuffer_load_dwordx4 %1, %2, %3 offen lds\n\ts_add_u32 m0, m0, 1024\n\ts_nop 0\n\tbuffer_load_dwordx4 %6, %2, %3 offen lds\n\ts_mov_b32 m0, %0"
                   : "=&s"(keep) : "v"(vo), "s"(prs), "s"(source(n)), "s"((unsigned)(size_t)stage.lds), "i"(wo_at(n)), "v"(v1) : "scc", "memory");
    }
    wait_bytes<0x80u>(smem, F_WO + 12, 1);
    dma_tag(smem, F_WO + 8, 1);
  } else dma_tag(smem, F_WO + 12, 1);
  __builtin_amdgcn_s_setprio(1);
  if (l) await_attention(smem, lane, tm, tn, ws, tag);
}

__device__ __forceinline__ void updates(char* smem, int w, int lane, int tm, int tn, char* ws) {
  const rsrc_t wrs = mk_rsrc(ws, WS_O);
  const unsigned tag = uni(lds_word(smem, F_TAG));
  const int g = lane >> 4, li = lane & 15;
  wait_bytes<0x80u>(smem, F_SELOK, 1);
  const unsigned selkey = *(const unsigned*)(smem + F_SEL + (lane & 7) * 4);
  unsigned mine = (unsigned)__ballot(((2047u - (selkey & 0xffffu)) >> 7) == (unsigned)tm) & 0xffu;
#pragma unroll
  for (int i = 0; i < 3; ++i) mine &= i < w ? mine - 1 : ~0u;
  if (mine != 0 && w < 3) {
    wait_bytes<0x80u>(smem, F_GO, 1);
    NAP(24);
  }
  for (; mine != 0; mine &= mine - 1, mine &= mine - 1, mine &= mine - 1, mine &= mine - 1) {
    const int c = __builtin_ctz(mine);
    const unsigned yoff = WS_Y + (unsigned)((g * 14 * 8 + c) * DM + tn * 64 + li * 4) * 4u;
    u32x4 yv[14];
    const unsigned key = (unsigned)__builtin_amdgcn_readlane((int)selkey, c), kb = key >> 16;
#pragma unroll 1
    for (unsigned n = 0; n < SPIN_CAP; ++n) {
      unsigned bad = 0;
#pragma unroll
      for (int j = 0; j < 14; ++j) yv[j] = ld16c(wrs, yoff, j * (8 * DM * 4));
#pragma unroll
      for (int j = 0; j < 14; ++j) bad = chk(bad, yv[j], tag);
      if (__all(bad == 0)) break;
      NAP(8);
    }
    float y[4] = {0.f, 0.f, 0.f, 0.f};
#pragma unroll
    for (int j = 0; j < 14; ++j) { y[0] += lo2f(yv[j][0]); y[1] += hi2f(yv[j][0]); y[2] += lo2f(yv[j][2]); y[3] += hi2f(yv[j][2]); }
    const float r = __uint_as_float(((kb & 0x8000u) ? (kb & 0x7fffu) : (~kb & 0xffffu)) << 16);
    const float rs = rbf(1.0f / (1.0f + __expf(-r)));
#pragma unroll
    for (int e = 0; e < 4; ++e) y[e] = rbf(rbf(xsum16_32(y[e])) * rs);
    wait_bytes<0x80808080u>(smem, F_FREE, 16);
    *(f32x4*)(smem + L_UP + c * 256 + li * 16) = f32x4{y[0], y[1], y[2], y[3]};
  }
  lds_post(smem, F_UP + w, 1);
}

__device__ __forceinline__ void side_wave(char* smem, int F, int lane, int bid, int tm, int tn, const u16* __restrict__ x,
                                          const u16* __restrict__ wgu, const u16* __restrict__ wd, const u16* __restrict__ wo, char* ws) {
  const rsrc_t wrs = mk_rsrc(ws, WS_O), xrs = mk_rsrc(x, S_TOK * DM * 2);
  const int u = unit_of(bid), cg = (bid & 7) * 7 + ((u >> 2) % 7), q = u & 3;
  const int g = lane >> 4, li = lane & 15;
  u32x4 fw[4][8];
  const rsrc_t grs = mk_rsrc(wgu, DM * FF * 2);
  const int cu = (bid & 7) * 7 + (((u >> 2) + F) % 7);
  const unsigned fo = (unsigned)(q * 256 + (li >> 3) * 128 + 8 * g) * (FF * 2) + cu * 128 + (li & 7) * 16;
#pragma unroll
  for (int s = 0; s < 4; ++s) asm volatile("" : "=v"(fw[s][0]), "=v"(fw[s][1]), "=v"(fw[s][2]), "=v"(fw[s][3]), "=v"(fw[s][4]), "=v"(fw[s][5]), "=v"(fw[s][6]), "=v"(fw[s][7]));
  const unsigned csb = WS_CS + (tn * 2 + F) * 4096;
  u32x4 cs[4], rec[2];
  unsigned ca = csb + lane * 16, ra = WS_R + (unsigned)max(tm * 128 - 16 + F * 72 + lane, 0) * 16, rb = WS_R + (unsigned)max(tm * 128 - 8 + F * 72 + lane, 0) * 16;
  asm volatile("" : "+v"(ca), "+v"(ra), "+v"(rb));
  const rsrc_t prs = mk_rsrc(wo, DM * DM * 2);
  const unsigned wa = lane < 32 ? (unsigned)(tm * 64 + F * 32 + lane) * 2048u + tn * 128 : 0x7fffffffu;
  unsigned td = 0;
  unsigned due = u < 28 ? (0x3012 >> ((tn & 3) * 4)) & 15 : 9, iss = 0;
#define FW(n) fw[(n) >> 3][(n) & 7]
#define LD(i) "s_mov_b32 %[t], (((%[n0]+" #i ")>>3)*32+((%[n0]+" #i ")&7))*7168\n\tbuffer_load_dwordx4 %" #i ", %[fo], %[rs], %[t] offen nt\n\t"
#define LD8 LD(0) LD(1) LD(2) LD(3) LD(4) LD(5) LD(6) LD(7)
#define R8(n) "+v"(FW(n)), "+v"(FW(n + 1)), "+v"(FW(n + 2)), "+v"(FW(n + 3)), "+v"(FW(n + 4)), "+v"(FW(n + 5)), "+v"(FW(n + 6)), "+v"(FW(n + 7))
#define SITE(lds_, ...) asm volatile("s_cmp_lg_u32 %[iss], %[k]\n\ts_cbranch_scc1 1f\n\ts_cmp_le_u32 %[nb], %[due]\n\ts_cbranch_scc1 1f\n\ts_sleep %[dly]\n\t" lds_ \
    "s_add_u32 %[iss], %[iss], 1\n\ts_add_u32 %[due], %[due], 4\n\t1:" : __VA_ARGS__, [iss] "+s"(iss), [due] "+s"(due), [t] "=&s"(t) \
    : [fo] "v"(fo), [rs] "s"(grs), [nb] "s"(nb), [k] "i"(K), [n0] "i"(n0), [dly] "i"(D), [ca] "v"(ca), [ra] "v"(ra), [rb] "v"(rb), [ws] "s"(wrs), [wa] "v"(wa), [wr] "s"(prs) : "scc", "memory")
#define C1K(i, a, o) "buffer_load_dwordx4 %" #i ", %[" #a "], %[ws], 0 offen offset:" #o " sc1\n\t"
#define G2(a, b, c, d, e, f) C1K(a, ca, 0) C1K(b, ca, 1024) C1K(c, ca, 2048) C1K(d, ca, 3072) C1K(e, ra, 0) C1K(f, rb, 0)
#define PING2() asm volatile("" : "+v"(cs[0]), "+v"(cs[1]), "+v"(cs[2]), "+v"(cs[3]), "+v"(rec[0]), "+v"(rec[1]))
  auto site = [&](auto kk, auto dd, unsigned nb) {
    constexpr int K = decltype(kk)::v, D = decltype(dd)::v, n0 = K == 0 ? 0 : 8 + K * 8;
    unsigned t;
    SCHED0();
    if constexpr (K == 0) SITE(LD8 LD(8) LD(9) LD(10) LD(11) LD(12) LD(13) LD(14) LD(15), R8(n0), R8(n0 + 8));
    else if constexpr (K == 1) SITE(G2(8, 9, 10, 11, 12, 13) LD8, R8(n0), "+v"(cs[0]), "+v"(cs[1]), "+v"(cs[2]), "+v"(cs[3]), "+v"(rec[0]), "+v"(rec[1]));
    else SITE(LD8 "buffer_load_dword %8, %[wa], %[wr], 0 offen\n\t", R8(n0), "+v"(td));
    SCHED0();
  };
#define CH(k, d, n) site(IC<k>{}, IC<d>{}, n)
  unsigned tag;
  {
    const int qq = (lane >> 2) & 3, hk = (qq >> 1) | ((g & 1) << 1);
    const char* pb = smem + XB + (8 * g + qq) * 128 + 8 * (lane & 3) + (((2 * F + (tm & 1)) ^ hk) << 5);
    const bf16x8 ones = {0x3f80, 0x3f80, 0x3f80, 0x3f80, 0x3f80, 0x3f80, 0x3f80, 0x3f80};
    f32x4 a[3];
#pragma unroll
    for (int t = 0; t < 2; ++t) {
      SCHED0(); BAR(); NAP(2); SCHED0();
      __builtin_amdgcn_s_setprio(3);
#pragma unroll
      for (int c = 0; c < 3; ++c) {
        a[c] = MFMA16(ones, lds_tr2(pb + t * WSL + c * 8192), t ? a[c] : (f32x4{0, 0, 0, 0}));
        a[c] = MFMA16(ones, lds_tr2(pb + t * WSL + c * 8192 + 4096), a[c]);
      }
      asm volatile("" : "+v"(a[0]), "+v"(a[1]), "+v"(a[2]));
      if (t == 0) { tag = uni(lds_word(smem, F_TAG)); CH(0, 4, 1); }
    }
    st16c(wrs, lane < 16 ? csb + ((tm & 12) + (tm & 1) * 2 + ((tm >> 1) & 1)) * 256 + lane * 16 : 0x7fffffffu,
          u32x4{__float_as_uint(a[0][0]), __float_as_uint(a[1][0]), __float_as_uint(a[2][0]), tag});
    __builtin_amdgcn_s_setprio(0);
    CH(0, 4, 2);
  }
  int nb = 2;
  tick(nb);
  const unsigned xs_off = WS_S + (unsigned)(((bid & 7) * 4 + q) * 6144 + lane * 16);
  uint2 tc[2], tq[2];
  unsigned ta[3], tv, todo = 1;
  auto gather2 = [&]() {
    asm volatile(G2(0, 1, 2, 3, 4, 5) : "=&v"(cs[0]), "=&v"(cs[1]), "=&v"(cs[2]), "=&v"(cs[3]), "=&v"(rec[0]), "=&v"(rec[1])
                 : [ca] "v"(ca), [ra] "v"(ra), [rb] "v"(rb), [ws] "s"(wrs) : "memory");
    SCHED0();
  };
  auto prepare = [&]() {
#pragma unroll 1
    for (unsigned n = 0; n < SPIN_CAP; ++n) {
      unsigned bad = chk(chk(0u, rec[0], tag), rec[1], tag);
#pragma unroll
      for (int j = 0; j < 4; ++j) bad = __builtin_amdgcn_bitop3_b32(bad, cs[j][3], tag, 0xF6);
      if (__all(bad == 0)) break;
      NAP(8);
      gather2();
      tick(nb); tick(nb); WAITV(0); PING2();
    }
    float cz[3];
#pragma unroll
    for (int z = 0; z < 3; ++z) {
      const unsigned v = __float_as_uint(opq(opq(f_of(cs[0][z]) + f_of(cs[1][z])) + opq(f_of(cs[2][z]) + f_of(cs[3][z]))));
      auto p = __builtin_amdgcn_permlane16_swap(v, v, false, false);
      cz[z] = opq(f_of(p[0]) + f_of(p[1]));
    }
    const float s0 = (g & 1) ? cz[1] : cz[0], s1 = (g & 1) ? cz[2] : cz[1];
    const float h0 = rbf(s0), d0 = opq(s0 - h0), c0 = rbf(d0), h1 = rbf(s1), d1 = opq(s1 - h1), c1 = rbf(d1);
    tc[0] = make_uint2(pack2(h0, c0), pack2(opq(d0 - c0), 0.f)); tc[1] = make_uint2(pack2(h1, c1), pack2(opq(d1 - c1), 0.f));
    asm volatile("" : "+v"(tc[0].x), "+v"(tc[0].y), "+v"(tc[1].x), "+v"(tc[1].y));
#pragma unroll
    for (int j = 0; j < 2; ++j) {
      const unsigned nm = (rec[j][0] & 0xffffu) ^ 0x8000u;
      tq[j] = make_uint2(nm | (nm << 16), __float_as_uint(__builtin_amdgcn_rcpf(f_of(rec[j][2]))));
      asm volatile("" : "+v"(tq[j].x), "+v"(tq[j].y));
    }
    ta[0] = lds_addr(smem + L_CSF + ((g & 1) * 64 + F * 32 + (g >> 1) * 16 + li) * 8);
    ta[1] = lds_addr(smem + L_QST + (F * 72 + lane) * 8);
    ta[2] = lds_addr(smem + (lane < 8 ? F_SEL + lane * 4 : lane == 8 ? F_SELOK : F_ST + F * 4));
    asm volatile("" : "+v"(ta[0]), "+v"(ta[1]), "+v"(ta[2]));
    SCHED0();
  };
  auto tables = [&](unsigned now) {
    asm volatile("s_and_b32 %0, %0, %8\n\ts_cbranch_scc0 1f\n\tds_write2_b64 %1, %2, %9 offset1:64\n\tds_write2_b64 %3, %4, %5 offset1:8\n\ts_mov_b64 exec, 0x3ff\n\tds_write_b32 %6, %7\n\ts_mov_b64 exec, -1\n\t1:"
                 : "+s"(now) : "v"(ta[0]), "v"(tc[0]), "v"(ta[1]), "v"(tq[0]), "v"(tq[1]), "v"(ta[2]), "v"(tv), "s"(todo), "v"(tc[1]) : "scc", "memory");
    todo &= ~now;
  };
  auto slice = [&]() {
    if (nb < NBAR) tick(nb);
    else { const u32x2 fl = *(const volatile LAS u32x2*)(LAS const char*)(smem + F_FULL); tables(uni(min(fl[0], fl[1])) >> 4); SCHED0(); }
  };
  if (u >= 28) {
    u32x4 kd[4];
#pragma unroll 1
    for (unsigned n = 0; n < SPIN_CAP; ++n) {
#define L1K(i, o) "buffer_load_dwordx4 %" #i ", %4, %5, 0 offen offset:" #o " sc1\n\t"
      asm volatile("s_nop 4\n\t" L1K(0, 0) L1K(1, 1024) L1K(2, 2048) L1K(3, 3072)
                   : "=&v"(kd[0]), "=&v"(kd[1]), "=&v"(kd[2]), "=&v"(kd[3]) : "v"(WS_K + F * 4096 + lane * 16), "s"(wrs) : "memory");
#undef L1K
      tick(nb);
      asm volatile("s_waitcnt vmcnt(0)" : "+v"(kd[0]), "+v"(kd[1]), "+v"(kd[2]), "+v"(kd[3]) :: "memory");
      if (__all(chk(chk(chk(chk(0u, kd[0], tag), kd[1], tag), kd[2], tag), kd[3], tag) == 0)) break;
      NAP(4);
    }
    unsigned m1 = 0, m2 = 0, m3 = 0, m4 = 0;
#pragma unroll
    for (int j = 0; j < 4; ++j) {
      kd[j] = u32x4{(kd[j][0] << 16) | (15 - 4 * j), (kd[j][0] & 0xffff0000u) | (14 - 4 * j), (kd[j][2] << 16) | (13 - 4 * j), (kd[j][2] & 0xffff0000u) | (12 - 4 * j)};
#pragma unroll
      for (int i = 0; i < 4; ++i) {
        const unsigned v = kd[j][i];
        m4 = med3(m3, m4, v); m3 = med3(m2, m3, v); m2 = med3(m1, m2, v); m1 = max(m1, v);
      }
    }
    const unsigned lb = ((63 - lane) << 5) | (F ? 0 : 16);
    m1 |= lb; m2 |= lb; m3 |= lb; m4 |= lb;
    asm volatile("" : "+v"(m1), "+v"(m2), "+v"(m3), "+v"(m4));
    tick(nb);
    unsigned best = 0;
#pragma unroll
    for (int r = 0; r < 8; ++r) {
      const unsigned wm = wave_max_u32(m1);
      best = (lane & 7) == r ? wm : best;
      const bool won = m1 == wm;
      m1 = won ? m2 : m1; m2 = won ? m3 : m2; m3 = won ? m4 : m3; m4 = won ? 0u : m4;
      if (r == 3) { asm volatile("" : "+v"(m1), "+v"(m2), "+v"(m3), "+v"(m4), "+v"(best)); tick(nb); }
    }
    if (__any(m1 == 0)) {
      unsigned wm = 0;
#pragma unroll 1
      for (int r = 0; r < 8; ++r) {
        if (r & 1) tick(nb);
        unsigned m = 0;
#pragma unroll
        for (int j = 0; j < 4; ++j)
#pragma unroll
          for (int i = 0; i < 4; ++i) m = max(m, (kd[j][i] | lb) - wm);
        wm += wave_max_u32(m);
        best = (lane & 7) == r ? wm : best;
      }
    }
    *(volatile LAS unsigned*)(LAS char*)(smem + F_CND + (F ? 60 - (lane & 7) * 4 : (lane & 7) * 4)) = best + 1;
    unsigned d;
    const unsigned ct = 2047u - (best & 0xffffu);
    asm volatile("buffer_load_dword %0, %1, %2, 0 offen\n\tbuffer_load_dword %0, %3, %4, 0 offen sc1" : "=&v"(d)
                 : "v"(lane < 32 ? ct * 2048u + q * 512 + (lane >> 3) * 128 : 0x7fffffffu), "s"(xrs), "v"(lane < 8 ? WS_R + ct * 16 : 0x7fffffffu), "s"(wrs) : "memory");
    gather2();
    unsigned ka, kb;
#pragma unroll 1
    for (unsigned n = 0; n < SPIN_CAP; ++n) {
      tick(nb);
      ka = *(const volatile LAS unsigned*)(LAS const char*)(smem + F_CND + (lane & 7) * 4);
      kb = *(const volatile LAS unsigned*)(LAS const char*)(smem + F_CND + 32 + (lane & 7) * 4);
      if (__all(min(ka, kb) != 0)) break;
      NAP(1);
    }
    const unsigned selkey = max(ka, kb) - 1, tok = 2047u - (selkey & 0xffffu);
    u32x4 xf[2], trec;
#pragma unroll 1
    for (unsigned n = 0; n < SPIN_CAP; ++n) {
      asm volatile("buffer_load_dwordx4 %0, %3, %4, 0 offen\n\tbuffer_load_dwordx4 %1, %3, %4, 0 offen offset:64\n\tbuffer_load_dwordx4 %2, %5, %6, 0 offen sc1"
                   : "=&v"(xf[0]), "=&v"(xf[1]), "=&v"(trec) : "v"(tok * 2048u + q * 512 + (li >> 3) * 256 + g * 16 + F * 128), "s"(xrs), "v"(WS_R + tok * 16), "s"(wrs) : "memory");
      if (n) tick(nb);
      asm volatile("s_waitcnt vmcnt(0)" : "+v"(xf[0]), "+v"(xf[1]), "+v"(trec), "+v"(d) :: "memory"); PING2();
      if (__all(chk(0u, trec, tag) == 0)) break;
      NAP(1);
    }
    const float mb = lo2f(trec[0]), rd = 1.0f / f_of(trec[2]);
#pragma unroll
    for (int s = 0; s < 2; ++s)
#pragma unroll
      for (int j = 0; j < 4; ++j) xf[s][j] = ln2s(xf[s][j], mb, rd);
    st16c(wrs, xs_off + F * 3072, u32x4{xf[0][0], xf[0][1], xf[0][2], tag});
    st16c(wrs, xs_off + F * 3072 + 1024, u32x4{xf[1][0], xf[1][1], xf[1][2], tag});
    st16c(wrs, xs_off + F * 3072 + 2048, u32x4{xf[0][3], xf[1][3], selkey, tag});
    tv = lane < 8 ? selkey : 1u;
    tick(nb);
    asm volatile("s_waitcnt vmcnt(0)\n\tbuffer_load_dword %0, %1, %2, 0 offen sc1" : "=&v"(d) : "v"(lane < 24 ? xs_off + F * 3072 + lane * 112 : 0x7fffffffu), "s"(wrs) : "memory");
    prepare();
    tick(nb); NAP(8);
    asm volatile("buffer_load_dword %0, %1, %2, 0 offen" : "+v"(td) : "v"(wa), "s"(prs) : "memory");
    tick(nb); tick(nb);
    asm volatile("s_waitcnt vmcnt(0)" : "+v"(d), "+v"(td) :: "memory");
    while (nb < NBAR) tick(nb);
    __builtin_amdgcn_s_setprio(1);
    wait_full(smem, F_FULL, 16);
    tables(1);
  } else {
    u32x4 xs[6];
    auto ask = [&]() {
#define X1K(i, o) "buffer_load_dwordx4 %" #i ", %6, %7, 0 offen offset:" #o " sc1\n\t"
#define X2K(i, o) "buffer_load_dwordx4 %" #i ", %6, %7, %8 offen offset:" #o " sc1\n\t"
      asm volatile(X1K(0, 0) X1K(1, 1024) X1K(2, 2048) X1K(3, 3072) X2K(4, 0) X2K(5, 1024)
                   : "=&v"(xs[0]), "=&v"(xs[1]), "=&v"(xs[2]), "=&v"(xs[3]), "=&v"(xs[4]), "=&v"(xs[5]) : "v"(xs_off), "s"(wrs), "s"(4096) : "memory");
      SCHED0();
    };
    auto good = [&]() {
      unsigned bad = xs[0][3] ^ tag;
#pragma unroll
      for (int j = 1; j < 6; ++j) bad = __builtin_amdgcn_bitop3_b32(bad, xs[j][3], tag, 0xF6);
      return __all(bad == 0);
    };
#define PIN6(a) asm volatile("" : "+v"(a[0]), "+v"(a[1]), "+v"(a[2]), "+v"(a[3]), "+v"(a[4]), "+v"(a[5]))
#define WAITS(snap, n) asm volatile("s_waitcnt vmcnt(%2)\n\ts_cmp_lg_u32 %0, %1\n\ts_cbranch_scc1 1f\n\ts_waitcnt vmcnt(0)\n\t1:" : "+s"(iss) : "s"(snap), "i"(n) : "scc", "memory")
    asm volatile("" : "=v"(cs[0]), "=v"(cs[1]), "=v"(cs[2]), "=v"(cs[3]), "=v"(rec[0]), "=v"(rec[1]));
    CH(0, 12 - 2, 3);
    tick(nb);
    CH(0, 12 - 2, 99);
    tick(nb);
    CH(1, 12 - 2, nb);
    tick(nb);
    WAITS(1, 14); PIN8(fw[0]); PIN8(fw[1]);
    tr_all(fw[0]);
    CH(1, 12 - 7, nb);
    tick(nb);
    tr_all(fw[1]);
    CH(1, 12 - 7, nb);
    tick(nb);
    CH(1, 12 - 2, 99);
    tick(nb);
    CH(2, 12 - 2, nb);
    tick(nb);
    WAITS(2, 9); PING2(); PIN8(fw[2]);
    ask();
    prepare();
    unsigned snap = iss;
    asm volatile("" : "+s"(snap));
    CH(2, 3, nb);
    tick(nb);
    WAITS(snap, 9); PIN6(xs);
    if (!good()) {
      CH(2, 0, 99);
#pragma unroll 1
      for (unsigned n = 0; n < SPIN_CAP; ++n) {
        ask(); tick(nb); WAITV(0); PIN6(xs);
        if (good()) break;
        NAP(4);
      }
    }
    tv = lane < 8 ? xs[2][2] : 1u;
    xs[0][3] = xs[2][0]; xs[1][3] = xs[2][1]; xs[3][3] = xs[5][0]; xs[4][3] = xs[5][1];
    PIN6(xs); asm volatile("" : "+v"(tv));
    if (nb == NBAR) { __builtin_amdgcn_s_setprio(1); wait_full(smem, F_FULL, 16); tables(1); }
    f32x4 acc[8];
    auto mma = [&](int s) {
      SCHED0(); __builtin_amdgcn_s_setprio(3); SCHED0();
#pragma unroll
      for (int i = 0; i < 8; ++i) acc[i] = MFMA16(__builtin_bit_cast(bf16x8, fw[s][i]), __builtin_bit_cast(bf16x8, xs[s + (s >> 1)]), s ? acc[i] : (f32x4{0, 0, 0, 0}));
      SCHED0(); __builtin_amdgcn_s_setprio(0); SCHED0();
      PIN8(acc);
    };
    mma(0);
    tr_all(fw[2]);
    CH(2, 12 - 9, 99);
    slice();
    mma(1);
    mma(2);
    slice();
    WAITV(0); PIN8(fw[3]); asm volatile("" : "+v"(td));
    tr_all(fw[3]);
    mma(3);
    {
#pragma unroll
      for (int r = 0; r < 4; ++r) {
        float v[2];
#pragma unroll
        for (int z = 0; z < 2; ++z) {
          auto a = __builtin_amdgcn_permlane32_swap(__float_as_uint(acc[z][r]), __float_as_uint(acc[z + 4][r]), false, false);
          auto b = __builtin_amdgcn_permlane32_swap(__float_as_uint(acc[z + 2][r]), __float_as_uint(acc[z + 6][r]), false, false);
          v[z] = f_of(li < 8 ? a[0] : b[1]) + dpp_f<0x128>(f_of(li < 8 ? b[0] : a[1]));
        }
        st16c(wrs, WS_GU + (unsigned)(((cu * 4 + q) * 2 + F) * 4096 + r * 1024 + lane * 16), u32x4{__float_as_uint(v[0]), tag, __float_as_uint(v[1]), tag});
      }
      SCHED0();
    }
    while (nb < NBAR) tick(nb);
    __builtin_amdgcn_s_setprio(1);
    u32x4 fd[2][8];
    {
      const rsrc_t drs = mk_rsrc(wd, DM * FF * 2);
      const unsigned dof = (unsigned)(cg * 64 + 8 * g) * (DM * 2) + q * 512 + F * 256 + li * 16;
      NAP(4);
#pragma unroll
      for (int s = 0; s < 2; ++s)
#pragma unroll
        for (int j = 0; j < 8; ++j) fd[s][j] = __builtin_amdgcn_raw_buffer_load_b128(drs, dof + (j & 1) * (DM * 2), (s * 32 + (j & 6)) * (DM * 2), 2);
      SCHED0();
    }
    wait_full(smem, F_FULL, 16);
    tables(1);
    if (bid == 0 && F == 0 && lane == 0) __hip_atomic_store((GAS unsigned*)(ws + WS_EP), tag, RLX_AGENT);
    hidden_half(smem, lane, F, cg, ws, tag, fd);
    wait_bytes<0x8080u>(smem, F_H, 1);
    const bf16x8 h0 = lds_b128(smem + L_HF + (li & 7) * 128 + g * 16), h1 = lds_b128(smem + L_HF + (li & 7) * 128 + 64 + g * 16);
#pragma unroll
    for (int i = 0; i < 8; ++i) {
      acc[i] = MFMA16(h0, __builtin_bit_cast(bf16x8, fd[0][i]), (f32x4{0, 0, 0, 0}));
      acc[i] = MFMA16(h1, __builtin_bit_cast(bf16x8, fd[1][i]), acc[i]);
    }
    const unsigned off = WS_Y + (unsigned)((cg * 8 + (g & 1) * 4) * DM + q * 256 + F * 128 + li * 8 + (g >> 1) * 4) * 4u;
#pragma unroll
    for (int r = 0; r < 4; ++r) {
      const unsigned lo = pack2(acc[0][r], acc[1][r]), hi = pack2(acc[2][r], acc[3][r]), lo2 = pack2(acc[4][r], acc[5][r]), hi2 = pack2(acc[6][r], acc[7][r]);
      st16c(wrs, off + r * (DM * 4), u32x4{g < 2 ? lo : lo2, tag, g < 2 ? hi : hi2, tag});
    }
  }
  __builtin_amdgcn_s_waitcnt(0x0F70);
}

__global__ __launch_bounds__(512, 2) void fused_block(const u16* __restrict__ x, const u16* __restrict__ wqkv, const u16* __restrict__ wo,
                                                      const u16* __restrict__ wr, const u16* __restrict__ wg, const u16* __restrict__ wu,
                                                      const u16* __restrict__ wd, char* ws, u16* __restrict__ out) {
  __shared__ __attribute__((aligned(128))) char smem[L_SIZE];
  unsigned long long pc;
  unsigned first;
  asm volatile("v_cmp_gt_u32 vcc, 24, %2\n\ts_getpc_b64 %0\n\ts_cbranch_vccz 1f\n\ts_mov_b64 exec, vcc\n\tv_lshlrev_b32 %1, 7, %2\n\tglobal_load_dword %1, %1, %0\n\ts_mov_b64 exec, -1\n\t1:"
               : "=s"(pc), "=&v"(first) : "v"(threadIdx.x) : "vcc", "memory");
  SCHED0();
  const int lane = threadIdx.x & 63;
  const int wid = __builtin_amdgcn_readfirstlane(threadIdx.x >> 6);
  const int bid = blockIdx.x, xcd = bid & 7, off = bid >> 3;
  const int tm = (xcd & 1) * 8 + (xcd & 2) * 2 + (off & 3), tn = (xcd >> 2) * 8 + (off >> 2);
  if (wid == 7) {
    unsigned d;
    asm volatile("s_getpc_b64 %0" : "=s"(pc));
    asm volatile("buffer_load_dword %0, %1, %2, 0 offen\n\ts_waitcnt vmcnt(0)" : "=&v"(d)
                 : "v"((unsigned)(lane * 32 + off) * 128u), "s"(mk_rsrc((const void*)(pc & ~255ull), CODE_BYTES)) : "memory");
  }
  asm volatile("" : "+s"(x), "+s"(wqkv), "+s"(wr), "+s"(ws), "+s"(out), "+s"(wo), "+s"(wd), "+s"(wg), "+s"(wu));
  unsigned rot2 = ((((unsigned)(0x65432106543210ull >> (off & 3) * 8) & 0xfffffffu) + ((tn & 7) + 1) * 0x1111111u) & 0x7777777u) * 2 | (unsigned)tn << 29;
  unsigned tag = 0;
  RowRegs rr;
  EpiRegs e;
  row_request(rr, x, wr, ws, tm * 128 + tn * 8 + wid * 2, lane, wid < 4);
  rr.keep = first;
  SCHED0();
  if ((wid >> 1) == 2) {
    __builtin_amdgcn_s_setprio(2);
    const Stager stage(smem, wid - 4, lane, tm, tn, x, wqkv);
    NAP(4);
    stage_x(stage, 0); stage_w(stage, 0); stage_x(stage, 1); stage_w(stage, 1);
    WAITV(21);
    BAR();
    stage_x(stage, 2); stage_w(stage, 2);
  }
  asm volatile("" ::: "memory");
  if (wid < 4) {
    __builtin_amdgcn_s_setprio(2);
    qkv_setup(smem, wid, lane, tm, tn, e);
    asm volatile("" : "+s"(rot2));
    SCHED0();
    tag = row_stats(rr, ws, tm * 128 + tn * 8 + wid * 2, lane);
    if (wid == 0) *(unsigned*)(smem + L_FLG + lane * 4) = lane * 4 == F_TAG - L_FLG ? tag : 0u;
  }
  asm volatile("" ::: "memory");
  if (wid < 4) {
    if (wid & 1) qkv_attention<1>(smem, wid, lane, tm, tn, ws, tag, x, e);
    else qkv_attention<0>(smem, wid, lane, tm, tn, ws, tag, x, e);
    out_proj(smem, wid, lane, tm, tn, ws, tag, out, e, rot2);
  } else {
    if (wid < 6) loader(smem, wid - 4, lane, tm, tn, rot2, x, wqkv, wo, wg, wu, wd, bid, ws);
    else side_wave(smem, wid - 6, lane, bid, tm, tn, x, wid == 6 ? wg : wu, wd, wo, ws);
    updates(smem, (0x2130 >> ((wid - 4) * 4)) & 3, lane, tm, tn, ws);
    __builtin_amdgcn_s_waitcnt(0x0F70);
  }
}

extern "C" void kernel_launch(void* const* d_in, const int* in_sizes, int n_in, void* d_out, int out_size, void* d_ws,
                              size_t ws_size, hipStream_t stream) {
  fused_block<<<256, 512, 0, stream>>>((const u16*)d_in[0], (const u16*)d_in[1], (const u16*)d_in[2], (const u16*)d_in[4],
                                       (const u16*)d_in[5], (const u16*)d_in[6], (const u16*)d_in[7], (char*)d_ws, (u16*)d_out);
}
```

```cpp
#include <hip/hip_runtime.h>
#include <hip/hip_bf16.h>
#include <stdint.h>

typedef __attribute__((ext_vector_type(8))) short bf16x8;
typedef __attribute__((ext_vector_type(4))) short s16x4;
typedef __attribute__((ext_vector_type(4))) float f32x4;
typedef __attribute__((ext_vector_type(2))) float f32x2;
typedef __attribute__((ext_vector_type(2))) __bf16 bf16x2_t;
typedef __attribute__((ext_vector_type(4))) unsigned u32x4;
typedef unsigned short u16;
#define LAS __attribute__((address_space(3)))
#define GAS __attribute__((address_space(1)))
#define RLX_AGENT __ATOMIC_RELAXED, __HIP_MEMORY_SCOPE_AGENT

#define S_TOK 2048
#define DM 1024
#define NQKV 3072
#define FF 3584

typedef __attribute__((ext_vector_type(2))) unsigned u32x2;

#define WS_EP   0
#define WS_R    4096
#define WS_K    (WS_R + 32768)
#define WS_OF   (WS_K + 8192)
#define WS_CS   (WS_OF + 4096)
#define WS_GU   (WS_CS + 16 * 2 * 4096)
#define WS_Y    (WS_GU + 56 * 4 * 8192)
#define WS_S    (WS_Y + 56 * 8 * DM * 4)
#define WS_O    (8u << 20)

__device__ const unsigned k_tags[32] = {0,  1,  2,  3,  4,  5,  6,  7,  8,  9,  10, 11, 12, 13, 14, 15,
                                        16, 17, 18, 19, 20, 21, 22, 23, 24, 25, 26, 27, 28, 29, 30, 31};

__device__ __forceinline__ unsigned pack2(float a, float b) {
  f32x2 v = {a, b};
  bf16x2_t r = __builtin_convertvector(v, bf16x2_t);
  return __builtin_bit_cast(unsigned, r);
}
__device__ __forceinline__ float rbf(float f) { return __uint_as_float(pack2(f, f) << 16); }
__device__ __forceinline__ float lo2f(unsigned w) { return __uint_as_float(w << 16); }
__device__ __forceinline__ float hi2f(unsigned w) { return __uint_as_float(w & 0xffff0000u); }

__device__ __forceinline__ bf16x8 lds_b128(const char* p) { return *(const bf16x8*)p; }
__device__ __forceinline__ bf16x8 lds_tr2(const char* p) {
  s16x4 lo = __builtin_amdgcn_ds_read_tr16_b64_v4i16((LAS s16x4*)p);
  s16x4 hi = __builtin_amdgcn_ds_read_tr16_b64_v4i16((LAS s16x4*)(p + 512));
  return __builtin_shufflevector(lo, hi, 0, 1, 2, 3, 4, 5, 6, 7);
}
#define MFMA16(a, b, c) __builtin_amdgcn_mfma_f32_16x16x32_bf16(a, b, c, 0, 0, 0)
#define WAITV(n) asm volatile("s_waitcnt vmcnt(%0)" ::"i"(n) : "memory")
#define WAITL0() __builtin_amdgcn_s_waitcnt(0xC07F)
#define BAR() __builtin_amdgcn_s_barrier()
#define SCHED0() __builtin_amdgcn_sched_barrier(0)
#define NAP(n) asm volatile("s_sleep %0" ::"i"(n) : "memory")
__device__ __forceinline__ float xsum16_32(float v) {
  unsigned u = __float_as_uint(v);
  auto a = __builtin_amdgcn_permlane16_swap(u, u, false, false);
  v = __uint_as_float(a[0]) + __uint_as_float(a[1]);
  u = __float_as_uint(v);
  auto b = __builtin_amdgcn_permlane32_swap(u, u, false, false);
  return __uint_as_float(b[0]) + __uint_as_float(b[1]);
}
__device__ __forceinline__ bf16x8 pack8(const f32x4& a, const f32x4& b) {
  u32x4 r = {pack2(a[0], a[1]), pack2(a[2], a[3]), pack2(b[0], b[1]), pack2(b[2], b[3])};
  return __builtin_bit_cast(bf16x8, r);
}
__device__ __forceinline__ unsigned wave_max_u32(unsigned v) {
  v = max(v, (unsigned)__builtin_amdgcn_update_dpp(0, (int)v, 0x111, 0xf, 0xf, false));
  v = max(v, (unsigned)__builtin_amdgcn_update_dpp(0, (int)v, 0x112, 0xf, 0xf, false));
  v = max(v, (unsigned)__builtin_amdgcn_update_dpp(0, (int)v, 0x114, 0xf, 0xf, false));
  v = max(v, (unsigned)__builtin_amdgcn_update_dpp(0, (int)v, 0x118, 0xf, 0xf, false));
  v = max(v, (unsigned)__builtin_amdgcn_update_dpp(0, (int)v, 0x142, 0xa, 0xf, false));
  v = max(v, (unsigned)__builtin_amdgcn_update_dpp(0, (int)v, 0x143, 0xc, 0xf, false));
  return (unsigned)__builtin_amdgcn_readlane((int)v, 63);
}
__device__ __forceinline__ bf16x8 tr_frag(const u32x4 (&w)[8], int i) {
  const unsigned sel = (i & 1) ? 0x07060302u : 0x05040100u;
  u32x4 r = {__builtin_amdgcn_perm(w[1][i >> 1], w[0][i >> 1], sel), __builtin_amdgcn_perm(w[3][i >> 1], w[2][i >> 1], sel),
             __builtin_amdgcn_perm(w[5][i >> 1], w[4][i >> 1], sel), __builtin_amdgcn_perm(w[7][i >> 1], w[6][i >> 1], sel)};
  return __builtin_bit_cast(bf16x8, r);
}

#define XSL_ 18432
#define WSL 24576
#define XB (3 * XSL_)
#define NBAR 14
#define L_UP 0
#define L_WA 6144
#define L_W3 (L_WA + 57344)
#define L_WB (L_W3 + 16384)
#define L_OWN (L_WB + 57344)
#define L_XCH (XB + 4 * WSL)
#define L_HF (L_XCH + 6144)
#define L_CSF (L_HF + 1024)
#define L_QST (L_CSF + 1536)
#define L_FLG (L_QST + 1152)
#define L_SIZE (L_FLG + 256)
#define F_FULL (L_FLG + 0)
#define F_ST (L_FLG + 8)
#define F_WO (L_FLG + 16)
#define F_FREE (L_FLG + 32)
#define F_XCH (L_FLG + 40)
#define F_ODONE (L_FLG + 48)
#define F_SELOK (L_FLG + 52)
#define F_GO (L_FLG + 56)
#define F_OL (L_FLG + 60)
#define F_H (L_FLG + 64)
#define F_TAG (L_FLG + 68)
#define F_UP (L_FLG + 72)
#define F_SEL (L_FLG + 96)
#define F_CND (L_FLG + 128)
#define OD 7
#define SPIN_CAP (1u << 18)
#define NB1 6
#define CODE_BYTES 54528

__device__ __forceinline__ unsigned lds_addr(const char* p) { return (unsigned)(size_t)(LAS const char*)p; }
__device__ __forceinline__ void lds_post(char* smem, int off, unsigned v) {
  asm volatile("s_mov_b64 exec, 1\n\tds_write_b8 %0, %1\n\ts_mov_b64 exec, -1" ::"v"(lds_addr(smem + off)), "v"(v) : "memory");
}
__device__ __forceinline__ unsigned lds_word(const char* smem, int off) { return *(const volatile LAS unsigned*)(LAS const char*)(smem + off); }
__device__ __forceinline__ u32x4 lds_word4(const char* smem, int off) { return *(const volatile LAS u32x4*)(LAS const char*)(smem + off); }
template <unsigned MASK, bool COND = false>
__device__ __forceinline__ unsigned wait_bytes(const char* smem, int off, unsigned need, unsigned skip = 0) {
  unsigned t, f;
  asm volatile(
      ".if %6\n\ts_cmp_lg_u32 %5, 0\n\ts_cbranch_scc1 2f\n\t.endif\n\t"
      "1:\n\t"
      "ds_read_b32 %1, %2\n\t"
      "s_waitcnt lgkmcnt(0)\n\t"
      "v_readfirstlane_b32 %0, %1\n\t"
      "s_or_b32 %0, %0, 0x80808080\n\t"
      "s_sub_u32 %0, %0, %3\n\t"
      "s_and_b32 %0, %0, %4\n\t"
      "s_cmp_eq_u32 %0, %4\n\t"
      "s_cbranch_scc1 2f\n\t"
      "s_sleep 1\n\t"
      "s_branch 1b\n\t"
      "2:"
      : "=&s"(t), "=&v"(f)
      : "v"(lds_addr(smem + off)), "s"(need * 0x01010101u), "s"(MASK), "s"(COND ? skip : MASK), "i"((int)COND)
      : "scc", "memory");
  return f;
}
__device__ __forceinline__ void wait_full(const char* smem, int off, unsigned need, unsigned cur = 0) {
  unsigned a, b;
  asm volatile(
      "s_cmp_ge_u32 %0, %4\n\t"
      "s_cbranch_scc1 3f\n\t"
      "s_branch 2f\n\t"
      "1:\n\t"
      "s_sleep 1\n\t"
      "2:\n\t"
      "ds_read_b32 %1, %3\n\t"
      "ds_read_b32 %2, %3 offset:4\n\t"
      "s_waitcnt lgkmcnt(0)\n\t"
      "v_min_u32 %1, %1, %2\n\t"
      "s_nop 0\n\t"
      "v_readfirstlane_b32 %0, %1\n\t"
      "s_cmp_lt_u32 %0, %4\n\t"
      "s_cbranch_scc1 1b\n\t"
      "3:"
      : "+s"(cur), "=&v"(a), "=&v"(b)
      : "v"(lds_addr(smem + off)), "s"(need)
      : "scc", "memory");
}
__device__ __forceinline__ unsigned uni(unsigned v) { return (unsigned)__builtin_amdgcn_readfirstlane((int)v); }
__device__ __forceinline__ void dma_tag(char* smem, int off, int n) {
  unsigned keep;
  asm volatile("s_nop 2\n\ts_mov_b32 %0, m0\n\ts_mov_b32 m0, %1\n\ts_mov_b64 exec, 1\n\tglobal_load_lds_dword %2, %3\n\ts_mov_b64 exec, -1\n\ts_mov_b32 m0, %0\n\ts_nop 0"
               : "=&s"(keep) : "s"(uni(lds_addr(smem + off))), "v"(n * 4), "s"(k_tags) : "memory");
}
template <int V> struct IC { static constexpr int v = V; };
struct QFrag { bf16x8 xa[5]; bf16x8 w[3][2]; };
__device__ __forceinline__ void q_read(QFrag& f, const char* sa, const char* sb0, const char* sb1, int ks) {
#pragma unroll
  for (int i = 0; i < 5; ++i) f.xa[i] = lds_b128(sa + i * 2048);
#pragma unroll
  for (int s = 0; s < 3; ++s) {
    f.w[s][0] = lds_tr2(sb0 + s * 8192 + ks * 4096);
    f.w[s][1] = lds_tr2(sb1 + s * 8192 + ks * 4096);
  }
}
__device__ constexpr int acc_no(int HALF, int m, int i, int t) {
  int n = 0;
  for (int ii = 0; ii < 5; ++ii)
    for (int tt = 0; tt < 2; ++tt)
      for (int mm = 0; mm < 3; ++mm) {
        const bool on = mm == 0 ? (HALF == 1 || (ii >= 1 && ii <= 3)) : (HALF == 0 || ii >= 1);
        if (mm == m && ii == i && tt == t) return on ? n : -1;
        if (on) ++n;
      }
  return -1;
}
__device__ constexpr int seq_of(int HALF, int z) {
  int n = 0;
  for (int a = 0; a < 30; ++a) {
    const int m = a / 10, t0 = (a / 5) % 2;
    const int i = m < 2 ? (t0 ? a % 5 : 4 - a % 5) : 4 - (a - 20) / 2, t = m < 2 ? t0 : (a ^ i) & 1;
    if (acc_no(HALF, m, i, t) >= 0) { if (n == z) return m * 100 + i * 10 + t; ++n; }
  }
  return -1;
}
template <int HALF, int Z, int E, bool FIRST>
__device__ __forceinline__ void q_one(const QFrag& f, f32x4 (&acc)[26]) {
  if constexpr (Z < E) {
    constexpr int m = seq_of(HALF, Z) / 100, i = seq_of(HALF, Z) / 10 % 10, t = seq_of(HALF, Z) % 10;
    constexpr int n = acc_no(HALF, m, i, t);
    const f32x4 c = FIRST ? f32x4{0, 0, 0, 0} : acc[n];
    acc[n] = m == 2 ? MFMA16(f.xa[i], f.w[2][t], c) : MFMA16(f.w[m][t], f.xa[i], c);
    q_one<HALF, Z + 1, E, FIRST>(f, acc);
  }
}
template <int HALF, int B, int E, int NRD, bool FIRST = false>
__device__ __forceinline__ void q_mma(const QFrag& f, f32x4 (&acc)[26]) {
  q_one<HALF, B, E, FIRST>(f, acc);
#pragma unroll
  for (int i = 0; i < NRD; ++i) {
    __builtin_amdgcn_sched_group_barrier(0x008, 1, 0);
    __builtin_amdgcn_sched_group_barrier(0x100, 1, 0);
  }
  __builtin_amdgcn_sched_group_barrier(0x008, E - B - NRD, 0);
}

__device__ __forceinline__ void tr_all(u32x4 (&w)[8]) {
  u32x4 t[8];
#pragma unroll
  for (int i = 0; i < 8; ++i) t[i] = __builtin_bit_cast(u32x4, tr_frag(w, i));
#pragma unroll
  for (int i = 0; i < 8; ++i) w[i] = t[i];
  asm volatile("" : "+v"(w[0]), "+v"(w[1]), "+v"(w[2]), "+v"(w[3]), "+v"(w[4]), "+v"(w[5]), "+v"(w[6]), "+v"(w[7]));
}
typedef __amdgpu_buffer_rsrc_t rsrc_t;
__device__ __forceinline__ rsrc_t mk_rsrc(const void* p, unsigned bytes) { return __builtin_amdgcn_make_buffer_rsrc((void*)p, 0, bytes, 0x00020000); }
__device__ __forceinline__ u32x4 ld16(rsrc_t rs, unsigned voff, unsigned soff) { return __builtin_amdgcn_raw_buffer_load_b128(rs, voff, soff, 0); }
__device__ __forceinline__ u32x4 ld16c(rsrc_t rs, unsigned voff, unsigned soff) { return __builtin_amdgcn_raw_buffer_load_b128(rs, voff, soff, 16); }
__device__ __forceinline__ void st16c(rsrc_t rs, unsigned voff, u32x4 v) { __builtin_amdgcn_raw_buffer_store_b128(v, rs, voff, 0, 16); }
__device__ __forceinline__ void st8c(rsrc_t rs, unsigned voff, unsigned v, unsigned tag) { __builtin_amdgcn_raw_buffer_store_b64(u32x2{v, tag}, rs, voff, 0, 16); }
__device__ __forceinline__ unsigned chk(unsigned bad, const u32x4& v, unsigned tag) {
  return __builtin_amdgcn_bitop3_b32(__builtin_amdgcn_bitop3_b32(bad, v[1], tag, 0xF6), v[3], tag, 0xF6);
}
__device__ __forceinline__ float f_of(unsigned u) { return __uint_as_float(u); }
#define LANE_VARS                                                                   \
  int lane = lane_in; asm volatile("" : "+v"(lane));                                \
  const int g = lane >> 4, r16 = lane & 15, q = (lane >> 2) & 3, p = lane & 3;      \
  const int hk = (q >> 1) | ((g & 1) << 1), brow = (8 * g + q) * 128 + 8 * p;       \
  (void)r16; (void)hk; (void)brow;

template <bool MAX>
__device__ __forceinline__ float rows_reduce(const float (&v)[4]) {
  auto op = [](unsigned a, unsigned b) {
    return __float_as_uint(MAX ? __builtin_elementwise_maximum(__uint_as_float(a), __uint_as_float(b)) : __uint_as_float(a) + __uint_as_float(b));
  };
  auto p = __builtin_amdgcn_permlane16_swap(__float_as_uint(v[0]), __float_as_uint(v[1]), false, false);
  auto q = __builtin_amdgcn_permlane16_swap(__float_as_uint(v[2]), __float_as_uint(v[3]), false, false);
  auto t = __builtin_amdgcn_permlane32_swap(op(p[0], p[1]), op(q[0], q[1]), false, false);
  return __uint_as_float(op(t[0], t[1]));
}
__device__ __forceinline__ void rows_spread(float t, float (&v)[4]) {
  auto h = __builtin_amdgcn_permlane32_swap(__float_as_uint(t), __float_as_uint(t), false, false);
  auto p = __builtin_amdgcn_permlane16_swap(h[0], h[0], false, false);
  auto q = __builtin_amdgcn_permlane16_swap(h[1], h[1], false, false);
  v[0] = __uint_as_float(p[0]); v[1] = __uint_as_float(p[1]); v[2] = __uint_as_float(q[0]); v[3] = __uint_as_float(q[1]);
}
typedef __attribute__((ext_vector_type(16))) unsigned u32x16;

template <int CTL>
__device__ __forceinline__ float dpp_f(float v) { return __uint_as_float(__builtin_amdgcn_update_dpp(0, (int)__float_as_uint(v), CTL, 0xf, 0xf, false)); }
__device__ __forceinline__ float sum16(float v) {
  v += dpp_f<0xB1>(v);
  v += dpp_f<0x4E>(v);
  v += dpp_f<0x141>(v);
  return v + dpp_f<0x140>(v);
}
__device__ __forceinline__ float dot2(unsigned a, unsigned b, float c) {
  return __builtin_amdgcn_fdot2_f32_bf16(__builtin_bit_cast(bf16x2_t, a), __builtin_bit_cast(bf16x2_t, b), c, false);
}
__device__ __forceinline__ float sum32(float v) {
  v = sum16(v);
  auto a = __builtin_amdgcn_permlane16_swap(__float_as_uint(v), __float_as_uint(v), false, false);
  return __uint_as_float(a[0]) + __uint_as_float(a[1]);
}
struct RowRegs { u32x4 x[4], w[2]; unsigned ep, keep; };
__device__ __forceinline__ void row_request(RowRegs& r, const u16* __restrict__ x, const u16* __restrict__ wr, char* ws, int row0, int lane, bool on) {
  if (!on) return;
  const rsrc_t xrs = mk_rsrc(x, S_TOK * DM * 2), rrs = mk_rsrc(wr, DM * 2);
  const unsigned xo = (unsigned)(row0 + (lane >> 5)) * 2048u + (lane & 31) * 16, wo = lane * 16;
  asm volatile("buffer_load_dword %0, off, %1, 0 sc1" : "=v"(r.ep) : "s"(mk_rsrc(ws + WS_EP, 4)) : "memory");
#pragma unroll
  for (int j = 0; j < 4; ++j) asm volatile("buffer_load_dwordx4 %0, %1, %2, 0 offen offset:%3" : "=v"(r.x[j]) : "v"(xo), "s"(xrs), "i"(j * 512) : "memory");
#pragma unroll
  for (int j = 0; j < 2; ++j) asm volatile("buffer_load_dwordx4 %0, %1, %2, 0 offen offset:%3" : "=v"(r.w[j]) : "v"(wo), "s"(rrs), "i"(j * 1024) : "memory");
}
__device__ __forceinline__ unsigned row_stats(RowRegs& r, char* ws, int row0, int lane) {
  float s0 = 0.f, s1 = 0.f;
  asm volatile("s_waitcnt vmcnt(2)" : "+v"(r.x[0]), "+v"(r.x[1]), "+v"(r.x[2]), "+v"(r.x[3]), "+v"(r.ep), "+v"(r.keep) :: "memory");
  const unsigned tag = uni(r.ep) + 1u;
#pragma unroll
  for (int j = 0; j < 4; ++j) {
    s0 = dot2(r.x[j][0], 0x3f803f80u, s0); s1 = dot2(r.x[j][1], 0x3f803f80u, s1);
    s0 = dot2(r.x[j][2], 0x3f803f80u, s0); s1 = dot2(r.x[j][3], 0x3f803f80u, s1);
  }
  const float mean = sum32(s0 + s1) * (1.0f / DM), mb = rbf(mean);
  s0 = 0.f; s1 = 0.f;
#pragma unroll
  for (int j = 0; j < 4; ++j)
#pragma unroll
    for (int i = 0; i < 4; ++i) {
      const float a = lo2f(r.x[j][i]) - mb, b = hi2f(r.x[j][i]) - mb;
      s0 = fmaf(a, a, s0); s1 = fmaf(b, b, s1);
      r.x[j][i] = pack2(a, b);
    }
  const float dm = mean - mb;
  const float var = rbf(fmaxf(sum32(s0 + s1) * (1.0f / DM) - dm * dm, 0.f));
  const float den = rbf(__builtin_amdgcn_sqrtf(rbf(var + 1e-5f))), rd = __builtin_amdgcn_rcpf(den);
  s0 = 0.f; s1 = 0.f;
  asm volatile("s_waitcnt vmcnt(0)" : "+v"(r.w[0]), "+v"(r.w[1]) :: "memory");
#pragma unroll
  for (int j = 0; j < 4; ++j)
#pragma unroll
    for (int i = 0; i < 4; ++i) {
      const unsigned n = pack2(lo2f(r.x[j][i]) * rd, hi2f(r.x[j][i]) * rd);
      const unsigned w = __builtin_amdgcn_permlane32_swap(r.w[j >> 1][i], r.w[j >> 1][i], false, false)[j & 1];
      if (i & 1) s1 = dot2(n, w, s1); else s0 = dot2(n, w, s0);
    }
  const float dot = sum32(s0 + s1);
  unsigned k = pack2(dot, dot) & 0xffffu;
  k = (k & 0x8000u) ? (~k & 0xffffu) : (k | 0x8000u);
  const rsrc_t wrs = mk_rsrc(ws, WS_O);
  st16c(wrs, (lane & 31) == 0 ? WS_R + (unsigned)(row0 + (lane >> 5)) * 16u : 0x7fffffffu,
        u32x4{(k << 16) | (__float_as_uint(mb) >> 16), tag, __float_as_uint(den), tag});
  const unsigned k01 = k | ((unsigned)__builtin_amdgcn_readlane((int)k, 32) << 16);
  st8c(wrs, lane == 0 ? WS_K + (unsigned)(((row0 >> 2) & 7) * 1024 + (row0 >> 5) * 16 + (row0 & 2) * 4) : 0x7fffffffu, k01, tag);
  return tag;
}
__device__ __forceinline__ void k_order(int tm, int tn, int& pa, int& pb) {
  pb = tn & 3;
  pa = (tm * 0x1111) ^ ((tn & 4) * 0x51 & 0x144) ^ ((~tn & 2) * 0x2200) ^ ((~tn & 1) << 9) ^ (pb == 3 ? 1 : tm & 1);
}
__device__ __forceinline__ int k_tile(int t, int pa, int pb) { return ((((t + pb) & 12) ? t + pb : t & 3) ^ (pa >> ((t + pb) & 12))) & 15; }
struct EpiRegs { int a0, b0, tx; unsigned xr, go; u32x4 xv[2][2]; };
__device__ __forceinline__ float opq(float v) { asm("" : "+v"(v)); return v; }
typedef __attribute__((ext_vector_type(10))) unsigned u32x10;
#define GRP(N, M, K, C) _Pragma("unroll") for (int z_ = 0; z_ < (N); ++z_) { __builtin_amdgcn_sched_group_barrier(0x008, M, 0); __builtin_amdgcn_sched_group_barrier(K, C, 0); }
__device__ constexpr int tail_of(int HALF, int z) {
  int n = 0;
  for (int a = 0; a < 30; ++a) {
    const int i = HALF ? a / 6 : 4 - a / 6, m = 2 - a % 6 / 2, t = a & 1;
    if (acc_no(HALF, m, i, t) >= 0 && (m < 2 || (HALF == 0 && i == 4))) { if (n == z) return m * 100 + i * 10 + t; ++n; }
  }
  return -1;
}
__device__ __forceinline__ void qkv_setup(char* smem, int wid, int lane_in, int tm, int tn, EpiRegs& e) {
  LANE_VARS
  e.a0 = (wid & 1) * 8192 + r16 * 128 + ((g ^ (r16 >> 1)) << 4);
  e.b0 = XB + brow + (((wid & 2) ^ hk) << 5);
  int pa, pb;
  k_order(tm, tn, pa, pb);
  e.tx = __builtin_ctz((unsigned)__ballot(k_tile(r16, pa, pb) == tn));
  e.xr = lds_addr(smem) + (16 + wid * 32 + r16) * 128 + ((((g & 1) * 2 + (g >> 1)) ^ (r16 >> 1)) << 4);
  asm volatile("" : "+v"(e.a0), "+v"(e.b0), "+v"(e.xr));
  asm volatile("" : "+s"(e.tx));
}
template <int HALF>
__device__ __forceinline__ void qkv_attention(char* smem, int wid, int lane_in, int tm, int tn, char* ws, unsigned tag,
                                              const u16* __restrict__ x, EpiRegs& e) {
  const int h = wid >> 1;
  f32x4 acc[26];
  float ra[5], rb[5], sa[4], sb[4];
  QFrag f0, f1;
  u32x4 fl;
  unsigned aq, ac, aa, ab, xa, xv, xf, ooff, sel[4];
  {
    LANE_VARS
    const int a0 = e.a0, b0 = e.b0, tx = uni(e.tx);
    const unsigned xr = e.xr;
    asm volatile("" : "=v"(e.xv[0][0]), "=v"(e.xv[0][1]), "=v"(e.xv[1][0]), "=v"(e.xv[1][1]));
    auto keep = [&](int t, int slot, int tx) {
      unsigned p0, p1;
      asm volatile("s_cmp_lg_u32 %7, %8\n\ts_cbranch_scc1 1f\n\tv_add_u32 %4, %9, %6\n\tv_xor_b32 %5, 64, %4\n\tds_read_b128 %0, %4\n\tds_read_b128 %1, %5\n\t"
                   "ds_read_b128 %2, %4 offset:2048\n\tds_read_b128 %3, %5 offset:2048\n\t1:"
                   : "+v"(e.xv[0][0]), "+v"(e.xv[0][1]), "+v"(e.xv[1][0]), "+v"(e.xv[1][1]), "=&v"(p0), "=&v"(p1) : "v"(xr), "s"(t), "s"(tx), "s"(slot) : "scc", "memory");
    };
    SCHED0();
    WAITL0();
    BAR();
    q_read(f0, smem + a0, smem + b0, smem + (b0 ^ 32), 0);
    SCHED0();
    q_read(f1, smem + (a0 ^ 64), smem + b0, smem + (b0 ^ 32), 1);
    int qa = XSL_ + a0, qc = XSL_ + (a0 ^ 64), qb = WSL + b0, qd = WSL + (b0 ^ 32), so = XSL_;
    q_mma<HALF, 0, 26, 17, true>(f0, acc);
    asm volatile("" : "+v"(qa), "+v"(qb), "+v"(qc), "+v"(qd));
    SCHED0();
    keep(0, 0, tx);
#pragma unroll 1
    for (int t = 1; t < NBAR - 1; ) {
      SCHED0();
      WAITL0();
      BAR();
      SCHED0();
      q_read(f0, smem + qa, smem + qb, smem + qd, 0);
      q_mma<HALF, 0, 26, 17>(f1, acc);
      SCHED0();
      q_read(f1, smem + qc, smem + qb, smem + qd, 1);
      q_mma<HALF, 0, 19, 17>(f0, acc);
      SCHED0();
      int d = so == 2 * XSL_ ? -2 * XSL_ : XSL_;
      asm volatile("" : "+s"(d));
      SCHED0();
      q_mma<HALF, 19, 20, 0>(f0, acc);
      SCHED0();
      keep(t, so, tx);
      SCHED0();
      q_mma<HALF, 20, 21, 0>(f0, acc);
      SCHED0(); qa += d; qc += d; asm volatile("" : "+v"(qa), "+v"(qc)); SCHED0();
      q_mma<HALF, 21, 22, 0>(f0, acc);
      SCHED0(); so += d; ++t; asm volatile("" : "+s"(so), "+s"(t)); SCHED0();
      q_mma<HALF, 22, 23, 0>(f0, acc);
      SCHED0(); int e = (t & 3) ? WSL : -3 * WSL; asm volatile("" : "+s"(e)); SCHED0();
      q_mma<HALF, 23, 24, 0>(f0, acc);
      SCHED0(); qb += e; qd += e; asm volatile("" : "+v"(qb), "+v"(qd)); SCHED0();
      q_mma<HALF, 24, 26, 0>(f0, acc);
      SCHED0();
    }
    WAITL0();
    BAR();
#pragma unroll 1
    for (int t = NBAR + 1; t < 17; ) {
      SCHED0();
      q_read(f0, smem + qa, smem + qb, smem + qd, 0);
      q_mma<HALF, 0, 26, 17>(f1, acc);
      SCHED0();
      q_read(f1, smem + qc, smem + qb, smem + qd, 1);
      const u32x4 fl = lds_word4(smem, F_FULL);
      q_mma<HALF, 0, 21, 18>(f0, acc);
      SCHED0();
      const unsigned cur = uni(min(fl[0], fl[1])), need = t;
      SCHED0();
      q_mma<HALF, 21, 22, 0>(f0, acc);
      SCHED0();
      keep(t, so, tx + 2);
      SCHED0();
      qa += XSL_; qb += WSL; qc += XSL_; qd += WSL; so += XSL_; ++t;
      q_one<HALF, 22, 25, false>(f0, acc);
      GRP(2, 1, 0x002, 2);
      asm volatile("" : "+v"(qa), "+v"(qb), "+v"(qc), "+v"(qd), "+s"(so), "+s"(t));
      SCHED0();
      q_mma<HALF, 25, 26, 0>(f0, acc);
      SCHED0();
      wait_full(smem, F_FULL, need, cur);
    }
    SCHED0();
    q_read(f0, smem + a0, smem + qb, smem + qd, 0);
    q_one<HALF, 0, 26, false>(f1, acc);
    {
      const int pa = 4 * g + (r16 & 1);
      const bool odd = r16 & 1, cur_a = pa <= r16, cur_b = pa + 2 <= r16;
      const unsigned qs = lds_addr(smem + L_QST + HALF * 512);
      aq = qs + r16 * 8; ac = lds_addr(smem + L_CSF + h * 256) + r16 * 8;
      aa = qs + pa * 8 + (cur_a ? 128 : 0); ab = qs + pa * 8 + (cur_b ? 128 : 0);
      xa = lds_addr(smem + L_XCH + h * 3072) + lane * 16; xv = xa + 2048 - lane * 8; xf = lds_addr(smem + F_XCH + h * 4);
      const unsigned la = odd ? 0x01000c0cu : 0x0c0c0100u, lb = odd ? 0x03020c0cu : 0x0c0c0302u;
      sel[0] = cur_a ? 0x0c0c0c0cu : la; sel[1] = cur_b ? 0x0c0c0c0cu : lb; sel[2] = cur_a ? la : 0x0c0c0c0cu; sel[3] = cur_b ? lb : 0x0c0c0c0cu;
      ooff = (unsigned)(tm * 8 + HALF * 4) * 32768u + (tn * 2 + h) * 1024 + lane * 16;
#pragma unroll
      for (int z = 0; z < 17; ++z) { GRP(1, 1, 0x100, 1); __builtin_amdgcn_sched_group_barrier(0x002, 1, 0); }
      GRP(9, 1, 0x002, 3);
      asm volatile("" : "+v"(aq), "+v"(ac), "+v"(aa), "+v"(ab), "+v"(xa), "+v"(xv), "+v"(xf), "+v"(ooff));
      asm volatile("" : "+v"(sel[0]), "+v"(sel[1]), "+v"(sel[2]), "+v"(sel[3]));
    }
    SCHED0();
#pragma unroll
    for (int i = 0; i < 5; ++i) f1.xa[i] = lds_b128(smem + (a0 ^ 64) + i * 2048);
#pragma unroll
    for (int s = 0; s < 3; ++s) {
      if (s == 2) fl = lds_word4(smem, F_FULL);
      f1.w[s][0] = lds_tr2(smem + qb + s * 8192 + 4096);
      f1.w[s][1] = lds_tr2(smem + qd + s * 8192 + 4096);
    }
    q_mma<HALF, 0, 26 - 0, 18>(f0, acc);
    SCHED0();
    keep(15, 0, tx);
    SCHED0();
  }
  LANE_VARS
  auto mm = [&](const QFrag& f, int m, int i, int t) {
    const int n = max(acc_no(HALF, m, i, t), 0);
    acc[n] = m == 2 ? MFMA16(f.xa[i], f.w[2][t], acc[n]) : MFMA16(f.w[m][t], f.xa[i], acc[n]);
  };
  auto last = [&](int z0, int z1) {
#pragma unroll
    for (int z = z0; z < z1; ++z) {
      const int c = tail_of(HALF, HALF ? z : z < 6 ? z + 2 : z < 8 ? z - 6 : z);
      mm(f1, c / 100, c / 10 % 10, c % 10);
    }
  };
  last(0, 3);
  SCHED0();
  const unsigned cur = uni(min(fl[2], fl[3]));
  SCHED0();
  last(3, 3 + 1);
  SCHED0();
  wait_full(smem, F_ST, 1, cur);
  SCHED0();
  u32x2 sq[5], cs[3][2];
#pragma unroll
  for (int i = 0; i < 5; ++i) sq[i] = *(const LAS u32x2*)(aq + i * 128);
#pragma unroll
  for (int s = 0; s < 3; ++s)
#pragma unroll
    for (int t = 0; t < 2; ++t) cs[s][t] = *(const LAS u32x2*)(ac + s * 512 + t * 128);
#pragma unroll
  for (int i = 1; i < 5; ++i) { ra[i] = *(const LAS float*)(aa + (i - 1) * 128 + 4); rb[i] = *(const LAS float*)(ab + (i - 1) * 128 + 20); }
  last(3 + 1, 3 + 5);
  __builtin_amdgcn_sched_group_barrier(0x100, 3, 0); GRP(3, 1, 0x100, 3);
  SCHED0();
  lds_post(smem, F_FREE + wid, 16);
  SCHED0();
  last(3 + 5, 18);
  const unsigned gm = g == 0 ? ~0u : 0u;
  u32x2 lx[5];
#pragma unroll
  for (int i = 0; i < 5; ++i) lx[i] = u32x2{sq[i][0] & gm, sq[i][0] & gm & 0xffffu};
#pragma unroll
  for (int i = 1; i < 5; ++i) {
    const float c = f_of(sq[i][1]) * (0.17677669529663687f * 1.4426950408889634f);
    sa[i - 1] = opq(c * ra[i]); sb[i - 1] = opq(c * rb[i]);
  }
  __builtin_amdgcn_sched_group_barrier(0x008, 5 - 3, 0); GRP(8, 1, 0x002, 3);
  asm volatile("" : "+v"(lx[0]), "+v"(lx[1]), "+v"(lx[2]), "+v"(lx[3]), "+v"(lx[4]));
  asm volatile("" : "+v"(sa[0]), "+v"(sa[1]), "+v"(sa[2]), "+v"(sa[3]), "+v"(sb[0]), "+v"(sb[1]), "+v"(sb[2]), "+v"(sb[3]));
  SCHED0();
  auto ln = [&](int m, int i, int t) {
    const int n = max(acc_no(HALF, m, i, t), 0);
    const s16x4 lxi = __builtin_bit_cast(s16x4, lx[i]), lw = __builtin_bit_cast(s16x4, cs[m][t]);
    acc[n] = m == 2 ? __builtin_amdgcn_mfma_f32_16x16x16bf16_1k(lxi, lw, acc[n], 0, 0, 0) : __builtin_amdgcn_mfma_f32_16x16x16bf16_1k(lw, lxi, acc[n], 0, 0, 0);
  };
#pragma unroll
  for (int z = 0; z < 18; ++z) ln(tail_of(HALF, z) / 100, tail_of(HALF, z) / 10 % 10, tail_of(HALF, z) % 10);
  bf16x8 qf[5], kf[5];
  u32x10 vf[2];
  auto pack_qk = [&](int i) {
    if (acc_no(HALF, 0, i, 0) >= 0) qf[i] = pack8(acc[max(acc_no(HALF, 0, i, 0), 0)], acc[max(acc_no(HALF, 0, i, 1), 0)]);
    if (acc_no(HALF, 1, i, 0) >= 0) kf[i] = pack8(acc[max(acc_no(HALF, 1, i, 0), 0)], acc[max(acc_no(HALF, 1, i, 1), 0)]);
  };
  auto pack_v = [&](int i, int t) {
    const f32x4& v = acc[max(acc_no(HALF, 2, i, t), 0)];
    return u32x2{pack2(v[0], v[1]), pack2(v[2], v[3])};
  };
  u32x2 vx[2];
  pack_qk(HALF == 0 ? 4 : 0);
  if (HALF == 0) {
    vx[0] = pack_v(4, 0); vx[1] = pack_v(4, 1);
    asm volatile("ds_write_b128 %0, %2 offset:1024\n\tds_write2st64_b64 %1, %3, %4 offset1:1" :: "v"(xa), "v"(xv), "v"(kf[4]), "v"(vx[0]), "v"(vx[1]) : "memory");
  } else {
    asm volatile("ds_write_b128 %0, %1" :: "v"(xa), "v"(qf[0]) : "memory");
  }
  asm volatile("s_mov_b64 exec, 1\n\tds_write_b8 %0, %0 offset:%1\n\ts_mov_b64 exec, -1" ::"v"(xf), "i"(HALF) : "memory");
  static_assert((F_XCH & 0xf) != 0 && ((F_XCH + 4) & 0xf) != 0, "the low byte of the address is the flag");
#pragma unroll
  for (int i = 0; i < 4; ++i) pack_qk(HALF == 0 ? 3 - i : i + 1);
  const f32x4 z4 = {0.f, 0.f, 0.f, 0.f};
  f32x4 sp[4], sc[4];
#pragma unroll
  for (int j = 0; j < 3; ++j) {
    const int i = HALF == 0 ? 3 - j : j + 2;
    sc[i - 1] = MFMA16(kf[i], qf[i], z4); sp[i - 1] = MFMA16(kf[i - 1], qf[i], z4);
  }
  if (HALF) sc[0] = MFMA16(kf[1], qf[1], z4);
  GRP(1, 6, 0x002, 4); GRP(12, 1, 0x002, 2); GRP(HALF ? 7 : 6, 1, 0x002, 3);
  SCHED0();
  unsigned flag;
  bf16x8 xq;
  if (HALF == 0) asm volatile("ds_read_b32 %0, %2\n\tds_read_b128 %1, %3" : "=&v"(flag), "=&v"(xq) : "v"(xf), "v"(xa) : "memory");
  else asm volatile("ds_read_b32 %0, %4\n\tds_read_b128 %1, %5 offset:1024\n\tds_read_b64 %2, %6\n\tds_read_b64 %3, %6 offset:512"
                    : "=&v"(flag), "=&v"(xq), "=&v"(vx[0]), "=&v"(vx[1]) : "v"(xf), "v"(xa), "v"(xv) : "memory");
  SCHED0();
  const bool odd = r16 & 1, cur_a = sel[0] == 0x0c0c0c0cu, cur_b = sel[1] == 0x0c0c0c0cu;
  float a[4], b[4], m[4];
  auto logits = [&](int j) {
    const f32x4 p4 = sp[j], c4 = sc[j];
    const float p0 = p4[0], p1 = p4[1], p2 = p4[2], p3 = p4[3], c0 = c4[0], c1 = c4[1], c2 = c4[2], c3 = c4[3];
    const float ca = odd ? c1 : c0, cb = odd ? c3 : c2, pa_ = odd ? p1 : p0, pb = odd ? p3 : p2;
    a[j] = opq((cur_a ? ca : pa_) * sa[j]);
    b[j] = opq((cur_b ? cb : pb) * sb[j]);
    if (HALF == 0 && j == 0) {
      a[j] = (!cur_a && tm == 0) ? -INFINITY : a[j];
      b[j] = (!cur_b && tm == 0) ? -INFINITY : b[j];
    }
    m[j] = __builtin_elementwise_maximum(a[j], b[j]);
  };
  auto late = [&](int z0, int z1) {
#pragma unroll
    for (int y = z0; y < z1; ++y) {
      const int c = seq_of(HALF, 18 + ((y + 8 - 0) & 7));
      if (y < 8 + 0) mm(y < 0 ? f0 : f1, 2, c / 10 % 10, c % 10);
    }
  };
#pragma unroll
  for (int j = 0; j < 2; ++j) logits(HALF == 0 ? 2 - j : j + 1);
  late(0, 6);
  GRP(6, 1, 0x002, 3);
  asm volatile("" : "+v"(m[1]), "+v"(m[2]));
  SCHED0();
  {
    unsigned t;
#define XWAIT(RD) "s_waitcnt lgkmcnt(0)\n\tv_readfirstlane_b32 %0, %1\n\ts_and_b32 %0, %0, %[mask]\n\ts_cbranch_scc1 2f\n\t1:\n\t" RD \
                  "\n\ts_waitcnt lgkmcnt(0)\n\tv_readfirstlane_b32 %0, %1\n\ts_and_b32 %0, %0, %[mask]\n\ts_cbranch_scc0 1b\n\t2:"
    if (HALF == 0) asm volatile(XWAIT("ds_read_b32 %1, %3\n\tds_read_b128 %2, %4") : "=&s"(t), "+v"(flag), "+v"(xq) : "v"(xf), "v"(xa), [mask] "s"(0xff00u) : "scc", "memory");
    else asm volatile(XWAIT("ds_read_b32 %1, %5\n\tds_read_b128 %2, %6 offset:1024\n\tds_read_b64 %3, %7\n\tds_read_b64 %4, %7 offset:512")
                      : "=&s"(t), "+v"(flag), "+v"(xq), "+v"(vx[0]), "+v"(vx[1]) : "v"(xf), "v"(xa), "v"(xv), [mask] "s"(0xffu) : "scc", "memory");
  }
  SCHED0();
  if (HALF == 0) { sc[3] = MFMA16(kf[4], xq, z4); sp[3] = MFMA16(kf[3], xq, z4); }
  else sp[0] = MFMA16(xq, qf[1], z4);
  logits(HALF == 0 ? 0 : 3);
  late(6, 8);
  GRP(1, HALF ? 1 : 2, 0x002, 3); GRP(2, 1, 0x002, 3);
  asm volatile("" : "+v"(m[HALF ? 3 : 0]));
  SCHED0();
  logits(HALF == 0 ? 3 : 0);
  rows_spread(rows_reduce<true>(m), m);
#pragma unroll
  for (int j = 0; j < 4; ++j) {
    a[j] = __builtin_amdgcn_exp2f(opq(a[j] - m[j]));
    b[j] = __builtin_amdgcn_exp2f(opq(b[j] - m[j]));
    m[j] = opq(a[j] + b[j]);
    a[j] = opq(a[j] * ra[j + 1]); b[j] = opq(b[j] * rb[j + 1]);
  }
  late(8, 16);
#pragma unroll
  for (int z = 0; z < 8; ++z) ln(2, seq_of(HALF, 18 + z) / 10 % 10, seq_of(HALF, 18 + z) % 10);
  GRP(0, 1, 0x402, 3); GRP(8, 1, 0x402, 2);
  asm volatile("" : "+v"(m[0]), "+v"(m[1]), "+v"(m[2]), "+v"(m[3]));
  SCHED0();
#pragma unroll
  for (int t = 0; t < 2; ++t) {
#pragma unroll
    for (int i = 0; i < 5; ++i) {
      const u32x2 v = i == (HALF ? 0 : 4) ? vx[t] : pack_v(i, t);
      vf[t][2 * i] = v[0]; vf[t][2 * i + 1] = v[1];
    }
    asm("" : "+v"(vf[t]));
  }
  rows_spread(__builtin_amdgcn_rcpf(rows_reduce<false>(m)), m);
  const rsrc_t ors = mk_rsrc(ws + WS_O, S_TOK * DM * 2);
#pragma unroll
  for (int j = 0; j < 4; ++j) {
    const unsigned pk = pack2(a[j] * m[j], b[j] * m[j]);
    const u32x4 pw = {__builtin_amdgcn_perm(0u, pk, sel[0]), __builtin_amdgcn_perm(0u, pk, sel[1]), __builtin_amdgcn_perm(0u, pk, sel[2]), __builtin_amdgcn_perm(0u, pk, sel[3])};
    const u32x4 v0 = {vf[0][2 * j], vf[0][2 * j + 1], vf[0][2 * j + 2], vf[0][2 * j + 3]}, v1 = {vf[1][2 * j], vf[1][2 * j + 1], vf[1][2 * j + 2], vf[1][2 * j + 3]};
    sp[j] = MFMA16(__builtin_bit_cast(bf16x8, v0), __builtin_bit_cast(bf16x8, pw), z4);
    sc[j] = MFMA16(__builtin_bit_cast(bf16x8, v1), __builtin_bit_cast(bf16x8, pw), z4);
  }
#pragma unroll
  for (int j = 0; j < 4; ++j) { qf[j] = pack8(sp[j], sc[j]); st16c(ors, ooff + j * 32768, __builtin_bit_cast(u32x4, qf[j])); }
  SCHED0();
  asm volatile("ds_read_b32 %0, %1 offset:%2" : "=v"(e.go) : "v"(xf - h * 4), "i"(F_GO - F_XCH) : "memory");
  wait_bytes<0x80808080u>(smem, F_FREE, 16);
  asm volatile("" : "+v"(e.go));
  asm volatile("ds_write_b128 %0, %1\n\tds_write_b128 %0, %2 offset:1024\n\tds_write_b128 %0, %3 offset:4096\n\tds_write_b128 %0, %4 offset:5120"
               :: "v"(lds_addr(smem + L_OWN) + HALF * 8192 + h * 2048 + lane * 16), "v"(qf[0]), "v"(qf[1]), "v"(qf[2]), "v"(qf[3]) : "memory");
  lds_post(smem, F_OL + wid, 1);
}

__device__ __forceinline__ unsigned ln2s(unsigned w, float mb, float r) {
  const unsigned d = pack2(opq(lo2f(w) - mb), opq(hi2f(w) - mb));
  return pack2(opq(lo2f(d) * r), opq(hi2f(d) * r));
}

__device__ __forceinline__ void await_attention(char* smem, int lane, int tm, int tn, char* ws, unsigned tag) {
  const rsrc_t wrs = mk_rsrc(ws, WS_O);
  const unsigned off = WS_OF + (tm * 64 + ((lane >> 3) & 1) * 32 + (lane & 7)) * 4;
  const int own = tn >> 3;
  bool first = true;
  unsigned seen = 0, since = 0;
#pragma unroll 1
  for (unsigned n = 0; n < SPIN_CAP; ++n) {
    unsigned want = ~seen & 0xffffu;
    if (since) ++since; else since = uni(lds_word(smem, F_ODONE)) >> 2;
    if (since <= (first ? 2 : 0)) want &= own ? 0x00ffu : 0xff00u;
    const unsigned f = __builtin_amdgcn_raw_buffer_load_b32(wrs, ((want >> (lane & 15)) & 1) ? off : 0x7fffffffu, 0, 16);
    seen |= (unsigned)__ballot(f == tag) & want;
    if (seen == 0xffffu) break;
    if (first && ((seen & 0xffu) == 0xffu || (seen >> 8) == 0xffu)) { lds_post(smem, F_GO, (seen & 0xffu) == 0xffu ? 1 : 2); first = false; NAP(8); }
    NAP(2);
  }
  lds_post(smem, F_GO + 1, 1);
  if (first) lds_post(smem, F_GO, 1);
}

__device__ __forceinline__ void out_proj(char* smem, int wid, int lane_in, int tm, int tn, char* ws, unsigned tag,
                                         u16* __restrict__ out, EpiRegs& e, unsigned rot2) {
  LANE_VARS
  const rsrc_t ors = mk_rsrc(ws + WS_O, S_TOK * DM * 2), yrs = mk_rsrc(out, S_TOK * DM * 2);
  const int tok = tm * 128 + wid * 32 + r16, co = (g & 1) * 16 + (g >> 1) * 8;
  unsigned eoff = (unsigned)(tok * DM + tn * 64 + co) * 2u;
  const unsigned vo = (unsigned)(tm * 8 + wid * 2) * 32768u + lane * 16;
  bf16x8 wb[3][4], oa[OD][2];
  int bc[4];
#pragma unroll
  for (int c = 0; c < 4; ++c) bc[c] = L_WA + brow + ((c ^ hk) << 5);
  f32x4 acc[2][4];
  SCHED0();
  unsigned go, oth = (tn & 8) * 2 ^ 16;
  asm volatile("" : "+s"(oth));
  auto o_request = [&](int s) {
    asm volatile("buffer_load_dwordx4 %0, %2, %4, %5 offen sc1\n\tbuffer_load_dwordx4 %1, %3, %4, %5 offen sc1" : "+v"(oa[s % OD][0]), "+v"(oa[s % OD][1])
                 : "v"(vo), "v"(vo + 32768), "s"(ors), "s"(((s >> 1 == 7 ? oth : (s & 16) ^ ((go & 2) * 8)) + ((rot2 >> (s & 14) * 2) & 14) + (s & 1)) * 1024) : "memory");
  };
  auto start = [&](unsigned word) {
    go = uni(word) & 3;
    if (go) {
#pragma unroll
      for (int s = 0; s < OD - 1; ++s) o_request(s);
    }
    SCHED0();
  };
#pragma unroll
  for (int s = 0; s < OD; ++s) asm volatile("" : "=v"(oa[s][0]), "=v"(oa[s][1]));
  {
    const unsigned w0 = uni(e.go);
    if (w0 & 3) wait_bytes<0x80808080u>(smem, F_OL, 1);
    start(w0);
  }
  const unsigned early = go;
  auto publish = [&](auto nn) {
    WAITV(decltype(nn)::v);
    unsigned old = 0;
    if (lane == 0) old = __hip_atomic_fetch_add((LAS unsigned*)(LAS char*)(smem + F_ODONE), 1u, __ATOMIC_RELAXED, __HIP_MEMORY_SCOPE_WORKGROUP);
    if (uni(old) == 3u) {
      if (lane == 0) __hip_atomic_store((GAS unsigned*)(ws + WS_OF) + tm * 64 + (tn >> 3) * 32 + (tn & 7), tag, RLX_AGENT);
    }
    SCHED0();
  };
  if (!early) publish(IC<0>{});
  const unsigned w1 = lds_word(smem, F_GO);
  const u32x2 fl = *(const volatile LAS u32x2*)(LAS const char*)(smem + F_FULL);
  const unsigned srow = 2047u - (*(const unsigned*)(smem + F_SEL + (lane & 7) * 4) & 0xffffu);
  f32x4 res[2][4];
  const uint2 st[2] = {*(const uint2*)(smem + L_QST + (16 + wid * 32 + r16) * 8), *(const uint2*)(smem + L_QST + (32 + wid * 32 + r16) * 8)};
  auto norm = [&](int u) {
    const float rd = f_of(st[u].y), k = 1.0f + rd, b = lo2f(st[u].x) * rd;
#pragma unroll
    for (int pr = 0; pr < 2; ++pr) {
      auto sa = __builtin_amdgcn_permlane16_swap(e.xv[u][pr][0], e.xv[u][pr][2], false, false);
      auto sb = __builtin_amdgcn_permlane16_swap(e.xv[u][pr][1], e.xv[u][pr][3], false, false);
#pragma unroll
      for (int z = 0; z < 2; ++z) res[u][2 * pr + z] = f32x4{lo2f(sa[z]), hi2f(sa[z]), lo2f(sb[z]), hi2f(sb[z])} * k + b;
    }
    asm volatile("" : "+v"(res[u][0]), "+v"(res[u][1]), "+v"(res[u][2]), "+v"(res[u][3]));
  };
  f32x4 ac2[2][4], (&ac3)[2][4] = res;
  auto w_frag = [&](int slot, const int (&b)[4], int z) {
#pragma unroll
    for (int c = 0; c < 4; ++c) wb[slot][c] = lds_tr2(smem + b[c] + z * 4096);
  };
  auto step = [&](f32x4 (&d)[2][4], int slot, const bf16x8 (&o)[2], bool fresh) {
#pragma unroll
    for (int c = 0; c < 4; ++c)
#pragma unroll
      for (int uu = 0; uu < 2; ++uu) {
        const int u = uu ^ (c & 1);
        d[u][c] = MFMA16(wb[slot][c], o[u], fresh ? (f32x4{0, 0, 0, 0}) : d[u][c]);
      }
  };
  int b3[4];
#pragma unroll
  for (int c = 0; c < 4; ++c) b3[c] = bc[c] + (L_W3 - L_WA);
  auto own_heads = [&](unsigned fu) {
    wait_full(smem, F_FULL, 17, fu);
    w_frag(0, b3, 2); w_frag(1, b3, 3);
    const unsigned w2 = lds_word(smem, F_GO);
    SCHED0();
    if (wid < 2) wait_bytes<0x00800080u>(smem, F_OL, 1); else wait_bytes<0x80008000u>(smem, F_OL, 1);
    if (!go) start(w2);
    bf16x8 own[2][2];
#pragma unroll
    for (int z = 0; z < 4; ++z) own[z >> 1][z & 1] = lds_b128(smem + L_OWN + wid * 4096 + lane * 16 + z * 1024);
    step(ac3, 0, own[0], false); step(ac3, 1, own[1], false);
    asm volatile("" : "+v"(ac3[0][0]), "+v"(ac3[0][1]), "+v"(ac3[0][2]), "+v"(ac3[0][3]), "+v"(ac3[1][0]), "+v"(ac3[1][1]), "+v"(ac3[1][2]), "+v"(ac3[1][3]));
    SCHED0();
  };
  unsigned mine = (unsigned)__ballot((srow >> 5) == (unsigned)(tm * 4 + wid)) & 0xffu;
  asm volatile("" : "+s"(mine));
  asm volatile("" : "+v"(eoff));
  if (!go) start(w1);
  norm(0);
  norm(1);
  SCHED0();
  if (early) publish(IC<2 * OD - 2>{});
  own_heads(uni(min(fl[0], fl[1])));
  if (!go) start(wait_bytes<0x80u>(smem, F_GO, 1));
  go = uni(go);
  int b2[4];
  const int mine1st = ((go >> 1) ^ (tn >> 3) ^ 1) & 1;
#pragma unroll
  for (int c = 0; c < 4; ++c) { b2[c] = bc[c] + (go & 1) * (L_WB - L_WA); bc[c] += (go >> 1) * (L_WB - L_WA); }
  unsigned so[15];
#pragma unroll
  for (int z = (OD - 1) / 2; z < 15; ++z) { so[z] = ((z == 7 ? oth : (2 * z & 16) ^ ((go & 2) * 8)) + ((rot2 >> (z & 7) * 4) & 14)) * 1024; asm volatile("" : "+s"(so[z])); }
  s16x4 wh[3][8];
  auto w_half = [&](int s, int z0, int z1) {
#pragma unroll
    for (int z = z0; z < z1; ++z)
      if (s < 30) wh[s % 3][z] = __builtin_amdgcn_ds_read_tr16_b64_v4i16((LAS s16x4*)(smem + (s < 14 ? bc[z >> 1] + s * 4096 : s < 16 ? b3[z >> 1] + (s - 14) * 4096 : b2[z >> 1] + (s - 16) * 4096) + (z & 1) * 512));
  };
  wait_full(smem, F_WO, 1);
  wait_bytes<0x80u, true>(smem, F_WO + 8, 1, go & 1);
  w_half(0, 0, 8); w_half(1, 0, 8);
  SCHED0();
  asm volatile("s_waitcnt vmcnt(%2)" : "+v"(oa[0][0]), "+v"(oa[0][1]) : "i"(2 * OD - 4) : "memory");
  auto one = [&](int s) __attribute__((always_inline)) {
    f32x4 (&d)[2][4] = s < 14 ? ac2 : s < 16 ? ac3 : acc;
    auto part = [&](int z0, int z1) {
      SCHED0();
      w_half(s + 2, z0, z1);
#pragma unroll
      for (int z = z0; z < z1; ++z) {
        const int c = z >> 1, u = (z ^ c) & 1;
        d[u][c] = MFMA16(__builtin_shufflevector(wh[s % 3][2 * c], wh[s % 3][2 * c + 1], 0, 1, 2, 3, 4, 5, 6, 7), oa[s % OD][u], s == 0 || s == 16 ? (f32x4{0, 0, 0, 0}) : d[u][c]);
        __builtin_amdgcn_sched_group_barrier(0x008, 1, 0);
        __builtin_amdgcn_sched_group_barrier(0x100, 1, 0);
      }
      SCHED0();
    };
    const int n = s + OD - 1;
    auto ask = [&](int u) {
      if (n < 30) asm volatile("buffer_load_dwordx4 %0, %1, %2, %3 offen offset:%4 sc1" : "+v"(oa[n % OD][u]) : "v"(vo + u * 32768), "s"(ors), "s"(so[n >> 1]), "i"((n & 1) * 1024) : "memory");
    };
    part(0, 1);
    if (n == 14 || n == 16) wait_bytes<0x8000u, true>(smem, F_GO, 1, n == 14 ? mine1st ^ 1 : mine1st);
    ask(0);
    part(1, 3);
    ask(1);
    if (s == 13) wait_bytes<0x80u>(smem, F_WO + 8, 1);
    part(3, 5);
    if (s < 29) asm volatile("s_waitcnt vmcnt(%2)" : "+v"(oa[(s + 1) % OD][0]), "+v"(oa[(s + 1) % OD][1]) : "i"(2 * ((n < 29 ? n : 29) - s - 1)) : "memory");
    part(5, 8);
    asm volatile("" : "+v"(d[0][0]), "+v"(d[0][1]), "+v"(d[0][2]), "+v"(d[0][3]), "+v"(d[1][0]), "+v"(d[1][1]), "+v"(d[1][2]), "+v"(d[1][3]));
  };
#pragma unroll
  for (int s = 0; s < 10; ++s) one(s);
#pragma unroll
  for (int s = 10; s < 20; ++s) one(s);
#pragma unroll
  for (int s = 20; s < 28; ++s) one(s);
  asm volatile("s_waitcnt vmcnt(0)" : "+v"(oa[29 % OD][0]), "+v"(oa[29 % OD][1]) :: "memory");
  if (mine != 0) {
    wait_bytes<0x80808080u>(smem, F_UP, 1);
#pragma unroll 1
    for (; mine != 0; mine &= mine - 1) {
      const int c = __builtin_ctz(mine), row = __builtin_amdgcn_readlane((int)srow, c) & 31;
#pragma unroll
      for (int u = 0; u < 2; ++u)
        if (row == u * 16 + r16) {
#pragma unroll
          for (int z = 0; z < 4; ++z) res[u][z] += *(const f32x4*)(smem + L_UP + c * 256 + (z * 16 + g * 4) * 4);
        }
    }
  }
  SCHED0();
#pragma unroll
  for (int u = 0; u < 2; ++u)
#pragma unroll
    for (int z = 0; z < 8; ++z)
      acc[u][z & 3] = MFMA16(__builtin_shufflevector(wh[(28 + (z >> 2)) % 3][2 * (z & 3)], wh[(28 + (z >> 2)) % 3][2 * (z & 3) + 1], 0, 1, 2, 3, 4, 5, 6, 7), oa[(28 + (z >> 2)) % OD][u], acc[u][z & 3]);
#pragma unroll
  for (int u = 0; u < 2; ++u)
#pragma unroll
    for (int pr = 0; pr < 2; ++pr) {
      const f32x4 c0 = (acc[u][2 * pr] + ac2[u][2 * pr]) + ac3[u][2 * pr], c1 = (acc[u][2 * pr + 1] + ac2[u][2 * pr + 1]) + ac3[u][2 * pr + 1];
      auto sa = __builtin_amdgcn_permlane16_swap(pack2(c0[0], c0[1]), pack2(c1[0], c1[1]), false, false);
      auto sb = __builtin_amdgcn_permlane16_swap(pack2(c0[2], c0[3]), pack2(c1[2], c1[3]), false, false);
      __builtin_amdgcn_raw_buffer_store_b128(u32x4{sa[0], sb[0], sa[1], sb[1]}, yrs, eoff + pr * 64 + u * 16 * DM * 2, 0, 16);
    }
  __builtin_amdgcn_sched_group_barrier(0x008, 9, 0);
  GRP(7, 1, 0x002, 4);
  SCHED0();
}

__device__ __forceinline__ unsigned med3(unsigned a, unsigned b, unsigned c) { return max(min(a, b), min(max(a, b), c)); }
#define PIN8(a) asm volatile("" : "+v"(a[0]), "+v"(a[1]), "+v"(a[2]), "+v"(a[3]), "+v"(a[4]), "+v"(a[5]), "+v"(a[6]), "+v"(a[7]))
__device__ __forceinline__ void tick(int& nb) {
  SCHED0();
  if (nb < NBAR) { BAR(); ++nb; NAP(2); }
  asm volatile("" ::: "memory");
  SCHED0();
}
__device__ __forceinline__ void hidden_half(char* smem, int lane, int l, int cg, char* ws, unsigned tag, u32x4 (&fd)[2][8]) {
  const rsrc_t wrs = mk_rsrc(ws, WS_O);
  const unsigned off = WS_GU + (unsigned)(cg * 32768 + l * 2048 + lane * 16);
  u32x4 ps[2][8];
  auto request = [&]() {
#pragma unroll
    for (int p = 0; p < 16; ++p) ps[p & 1][p >> 1] = ld16c(wrs, off + (p & 1) * 1024, (p >> 1) * 4096);
    SCHED0();
  };
  auto good = [&]() {
    unsigned bad = 0;
#pragma unroll
    for (int p = 0; p < 16; ++p) bad = chk(bad, ps[p & 1][p >> 1], tag);
    return __all(bad == 0);
  };
#pragma unroll 1
  for (unsigned n = 0; n < SPIN_CAP; ++n) {
    const unsigned t = __builtin_amdgcn_raw_buffer_load_b32(wrs, WS_GU + (unsigned)(cg * 32768 + (lane >> 3) * 4096 + l * 2048 + (lane & 7) * 256 + 132), 0, 16);
    if (__all(t == tag)) break;
    NAP(8);
  }
  request();
  tr_all(fd[0]); tr_all(fd[1]);
  SCHED0();
#pragma unroll 1
  for (unsigned n = 0; n < SPIN_CAP && !good(); ++n) { NAP(4); request(); }
  unsigned hp[2];
#pragma unroll
  for (int j = 0; j < 2; ++j) {
    float hv[2];
#pragma unroll
    for (int z = 0; z < 2; ++z) {
      const float gb = rbf((f_of(ps[j][0][2 * z]) + f_of(ps[j][2][2 * z])) + (f_of(ps[j][4][2 * z]) + f_of(ps[j][6][2 * z])));
      const float ub = rbf((f_of(ps[j][1][2 * z]) + f_of(ps[j][3][2 * z])) + (f_of(ps[j][5][2 * z]) + f_of(ps[j][7][2 * z])));
      hv[z] = rbf(gb / (1.0f + __expf(-gb))) * ub;
    }
    hp[j] = pack2(hv[0], hv[1]);
  }
#pragma unroll
  for (int j = 0; j < 2; ++j)
    *(unsigned*)(smem + L_HF + (lane & 7) * 128 + (((lane >> 4) & 1) * 4 + 2 * l + j) * 16 + ((lane >> 3) & 1) * 4 + (lane >> 5) * 8) = hp[j];
  lds_post(smem, F_H + l, 1);
}

struct Stager {
  rsrc_t xrs, qrs;
  LAS char* lds;
  unsigned va, vb;
  int arow, pa, pb;
  __device__ __forceinline__ Stager(char* smem, int l, int lane, int tm, int tn, const u16* x, const u16* w) {
    asm volatile("" : "+v"(lane));
    const int kr = lane >> 3;
    xrs = mk_rsrc(x, S_TOK * DM * 2); qrs = mk_rsrc(w, DM * NQKV * 2);
    lds = (LAS char*)smem + l * 1024;
    va = (unsigned)(kr + 8 * l) * 2048u + (((lane & 7) ^ (kr >> 1) ^ (l << 2)) << 4);
    vb = (unsigned)(kr + 8 * l) * 6144u + tn * 128 + (((lane & 7) ^ (((kr >> 1) & 1) << 1) ^ (l << 2)) << 4);
    arow = tm * 128 - 16;
    k_order(tm, tn, pa, pb);
  }
};
#define DM1(l, o) "s_add_u32 m0, %2, " #l "\n\ts_add_u32 %0, %5, " #o "\n\tbuffer_load_dwordx4 %3, %4, %0 offen lds\n\t"
#define DMA_(str, v_, rs_, so_, s0_) asm volatile("s_mov_b32 %1, m0\n\t" str "s_mov_b32 m0, %1" : "=&s"(tmp), "=&s"(keep) : "s"(base), "v"(v_), "s"(rs_), "s"(so_), "s"(s0_) : "scc", "memory")
__device__ __forceinline__ void stage_x(const Stager& g, int t) {
  const int kt = k_tile(t, g.pa, g.pb);
  const unsigned base = (unsigned)(size_t)g.lds + (t % 3) * XSL_;
  unsigned keep, tmp;
  DMA_("s_mov_b32 m0, %2\n\ts_nop 0\n\tbuffer_load_dwordx4 %3, %4, %6 offen lds\n\t" DM1(2048, 32768) DM1(4096, 65536) DM1(6144, 98304) DM1(8192, 131072)
       DM1(10240, 163840) DM1(12288, 196608) DM1(14336, 229376) DM1(16384, 262144), g.va, g.xrs, g.arow * 2048 + kt * 128, max(g.arow, 0) * 2048 + kt * 128);
}
__device__ __forceinline__ void stage_w(const Stager& g, int t) {
  const int kt = k_tile(t, g.pa, g.pb);
  const unsigned base = (unsigned)(size_t)g.lds + XB + (t & 3) * WSL;
  unsigned keep, tmp;
  DMA_(DM1(0, 0) DM1(2048, 98304) DM1(4096, 196608) DM1(6144, 294912) DM1(8192, 2048) DM1(10240, 100352) DM1(12288, 198656) DM1(14336, 296960)
       DM1(16384, 4096) DM1(18432, 102400) DM1(20480, 200704) DM1(22528, 299008), g.vb, g.qrs, kt * 64 * 6144, 0);
}
__device__ __forceinline__ int unit_of(int bid) {
  const int off = bid >> 3;
  return (off >> 2) == 3 ? off + 16 : (off >> 2) == 7 ? off - 16 : off;
}
__device__ constexpr int wo_at(int n) { return (n < 28 ? L_WA + n * 2048 : n < 56 ? L_WB + (n - 28) * 2048 : L_W3 + (n - 56) * 2048); }
__device__ __forceinline__ void loader(char* smem, int l, int lane, int tm, int tn, unsigned rot2, const u16* __restrict__ x,
                                       const u16* __restrict__ w, const u16* __restrict__ wo, char* ws) {
  const Stager stage(smem, l, lane, tm, tn, x, w);
  dma_tag(smem, F_FULL + l * 4, 0);
  const rsrc_t prs = mk_rsrc(wo, DM * DM * 2);
  const int kr = lane >> 3;
  const unsigned vo = (unsigned)((kr < 4 ? kr : 12 + kr) + 4 * l) * 2048u + tn * 128 + (((lane & 7) ^ (((kr >> 1) & 1) << 1) ^ (l << 2)) << 4);
  int oth = (tn & 8) * 2 ^ 16;
  asm volatile("" : "+s"(oth));
  auto source = [&](int n) {
    const int k = n >> 1, half = k < 14 ? 0 : k < 28 ? 16 : k >= 30 ? (tn & 8) * 2 : oth;
    return (unsigned)((half + ((rot2 >> ((k < 14 ? k : k < 28 ? k - 14 : 14) & 14) * 2) & 14) + (k & 1)) * 32 + 8 * (n & 1)) * 2048;
  };
  auto fetch = [&](int n) {
    unsigned keep;
    asm volatile("s_mov_b32 %0, m0\n\ts_add_u32 m0, %4, %5\n\ts_nop 2\n\tbuffer_load_dwordx4 %1, %2, %3 offen lds\n\ts_mov_b32 m0, %0"
                 : "=&s"(keep) : "v"(vo), "s"(prs), "s"(source(n)), "s"((unsigned)(size_t)stage.lds), "i"(wo_at(n)) : "scc", "memory");
  };
  static_assert(L_WA + 24 * 2048 == XB && L_WB == XB + WSL && L_OWN + 16384 <= L_XCH, "layout");
#pragma unroll
  for (int t = -1; t < NBAR - 2; ++t) {
    if (t == 0) stage_w(stage, 3);
    if (t >= 0) stage_x(stage, t + 3);
    if (t == NBAR - 3) dma_tag(smem, F_FULL + l * 4, 15);
    if (t >= 0) stage_w(stage, t + 4);
    SCHED0();
    WAITV(t < 0 ? 22 : 33 + (t == 0 || t == NBAR - 3));
    BAR();
  }
  stage_x(stage, 15);
  dma_tag(smem, F_FULL + l * 4, 16);
  SCHED0();
#pragma unroll
  for (int y = 0; y < 12; ++y) fetch(y < 8 ? 63 - y : 16 + y);
  dma_tag(smem, F_FULL + l * 4, 17);
  const unsigned tag = uni(lds_word(smem, F_TAG));
  wait_bytes<0x80808080u>(smem, F_FREE, 16);
#pragma unroll
  for (int n = 0; n < 24; ++n) fetch(n);
  dma_tag(smem, F_WO + l * 4, 1);
#pragma unroll
  for (int n = 28; n < 28 + NB1; ++n) fetch(n);
  if (l == 0) {
    const unsigned v1 = (vo + 4 * 2048) ^ 64;
    wait_bytes<0x80808080u>(smem, F_OL, 1);
#pragma unroll
    for (int n = 28 + NB1; n < 56; ++n) {
      unsigned keep;
      asm volatile("s_mov_b32 %0, m0\n\ts_add_u32 m0, %4, %5\n\ts_nop 0\n\tbuffer_load_dwordx4 %1, %2, %3 offen lds\n\ts_add_u32 m0, m0, 1024\n\ts_nop 0\n\tbuffer_load_dwordx4 %6, %2, %3 offen lds\n\ts_mov_b32 m0, %0"
                   : "=&s"(keep) : "v"(vo), "s"(prs), "s"(source(n)), "s"((unsigned)(size_t)stage.lds), "i"(wo_at(n)), "v"(v1) : "scc", "memory");
    }
    wait_bytes<0x80u>(smem, F_WO + 12, 1);
    dma_tag(smem, F_WO + 8, 1);
  } else dma_tag(smem, F_WO + 12, 1);
  __builtin_amdgcn_s_setprio(1);
  if (l) await_attention(smem, lane, tm, tn, ws, tag);
}

__device__ __forceinline__ void updates(char* smem, int w, int lane, int tm, int tn, char* ws) {
  const rsrc_t wrs = mk_rsrc(ws, WS_O);
  const unsigned tag = uni(lds_word(smem, F_TAG));
  const int g = lane >> 4, li = lane & 15;
  wait_bytes<0x80u>(smem, F_SELOK, 1);
  const unsigned selkey = *(const unsigned*)(smem + F_SEL + (lane & 7) * 4);
  unsigned mine = (unsigned)__ballot(((2047u - (selkey & 0xffffu)) >> 7) == (unsigned)tm) & 0xffu;
#pragma unroll
  for (int i = 0; i < 3; ++i) mine &= i < w ? mine - 1 : ~0u;
  if (mine != 0 && w < 3) {
    wait_bytes<0x80u>(smem, F_GO, 1);
    NAP(24);
  }
  for (; mine != 0; mine &= mine - 1, mine &= mine - 1, mine &= mine - 1, mine &= mine - 1) {
    const int c = __builtin_ctz(mine);
    const unsigned yoff = WS_Y + (unsigned)((g * 14 * 8 + c) * DM + tn * 64 + li * 4) * 4u;
    u32x4 yv[14];
    const unsigned key = (unsigned)__builtin_amdgcn_readlane((int)selkey, c), kb = key >> 16;
#pragma unroll 1
    for (unsigned n = 0; n < SPIN_CAP; ++n) {
      unsigned bad = 0;
#pragma unroll
      for (int j = 0; j < 14; ++j) yv[j] = ld16c(wrs, yoff, j * (8 * DM * 4));
#pragma unroll
      for (int j = 0; j < 14; ++j) bad = chk(bad, yv[j], tag);
      if (__all(bad == 0)) break;
      NAP(8);
    }
    float y[4] = {0.f, 0.f, 0.f, 0.f};
#pragma unroll
    for (int j = 0; j < 14; ++j) { y[0] += lo2f(yv[j][0]); y[1] += hi2f(yv[j][0]); y[2] += lo2f(yv[j][2]); y[3] += hi2f(yv[j][2]); }
    const float r = __uint_as_float(((kb & 0x8000u) ? (kb & 0x7fffu) : (~kb & 0xffffu)) << 16);
    const float rs = rbf(1.0f / (1.0f + __expf(-r)));
#pragma unroll
    for (int e = 0; e < 4; ++e) y[e] = rbf(rbf(xsum16_32(y[e])) * rs);
    wait_bytes<0x80808080u>(smem, F_FREE, 16);
    *(f32x4*)(smem + L_UP + c * 256 + li * 16) = f32x4{y[0], y[1], y[2], y[3]};
  }
  lds_post(smem, F_UP + w, 1);
}

__device__ __forceinline__ void side_wave(char* smem, int F, int lane, int bid, int tm, int tn, const u16* __restrict__ x,
                                          const u16* __restrict__ wgu, const u16* __restrict__ wd, const u16* __restrict__ wo, char* ws) {
  const rsrc_t wrs = mk_rsrc(ws, WS_O), xrs = mk_rsrc(x, S_TOK * DM * 2);
  const int u = unit_of(bid), cg = (bid & 7) * 7 + ((u >> 2) % 7), q = u & 3;
  const int g = lane >> 4, li = lane & 15;
  u32x4 fw[4][8];
  const rsrc_t grs = mk_rsrc(wgu, DM * FF * 2);
  const int cu = (bid & 7) * 7 + (((u >> 2) + F) % 7);
  const unsigned fo = (unsigned)(q * 256 + (li >> 3) * 128 + 8 * g) * (FF * 2) + cu * 128 + (li & 7) * 16;
#pragma unroll
  for (int s = 0; s < 4; ++s) asm volatile("" : "=v"(fw[s][0]), "=v"(fw[s][1]), "=v"(fw[s][2]), "=v"(fw[s][3]), "=v"(fw[s][4]), "=v"(fw[s][5]), "=v"(fw[s][6]), "=v"(fw[s][7]));
  const unsigned csb = WS_CS + (tn * 2 + F) * 4096;
  u32x4 cs[4], rec[2];
  unsigned ca = csb + lane * 16, ra = WS_R + (unsigned)max(tm * 128 - 16 + F * 72 + lane, 0) * 16, rb = WS_R + (unsigned)max(tm * 128 - 8 + F * 72 + lane, 0) * 16;
  asm volatile("" : "+v"(ca), "+v"(ra), "+v"(rb));
  const rsrc_t prs = mk_rsrc(wo, DM * DM * 2);
  const unsigned wa = lane < 32 ? (unsigned)(tm * 64 + F * 32 + lane) * 2048u + tn * 128 : 0x7fffffffu;
  unsigned td = 0;
  unsigned due = u < 28 ? (0x3012 >> ((tn & 3) * 4)) & 15 : 9, iss = 0;
#define FW(n) fw[(n) >> 3][(n) & 7]
#define LD(i) "s_mov_b32 %[t], (((%[n0]+" #i ")>>3)*32+((%[n0]+" #i ")&7))*7168\n\tbuffer_load_dwordx4 %" #i ", %[fo], %[rs], %[t] offen nt\n\t"
#define LD8 LD(0) LD(1) LD(2) LD(3) LD(4) LD(5) LD(6) LD(7)
#define R8(n) "+v"(FW(n)), "+v"(FW(n + 1)), "+v"(FW(n + 2)), "+v"(FW(n + 3)), "+v"(FW(n + 4)), "+v"(FW(n + 5)), "+v"(FW(n + 6)), "+v"(FW(n + 7))
#define SITE(lds_, ...) asm volatile("s_cmp_lg_u32 %[iss], %[k]\n\ts_cbranch_scc1 1f\n\ts_cmp_le_u32 %[nb], %[due]\n\ts_cbranch_scc1 1f\n\ts_sleep %[dly]\n\t" lds_ \
    "s_add_u32 %[iss], %[iss], 1\n\ts_add_u32 %[due], %[due], 4\n\t1:" : __VA_ARGS__, [iss] "+s"(iss), [due] "+s"(due), [t] "=&s"(t) \
    : [fo] "v"(fo), [rs] "s"(grs), [nb] "s"(nb), [k] "i"(K), [n0] "i"(n0), [dly] "i"(D), [ca] "v"(ca), [ra] "v"(ra), [rb] "v"(rb), [ws] "s"(wrs), [wa] "v"(wa), [wr] "s"(prs) : "scc", "memory")
#define C1K(i, a, o) "buffer_load_dwordx4 %" #i ", %[" #a "], %[ws], 0 offen offset:" #o " sc1\n\t"
#define G2(a, b, c, d, e, f) C1K(a, ca, 0) C1K(b, ca, 1024) C1K(c, ca, 2048) C1K(d, ca, 3072) C1K(e, ra, 0) C1K(f, rb, 0)
#define PING2() asm volatile("" : "+v"(cs[0]), "+v"(cs[1]), "+v"(cs[2]), "+v"(cs[3]), "+v"(rec[0]), "+v"(rec[1]))
  auto site = [&](auto kk, auto dd, unsigned nb) {
    constexpr int K = decltype(kk)::v, D = decltype(dd)::v, n0 = K == 0 ? 0 : 8 + K * 8;
    unsigned t;
    SCHED0();
    if constexpr (K == 0) SITE(LD8 LD(8) LD(9) LD(10) LD(11) LD(12) LD(13) LD(14) LD(15), R8(n0), R8(n0 + 8));
    else if constexpr (K == 1) SITE(G2(8, 9, 10, 11, 12, 13) LD8, R8(n0), "+v"(cs[0]), "+v"(cs[1]), "+v"(cs[2]), "+v"(cs[3]), "+v"(rec[0]), "+v"(rec[1]));
    else SITE(LD8 "buffer_load_dword %8, %[wa], %[wr], 0 offen\n\t", R8(n0), "+v"(td));
    SCHED0();
  };
#define CH(k, d, n) site(IC<k>{}, IC<d>{}, n)
  unsigned tag;
  {
    const int qq = (lane >> 2) & 3, hk = (qq >> 1) | ((g & 1) << 1);
    const char* pb = smem + XB + (8 * g + qq) * 128 + 8 * (lane & 3) + (((2 * F + (tm & 1)) ^ hk) << 5);
    const bf16x8 ones = {0x3f80, 0x3f80, 0x3f80, 0x3f80, 0x3f80, 0x3f80, 0x3f80, 0x3f80};
    f32x4 a[3];
#pragma unroll
    for (int t = 0; t < 2; ++t) {
      SCHED0(); BAR(); NAP(2); SCHED0();
      __builtin_amdgcn_s_setprio(3);
#pragma unroll
      for (int c = 0; c < 3; ++c) {
        a[c] = MFMA16(ones, lds_tr2(pb + t * WSL + c * 8192), t ? a[c] : (f32x4{0, 0, 0, 0}));
        a[c] = MFMA16(ones, lds_tr2(pb + t * WSL + c * 8192 + 4096), a[c]);
      }
      asm volatile("" : "+v"(a[0]), "+v"(a[1]), "+v"(a[2]));
      if (t == 0) { tag = uni(lds_word(smem, F_TAG)); CH(0, 4, 1); }
    }
    st16c(wrs, lane < 16 ? csb + ((tm & 12) + (tm & 1) * 2 + ((tm >> 1) & 1)) * 256 + lane * 16 : 0x7fffffffu,
          u32x4{__float_as_uint(a[0][0]), __float_as_uint(a[1][0]), __float_as_uint(a[2][0]), tag});
    __builtin_amdgcn_s_setprio(0);
    CH(0, 4, 2);
  }
  int nb = 2;
  tick(nb);
  const unsigned xs_off = WS_S + (unsigned)(((bid & 7) * 4 + q) * 6144 + lane * 16);
  uint2 tc[2], tq[2];
  unsigned ta[3], tv, todo = 1;
  auto gather2 = [&]() {
    asm volatile(G2(0, 1, 2, 3, 4, 5) : "=&v"(cs[0]), "=&v"(cs[1]), "=&v"(cs[2]), "=&v"(cs[3]), "=&v"(rec[0]), "=&v"(rec[1])
                 : [ca] "v"(ca), [ra] "v"(ra), [rb] "v"(rb), [ws] "s"(wrs) : "memory");
    SCHED0();
  };
  auto prepare = [&]() {
#pragma unroll 1
    for (unsigned n = 0; n < SPIN_CAP; ++n) {
      unsigned bad = chk(chk(0u, rec[0], tag), rec[1], tag);
#pragma unroll
      for (int j = 0; j < 4; ++j) bad = __builtin_amdgcn_bitop3_b32(bad, cs[j][3], tag, 0xF6);
      if (__all(bad == 0)) break;
      NAP(8);
      gather2();
      tick(nb); tick(nb); WAITV(0); PING2();
    }
    float cz[3];
#pragma unroll
    for (int z = 0; z < 3; ++z) {
      const unsigned v = __float_as_uint(opq(opq(f_of(cs[0][z]) + f_of(cs[1][z])) + opq(f_of(cs[2][z]) + f_of(cs[3][z]))));
      auto p = __builtin_amdgcn_permlane16_swap(v, v, false, false);
      cz[z] = opq(f_of(p[0]) + f_of(p[1]));
    }
    const float s0 = (g & 1) ? cz[1] : cz[0], s1 = (g & 1) ? cz[2] : cz[1];
    const float h0 = rbf(s0), d0 = opq(s0 - h0), c0 = rbf(d0), h1 = rbf(s1), d1 = opq(s1 - h1), c1 = rbf(d1);
    tc[0] = make_uint2(pack2(h0, c0), pack2(opq(d0 - c0), 0.f)); tc[1] = make_uint2(pack2(h1, c1), pack2(opq(d1 - c1), 0.f));
    asm volatile("" : "+v"(tc[0].x), "+v"(tc[0].y), "+v"(tc[1].x), "+v"(tc[1].y));
#pragma unroll
    for (int j = 0; j < 2; ++j) {
      const unsigned nm = (rec[j][0] & 0xffffu) ^ 0x8000u;
      tq[j] = make_uint2(nm | (nm << 16), __float_as_uint(__builtin_amdgcn_rcpf(f_of(rec[j][2]))));
      asm volatile("" : "+v"(tq[j].x), "+v"(tq[j].y));
    }
    ta[0] = lds_addr(smem + L_CSF + ((g & 1) * 64 + F * 32 + (g >> 1) * 16 + li) * 8);
    ta[1] = lds_addr(smem + L_QST + (F * 72 + lane) * 8);
    ta[2] = lds_addr(smem + (lane < 8 ? F_SEL + lane * 4 : lane == 8 ? F_SELOK : F_ST + F * 4));
    asm volatile("" : "+v"(ta[0]), "+v"(ta[1]), "+v"(ta[2]));
    SCHED0();
  };
  auto tables = [&](unsigned now) {
    asm volatile("s_and_b32 %0, %0, %8\n\ts_cbranch_scc0 1f\n\tds_write2_b64 %1, %2, %9 offset1:64\n\tds_write2_b64 %3, %4, %5 offset1:8\n\ts_mov_b64 exec, 0x3ff\n\tds_write_b32 %6, %7\n\ts_mov_b64 exec, -1\n\t1:"
                 : "+s"(now) : "v"(ta[0]), "v"(tc[0]), "v"(ta[1]), "v"(tq[0]), "v"(tq[1]), "v"(ta[2]), "v"(tv), "s"(todo), "v"(tc[1]) : "scc", "memory");
    todo &= ~now;
  };
  auto slice = [&]() {
    if (nb < NBAR) tick(nb);
    else { const u32x2 fl = *(const volatile LAS u32x2*)(LAS const char*)(smem + F_FULL); tables(uni(min(fl[0], fl[1])) >> 4); SCHED0(); }
  };
  if (u >= 28) {
    u32x4 kd[4];
#pragma unroll 1
    for (unsigned n = 0; n < SPIN_CAP; ++n) {
#define L1K(i, o) "buffer_load_dwordx4 %" #i ", %4, %5, 0 offen offset:" #o " sc1\n\t"
      asm volatile("s_nop 4\n\t" L1K(0, 0) L1K(1, 1024) L1K(2, 2048) L1K(3, 3072)
                   : "=&v"(kd[0]), "=&v"(kd[1]), "=&v"(kd[2]), "=&v"(kd[3]) : "v"(WS_K + F * 4096 + lane * 16), "s"(wrs) : "memory");
#undef L1K
      tick(nb);
      asm volatile("s_waitcnt vmcnt(0)" : "+v"(kd[0]), "+v"(kd[1]), "+v"(kd[2]), "+v"(kd[3]) :: "memory");
      if (__all(chk(chk(chk(chk(0u, kd[0], tag), kd[1], tag), kd[2], tag), kd[3], tag) == 0)) break;
      NAP(4);
    }
    unsigned m1 = 0, m2 = 0, m3 = 0, m4 = 0;
#pragma unroll
    for (int j = 0; j < 4; ++j) {
      kd[j] = u32x4{(kd[j][0] << 16) | (15 - 4 * j), (kd[j][0] & 0xffff0000u) | (14 - 4 * j), (kd[j][2] << 16) | (13 - 4 * j), (kd[j][2] & 0xffff0000u) | (12 - 4 * j)};
#pragma unroll
      for (int i = 0; i < 4; ++i) {
        const unsigned v = kd[j][i];
        m4 = med3(m3, m4, v); m3 = med3(m2, m3, v); m2 = med3(m1, m2, v); m1 = max(m1, v);
      }
    }
    const unsigned lb = ((63 - lane) << 5) | (F ? 0 : 16);
    m1 |= lb; m2 |= lb; m3 |= lb; m4 |= lb;
    asm volatile("" : "+v"(m1), "+v"(m2), "+v"(m3), "+v"(m4));
    tick(nb);
    unsigned best = 0;
#pragma unroll
    for (int r = 0; r < 8; ++r) {
      const unsigned wm = wave_max_u32(m1);
      best = (lane & 7) == r ? wm : best;
      const bool won = m1 == wm;
      m1 = won ? m2 : m1; m2 = won ? m3 : m2; m3 = won ? m4 : m3; m4 = won ? 0u : m4;
      if (r == 3) { asm volatile("" : "+v"(m1), "+v"(m2), "+v"(m3), "+v"(m4), "+v"(best)); tick(nb); }
    }
    if (__any(m1 == 0)) {
      unsigned wm = 0;
#pragma unroll 1
      for (int r = 0; r < 8; ++r) {
        if (r & 1) tick(nb);
        unsigned m = 0;
#pragma unroll
        for (int j = 0; j < 4; ++j)
#pragma unroll
          for (int i = 0; i < 4; ++i) m = max(m, (kd[j][i] | lb) - wm);
        wm += wave_max_u32(m);
        best = (lane & 7) == r ? wm : best;
      }
    }
    *(volatile LAS unsigned*)(LAS char*)(smem + F_CND + (F ? 60 - (lane & 7) * 4 : (lane & 7) * 4)) = best + 1;
    unsigned d;
    const unsigned ct = 2047u - (best & 0xffffu);
    asm volatile("buffer_load_dword %0, %1, %2, 0 offen\n\tbuffer_load_dword %0, %3, %4, 0 offen sc1" : "=&v"(d)
                 : "v"(lane < 32 ? ct * 2048u + q * 512 + (lane >> 3) * 128 : 0x7fffffffu), "s"(xrs), "v"(lane < 8 ? WS_R + ct * 16 : 0x7fffffffu), "s"(wrs) : "memory");
    gather2();
    unsigned ka, kb;
#pragma unroll 1
    for (unsigned n = 0; n < SPIN_CAP; ++n) {
      tick(nb);
      ka = *(const volatile LAS unsigned*)(LAS const char*)(smem + F_CND + (lane & 7) * 4);
      kb = *(const volatile LAS unsigned*)(LAS const char*)(smem + F_CND + 32 + (lane & 7) * 4);
      if (__all(min(ka, kb) != 0)) break;
      NAP(1);
    }
    const unsigned selkey = max(ka, kb) - 1, tok = 2047u - (selkey & 0xffffu);
    u32x4 xf[2], trec;
#pragma unroll 1
    for (unsigned n = 0; n < SPIN_CAP; ++n) {
      asm volatile("buffer_load_dwordx4 %0, %3, %4, 0 offen\n\tbuffer_load_dwordx4 %1, %3, %4, 0 offen offset:64\n\tbuffer_load_dwordx4 %2, %5, %6, 0 offen sc1"
                   : "=&v"(xf[0]), "=&v"(xf[1]), "=&v"(trec) : "v"(tok * 2048u + q * 512 + (li >> 3) * 256 + g * 16 + F * 128), "s"(xrs), "v"(WS_R + tok * 16), "s"(wrs) : "memory");
      if (n) tick(nb);
      asm volatile("s_waitcnt vmcnt(0)" : "+v"(xf[0]), "+v"(xf[1]), "+v"(trec), "+v"(d) :: "memory"); PING2();
      if (__all(chk(0u, trec, tag) == 0)) break;
      NAP(1);
    }
    const float mb = lo2f(trec[0]), rd = 1.0f / f_of(trec[2]);
#pragma unroll
    for (int s = 0; s < 2; ++s)
#pragma unroll
      for (int j = 0; j < 4; ++j) xf[s][j] = ln2s(xf[s][j], mb, rd);
    st16c(wrs, xs_off + F * 3072, u32x4{xf[0][0], xf[0][1], xf[0][2], tag});
    st16c(wrs, xs_off + F * 3072 + 1024, u32x4{xf[1][0], xf[1][1], xf[1][2], tag});
    st16c(wrs, xs_off + F * 3072 + 2048, u32x4{xf[0][3], xf[1][3], selkey, tag});
    tv = lane < 8 ? selkey : 1u;
    tick(nb);
    asm volatile("s_waitcnt vmcnt(0)\n\tbuffer_load_dword %0, %1, %2, 0 offen sc1" : "=&v"(d) : "v"(lane < 24 ? xs_off + F * 3072 + lane * 112 : 0x7fffffffu), "s"(wrs) : "memory");
    prepare();
    tick(nb); NAP(8);
    asm volatile("buffer_load_dword %0, %1, %2, 0 offen" : "+v"(td) : "v"(wa), "s"(prs) : "memory");
    tick(nb); tick(nb);
    asm volatile("s_waitcnt vmcnt(0)" : "+v"(d), "+v"(td) :: "memory");
    while (nb < NBAR) tick(nb);
    __builtin_amdgcn_s_setprio(1);
    wait_full(smem, F_FULL, 16);
    tables(1);
  } else {
    u32x4 xs[6];
    auto ask = [&]() {
#define X1K(i, o) "buffer_load_dwordx4 %" #i ", %6, %7, 0 offen offset:" #o " sc1\n\t"
#define X2K(i, o) "buffer_load_dwordx4 %" #i ", %6, %7, %8 offen offset:" #o " sc1\n\t"
      asm volatile(X1K(0, 0) X1K(1, 1024) X1K(2, 2048) X1K(3, 3072) X2K(4, 0) X2K(5, 1024)
                   : "=&v"(xs[0]), "=&v"(xs[1]), "=&v"(xs[2]), "=&v"(xs[3]), "=&v"(xs[4]), "=&v"(xs[5]) : "v"(xs_off), "s"(wrs), "s"(4096) : "memory");
      SCHED0();
    };
    auto good = [&]() {
      unsigned bad = xs[0][3] ^ tag;
#pragma unroll
      for (int j = 1; j < 6; ++j) bad = __builtin_amdgcn_bitop3_b32(bad, xs[j][3], tag, 0xF6);
      return __all(bad == 0);
    };
#define PIN6(a) asm volatile("" : "+v"(a[0]), "+v"(a[1]), "+v"(a[2]), "+v"(a[3]), "+v"(a[4]), "+v"(a[5]))
#define WAITS(snap, n) asm volatile("s_waitcnt vmcnt(%2)\n\ts_cmp_lg_u32 %0, %1\n\ts_cbranch_scc1 1f\n\ts_waitcnt vmcnt(0)\n\t1:" : "+s"(iss) : "s"(snap), "i"(n) : "scc", "memory")
    asm volatile("" : "=v"(cs[0]), "=v"(cs[1]), "=v"(cs[2]), "=v"(cs[3]), "=v"(rec[0]), "=v"(rec[1]));
    CH(0, 10 - 2, 3);
    tick(nb);
    CH(0, 10 - 2, 99);
    tick(nb);
    CH(1, 10 - 2, nb);
    tick(nb);
    WAITS(1, 14); PIN8(fw[0]); PIN8(fw[1]);
    tr_all(fw[0]);
    CH(1, 10 - 7, nb);
    tick(nb);
    tr_all(fw[1]);
    CH(1, 10 - 7, nb);
    tick(nb);
    CH(1, 10 - 2, 99);
    tick(nb);
    CH(2, 10 - 2, nb);
    tick(nb);
    WAITS(2, 9); PING2(); PIN8(fw[2]);
    ask();
    prepare();
    unsigned snap = iss;
    asm volatile("" : "+s"(snap));
    CH(2, 3, nb);
    tick(nb);
    WAITS(snap, 9); PIN6(xs);
    if (!good()) {
      CH(2, 0, 99);
#pragma unroll 1
      for (unsigned n = 0; n < SPIN_CAP; ++n) {
        ask(); tick(nb); WAITV(0); PIN6(xs);
        if (good()) break;
        NAP(4);
      }
    }
    tv = lane < 8 ? xs[2][2] : 1u;
    xs[0][3] = xs[2][0]; xs[1][3] = xs[2][1]; xs[3][3] = xs[5][0]; xs[4][3] = xs[5][1];
    PIN6(xs); asm volatile("" : "+v"(tv));
    if (nb == NBAR) { __builtin_amdgcn_s_setprio(1); wait_full(smem, F_FULL, 16); tables(1); }
    f32x4 acc[8];
    auto mma = [&](int s) {
      SCHED0(); __builtin_amdgcn_s_setprio(3); SCHED0();
#pragma unroll
      for (int i = 0; i < 8; ++i) acc[i] = MFMA16(__builtin_bit_cast(bf16x8, fw[s][i]), __builtin_bit_cast(bf16x8, xs[s + (s >> 1)]), s ? acc[i] : (f32x4{0, 0, 0, 0}));
      SCHED0(); __builtin_amdgcn_s_setprio(0); SCHED0();
      PIN8(acc);
    };
    mma(0);
    tr_all(fw[2]);
    CH(2, 10 - 9, 99);
    slice();
    mma(1);
    mma(2);
    slice();
    WAITV(0); PIN8(fw[3]); asm volatile("" : "+v"(td));
    tr_all(fw[3]);
    mma(3);
    {
#pragma unroll
      for (int r = 0; r < 4; ++r) {
        float v[2];
#pragma unroll
        for (int z = 0; z < 2; ++z) {
          auto a = __builtin_amdgcn_permlane32_swap(__float_as_uint(acc[z][r]), __float_as_uint(acc[z + 4][r]), false, false);
          auto b = __builtin_amdgcn_permlane32_swap(__float_as_uint(acc[z + 2][r]), __float_as_uint(acc[z + 6][r]), false, false);
          v[z] = f_of(li < 8 ? a[0] : b[1]) + dpp_f<0x128>(f_of(li < 8 ? b[0] : a[1]));
        }
        st16c(wrs, WS_GU + (unsigned)(((cu * 4 + q) * 2 + F) * 4096 + r * 1024 + lane * 16), u32x4{__float_as_uint(v[0]), tag, __float_as_uint(v[1]), tag});
      }
      SCHED0();
    }
    while (nb < NBAR) tick(nb);
    __builtin_amdgcn_s_setprio(1);
    u32x4 fd[2][8];
    {
      const rsrc_t drs = mk_rsrc(wd, DM * FF * 2);
      const unsigned dof = (unsigned)(cg * 64 + 8 * g) * (DM * 2) + q * 512 + F * 256 + li * 16;
      NAP(6);
#pragma unroll
      for (int s = 0; s < 2; ++s)
#pragma unroll
        for (int j = 0; j < 8; ++j) fd[s][j] = __builtin_amdgcn_raw_buffer_load_b128(drs, dof + (j & 1) * (DM * 2), (s * 32 + (j & 6)) * (DM * 2), 2);
      SCHED0();
    }
    wait_full(smem, F_FULL, 16);
    tables(1);
    if (bid == 0 && F == 0 && lane == 0) __hip_atomic_store((GAS unsigned*)(ws + WS_EP), tag, RLX_AGENT);
    hidden_half(smem, lane, F, cg, ws, tag, fd);
    wait_bytes<0x8080u>(smem, F_H, 1);
    const bf16x8 h0 = lds_b128(smem + L_HF + (li & 7) * 128 + g * 16), h1 = lds_b128(smem + L_HF + (li & 7) * 128 + 64 + g * 16);
#pragma unroll
    for (int i = 0; i < 8; ++i) {
      acc[i] = MFMA16(h0, __builtin_bit_cast(bf16x8, fd[0][i]), (f32x4{0, 0, 0, 0}));
      acc[i] = MFMA16(h1, __builtin_bit_cast(bf16x8, fd[1][i]), acc[i]);
    }
    const unsigned off = WS_Y + (unsigned)((cg * 8 + (g & 1) * 4) * DM + q * 256 + F * 128 + li * 8 + (g >> 1) * 4) * 4u;
#pragma unroll
    for (int r = 0; r < 4; ++r) {
      const unsigned lo = pack2(acc[0][r], acc[1][r]), hi = pack2(acc[2][r], acc[3][r]), lo2 = pack2(acc[4][r], acc[5][r]), hi2 = pack2(acc[6][r], acc[7][r]);
      st16c(wrs, off + r * (DM * 4), u32x4{g < 2 ? lo : lo2, tag, g < 2 ? hi : hi2, tag});
    }
  }
  __builtin_amdgcn_s_waitcnt(0x0F70);
}

__global__ __launch_bounds__(512, 2) void fused_block(const u16* __restrict__ x, const u16* __restrict__ wqkv, const u16* __restrict__ wo,
                                                      const u16* __restrict__ wr, const u16* __restrict__ wg, const u16* __restrict__ wu,
                                                      const u16* __restrict__ wd, char* ws, u16* __restrict__ out) {
  __shared__ __attribute__((aligned(128))) char smem[L_SIZE];
  unsigned long long pc;
  unsigned first;
  asm volatile("v_cmp_gt_u32 vcc, 24, %2\n\ts_getpc_b64 %0\n\ts_cbranch_vccz 1f\n\ts_mov_b64 exec, vcc\n\tv_lshlrev_b32 %1, 7, %2\n\tglobal_load_dword %1, %1, %0\n\ts_mov_b64 exec, -1\n\t1:"
               : "=s"(pc), "=&v"(first) : "v"(threadIdx.x) : "vcc", "memory");
  SCHED0();
  const int lane = threadIdx.x & 63;
  const int wid = __builtin_amdgcn_readfirstlane(threadIdx.x >> 6);
  const int bid = blockIdx.x, xcd = bid & 7, off = bid >> 3;
  const int tm = (xcd & 1) * 8 + (xcd & 2) * 2 + (off & 3), tn = (xcd >> 2) * 8 + (off >> 2);
  if (wid == 7) {
    unsigned d;
    asm volatile("s_getpc_b64 %0" : "=s"(pc));
    asm volatile("buffer_load_dword %0, %1, %2, 0 offen\n\ts_waitcnt vmcnt(0)" : "=&v"(d)
                 : "v"((unsigned)(lane * 32 + off) * 128u), "s"(mk_rsrc((const void*)(pc & ~255ull), CODE_BYTES)) : "memory");
  }
  asm volatile("" : "+s"(x), "+s"(wqkv), "+s"(wr), "+s"(ws), "+s"(out), "+s"(wo), "+s"(wd), "+s"(wg), "+s"(wu));
  unsigned rot2 = ((((unsigned)(0x65432106543210ull >> (off & 3) * 8) & 0xfffffffu) + ((tn & 7) + 1) * 0x1111111u) & 0x7777777u) * 2 | (unsigned)tn << 29;
  unsigned tag = 0;
  RowRegs rr;
  EpiRegs e;
  row_request(rr, x, wr, ws, tm * 128 + tn * 8 + wid * 2, lane, wid < 4);
  rr.keep = first;
  SCHED0();
  if ((wid >> 1) == 2) {
    __builtin_amdgcn_s_setprio(2);
    const Stager stage(smem, wid - 4, lane, tm, tn, x, wqkv);
    NAP(4);
    stage_x(stage, 0); stage_w(stage, 0); stage_x(stage, 1); stage_w(stage, 1);
    WAITV(21);
    BAR();
    stage_x(stage, 2); stage_w(stage, 2);
  }
  asm volatile("" ::: "memory");
  if (wid < 4) {
    __builtin_amdgcn_s_setprio(2);
    qkv_setup(smem, wid, lane, tm, tn, e);
    asm volatile("" : "+s"(rot2));
    SCHED0();
    tag = row_stats(rr, ws, tm * 128 + tn * 8 + wid * 2, lane);
    if (wid == 0) *(unsigned*)(smem + L_FLG + lane * 4) = lane * 4 == F_TAG - L_FLG ? tag : 0u;
  }
  asm volatile("" ::: "memory");
  if (wid < 4) {
    if (wid & 1) qkv_attention<1>(smem, wid, lane, tm, tn, ws, tag, x, e);
    else qkv_attention<0>(smem, wid, lane, tm, tn, ws, tag, x, e);
    out_proj(smem, wid, lane, tm, tn, ws, tag, out, e, rot2);
  } else {
    if (wid < 6) loader(smem, wid - 4, lane, tm, tn, rot2, x, wqkv, wo, ws);
    else side_wave(smem, wid - 6, lane, bid, tm, tn, x, wid == 6 ? wg : wu, wd, wo, ws);
    updates(smem, (0x2130 >> ((wid - 4) * 4)) & 3, lane, tm, tn, ws);
    __builtin_amdgcn_s_waitcnt(0x0F70);
  }
}

extern "C" void kernel_launch(void* const* d_in, const int* in_sizes, int n_in, void* d_out, int out_size, void* d_ws,
                              size_t ws_size, hipStream_t stream) {
  fused_block<<<256, 512, 0, stream>>>((const u16*)d_in[0], (const u16*)d_in[1], (const u16*)d_in[2], (const u16*)d_in[4],
                                       (const u16*)d_in[5], (const u16*)d_in[6], (const u16*)d_in[7], (char*)d_ws, (u16*)d_out);
}
```
